```python
import torch

def _softmax(x, axis=-1):
    return torch.softmax(x, dim=axis)
def _gelu(x):
    return torch.nn.functional.gelu(x, approximate="tanh")
def _silu(x):
    return torch.nn.functional.silu(x)
def _sigmoid(x):
    return torch.sigmoid(x)
def _layernorm(x, axis=-1, eps=1e-5):
    m = x.mean(dim=axis, keepdim=True)
    v = x.var(dim=axis, keepdim=True, unbiased=False)
    return (x - m) / torch.sqrt(v + eps)
def _scatter_add(tgt, idx, src, axis=0):
    return tgt.clone().index_add_(axis, idx, src)
def _segment_sum(src, seg, n):
    out = torch.zeros((n,) + src.shape[1:], dtype=src.dtype)
    return out.index_add_(0, seg, src)
def _pad(x, pw):
    flat = tuple(v for pair in reversed(pw) for v in pair)
    return torch.nn.functional.pad(x, flat)

kernel_name = 'parallel_sum__compressed_kv_cla__dense_activation_gelu__prompt'

def setup_inputs():

    _w = (lambda a: (lambda t: t.to(torch.bfloat16) if t.is_floating_point() else t)(torch.as_tensor(a)))
    def _A_setup():
        B, H, Sq, Sk, D, M = 1, 32, 2048, 2048, 128, 1024
        Dc = 4 * D
        return {
            "x": _w((lambda *s: torch.randn(*s, dtype=torch.bfloat16) * 0.05)(B, Sq, M)),
            "c_kv": _w((lambda *s: torch.randn(*s, dtype=torch.bfloat16) * 0.4)(B, Sk, Dc)),
            "w_q": _w((lambda *s: torch.randn(*s, dtype=torch.bfloat16) * 3.2)(M, H, D) * (M ** -0.5)),
            "w_uk": _w((lambda *s: torch.randn(*s, dtype=torch.bfloat16) * 3.2)(Dc, H, D) * (Dc ** -0.5)),
            "w_uv": _w((lambda *s: torch.randn(*s, dtype=torch.bfloat16) * 0.4)(Dc, H, D) * (Dc ** -0.5)),
            "w_o": _w((lambda *s: torch.randn(*s, dtype=torch.bfloat16) * 0.4)(H, D, M) * ((H * D) ** -0.5)),
        }
    _A_in = _A_setup()

    def _B_setup():
        B, S, D, F = 1, 2048, 1024, 3584
        return {
            "x": _w((lambda *s: torch.randn(*s, dtype=torch.bfloat16) * 0.05)(B, S, D)),
            "w1": _w((lambda *s: torch.randn(*s, dtype=torch.bfloat16) * 0.4)(D, F) * (D ** -0.5)),
            "b1": _w((lambda *s: torch.randn(*s, dtype=torch.bfloat16) * 12.8)(F) * 0.01),
            "w2": _w((lambda *s: torch.randn(*s, dtype=torch.bfloat16) * 0.2)(F, D) * (F ** -0.5)),
            "b2": _w((lambda *s: torch.randn(*s, dtype=torch.bfloat16) * 1.6)(D) * 0.01),
        }
    _B_in = _B_setup()

    return {
        "A_x": _A_in["x"],
        "A_c_kv": _A_in["c_kv"],
        "A_w_q": _A_in["w_q"],
        "A_w_uk": _A_in["w_uk"],
        "A_w_uv": _A_in["w_uv"],
        "A_w_o": _A_in["w_o"],
        "B_x": _B_in["x"],
        "B_w1": _B_in["w1"],
        "B_b1": _B_in["b1"],
        "B_w2": _B_in["w2"],
        "B_b2": _B_in["b2"],
    }

def reference(A_x, A_c_kv, A_w_q, A_w_uk, A_w_uv, A_w_o, B_x, B_w1, B_b1, B_w2, B_b2):
    def _A_ref(x, c_kv, w_q, w_uk, w_uv, w_o):
        B, Sq, M = x.shape[0], x.shape[1], x.shape[2]
        Sk = c_kv.shape[1]
        H, D = w_q.shape[1], w_q.shape[2]
        q = torch.einsum("bqm,mhd->bhqd", x, w_q)
        k = torch.einsum("bkc,chd->bhkd", c_kv, w_uk)
        v = torch.einsum("bkc,chd->bhkd", c_kv, w_uv)
        scores = torch.einsum("bhqd,bhkd->bhqk", q, k) * (D ** -0.5)
        off = Sk - Sq
        qi = torch.arange(Sq)[:, None]
        kj = torch.arange(Sk)[None, :]
        cmask = kj <= (qi + off)
        scores = torch.where(cmask[None, None], scores, -1e9)
        p = _softmax(scores, axis=-1)
        ctx = torch.einsum("bhqk,bhkd->bhqd", p, v)
        return torch.einsum("bhqd,hdm->bqm", ctx, w_o)

    def _B_ref(x, w1, b1, w2, b2):
        h = torch.einsum("bsd,df->bsf", x, w1) + b1
        h = _gelu(h)
        return torch.einsum("bsf,fd->bsd", h, w2) + b2

    x = A_x
    xn = _layernorm(x, axis=-1)
    return x + _A_ref(xn, A_c_kv, A_w_q, A_w_uk, A_w_uv, A_w_o) + _B_ref(xn, B_w1, B_b1, B_w2, B_b2)
```

```cpp
#include <hip/hip_runtime.h>
#include <cstdint>

#define LAS __attribute__((address_space(3)))
#define DI __device__ __forceinline__
typedef unsigned short bf16_t;
typedef short bf16x8 __attribute__((ext_vector_type(8)));
typedef short s16x4 __attribute__((ext_vector_type(4)));
typedef short v4i16_t __attribute__((ext_vector_type(4)));
typedef float f32x2 __attribute__((ext_vector_type(2)));
typedef float f32x4 __attribute__((ext_vector_type(4)));
typedef float f32x16 __attribute__((ext_vector_type(16)));
typedef unsigned u32x2 __attribute__((ext_vector_type(2)));
typedef unsigned u32x4 __attribute__((ext_vector_type(4)));
typedef __bf16 bf16x2_t __attribute__((ext_vector_type(2)));
typedef LAS unsigned char* lds_ptr;
typedef LAS const unsigned char* lds_cptr;

constexpr int SEQ = 2048, DM = 1024, NH = 32, HD = 128, DC = 512, FF = 3584, NQ = NH * HD;
constexpr size_t MiB = 1u << 20;
constexpr size_t WS_FLAG = 0;
constexpr size_t WS_XN = 1 * MiB;
constexpr size_t WS_Q = 8 * MiB;
constexpr size_t WS_K = 24 * MiB;
constexpr size_t WS_VT = 40 * MiB;
constexpr size_t WS_CTX = 56 * MiB;
constexpr size_t WS_H = 72 * MiB;
constexpr size_t WS_PART = 88 * MiB;

DI unsigned cvtpk(float lo, float hi) { f32x2 v = {lo, hi}; bf16x2_t b = __builtin_convertvector(v, bf16x2_t); return __builtin_bit_cast(unsigned, b); }
DI float bflo(unsigned w) { return __uint_as_float(w << 16); }
DI float bfhi(unsigned w) { return __uint_as_float(w & 0xffff0000u); }
DI s16x4 lds_tr(lds_cptr p) { return __builtin_bit_cast(s16x4, __builtin_amdgcn_ds_read_tr16_b64_v4i16((LAS v4i16_t*)p)); }
DI bf16x8 lds_rd(lds_cptr p) { return *(const LAS bf16x8*)p; }
DI bf16x8 lds_rdv(lds_cptr p) { return *(const volatile LAS bf16x8*)p; }
DI bf16x8 cat8(s16x4 a, s16x4 b) { return __builtin_shufflevector(a, b, 0, 1, 2, 3, 4, 5, 6, 7); }
#define SBAR() __builtin_amdgcn_sched_barrier(0)
#define PIN(x) asm volatile("" : "+v"(x))
#define WAIT_V(n) asm volatile("s_waitcnt vmcnt(" #n ")" ::: "memory")
#define WAIT_L(n) asm volatile("s_waitcnt lgkmcnt(" #n ")" ::: "memory")
#define BAR() __builtin_amdgcn_s_barrier()
#define MFMA16(a, b, c) __builtin_amdgcn_mfma_f32_16x16x32_bf16(a, b, c, 0, 0, 0)
#define MFMA32(a, b, c) __builtin_amdgcn_mfma_f32_32x32x16_bf16(a, b, c, 0, 0, 0)
#define GLDS(g, l) __builtin_amdgcn_global_load_lds((const unsigned*)(g), (LAS unsigned*)(l), 16, 0, 0)

__global__ __launch_bounds__(256) void k0_ln(const bf16_t* __restrict__ x, bf16_t* __restrict__ xn, unsigned* __restrict__ flags) {
    const int lane = threadIdx.x & 63, row = blockIdx.x * 4 + (threadIdx.x >> 6);
    const u32x4* src = (const u32x4*)(x + (size_t)row * DM);
    const u32x4 v0 = src[lane], v1 = src[lane + 64];
    float f[16];
#pragma unroll
    for (int i = 0; i < 4; ++i) { f[2 * i] = bflo(v0[i]); f[2 * i + 1] = bfhi(v0[i]); f[8 + 2 * i] = bflo(v1[i]); f[8 + 2 * i + 1] = bfhi(v1[i]); }
    float s = 0.f;
#pragma unroll
    for (int i = 0; i < 16; ++i) s += f[i];
#pragma unroll
    for (int o = 32; o >= 1; o >>= 1) s += __shfl_xor(s, o);
    const float mean = s * (1.0f / DM);
    float q = 0.f;
#pragma unroll
    for (int i = 0; i < 16; ++i) { f[i] -= mean; q += f[i] * f[i]; }
#pragma unroll
    for (int o = 32; o >= 1; o >>= 1) q += __shfl_xor(q, o);
    const float rstd = 1.0f / sqrtf(q * (1.0f / DM) + 1e-5f);
    u32x4 w0, w1;
#pragma unroll
    for (int i = 0; i < 4; ++i) { w0[i] = cvtpk(f[2 * i] * rstd, f[2 * i + 1] * rstd); w1[i] = cvtpk(f[8 + 2 * i] * rstd, f[8 + 2 * i + 1] * rstd); }
    const int mh = row >> 7, r = row & 127, c = lane & 7, kt = lane >> 3, sw = (c ^ ((r >> 1) & 7)) * 16;
    char* base = (char*)xn + (size_t)mh * 16 * 16384 + r * 128 + sw;
    *(u32x4*)(base + (size_t)kt * 16384) = w0;
    *(u32x4*)(base + (size_t)(kt + 8) * 16384) = w1;
    if (blockIdx.x == 0 && threadIdx.x < 128) flags[threadIdx.x * 64] = 0u;
}

namespace k1 {
constexpr int HTB = 16384, LDS_BYTES = 8 * HTB;
enum { T_K = 0, T_V = 1, T_Q = 2, T_H = 3 };
struct Src {
    const char* a; const char* b;
    unsigned aK, aH, aD, bK, bD;
    unsigned va, vb;
};
struct Frag { bf16x8 At[4][2], B0[2][2], B1[2][2]; };

DI float gelu_tanh(float v) {
    const float z = v * (v * v * -0.10294324f + -2.3022082f);
    return v * __builtin_amdgcn_rcpf(1.0f + __builtin_amdgcn_exp2f(z));
}

#define K1_SA(b, h) (((b) * 2 + (h)) * HTB)
#define K1_SB(b, h) ((4 + (b) * 2 + (h)) * HTB)
#define K1_STAGE(bufoff, gbase, voff, d2) do { GLDS((const char*)(gbase) + (voff), lds + (bufoff) + ldsw); GLDS((const char*)(gbase) + (d2) + (voff), lds + (bufoff) + ldsw + 8192); } while (0)
#define K1_LDA(dst, b, h) do { _Pragma("unroll") for (int m = 0; m < 4; ++m) { dst[m][0] = lds_rd(lds + K1_SA(b, h) + aoff0 + m * 2048); dst[m][1] = lds_rd(lds + K1_SA(b, h) + aoff1 + m * 2048); } } while (0)
#define K1_LDB(dst, b, h) do { _Pragma("unroll") for (int k = 0; k < 2; ++k) { \
        dst[0][k] = cat8(lds_tr(lds + K1_SB(b, h) + boff0 + k * 8192), lds_tr(lds + K1_SB(b, h) + boff0 + k * 8192 + 256)); \
        dst[1][k] = cat8(lds_tr(lds + K1_SB(b, h) + boff1 + k * 8192), lds_tr(lds + K1_SB(b, h) + boff1 + k * 8192 + 256)); } } while (0)
#define K1_MMA(ai, bj, At, Bt) do { __builtin_amdgcn_s_setprio(1); _Pragma("unroll") for (int m = 0; m < 4; ++m) _Pragma("unroll") for (int n = 0; n < 2; ++n) _Pragma("unroll") for (int k = 0; k < 2; ++k) \
        acc[ai][bj][m][n] = SWAP ? MFMA16(At[m][k], Bt[n][k], acc[ai][bj][m][n]) : MFMA16(Bt[n][k], At[m][k], acc[ai][bj][m][n]); __builtin_amdgcn_s_setprio(0); } while (0)

template <bool SWAP>
DI void kloop(lds_ptr lds, f32x4 (&acc)[2][2][4][2], const Src& cur, const Src& nxt, int nt, unsigned ldsw, int aoff0, int aoff1, int boff0, int boff1) {
    bf16x8 At[4][2], B0[2][2], B1[2][2];
    for (int t = 0; t < nt; t += 2) {
        const bool last = (t == nt - 2);
        const char* a1h = cur.a + (size_t)(t + 1) * cur.aK + cur.aH;
        const char* a2 = last ? nxt.a : cur.a + (size_t)(t + 2) * cur.aK; const char* b2 = last ? nxt.b : cur.b + (size_t)(t + 2) * cur.bK;
        const unsigned aK = last ? nxt.aK : cur.aK, aH = last ? nxt.aH : cur.aH, aD = last ? nxt.aD : cur.aD, bK = last ? nxt.bK : cur.bK, bD = last ? nxt.bD : cur.bD;
        const unsigned va = last ? nxt.va : cur.va, vb = last ? nxt.vb : cur.vb;
        const char* a3 = a2 + aK; const char* b3 = b2 + bK;
        K1_LDB(B0, 0, 0); K1_LDB(B1, 0, 1); SBAR(); K1_LDA(At, 0, 0); K1_STAGE(K1_SA(1, 1), a1h, cur.va, cur.aD);
        WAIT_V(8); WAIT_L(0); BAR(); K1_MMA(0, 0, At, B0); K1_MMA(0, 1, At, B1); BAR(); SBAR();
        K1_LDA(At, 0, 1); K1_STAGE(K1_SB(0, 0), b2, vb, bD); K1_STAGE(K1_SB(0, 1), b2 + 256, vb, bD); K1_STAGE(K1_SA(0, 0), a2, va, aD);
        WAIT_V(8); WAIT_L(0); BAR(); K1_MMA(1, 0, At, B0); K1_MMA(1, 1, At, B1); BAR(); SBAR();
        K1_LDB(B0, 1, 0); K1_LDB(B1, 1, 1); SBAR(); K1_LDA(At, 1, 0); K1_STAGE(K1_SA(0, 1), a2 + aH, va, aD);
        WAIT_V(8); WAIT_L(0); BAR(); K1_MMA(0, 0, At, B0); K1_MMA(0, 1, At, B1); BAR(); SBAR();
        K1_LDA(At, 1, 1); K1_STAGE(K1_SB(1, 0), b3, vb, bD); K1_STAGE(K1_SB(1, 1), b3 + 256, vb, bD); K1_STAGE(K1_SA(1, 0), a3, va, aD);
        WAIT_V(8); WAIT_L(0); BAR(); K1_MMA(1, 0, At, B0); K1_MMA(1, 1, At, B1); BAR(); SBAR();
    }
}

DI void zero_acc(f32x4 (&acc)[2][2][4][2]) {
#pragma unroll
    for (int a = 0; a < 2; ++a)
#pragma unroll
        for (int b = 0; b < 2; ++b)
#pragma unroll
            for (int m = 0; m < 4; ++m)
#pragma unroll
                for (int n = 0; n < 2; ++n) acc[a][b][m][n] = (f32x4){0.f, 0.f, 0.f, 0.f};
}
DI u32x4 pack_swap16(f32x4 v0, f32x4 v1) {
    const unsigned x0 = cvtpk(v0[0], v0[1]), x1 = cvtpk(v0[2], v0[3]), y0 = cvtpk(v1[0], v1[1]), y1 = cvtpk(v1[2], v1[3]);
    const auto r0 = __builtin_amdgcn_permlane16_swap(x0, y0, false, false), r1 = __builtin_amdgcn_permlane16_swap(x1, y1, false, false);
    return (u32x4){r0[0], r1[0], r0[1], r1[1]};
}
DI u32x4 pack_swap32(f32x4 v0, f32x4 v1) {
    const unsigned x0 = cvtpk(v0[0], v0[1]), x1 = cvtpk(v0[2], v0[3]), y0 = cvtpk(v1[0], v1[1]), y1 = cvtpk(v1[2], v1[3]);
    const auto r0 = __builtin_amdgcn_permlane32_swap(x0, y0, false, false), r1 = __builtin_amdgcn_permlane32_swap(x1, y1, false, false);
    return (u32x4){r0[0], r1[0], r0[1], r1[1]};
}
DI void epi_qk(const f32x4 (&acc)[2][2][4][2], char* dst, int pm, int pn, float sc, int wr, int wc, int fr, int fq) {
    const int cg = ((fq & 1) << 1) | (fq >> 1);
    char* p = dst + (size_t)(2 * pn * 32 + 4 * pm + wr) * 16384 + (4 * wc + cg) * 1024 + fr * 16;
#pragma unroll
    for (int ai = 0; ai < 2; ++ai)
#pragma unroll
        for (int bj = 0; bj < 2; ++bj)
#pragma unroll
            for (int m = 0; m < 4; ++m)
                *(u32x4*)(p + (size_t)(bj * 32 + ai * 2) * 16384 + m * 256) = pack_swap16(acc[ai][bj][m][0] * sc, acc[ai][bj][m][1] * sc);
}
DI void epi_v(const f32x4 (&acc)[2][2][4][2], char* dst, int pm, int pn, int wr, int wc, int fr, int fq) {
    char* p = dst + (size_t)(2 * pn * 32 + 4 * pm + wr) * 16384 + fq * 2048 + (32 * wc + fr) * 16;
#pragma unroll
    for (int ai = 0; ai < 2; ++ai)
#pragma unroll
        for (int bj = 0; bj < 2; ++bj)
#pragma unroll
            for (int m = 0; m < 4; m += 2)
#pragma unroll
                for (int n = 0; n < 2; ++n)
                    *(u32x4*)(p + (size_t)(bj * 32 + ai * 2) * 16384 + m * 4096 + n * 256) = pack_swap32(acc[ai][bj][m][n], acc[ai][bj][m + 1][n]);
}
DI void epi_h(const f32x4 (&acc)[2][2][4][2], char* dst, const bf16_t* b1, int pm, int pn, int wr, int wc, int fr, int fq) {
    const int cg = ((fq & 1) << 1) | (fq >> 1);
    f32x4 bv[2][2];
#pragma unroll
    for (int bj = 0; bj < 2; ++bj)
#pragma unroll
        for (int n = 0; n < 2; ++n) { const u32x2 w = *(const u32x2*)(b1 + pn * 256 + bj * 128 + wc * 32 + n * 16 + fq * 4); bv[bj][n] = (f32x4){bflo(w[0]), bfhi(w[0]), bflo(w[1]), bfhi(w[1])}; }
    char* p = dst + ((size_t)(2 * pm * 56 + 4 * pn + (wc >> 1)) * 8 + 4 * (wc & 1) + cg) * 2048 + (64 * wr + fr) * 16;
#pragma unroll
    for (int ai = 0; ai < 2; ++ai)
#pragma unroll
        for (int bj = 0; bj < 2; ++bj)
#pragma unroll
            for (int m = 0; m < 4; ++m) {
                f32x4 v0 = acc[ai][bj][m][0] + bv[bj][0], v1 = acc[ai][bj][m][1] + bv[bj][1];
#pragma unroll
                for (int j = 0; j < 4; ++j) { v0[j] = gelu_tanh(v0[j]); v1[j] = gelu_tanh(v1[j]); }
                *(u32x4*)(p + (size_t)(ai * 56 + bj * 2) * 16384 + m * 256) = pack_swap16(v0, v1);
            }
}

__global__ __launch_bounds__(512, 2) void k1_proj(const bf16_t* __restrict__ ckv, const char* __restrict__ xn, const bf16_t* __restrict__ wuk, const bf16_t* __restrict__ wuv,
                                                  const bf16_t* __restrict__ wq, const bf16_t* __restrict__ w1, const bf16_t* __restrict__ b1,
                                                  char* __restrict__ Qo, char* __restrict__ Ko, char* __restrict__ VTo, char* __restrict__ Ho) {
    extern __shared__ __attribute__((aligned(16))) unsigned char smem[];
    lds_ptr lds = (lds_ptr)smem;
    const int tid = threadIdx.x, wid = __builtin_amdgcn_readfirstlane(tid >> 6), lane = tid & 63, wr = wid >> 2, wc = wid & 3, fr = lane & 15, fq = lane >> 4;
    const int xcd = blockIdx.x & 7, idx = blockIdx.x >> 3, pm = idx & 7, pj = xcd + 8 * (idx >> 3);
    const bool isV = pj >= 16, has2 = pj < 30, isH = pj >= 16;
    const unsigned ldsw = (unsigned)wid * 1024u;
    const int aoff0 = (wr * 64 + fr) * 128 + ((fq ^ (fr >> 1)) * 16), aoff1 = aoff0 ^ 64;
    const int boff0 = fq * 2048 + wc * 512 + (fr >> 2) * 64 + (fq & 1) * 32 + (fr & 3) * 8, boff1 = boff0 ^ 32;
    const unsigned brow = 8 * (wid >> 1) + ((lane & 31) >> 2), bcol = (64 * (wid & 1) + 32 * (lane >> 5) + 8 * ((lane & 3) ^ (wid & 2))) * 2;
    Src u0, u1;
    u0.a = (const char*)ckv + (size_t)pm * 256 * DC * 2; u0.aK = 128; u0.aH = 128 * DC * 2; u0.aD = 64 * DC * 2;
    u0.va = (tid >> 3) * (DC * 2) + (((tid & 7) ^ ((tid >> 4) & 7)) * 16);
    u0.b = (const char*)(isV ? wuv : wuk) + (pj & 15) * 512; u0.bK = 64 * NQ * 2; u0.bD = 32 * NQ * 2; u0.vb = brow * (NQ * 2) + bcol;
    if (has2) {
        const unsigned ldb2 = isH ? FF * 2 : NQ * 2;
        u1.a = xn + (size_t)pm * 2 * 16 * 16384; u1.aK = 16384; u1.aH = 16 * 16384; u1.aD = 8192; u1.va = tid * 16;
        u1.b = (const char*)(isH ? w1 : wq) + (pj & 15) * 512; u1.bK = 64 * ldb2; u1.bD = 32 * ldb2; u1.vb = brow * ldb2 + bcol;
    } else u1 = u0;
    f32x4 acc[2][2][4][2];
    zero_acc(acc);
    K1_STAGE(K1_SB(0, 0), u0.b, u0.vb, u0.bD); K1_STAGE(K1_SB(0, 1), u0.b + 256, u0.vb, u0.bD); K1_STAGE(K1_SA(0, 0), u0.a, u0.va, u0.aD); K1_STAGE(K1_SA(0, 1), u0.a + u0.aH, u0.va, u0.aD);
    if (wr == 1) BAR();
    WAIT_V(2); BAR();
    K1_STAGE(K1_SB(1, 0), u0.b + u0.bK, u0.vb, u0.bD); K1_STAGE(K1_SA(1, 0), u0.a + u0.aK, u0.va, u0.aD); K1_STAGE(K1_SB(1, 1), u0.b + u0.bK + 256, u0.vb, u0.bD);
    WAIT_V(6); BAR();
    if (isV) kloop<true>(lds, acc, u0, u1, DC / 64, ldsw, aoff0, aoff1, boff0, boff1);
    else     kloop<false>(lds, acc, u0, u1, DC / 64, ldsw, aoff0, aoff1, boff0, boff1);
    if (wr == 0) BAR();
    if (isV) epi_v(acc, VTo, pm, pj & 15, wr, wc, fr, fq); else epi_qk(acc, Ko, pm, pj & 15, 1.0f, wr, wc, fr, fq);
    if (has2) {
        zero_acc(acc);
        if (wr == 1) BAR();
        kloop<false>(lds, acc, u1, u1, DM / 64, ldsw, aoff0, aoff1, boff0, boff1);
        if (wr == 0) BAR();
        if (isH) epi_h(acc, Ho, b1, pm, pj & 15, wr, wc, fr, fq); else epi_qk(acc, Qo, pm, pj & 15, 0.12751743082459868f, wr, wc, fr, fq);
    }
    WAIT_V(0);
}
#undef K1_SA
#undef K1_SB
#undef K1_STAGE
#undef K1_LDA
#undef K1_LDB
#undef K1_MMA
}

namespace k2 {
constexpr int SLOT = 16384, RK = 4, RV = 4, LDS_K = 0, LDS_V = RK * SLOT, LDS_BYTES = (RK + RV) * SLOT;
constexpr float THRL = 8.0f;
DI void dma2(const char* sbase, unsigned voff, unsigned lds_dst) {
    unsigned keep;
    asm volatile("s_mov_b32 %0, m0\n\ts_mov_b32 m0, %3\n\ts_nop 0\n\tglobal_load_lds_dwordx4 %1, %2\n\tglobal_load_lds_dwordx4 %1, %2 offset:1024\n\ts_mov_b32 m0, %0"
                 : "=&s"(keep) : "v"(voff), "s"(sbase), "s"(lds_dst) : "memory");
}
#define MX3(a, b, c) __builtin_fmaxf(__builtin_fmaxf((a), (b)), (c))
DI float max16(const f32x16& p) {
    float a = MX3(p[0], p[1], p[2]), b = MX3(p[3], p[4], p[5]);
    a = MX3(a, p[6], p[7]); b = MX3(b, p[8], p[9]); a = MX3(a, p[10], p[11]); b = MX3(b, p[12], p[13]); a = MX3(a, p[14], p[15]);
    return __builtin_fmaxf(a, b);
}
DI float halfmax(float m) { auto rr = __builtin_amdgcn_permlane32_swap(__float_as_uint(m), __float_as_uint(m), false, false); return __builtin_fmaxf(__uint_as_float(rr[0]), __uint_as_float(rr[1])); }
DI void cmask(f32x16& p, int r32, int hi) {
#pragma unroll
    for (int r = 0; r < 16; ++r) { const int kv = 4 * hi + (r & 3) + 8 * (r >> 2); if (kv > r32) p[r] = -INFINITY; }
}
#define WAITBAR(N) asm volatile("s_waitcnt vmcnt(" #N ")\n\ts_barrier" ::: "memory")

__global__ __launch_bounds__(512, 2) void k2_attn(const char* __restrict__ Q, const char* __restrict__ K, const char* __restrict__ VT, char* __restrict__ CTX) {
    extern __shared__ __attribute__((aligned(16))) unsigned char smem[];
    const lds_cptr lds = (lds_cptr)smem;
    const int tid = threadIdx.x, lane = tid & 63, r32 = lane & 31, hi = lane >> 5, wid = __builtin_amdgcn_readfirstlane(tid >> 6);
    const int xcd = blockIdx.x & 7, idx = blockIdx.x >> 3, h = xcd * 4 + (idx >> 3), ib = idx & 7;
    const int G = wid < 4 ? 4 * ib + wid : 63 - 4 * ib - (wid - 4);
    const int NT = 32 - 2 * ib;
    const char* Kh = K + (size_t)h * 32 * SLOT; const char* Vh = VT + (size_t)h * 32 * SLOT;
    const unsigned lds0 = (unsigned)(uintptr_t)smem;
    const unsigned dvo = wid * 2048 + lane * 16, kdst = lds0 + LDS_K + wid * 2048, vdst = lds0 + LDS_V + wid * 2048;
#define DMA_K(t, sl) dma2(Kh + (size_t)(t) * SLOT, dvo, (unsigned)__builtin_amdgcn_readfirstlane(kdst + ((sl) & 3) * SLOT))
#define DMA_V(t, sl) dma2(Vh + (size_t)(t) * SLOT, dvo, (unsigned)__builtin_amdgcn_readfirstlane(vdst + ((sl) & 3) * SLOT))
#define DMA_STEP(s) do { DMA_K(min((s) + 3, NT - 1), (s) + 3); DMA_V(min((s) + 2, NT - 1), (s) + 2); } while (0)
#define END_STEP() WAITBAR(4)
#define END_STEP8() WAITBAR(12)
    bf16x8 qr[8];
    { const char* qp = Q + ((size_t)h * 32 + (G >> 1)) * SLOT; const unsigned qo = hi * 1024 + ((G & 1) * 32 + r32) * 16;
#define QLD(d) asm volatile("global_load_dwordx4 %0, %1, %2 offset:%c3" : "=&v"(qr[d]) : "v"(qo + ((d) >> 1) * 4096), "s"(qp), "i"(((d) & 1) * 2048) : "memory")
      QLD(0); QLD(1); QLD(2); QLD(3); QLD(4); QLD(5); QLD(6); QLD(7);
#undef QLD
    }
    DMA_K(0, 0); DMA_V(0, 0); DMA_K(1, 1); DMA_K(min(2, NT - 1), 2); DMA_V(1, 1);
    const lds_cptr kp0 = lds + LDS_K + hi * 1024 + r32 * 16, vp0 = lds + LDS_V + hi * 2048 + r32 * 16;
    float mhat = 0.f, l_reg = 0.f, fct = 1.f; bool resc = false;
    f32x16 o[4]; o[0] = f32x16{}; o[1] = f32x16{}; o[2] = f32x16{}; o[3] = f32x16{};
    f32x16 negm = f32x16{}; PIN(negm);
    f32x16 pA, pB; bf16x8 kc0, kc1, kc2; u32x4 pw0, pw1; float e[8];
#pragma unroll
    for (int r = 0; r < 16; ++r) pB[r] = -INFINITY;
#pragma unroll
    for (int r = 0; r < 8; ++r) e[r] = 0.f;
#define EX(v) __builtin_amdgcn_exp2f(v)
    asm volatile("s_waitcnt vmcnt(4)\n\ts_barrier" : "+v"(qr[0]), "+v"(qr[1]), "+v"(qr[2]), "+v"(qr[3]), "+v"(qr[4]), "+v"(qr[5]), "+v"(qr[6]), "+v"(qr[7]) :: "memory");
    kc0 = lds_rdv(kp0); kc1 = lds_rdv(kp0 + 2048); kc2 = lds_rdv(kp0 + 4096);
#define VOFF(KB, j) ((2 * (KB) + ((j) >> 2)) * 4096 + ((j) & 3) * 512)
#define GAPA(j, C, CIN, P, VKB) do { \
        C = MFMA32(kf[j], qr[j], CIN); PIN(C); \
        if ((j) + 3 < 8) kf[((j) + 3) & 7] = lds_rdv(kp_ + (((j) + 3) & 7) * 2048); else vf[((j) + 3) & 7] = lds_rdv(vp_ + VOFF(VKB, ((j) + 3) & 7)); \
        if ((j) < 6) { f[(j) & 7] = EX(P[8 + ((j) & 7)]); PIN(f[(j) & 7]); } \
        if ((j) == 5) { f[6] = EX(P[14]); PIN(f[6]); } \
        if ((j) == 6) { f[7] = EX(P[15]); PIN(f[7]); } \
        { const float x0_ = (j) < 4 ? e[(2 * (j)) & 7] : f[(2 * (j)) & 7], x1_ = (j) < 4 ? e[(2 * (j) + 1) & 7] : f[(2 * (j) + 1) & 7]; \
          s0 += x0_; s1 += x1_; if ((j) < 4) { pw0[(j) & 3] = cvtpk(x0_, x1_); PIN(pw0); } else { pw1[(j) & 3] = cvtpk(x0_, x1_); PIN(pw1); } } \
        PIN(s0); PIN(s1); SBAR(); } while (0)
#define GAPB(j, VKB) do { o[(j) & 3] = MFMA32(vf[j], __builtin_bit_cast(bf16x8, ((j) < 4) ? pw0 : pw1), o[(j) & 3]); PIN(o[(j) & 3]); \
        if ((j) + 3 < 8) vf[((j) + 3) & 7] = lds_rdv(vp_ + VOFF(VKB, ((j) + 3) & 7)); } while (0)
#define EXE(C, i) do { e[i] = EX(C[i]); PIN(e[i]); } while (0)
#define UNIT(C, P, u, MASKC, KP, VP, VKB, KN, DMA) do { \
        const lds_cptr kp_ = (KP), vp_ = (VP), kn_ = (KN); \
        bf16x8 kf[8], vf[8]; kf[0] = kc0; kf[1] = kc1; kf[2] = kc2; float s0 = l_reg, s1 = 0.f, ma, mb, f[8]; \
        SBAR(); \
        GAPA(0, C, negm, P, VKB); GAPA(1, C, C, P, VKB); GAPA(2, C, C, P, VKB); GAPA(3, C, C, P, VKB); \
        GAPA(4, C, C, P, VKB); GAPA(5, C, C, P, VKB); GAPA(6, C, C, P, VKB); GAPA(7, C, C, P, VKB); \
        GAPB(0, VKB); DMA; SBAR(); \
        GAPB(1, VKB); l_reg = s0 + s1; PIN(l_reg); SBAR(); \
        if (MASKC) cmask(C, r32, hi); \
        SBAR(); \
        GAPB(2, VKB); ma = MX3(C[0], C[1], C[2]); mb = MX3(C[3], C[4], C[5]); ma = MX3(ma, C[6], C[7]); mb = MX3(mb, C[8], C[9]); PIN(ma); PIN(mb); SBAR(); \
        GAPB(3, VKB); ma = MX3(ma, C[10], C[11]); mb = MX3(mb, C[12], C[13]); ma = MX3(ma, C[14], C[15]); PIN(ma); PIN(mb); SBAR(); \
        GAPB(4, VKB); \
        { const float rm = halfmax(__builtin_fmaxf(ma, mb)); resc = false; \
          if (__builtin_expect((u) == 0 || __any(rm > THRL), 0)) { const float dl = (u) == 0 ? rm : __builtin_fmaxf(rm, 0.f); mhat += dl; \
              const float nm_ = -mhat; \
              _Pragma("unroll") for (int r = 0; r < 16; ++r) { asm volatile("v_sub_f32 %0, %0, %1" : "+v"(C[r]) : "v"(dl)); asm volatile("v_mov_b32 %0, %1" : "+v"(negm[r]) : "v"(nm_)); } \
              if ((u) != 0) { fct = EX(-dl); l_reg *= fct; resc = true; } } } \
        SBAR(); \
        GAPB(5, VKB); kc0 = lds_rdv(kn_); EXE(C, 0); EXE(C, 1); SBAR(); \
        GAPB(6, VKB); kc1 = lds_rdv(kn_ + 2048); EXE(C, 2); EXE(C, 3); EXE(C, 4); SBAR(); \
        GAPB(7, VKB); kc2 = lds_rdv(kn_ + 4096); EXE(C, 5); EXE(C, 6); EXE(C, 7); SBAR(); \
        if (resc) { asm volatile("s_nop 15" ::: "memory"); \
            _Pragma("unroll") for (int d_ = 0; d_ < 4; ++d_) _Pragma("unroll") for (int r = 0; r < 16; ++r) asm volatile("v_mul_f32 %0, %1, %0" : "+v"(o[d_][r]) : "v"(fct)); } \
        } while (0)
#define DRAIN(P, VP, VKB) do { \
        const lds_cptr vp_ = (VP); bf16x8 vf[8]; float f[8]; \
        _Pragma("unroll") for (int j = 0; j < 8; ++j) vf[j] = lds_rdv(vp_ + VOFF(VKB, j)); \
        _Pragma("unroll") for (int r = 0; r < 8; ++r) f[r] = EX(P[8 + r]); \
        float s0 = 0.f, s1 = 0.f; \
        _Pragma("unroll") for (int r = 0; r < 8; r += 2) { s0 += e[r]; s1 += e[r + 1]; pw0[r >> 1] = cvtpk(e[r], e[r + 1]); } \
        _Pragma("unroll") for (int r = 0; r < 8; r += 2) { s0 += f[r]; s1 += f[r + 1]; pw1[r >> 1] = cvtpk(f[r], f[r + 1]); } \
        l_reg += s0 + s1; \
        _Pragma("unroll") for (int j = 0; j < 8; ++j) o[j & 3] = MFMA32(vf[j], __builtin_bit_cast(bf16x8, (j < 4) ? pw0 : pw1), o[j & 3]); \
        } while (0)
    const int nfull = (G + 1) >> 1;
    int t = 0;
    for (; t < nfull; ++t) {
        const int ks = (t & 3) * SLOT, kn = ((t + 1) & 3) * SLOT, vprev = t ? ((t - 1) & 3) * SLOT : 0;
        UNIT(pA, pB, 2 * t, false, kp0 + ks, vp0 + vprev, 1, kp0 + ks + 512, DMA_STEP(t));
        UNIT(pB, pA, 2 * t + 1, 2 * t + 1 == G, kp0 + ks + 512, vp0 + ks, 0, kp0 + kn, (void)0);
        END_STEP();
    }
    {
        const int ks = (t & 3) * SLOT, vprev = t ? ((t - 1) & 3) * SLOT : 0;
        if (G & 1) { DMA_STEP(t); DRAIN(pB, vp0 + vprev, 1); }
        else { UNIT(pA, pB, 2 * t, true, kp0 + ks, vp0 + vprev, 1, kp0 + ks + 512, DMA_STEP(t)); DRAIN(pA, vp0 + ks, 0); }
    }
    {
        auto rr = __builtin_amdgcn_permlane32_swap(__float_as_uint(l_reg), __float_as_uint(l_reg), false, false);
        const float inv = 1.0f / (__uint_as_float(rr[0]) + __uint_as_float(rr[1]));
        char* op = CTX + ((size_t)((G >> 2) * 64 + 2 * h) * 8 + hi) * 2048 + ((G & 3) * 32 + r32) * 16;
#pragma unroll
        for (int db = 0; db < 4; ++db)
#pragma unroll
            for (int g = 0; g < 4; g += 2) {
                const unsigned x0 = cvtpk(o[db][4 * g] * inv, o[db][4 * g + 1] * inv), x1 = cvtpk(o[db][4 * g + 2] * inv, o[db][4 * g + 3] * inv);
                const unsigned y0 = cvtpk(o[db][4 * g + 4] * inv, o[db][4 * g + 5] * inv), y1 = cvtpk(o[db][4 * g + 6] * inv, o[db][4 * g + 7] * inv);
                const auto r0 = __builtin_amdgcn_permlane32_swap(x0, y0, false, false), r1 = __builtin_amdgcn_permlane32_swap(x1, y1, false, false);
                *(u32x4*)(op + (size_t)((db >> 1) * 8 + 4 * (db & 1) + g) * 2048) = (u32x4){r0[0], r1[0], r0[1], r1[1]};
            }
    }
    if (t < NT) { END_STEP8(); ++t;
        if (t < NT) { DMA_STEP(t); END_STEP8(); ++t;
            for (; t < NT; ++t) { DMA_STEP(t); END_STEP(); } } }
    WAIT_V(0);
}
}

namespace k3 {
constexpr int NS = 4, STG = 32768, LDS_BYTES = NS * STG;
typedef __attribute__((address_space(1))) unsigned gu32;
struct Fr { bf16x8 a[4][2], b[2][2]; };

__global__ __launch_bounds__(512, 2) void k3_out(const char* __restrict__ ctx, const char* __restrict__ H, const bf16_t* __restrict__ wo, const bf16_t* __restrict__ w2,
                                                 const bf16_t* __restrict__ b2, const bf16_t* __restrict__ x, float* part, unsigned* flags, bf16_t* __restrict__ out) {
    extern __shared__ __attribute__((aligned(16))) unsigned char smem[];
    lds_ptr lds = (lds_ptr)smem;
    const int tid = threadIdx.x, wid = __builtin_amdgcn_readfirstlane(tid >> 6), lane = tid & 63, wr = wid >> 2, wc = wid & 3, fr = lane & 15, fq = lane >> 4;
    const int b = blockIdx.x, idx = b >> 3, cons = (b >> 2) & 1, mt = 4 * (b & 3) + (idx >> 3), ntile = idx & 7, tile = mt * 8 + ntile;
    const int nt = cons ? 64 : 56;
    const char* abase = cons ? ctx + (size_t)mt * 64 * 16384 : H + (size_t)mt * 56 * 16384;
    const char* bbase = (const char*)(cons ? wo : w2) + ntile * 256;
    const unsigned va = wid * 2048 + lane * 16;
    const unsigned vb = (8 * (wid >> 1) + ((lane & 31) >> 2)) * (DM * 2) + (64 * (wid & 1) + 32 * (lane >> 5) + 8 * ((lane & 3) ^ (wid & 2))) * 2;
    const int aoff = fq * 2048 + (wr * 64 + fr) * 16;
    const int boff0 = 16384 + fq * 2048 + wc * 512 + (fr >> 2) * 64 + (fq & 1) * 32 + (fr & 3) * 8, boff1 = boff0 ^ 32;
#define K3_STAGE(t) do { const unsigned sl_ = ((t) & 3) * STG; const char* a_ = abase + (size_t)(t) * 16384 + va; const char* b_ = bbase + (size_t)(t) * (64 * DM * 2) + vb; \
        GLDS(a_, lds + sl_ + wid * 2048); GLDS(a_ + 1024, lds + sl_ + wid * 2048 + 1024); \
        GLDS(b_, lds + sl_ + 16384 + wid * 1024); GLDS(b_ + 32 * DM * 2, lds + sl_ + 16384 + wid * 1024 + 8192); } while (0)
#define K3_RDA(F, j) F.a[(j) >> 1][(j) & 1] = lds_rd(lds + sl_ + aoff + ((j) & 1) * 8192 + ((j) >> 1) * 256)
#define K3_RDB(F, j) F.b[(j) >> 1][(j) & 1] = cat8(lds_tr(lds + sl_ + (((j) >> 1) ? boff1 : boff0) + ((j) & 1) * 8192), lds_tr(lds + sl_ + (((j) >> 1) ? boff1 : boff0) + ((j) & 1) * 8192 + 256))
#define K3_MF(F, j) acc[((j) >> 1) & 3][(j) & 1] = MFMA16(F.b[(j) & 1][(j) >> 3], F.a[((j) >> 1) & 3][(j) >> 3], acc[((j) >> 1) & 3][(j) & 1])
#define K3_STEP(t, C, N) do { \
        if ((t) + 3 < nt) { WAIT_V(8); } else if ((t) + 2 < nt) { WAIT_V(4); } else { WAIT_V(0); } \
        __builtin_amdgcn_s_waitcnt(0xC07F); BAR(); SBAR(); \
        if ((t) + 4 < nt) K3_STAGE((t) + 4); \
        const unsigned sl_ = (((t) + 1) & 3) * STG; \
        SBAR(); \
        K3_MF(C, 0); K3_RDB(N, 0); SBAR(); K3_MF(C, 1); K3_RDA(N, 0); SBAR(); K3_MF(C, 2); K3_RDA(N, 2); SBAR(); K3_MF(C, 3); K3_RDB(N, 2); SBAR(); \
        K3_MF(C, 4); K3_RDA(N, 4); SBAR(); K3_MF(C, 5); K3_RDA(N, 6); SBAR(); K3_MF(C, 6); K3_RDB(N, 1); SBAR(); K3_MF(C, 7); K3_RDA(N, 1); SBAR(); \
        K3_MF(C, 8); K3_RDA(N, 3); SBAR(); K3_MF(C, 9); K3_RDB(N, 3); SBAR(); K3_MF(C, 10); K3_RDA(N, 5); SBAR(); K3_MF(C, 11); K3_RDA(N, 7); SBAR(); \
        K3_MF(C, 12); K3_MF(C, 13); K3_MF(C, 14); K3_MF(C, 15); SBAR(); \
        } while (0)
    f32x4 acc[4][2];
#pragma unroll
    for (int m = 0; m < 4; ++m) { acc[m][0] = (f32x4){0.f, 0.f, 0.f, 0.f}; acc[m][1] = (f32x4){0.f, 0.f, 0.f, 0.f}; }
    Fr FA, FB;
    K3_STAGE(0); K3_STAGE(1); K3_STAGE(2); K3_STAGE(3);
    WAIT_V(12); BAR(); SBAR();
    { const unsigned sl_ = 0;
#pragma unroll
      for (int j = 0; j < 4; ++j) { K3_RDB(FA, j); }
#pragma unroll
      for (int j = 0; j < 8; ++j) { K3_RDA(FA, j); } }
    for (int t = 0; t < nt; t += 2) {
        K3_STEP(t, FA, FB);
        K3_STEP(t + 1, FB, FA);
    }
    const auto rs = __builtin_amdgcn_make_buffer_rsrc((void*)((char*)part + (size_t)tile * 65536), 0, 65536, 0x00020000);
    gu32* flag = (gu32*)(flags + tile * 64);
    if (!cons) {
#pragma unroll
        for (int m = 0; m < 4; ++m)
#pragma unroll
            for (int n = 0; n < 2; ++n) {
                const int row = mt * 128 + wr * 64 + m * 16 + fr, col = ntile * 128 + wc * 32 + n * 16 + fq * 4;
                const u32x2 xb = *(const u32x2*)(x + (size_t)row * DM + col), bb = *(const u32x2*)(b2 + col);
                const f32x4 v = acc[m][n] + (f32x4){bflo(xb[0]) + bflo(bb[0]), bfhi(xb[0]) + bfhi(bb[0]), bflo(xb[1]) + bflo(bb[1]), bfhi(xb[1]) + bfhi(bb[1])};
                __builtin_amdgcn_raw_buffer_store_b128(__builtin_bit_cast(u32x4, v), rs, (m * 2 + n) * 8192 + tid * 16, 0, 16);
            }
        WAIT_V(0);
        __syncthreads();
        if (tid == 0) __hip_atomic_store(flag, 1u, __ATOMIC_RELAXED, __HIP_MEMORY_SCOPE_AGENT);
    } else {
        if (wid == 0) {
            for (int spin = 0; spin < (1 << 20); ++spin) {
                if (__builtin_amdgcn_readfirstlane(__hip_atomic_load(flag, __ATOMIC_RELAXED, __HIP_MEMORY_SCOPE_AGENT)) == 1u) break;
                __builtin_amdgcn_s_sleep(1);
            }
            __builtin_amdgcn_fence(__ATOMIC_ACQUIRE, "agent");
            WAIT_V(0);
        }
        __syncthreads();
        u32x4 pv[8];
#pragma unroll
        for (int j = 0; j < 8; ++j) pv[j] = __builtin_amdgcn_raw_buffer_load_b128(rs, j * 8192 + tid * 16, 0, 16);
        const int cg = ((fq & 1) << 1) | (fq >> 1);
#pragma unroll
        for (int m = 0; m < 4; ++m) {
            const f32x4 v0 = acc[m][0] + __builtin_bit_cast(f32x4, pv[m * 2]), v1 = acc[m][1] + __builtin_bit_cast(f32x4, pv[m * 2 + 1]);
            const int row = mt * 128 + wr * 64 + m * 16 + fr;
            *(u32x4*)(out + (size_t)row * DM + ntile * 128 + wc * 32 + cg * 8) = k1::pack_swap16(v0, v1);
        }
    }
}
}

extern "C" void kernel_launch(void* const* d_in, const int* in_sizes, int n_in, void* d_out, int out_size, void* d_ws, size_t ws_size, hipStream_t stream) {
    const bf16_t* x = (const bf16_t*)d_in[0]; const bf16_t* ckv = (const bf16_t*)d_in[1];
    const bf16_t* wq = (const bf16_t*)d_in[2]; const bf16_t* wuk = (const bf16_t*)d_in[3]; const bf16_t* wuv = (const bf16_t*)d_in[4]; const bf16_t* wo = (const bf16_t*)d_in[5];
    const bf16_t* w1 = (const bf16_t*)d_in[7]; const bf16_t* b1 = (const bf16_t*)d_in[8]; const bf16_t* w2 = (const bf16_t*)d_in[9]; const bf16_t* b2 = (const bf16_t*)d_in[10];
    char* ws = (char*)d_ws;
    (void)hipFuncSetAttribute((const void*)k1::k1_proj, hipFuncAttributeMaxDynamicSharedMemorySize, k1::LDS_BYTES);
    (void)hipFuncSetAttribute((const void*)k2::k2_attn, hipFuncAttributeMaxDynamicSharedMemorySize, k2::LDS_BYTES);
    (void)hipFuncSetAttribute((const void*)k3::k3_out, hipFuncAttributeMaxDynamicSharedMemorySize, k3::LDS_BYTES);
    k0_ln<<<SEQ / 4, 256, 0, stream>>>(x, (bf16_t*)(ws + WS_XN), (unsigned*)(ws + WS_FLAG));
    k1::k1_proj<<<256, 512, k1::LDS_BYTES, stream>>>(ckv, ws + WS_XN, wuk, wuv, wq, w1, b1, ws + WS_Q, ws + WS_K, ws + WS_VT, ws + WS_H);
    k2::k2_attn<<<256, 512, k2::LDS_BYTES, stream>>>(ws + WS_Q, ws + WS_K, ws + WS_VT, ws + WS_CTX);
    k3::k3_out<<<256, 512, k3::LDS_BYTES, stream>>>(ws + WS_CTX, ws + WS_H, wo, w2, b2, x, (float*)(ws + WS_PART), (unsigned*)(ws + WS_FLAG), (bf16_t*)d_out);
}
```

```cpp
#include <hip/hip_runtime.h>
#include <cstdint>

#define STAMP(k, i) do {} while (0)
#define LAS __attribute__((address_space(3)))
#define DI __device__ __forceinline__
typedef unsigned short bf16_t;
typedef short bf16x8 __attribute__((ext_vector_type(8)));
typedef short s16x4 __attribute__((ext_vector_type(4)));
typedef short v4i16_t __attribute__((ext_vector_type(4)));
typedef float f32x2 __attribute__((ext_vector_type(2)));
typedef float f32x4 __attribute__((ext_vector_type(4)));
typedef float f32x16 __attribute__((ext_vector_type(16)));
typedef unsigned u32x2 __attribute__((ext_vector_type(2)));
typedef unsigned u32x4 __attribute__((ext_vector_type(4)));
typedef __bf16 bf16x2_t __attribute__((ext_vector_type(2)));
typedef LAS unsigned char* lds_ptr;
typedef LAS const unsigned char* lds_cptr;

constexpr int SEQ = 2048, DM = 1024, NH = 32, HD = 128, DC = 512, FF = 3584, NQ = NH * HD;
constexpr size_t MiB = 1u << 20;
constexpr size_t WS_FLAG = 0;
constexpr size_t WS_XN = 1 * MiB;
constexpr size_t WS_Q = 8 * MiB;
constexpr size_t WS_K = 24 * MiB;
constexpr size_t WS_VT = 40 * MiB;
constexpr size_t WS_CTX = 56 * MiB;
constexpr size_t WS_H = 72 * MiB;
constexpr size_t WS_PART = 88 * MiB;

DI unsigned cvtpk(float lo, float hi) { f32x2 v = {lo, hi}; bf16x2_t b = __builtin_convertvector(v, bf16x2_t); return __builtin_bit_cast(unsigned, b); }
DI float bflo(unsigned w) { return __uint_as_float(w << 16); }
DI float bfhi(unsigned w) { return __uint_as_float(w & 0xffff0000u); }
DI s16x4 lds_tr(lds_cptr p) { return __builtin_bit_cast(s16x4, __builtin_amdgcn_ds_read_tr16_b64_v4i16((LAS v4i16_t*)p)); }
DI bf16x8 lds_rd(lds_cptr p) { return *(const LAS bf16x8*)p; }
DI bf16x8 lds_rdv(lds_cptr p) { return *(const volatile LAS bf16x8*)p; }
DI bf16x8 cat8(s16x4 a, s16x4 b) { return __builtin_shufflevector(a, b, 0, 1, 2, 3, 4, 5, 6, 7); }
#define SBAR() __builtin_amdgcn_sched_barrier(0)
#define PIN(x) asm volatile("" : "+v"(x))
#define WAIT_V(n) asm volatile("s_waitcnt vmcnt(" #n ")" ::: "memory")
#define WAIT_L(n) asm volatile("s_waitcnt lgkmcnt(" #n ")" ::: "memory")
#define BAR() __builtin_amdgcn_s_barrier()
#define MFMA16(a, b, c) __builtin_amdgcn_mfma_f32_16x16x32_bf16(a, b, c, 0, 0, 0)
#define MFMA32(a, b, c) __builtin_amdgcn_mfma_f32_32x32x16_bf16(a, b, c, 0, 0, 0)
DI void glds(const char* sbase, unsigned voff, unsigned ldsaddr) {
    asm volatile("s_mov_b32 m0, %2\n\ts_nop 0\n\tglobal_load_lds_dwordx4 %0, %1" :: "v"(voff), "s"(sbase), "s"(ldsaddr) : "memory");
}
DI void glds2(const char* sbase, unsigned voff, unsigned ldsaddr) {
    asm volatile("s_mov_b32 m0, %2\n\ts_nop 0\n\tglobal_load_lds_dwordx4 %0, %1\n\tglobal_load_lds_dwordx4 %0, %1 offset:1024" :: "v"(voff), "s"(sbase), "s"(ldsaddr) : "memory");
}

__global__ __launch_bounds__(256) void k0_ln(const bf16_t* __restrict__ x, bf16_t* __restrict__ xn, unsigned* __restrict__ flags) {
    const int lane = threadIdx.x & 63, row = blockIdx.x * 4 + (threadIdx.x >> 6);
    const u32x4* src = (const u32x4*)(x + (size_t)row * DM);
    const u32x4 v0 = src[lane], v1 = src[lane + 64];
    float f[16];
#pragma unroll
    for (int i = 0; i < 4; ++i) { f[2 * i] = bflo(v0[i]); f[2 * i + 1] = bfhi(v0[i]); f[8 + 2 * i] = bflo(v1[i]); f[8 + 2 * i + 1] = bfhi(v1[i]); }
    float s = 0.f;
#pragma unroll
    for (int i = 0; i < 16; ++i) s += f[i];
#pragma unroll
    for (int o = 32; o >= 1; o >>= 1) s += __shfl_xor(s, o);
    const float mean = s * (1.0f / DM);
    float q = 0.f;
#pragma unroll
    for (int i = 0; i < 16; ++i) { f[i] -= mean; q += f[i] * f[i]; }
#pragma unroll
    for (int o = 32; o >= 1; o >>= 1) q += __shfl_xor(q, o);
    const float rstd = 1.0f / sqrtf(q * (1.0f / DM) + 1e-5f);
    u32x4 w0, w1;
#pragma unroll
    for (int i = 0; i < 4; ++i) { w0[i] = cvtpk(f[2 * i] * rstd, f[2 * i + 1] * rstd); w1[i] = cvtpk(f[8 + 2 * i] * rstd, f[8 + 2 * i + 1] * rstd); }
    const int mh = row >> 7, r = row & 127, c = lane & 7, kt = lane >> 3, sw = (c ^ ((r >> 1) & 7)) * 16;
    char* base = (char*)xn + (size_t)mh * 16 * 16384 + r * 128 + sw;
    *(u32x4*)(base + (size_t)kt * 16384) = w0;
    *(u32x4*)(base + (size_t)(kt + 8) * 16384) = w1;
    if (blockIdx.x == 0 && threadIdx.x < 128) flags[threadIdx.x * 64] = 0u;
}

namespace k1 {
constexpr int HTB = 16384, LDS_BYTES = 8 * HTB;
enum { T_K = 0, T_V = 1, T_Q = 2, T_H = 3 };
struct Src {
    const char* a; const char* b;
    unsigned aK, aH, aD, bK, bD;
    unsigned va, vb;
};
struct Frag { bf16x8 At[4][2], B0[2][2], B1[2][2]; };

DI float gelu_tanh(float v) {
    const float z = v * (v * v * -0.10294324f + -2.3022082f);
    return v * __builtin_amdgcn_rcpf(1.0f + __builtin_amdgcn_exp2f(z));
}

#define K1_SA(b, h) (((b) * 2 + (h)) * HTB)
#define K1_SB(b, h) ((4 + (b) * 2 + (h)) * HTB)
#define K1_STAGE(bufoff, gbase, voff, d2) do { glds((const char*)(gbase), voff, ldsw + (bufoff)); glds((const char*)(gbase) + (d2), voff, ldsw + (bufoff) + 8192); } while (0)
#define K1_LDA(dst, b, h) do { _Pragma("unroll") for (int m = 0; m < 4; ++m) { dst[m][0] = lds_rd(lds + K1_SA(b, h) + aoff0 + m * 2048); dst[m][1] = lds_rd(lds + K1_SA(b, h) + aoff1 + m * 2048); } } while (0)
#define K1_LDB(dst, b, h) do { _Pragma("unroll") for (int k = 0; k < 2; ++k) { \
        dst[0][k] = cat8(lds_tr(lds + K1_SB(b, h) + boff0 + k * 8192), lds_tr(lds + K1_SB(b, h) + boff0 + k * 8192 + 256)); \
        dst[1][k] = cat8(lds_tr(lds + K1_SB(b, h) + boff1 + k * 8192), lds_tr(lds + K1_SB(b, h) + boff1 + k * 8192 + 256)); } } while (0)
#define K1_MMA(ai, bj, At, Bt) do { __builtin_amdgcn_s_setprio(1); _Pragma("unroll") for (int m = 0; m < 4; ++m) _Pragma("unroll") for (int n = 0; n < 2; ++n) _Pragma("unroll") for (int k = 0; k < 2; ++k) \
        acc[ai][bj][m][n] = SWAP ? MFMA16(At[m][k], Bt[n][k], acc[ai][bj][m][n]) : MFMA16(Bt[n][k], At[m][k], acc[ai][bj][m][n]); __builtin_amdgcn_s_setprio(0); } while (0)

template <bool SWAP>
DI void kloop(lds_ptr lds, f32x4 (&acc)[2][2][4][2], const Src& cur, const Src& nxt, int nt, unsigned ldsw, int aoff0, int aoff1, int boff0, int boff1) {
    bf16x8 At[4][2], B0[2][2], B1[2][2];
    for (int t = 0; t < nt; t += 2) {
        const bool last = (t == nt - 2);
        const char* a1h = cur.a + (size_t)(t + 1) * cur.aK + cur.aH;
        const char* a2 = last ? nxt.a : cur.a + (size_t)(t + 2) * cur.aK; const char* b2 = last ? nxt.b : cur.b + (size_t)(t + 2) * cur.bK;
        const unsigned aK = last ? nxt.aK : cur.aK, aH = last ? nxt.aH : cur.aH, aD = last ? nxt.aD : cur.aD, bK = last ? nxt.bK : cur.bK, bD = last ? nxt.bD : cur.bD;
        const unsigned va = last ? nxt.va : cur.va, vb = last ? nxt.vb : cur.vb;
        const char* a3 = a2 + aK; const char* b3 = b2 + bK;
        K1_LDB(B0, 0, 0); K1_LDB(B1, 0, 1); SBAR(); K1_LDA(At, 0, 0); K1_STAGE(K1_SA(1, 1), a1h, cur.va, cur.aD);
        WAIT_V(8); WAIT_L(0); BAR(); K1_MMA(0, 0, At, B0); K1_MMA(0, 1, At, B1); BAR(); SBAR();
        K1_LDA(At, 0, 1); K1_STAGE(K1_SB(0, 0), b2, vb, bD); K1_STAGE(K1_SB(0, 1), b2 + 256, vb, bD); K1_STAGE(K1_SA(0, 0), a2, va, aD);
        WAIT_V(8); WAIT_L(0); BAR(); K1_MMA(1, 0, At, B0); K1_MMA(1, 1, At, B1); BAR(); SBAR();
        K1_LDB(B0, 1, 0); K1_LDB(B1, 1, 1); SBAR(); K1_LDA(At, 1, 0); K1_STAGE(K1_SA(0, 1), a2 + aH, va, aD);
        WAIT_V(8); WAIT_L(0); BAR(); K1_MMA(0, 0, At, B0); K1_MMA(0, 1, At, B1); BAR(); SBAR();
        K1_LDA(At, 1, 1); K1_STAGE(K1_SB(1, 0), b3, vb, bD); K1_STAGE(K1_SB(1, 1), b3 + 256, vb, bD); K1_STAGE(K1_SA(1, 0), a3, va, aD);
        WAIT_V(8); WAIT_L(0); BAR(); K1_MMA(1, 0, At, B0); K1_MMA(1, 1, At, B1); BAR(); SBAR();
    }
}

DI void zero_acc(f32x4 (&acc)[2][2][4][2]) {
#pragma unroll
    for (int a = 0; a < 2; ++a)
#pragma unroll
        for (int b = 0; b < 2; ++b)
#pragma unroll
            for (int m = 0; m < 4; ++m)
#pragma unroll
                for (int n = 0; n < 2; ++n) acc[a][b][m][n] = (f32x4){0.f, 0.f, 0.f, 0.f};
}
DI u32x4 pack_swap16(f32x4 v0, f32x4 v1) {
    const unsigned x0 = cvtpk(v0[0], v0[1]), x1 = cvtpk(v0[2], v0[3]), y0 = cvtpk(v1[0], v1[1]), y1 = cvtpk(v1[2], v1[3]);
    const auto r0 = __builtin_amdgcn_permlane16_swap(x0, y0, false, false), r1 = __builtin_amdgcn_permlane16_swap(x1, y1, false, false);
    return (u32x4){r0[0], r1[0], r0[1], r1[1]};
}
DI u32x4 pack_swap32(f32x4 v0, f32x4 v1) {
    const unsigned x0 = cvtpk(v0[0], v0[1]), x1 = cvtpk(v0[2], v0[3]), y0 = cvtpk(v1[0], v1[1]), y1 = cvtpk(v1[2], v1[3]);
    const auto r0 = __builtin_amdgcn_permlane32_swap(x0, y0, false, false), r1 = __builtin_amdgcn_permlane32_swap(x1, y1, false, false);
    return (u32x4){r0[0], r1[0], r0[1], r1[1]};
}
DI void epi_qk(const f32x4 (&acc)[2][2][4][2], char* dst, int pm, int pn, float sc, int wr, int wc, int fr, int fq) {
    const int cg = ((fq & 1) << 1) | (fq >> 1);
    char* p = dst + (size_t)(2 * pn * 32 + 4 * pm + wr) * 16384 + (4 * wc + cg) * 1024 + fr * 16;
#pragma unroll
    for (int ai = 0; ai < 2; ++ai)
#pragma unroll
        for (int bj = 0; bj < 2; ++bj)
#pragma unroll
            for (int m = 0; m < 4; ++m)
                *(u32x4*)(p + (size_t)(bj * 32 + ai * 2) * 16384 + m * 256) = pack_swap16(acc[ai][bj][m][0] * sc, acc[ai][bj][m][1] * sc);
}
DI void epi_v(const f32x4 (&acc)[2][2][4][2], char* dst, int pm, int pn, int wr, int wc, int fr, int fq) {
    char* p = dst + (size_t)(2 * pn * 32 + 4 * pm + wr) * 16384 + fq * 2048 + (32 * wc + fr) * 16;
#pragma unroll
    for (int ai = 0; ai < 2; ++ai)
#pragma unroll
        for (int bj = 0; bj < 2; ++bj)
#pragma unroll
            for (int m = 0; m < 4; m += 2)
#pragma unroll
                for (int n = 0; n < 2; ++n)
                    *(u32x4*)(p + (size_t)(bj * 32 + ai * 2) * 16384 + m * 4096 + n * 256) = pack_swap32(acc[ai][bj][m][n], acc[ai][bj][m + 1][n]);
}
DI void epi_h(const f32x4 (&acc)[2][2][4][2], char* dst, const bf16_t* b1, int pm, int pn, int wr, int wc, int fr, int fq) {
    const int cg = ((fq & 1) << 1) | (fq >> 1);
    f32x4 bv[2][2];
#pragma unroll
    for (int bj = 0; bj < 2; ++bj)
#pragma unroll
        for (int n = 0; n < 2; ++n) { const u32x2 w = *(const u32x2*)(b1 + pn * 256 + bj * 128 + wc * 32 + n * 16 + fq * 4); bv[bj][n] = (f32x4){bflo(w[0]), bfhi(w[0]), bflo(w[1]), bfhi(w[1])}; }
    char* p = dst + ((size_t)(2 * pm * 56 + 4 * pn + (wc >> 1)) * 8 + 4 * (wc & 1) + cg) * 2048 + (64 * wr + fr) * 16;
#pragma unroll
    for (int ai = 0; ai < 2; ++ai)
#pragma unroll
        for (int bj = 0; bj < 2; ++bj)
#pragma unroll
            for (int m = 0; m < 4; ++m) {
                f32x4 v0 = acc[ai][bj][m][0] + bv[bj][0], v1 = acc[ai][bj][m][1] + bv[bj][1];
#pragma unroll
                for (int j = 0; j < 4; ++j) { v0[j] = gelu_tanh(v0[j]); v1[j] = gelu_tanh(v1[j]); }
                *(u32x4*)(p + (size_t)(ai * 56 + bj * 2) * 16384 + m * 256) = pack_swap16(v0, v1);
            }
}

__global__ __launch_bounds__(512, 2) void k1_proj(const bf16_t* __restrict__ ckv, const char* __restrict__ xn, const bf16_t* __restrict__ wuk, const bf16_t* __restrict__ wuv,
                                                  const bf16_t* __restrict__ wq, const bf16_t* __restrict__ w1, const bf16_t* __restrict__ b1,
                                                  char* __restrict__ Qo, char* __restrict__ Ko, char* __restrict__ VTo, char* __restrict__ Ho) {
    extern __shared__ __attribute__((aligned(16))) unsigned char smem[];
    lds_ptr lds = (lds_ptr)smem;
    const int tid = threadIdx.x, wid = __builtin_amdgcn_readfirstlane(tid >> 6), lane = tid & 63, wr = wid >> 2, wc = wid & 3, fr = lane & 15, fq = lane >> 4;
    const int xcd = blockIdx.x & 7, idx = blockIdx.x >> 3, pm = idx & 7, pj = xcd + 8 * (idx >> 3);
    const bool isV = pj >= 16, has2 = pj < 30, isH = pj >= 16;
    const unsigned ldsw = (unsigned)(uintptr_t)smem + (unsigned)wid * 1024u;
    const int aoff0 = (wr * 64 + fr) * 128 + ((fq ^ (fr >> 1)) * 16), aoff1 = aoff0 ^ 64;
    const int boff0 = fq * 2048 + wc * 512 + (fr >> 2) * 64 + (fq & 1) * 32 + (fr & 3) * 8, boff1 = boff0 ^ 32;
    const unsigned brow = 8 * (wid >> 1) + ((lane & 31) >> 2), bcol = (64 * (wid & 1) + 32 * (lane >> 5) + 8 * ((lane & 3) ^ (wid & 2))) * 2;
    Src u0, u1;
    u0.a = (const char*)ckv + (size_t)pm * 256 * DC * 2; u0.aK = 128; u0.aH = 128 * DC * 2; u0.aD = 64 * DC * 2;
    u0.va = (tid >> 3) * (DC * 2) + (((tid & 7) ^ ((tid >> 4) & 7)) * 16);
    u0.b = (const char*)(isV ? wuv : wuk) + (pj & 15) * 512; u0.bK = 64 * NQ * 2; u0.bD = 32 * NQ * 2; u0.vb = brow * (NQ * 2) + bcol;
    if (has2) {
        const unsigned ldb2 = isH ? FF * 2 : NQ * 2;
        u1.a = xn + (size_t)pm * 2 * 16 * 16384; u1.aK = 16384; u1.aH = 16 * 16384; u1.aD = 8192; u1.va = tid * 16;
        u1.b = (const char*)(isH ? w1 : wq) + (pj & 15) * 512; u1.bK = 64 * ldb2; u1.bD = 32 * ldb2; u1.vb = brow * ldb2 + bcol;
    } else u1 = u0;
    f32x4 acc[2][2][4][2];
    zero_acc(acc);
    STAMP(1, 0);
    K1_STAGE(K1_SB(0, 0), u0.b, u0.vb, u0.bD); K1_STAGE(K1_SB(0, 1), u0.b + 256, u0.vb, u0.bD); K1_STAGE(K1_SA(0, 0), u0.a, u0.va, u0.aD); K1_STAGE(K1_SA(0, 1), u0.a + u0.aH, u0.va, u0.aD);
    if (wr == 1) BAR();
    WAIT_V(2); BAR();
    K1_STAGE(K1_SB(1, 0), u0.b + u0.bK, u0.vb, u0.bD); K1_STAGE(K1_SA(1, 0), u0.a + u0.aK, u0.va, u0.aD); K1_STAGE(K1_SB(1, 1), u0.b + u0.bK + 256, u0.vb, u0.bD);
    WAIT_V(6); BAR();
    STAMP(1, 1);
    if (isV) kloop<true>(lds, acc, u0, u1, DC / 64, ldsw, aoff0, aoff1, boff0, boff1);
    else     kloop<false>(lds, acc, u0, u1, DC / 64, ldsw, aoff0, aoff1, boff0, boff1);
    STAMP(1, 2);
    if (wr == 0) BAR();
    if (isV) epi_v(acc, VTo, pm, pj & 15, wr, wc, fr, fq); else epi_qk(acc, Ko, pm, pj & 15, 1.0f, wr, wc, fr, fq);
    if (has2) {
        zero_acc(acc);
        STAMP(1, 3);
        if (wr == 1) BAR();
        kloop<false>(lds, acc, u1, u1, DM / 64, ldsw, aoff0, aoff1, boff0, boff1);
        STAMP(1, 4);
        if (wr == 0) BAR();
        if (isH) epi_h(acc, Ho, b1, pm, pj & 15, wr, wc, fr, fq); else epi_qk(acc, Qo, pm, pj & 15, 0.12751743082459868f, wr, wc, fr, fq);
    }
    STAMP(1, 5);
    WAIT_V(0);
    STAMP(1, 6);
}
#undef K1_SA
#undef K1_SB
#undef K1_STAGE
#undef K1_LDA
#undef K1_LDB
#undef K1_MMA
}

namespace k2 {
constexpr int SLOT = 16384, LDS_K = 0, LDS_V = 4 * SLOT, LDS_BYTES = 8 * SLOT;
#define K2_ASM \
    "s_nop 1\n\t" \
    "s_cmp_eq_u32 %[n], 0\n\t" \
    "s_cbranch_scc1 TAIL_%=\n\t" \
    "LOOP_%=:\n\t" \
    "s_cmp_lg_u32 %[dmaw], 0\n\t" \
    "s_cbranch_scc1 DMAL_%=\n\t" \
    "DMABL_%=:\n\t" \
    "s_waitcnt lgkmcnt(0)\n\t" \
    "v_mfma_f32_32x32x16_bf16 v[112:127], v[168:171], v[64:67], v[96:111]\n\t" \
    "ds_read_b128 v[200:203], v235 offset:8192\n\t" \
    "ds_read_b128 v[204:207], v235 offset:8704\n\t" \
    "v_add_f32 v237, v144, v145\n\t" \
    "v_cvt_pk_bf16_f32 v160, v144, v145\n\t" \
    "v_mfma_f32_32x32x16_bf16 v[112:127], v[172:175], v[68:71], v[112:127]\n\t" \
    "ds_read_b128 v[208:211], v235 offset:9216\n\t" \
    "ds_read_b128 v[212:215], v235 offset:9728\n\t" \
    "v_add_f32 v237, v237, v146\n\t" \
    "v_add_f32 v237, v237, v147\n\t" \
    "v_cvt_pk_bf16_f32 v161, v146, v147\n\t" \
    "v_mfma_f32_32x32x16_bf16 v[112:127], v[176:179], v[72:75], v[112:127]\n\t" \
    "ds_read_b128 v[216:219], v235 offset:12288\n\t" \
    "ds_read_b128 v[220:223], v235 offset:12800\n\t" \
    "v_add_f32 v237, v237, v148\n\t" \
    "v_add_f32 v237, v237, v149\n\t" \
    "v_cvt_pk_bf16_f32 v162, v148, v149\n\t" \
    "v_mfma_f32_32x32x16_bf16 v[112:127], v[180:183], v[76:79], v[112:127]\n\t" \
    "ds_read_b128 v[224:227], v235 offset:13312\n\t" \
    "ds_read_b128 v[228:231], v235 offset:13824\n\t" \
    "v_add_f32 v237, v237, v150\n\t" \
    "v_add_f32 v237, v237, v151\n\t" \
    "v_cvt_pk_bf16_f32 v163, v150, v151\n\t" \
    "v_mfma_f32_32x32x16_bf16 v[112:127], v[184:187], v[80:83], v[112:127]\n\t" \
    "v_add_f32 v237, v237, v152\n\t" \
    "v_add_f32 v237, v237, v153\n\t" \
    "v_cvt_pk_bf16_f32 v164, v152, v153\n\t" \
    "v_mfma_f32_32x32x16_bf16 v[112:127], v[188:191], v[84:87], v[112:127]\n\t" \
    "v_add_f32 v237, v237, v154\n\t" \
    "v_add_f32 v237, v237, v155\n\t" \
    "v_cvt_pk_bf16_f32 v165, v154, v155\n\t" \
    "v_mfma_f32_32x32x16_bf16 v[112:127], v[192:195], v[88:91], v[112:127]\n\t" \
    "v_add_f32 v237, v237, v156\n\t" \
    "v_add_f32 v237, v237, v157\n\t" \
    "v_cvt_pk_bf16_f32 v166, v156, v157\n\t" \
    "v_mfma_f32_32x32x16_bf16 v[112:127], v[196:199], v[92:95], v[112:127]\n\t" \
    "v_add_f32 v237, v237, v158\n\t" \
    "v_add_f32 v237, v237, v159\n\t" \
    "v_cvt_pk_bf16_f32 v167, v158, v159\n\t" \
    "v_cmp_lt_f32 vcc, %[thr], v237\n\t" \
    "s_cbranch_vccnz RAREA_%=\n\t" \
    "BACKA_%=:\n\t" \
    "s_waitcnt lgkmcnt(0)\n\t" \
    "v_mfma_f32_32x32x16_bf16 v[0:15], v[200:203], v[160:163], v[0:15]\n\t" \
    "ds_read_b128 v[168:171], v232 offset:512\n\t" \
    "ds_read_b128 v[172:175], v232 offset:2560\n\t" \
    "v_add_f32 v236, v236, v237\n\t" \
    "v_mfma_f32_32x32x16_bf16 v[16:31], v[204:207], v[160:163], v[16:31]\n\t" \
    "ds_read_b128 v[176:179], v232 offset:4608\n\t" \
    "ds_read_b128 v[180:183], v232 offset:6656\n\t" \
    "v_exp_f32 v144, v112\n\t" \
    "v_exp_f32 v145, v113\n\t" \
    "v_mfma_f32_32x32x16_bf16 v[32:47], v[208:211], v[160:163], v[32:47]\n\t" \
    "ds_read_b128 v[184:187], v232 offset:8704\n\t" \
    "ds_read_b128 v[188:191], v232 offset:10752\n\t" \
    "v_exp_f32 v146, v114\n\t" \
    "v_exp_f32 v147, v115\n\t" \
    "v_mfma_f32_32x32x16_bf16 v[48:63], v[212:215], v[160:163], v[48:63]\n\t" \
    "ds_read_b128 v[192:195], v232 offset:12800\n\t" \
    "ds_read_b128 v[196:199], v232 offset:14848\n\t" \
    "v_exp_f32 v148, v116\n\t" \
    "v_exp_f32 v149, v117\n\t" \
    "v_mfma_f32_32x32x16_bf16 v[0:15], v[216:219], v[164:167], v[0:15]\n\t" \
    "v_exp_f32 v150, v118\n\t" \
    "v_exp_f32 v151, v119\n\t" \
    "v_exp_f32 v152, v120\n\t" \
    "v_mfma_f32_32x32x16_bf16 v[16:31], v[220:223], v[164:167], v[16:31]\n\t" \
    "v_exp_f32 v153, v121\n\t" \
    "v_exp_f32 v154, v122\n\t" \
    "v_exp_f32 v155, v123\n\t" \
    "v_mfma_f32_32x32x16_bf16 v[32:47], v[224:227], v[164:167], v[32:47]\n\t" \
    "v_exp_f32 v156, v124\n\t" \
    "v_exp_f32 v157, v125\n\t" \
    "v_mfma_f32_32x32x16_bf16 v[48:63], v[228:231], v[164:167], v[48:63]\n\t" \
    "v_exp_f32 v158, v126\n\t" \
    "v_exp_f32 v159, v127\n\t" \
    "s_waitcnt lgkmcnt(0)\n\t" \
    "v_mfma_f32_32x32x16_bf16 v[128:143], v[168:171], v[64:67], v[96:111]\n\t" \
    "ds_read_b128 v[200:203], v234 offset:0\n\t" \
    "ds_read_b128 v[204:207], v234 offset:512\n\t" \
    "v_add_f32 v237, v144, v145\n\t" \
    "v_cvt_pk_bf16_f32 v160, v144, v145\n\t" \
    "v_mfma_f32_32x32x16_bf16 v[128:143], v[172:175], v[68:71], v[128:143]\n\t" \
    "ds_read_b128 v[208:211], v234 offset:1024\n\t" \
    "ds_read_b128 v[212:215], v234 offset:1536\n\t" \
    "v_add_f32 v237, v237, v146\n\t" \
    "v_add_f32 v237, v237, v147\n\t" \
    "v_cvt_pk_bf16_f32 v161, v146, v147\n\t" \
    "v_mfma_f32_32x32x16_bf16 v[128:143], v[176:179], v[72:75], v[128:143]\n\t" \
    "ds_read_b128 v[216:219], v234 offset:4096\n\t" \
    "ds_read_b128 v[220:223], v234 offset:4608\n\t" \
    "v_add_f32 v237, v237, v148\n\t" \
    "v_add_f32 v237, v237, v149\n\t" \
    "v_cvt_pk_bf16_f32 v162, v148, v149\n\t" \
    "v_mfma_f32_32x32x16_bf16 v[128:143], v[180:183], v[76:79], v[128:143]\n\t" \
    "ds_read_b128 v[224:227], v234 offset:5120\n\t" \
    "ds_read_b128 v[228:231], v234 offset:5632\n\t" \
    "v_add_f32 v237, v237, v150\n\t" \
    "v_add_f32 v237, v237, v151\n\t" \
    "v_cvt_pk_bf16_f32 v163, v150, v151\n\t" \
    "v_mfma_f32_32x32x16_bf16 v[128:143], v[184:187], v[80:83], v[128:143]\n\t" \
    "v_add_f32 v237, v237, v152\n\t" \
    "v_add_f32 v237, v237, v153\n\t" \
    "v_cvt_pk_bf16_f32 v164, v152, v153\n\t" \
    "v_mfma_f32_32x32x16_bf16 v[128:143], v[188:191], v[84:87], v[128:143]\n\t" \
    "v_add_f32 v237, v237, v154\n\t" \
    "v_add_f32 v237, v237, v155\n\t" \
    "v_cvt_pk_bf16_f32 v165, v154, v155\n\t" \
    "v_mfma_f32_32x32x16_bf16 v[128:143], v[192:195], v[88:91], v[128:143]\n\t" \
    "v_add_f32 v237, v237, v156\n\t" \
    "v_add_f32 v237, v237, v157\n\t" \
    "v_cvt_pk_bf16_f32 v166, v156, v157\n\t" \
    "v_mfma_f32_32x32x16_bf16 v[128:143], v[196:199], v[92:95], v[128:143]\n\t" \
    "v_add_f32 v237, v237, v158\n\t" \
    "v_add_f32 v237, v237, v159\n\t" \
    "v_cvt_pk_bf16_f32 v167, v158, v159\n\t" \
    "v_cmp_lt_f32 vcc, %[thr], v237\n\t" \
    "s_cbranch_vccnz RAREB_%=\n\t" \
    "BACKB_%=:\n\t" \
    "s_waitcnt lgkmcnt(0)\n\t" \
    "v_mfma_f32_32x32x16_bf16 v[0:15], v[200:203], v[160:163], v[0:15]\n\t" \
    "ds_read_b128 v[168:171], v233 offset:0\n\t" \
    "ds_read_b128 v[172:175], v233 offset:2048\n\t" \
    "v_add_f32 v236, v236, v237\n\t" \
    "v_mfma_f32_32x32x16_bf16 v[16:31], v[204:207], v[160:163], v[16:31]\n\t" \
    "ds_read_b128 v[176:179], v233 offset:4096\n\t" \
    "ds_read_b128 v[180:183], v233 offset:6144\n\t" \
    "v_exp_f32 v144, v128\n\t" \
    "v_exp_f32 v145, v129\n\t" \
    "v_mfma_f32_32x32x16_bf16 v[32:47], v[208:211], v[160:163], v[32:47]\n\t" \
    "ds_read_b128 v[184:187], v233 offset:8192\n\t" \
    "ds_read_b128 v[188:191], v233 offset:10240\n\t" \
    "v_exp_f32 v146, v130\n\t" \
    "v_exp_f32 v147, v131\n\t" \
    "v_mfma_f32_32x32x16_bf16 v[48:63], v[212:215], v[160:163], v[48:63]\n\t" \
    "ds_read_b128 v[192:195], v233 offset:12288\n\t" \
    "ds_read_b128 v[196:199], v233 offset:14336\n\t" \
    "v_exp_f32 v148, v132\n\t" \
    "v_exp_f32 v149, v133\n\t" \
    "v_mfma_f32_32x32x16_bf16 v[0:15], v[216:219], v[164:167], v[0:15]\n\t" \
    "v_exp_f32 v150, v134\n\t" \
    "v_exp_f32 v151, v135\n\t" \
    "v_exp_f32 v152, v136\n\t" \
    "v_mfma_f32_32x32x16_bf16 v[16:31], v[220:223], v[164:167], v[16:31]\n\t" \
    "v_exp_f32 v153, v137\n\t" \
    "v_exp_f32 v154, v138\n\t" \
    "v_exp_f32 v155, v139\n\t" \
    "s_add_u32 %[soff], %[soff], 0x4000\n\t" \
    "s_and_b32 %[soff], %[soff], 0xffff\n\t" \
    "v_mfma_f32_32x32x16_bf16 v[32:47], v[224:227], v[164:167], v[32:47]\n\t" \
    "v_exp_f32 v156, v140\n\t" \
    "v_exp_f32 v157, v141\n\t" \
    "v_mov_b32 v235, v234\n\t" \
    "v_mov_b32 v232, v233\n\t" \
    "v_mfma_f32_32x32x16_bf16 v[48:63], v[228:231], v[164:167], v[48:63]\n\t" \
    "v_exp_f32 v158, v142\n\t" \
    "v_exp_f32 v159, v143\n\t" \
    "v_add_u32 v234, %[soff], %[vp0]\n\t" \
    "s_add_u32 s60, %[soff], 0x4000\n\t" \
    "s_and_b32 s60, s60, 0xffff\n\t" \
    "v_add_u32 v233, s60, %[kp0]\n\t" \
    "s_waitcnt vmcnt(8)\n\t" \
    "s_barrier\n\t" \
    "s_add_u32 %[t], %[t], 1\n\t" \
    "s_cmp_lt_u32 %[t], %[n]\n\t" \
    "s_cbranch_scc1 LOOP_%=\n\t" \
    "TAIL_%=:\n\t" \
    "s_cmp_lg_u32 %[dmaw], 0\n\t" \
    "s_cbranch_scc1 DMAT_%=\n\t" \
    "DMABT_%=:\n\t" \
    "s_cmp_eq_u32 %[odd], 0\n\t" \
    "s_cbranch_scc1 EVEN_%=\n\t" \
    "s_waitcnt lgkmcnt(0)\n\t" \
    "v_mfma_f32_32x32x16_bf16 v[112:127], v[168:171], v[64:67], v[96:111]\n\t" \
    "ds_read_b128 v[200:203], v235 offset:8192\n\t" \
    "ds_read_b128 v[204:207], v235 offset:8704\n\t" \
    "v_add_f32 v237, v144, v145\n\t" \
    "v_cvt_pk_bf16_f32 v160, v144, v145\n\t" \
    "v_mfma_f32_32x32x16_bf16 v[112:127], v[172:175], v[68:71], v[112:127]\n\t" \
    "ds_read_b128 v[208:211], v235 offset:9216\n\t" \
    "ds_read_b128 v[212:215], v235 offset:9728\n\t" \
    "v_add_f32 v237, v237, v146\n\t" \
    "v_add_f32 v237, v237, v147\n\t" \
    "v_cvt_pk_bf16_f32 v161, v146, v147\n\t" \
    "v_mfma_f32_32x32x16_bf16 v[112:127], v[176:179], v[72:75], v[112:127]\n\t" \
    "ds_read_b128 v[216:219], v235 offset:12288\n\t" \
    "ds_read_b128 v[220:223], v235 offset:12800\n\t" \
    "v_add_f32 v237, v237, v148\n\t" \
    "v_add_f32 v237, v237, v149\n\t" \
    "v_cvt_pk_bf16_f32 v162, v148, v149\n\t" \
    "v_mfma_f32_32x32x16_bf16 v[112:127], v[180:183], v[76:79], v[112:127]\n\t" \
    "ds_read_b128 v[224:227], v235 offset:13312\n\t" \
    "ds_read_b128 v[228:231], v235 offset:13824\n\t" \
    "v_add_f32 v237, v237, v150\n\t" \
    "v_add_f32 v237, v237, v151\n\t" \
    "v_cvt_pk_bf16_f32 v163, v150, v151\n\t" \
    "v_mfma_f32_32x32x16_bf16 v[112:127], v[184:187], v[80:83], v[112:127]\n\t" \
    "v_add_f32 v237, v237, v152\n\t" \
    "v_add_f32 v237, v237, v153\n\t" \
    "v_cvt_pk_bf16_f32 v164, v152, v153\n\t" \
    "v_mfma_f32_32x32x16_bf16 v[112:127], v[188:191], v[84:87], v[112:127]\n\t" \
    "v_add_f32 v237, v237, v154\n\t" \
    "v_add_f32 v237, v237, v155\n\t" \
    "v_cvt_pk_bf16_f32 v165, v154, v155\n\t" \
    "v_mfma_f32_32x32x16_bf16 v[112:127], v[192:195], v[88:91], v[112:127]\n\t" \
    "v_add_f32 v237, v237, v156\n\t" \
    "v_add_f32 v237, v237, v157\n\t" \
    "v_cvt_pk_bf16_f32 v166, v156, v157\n\t" \
    "v_mfma_f32_32x32x16_bf16 v[112:127], v[196:199], v[92:95], v[112:127]\n\t" \
    "v_add_f32 v237, v237, v158\n\t" \
    "v_add_f32 v237, v237, v159\n\t" \
    "v_cvt_pk_bf16_f32 v167, v158, v159\n\t" \
    "v_cmp_lt_f32 vcc, %[thr], v237\n\t" \
    "s_cbranch_vccnz RAREC_%=\n\t" \
    "BACKC_%=:\n\t" \
    "s_waitcnt lgkmcnt(0)\n\t" \
    "v_mfma_f32_32x32x16_bf16 v[0:15], v[200:203], v[160:163], v[0:15]\n\t" \
    "ds_read_b128 v[168:171], v232 offset:512\n\t" \
    "ds_read_b128 v[172:175], v232 offset:2560\n\t" \
    "v_add_f32 v236, v236, v237\n\t" \
    "v_mfma_f32_32x32x16_bf16 v[16:31], v[204:207], v[160:163], v[16:31]\n\t" \
    "ds_read_b128 v[176:179], v232 offset:4608\n\t" \
    "ds_read_b128 v[180:183], v232 offset:6656\n\t" \
    "v_exp_f32 v144, v112\n\t" \
    "v_exp_f32 v145, v113\n\t" \
    "v_mfma_f32_32x32x16_bf16 v[32:47], v[208:211], v[160:163], v[32:47]\n\t" \
    "ds_read_b128 v[184:187], v232 offset:8704\n\t" \
    "ds_read_b128 v[188:191], v232 offset:10752\n\t" \
    "v_exp_f32 v146, v114\n\t" \
    "v_exp_f32 v147, v115\n\t" \
    "v_mfma_f32_32x32x16_bf16 v[48:63], v[212:215], v[160:163], v[48:63]\n\t" \
    "ds_read_b128 v[192:195], v232 offset:12800\n\t" \
    "ds_read_b128 v[196:199], v232 offset:14848\n\t" \
    "v_exp_f32 v148, v116\n\t" \
    "v_exp_f32 v149, v117\n\t" \
    "v_mfma_f32_32x32x16_bf16 v[0:15], v[216:219], v[164:167], v[0:15]\n\t" \
    "v_exp_f32 v150, v118\n\t" \
    "v_exp_f32 v151, v119\n\t" \
    "v_exp_f32 v152, v120\n\t" \
    "v_mfma_f32_32x32x16_bf16 v[16:31], v[220:223], v[164:167], v[16:31]\n\t" \
    "v_exp_f32 v153, v121\n\t" \
    "v_exp_f32 v154, v122\n\t" \
    "v_exp_f32 v155, v123\n\t" \
    "v_mfma_f32_32x32x16_bf16 v[32:47], v[224:227], v[164:167], v[32:47]\n\t" \
    "v_exp_f32 v156, v124\n\t" \
    "v_exp_f32 v157, v125\n\t" \
    "v_mfma_f32_32x32x16_bf16 v[48:63], v[228:231], v[164:167], v[48:63]\n\t" \
    "v_exp_f32 v158, v126\n\t" \
    "v_exp_f32 v159, v127\n\t" \
    "v_mov_b32 v239, 0xff800000\n\t" \
    "v_cmp_gt_i32 vcc, 0, %[dq]\n\t" \
    "s_nop 1\n\t" \
    "v_cndmask_b32 v96, v96, v239, vcc\n\t" \
    "v_cmp_gt_i32 vcc, 1, %[dq]\n\t" \
    "s_nop 1\n\t" \
    "v_cndmask_b32 v97, v97, v239, vcc\n\t" \
    "v_cmp_gt_i32 vcc, 2, %[dq]\n\t" \
    "s_nop 1\n\t" \
    "v_cndmask_b32 v98, v98, v239, vcc\n\t" \
    "v_cmp_gt_i32 vcc, 3, %[dq]\n\t" \
    "s_nop 1\n\t" \
    "v_cndmask_b32 v99, v99, v239, vcc\n\t" \
    "v_cmp_gt_i32 vcc, 8, %[dq]\n\t" \
    "s_nop 1\n\t" \
    "v_cndmask_b32 v100, v100, v239, vcc\n\t" \
    "v_cmp_gt_i32 vcc, 9, %[dq]\n\t" \
    "s_nop 1\n\t" \
    "v_cndmask_b32 v101, v101, v239, vcc\n\t" \
    "v_cmp_gt_i32 vcc, 10, %[dq]\n\t" \
    "s_nop 1\n\t" \
    "v_cndmask_b32 v102, v102, v239, vcc\n\t" \
    "v_cmp_gt_i32 vcc, 11, %[dq]\n\t" \
    "s_nop 1\n\t" \
    "v_cndmask_b32 v103, v103, v239, vcc\n\t" \
    "v_cmp_gt_i32 vcc, 16, %[dq]\n\t" \
    "s_nop 1\n\t" \
    "v_cndmask_b32 v104, v104, v239, vcc\n\t" \
    "v_cmp_gt_i32 vcc, 17, %[dq]\n\t" \
    "s_nop 1\n\t" \
    "v_cndmask_b32 v105, v105, v239, vcc\n\t" \
    "v_cmp_gt_i32 vcc, 18, %[dq]\n\t" \
    "s_nop 1\n\t" \
    "v_cndmask_b32 v106, v106, v239, vcc\n\t" \
    "v_cmp_gt_i32 vcc, 19, %[dq]\n\t" \
    "s_nop 1\n\t" \
    "v_cndmask_b32 v107, v107, v239, vcc\n\t" \
    "v_cmp_gt_i32 vcc, 24, %[dq]\n\t" \
    "s_nop 1\n\t" \
    "v_cndmask_b32 v108, v108, v239, vcc\n\t" \
    "v_cmp_gt_i32 vcc, 25, %[dq]\n\t" \
    "s_nop 1\n\t" \
    "v_cndmask_b32 v109, v109, v239, vcc\n\t" \
    "v_cmp_gt_i32 vcc, 26, %[dq]\n\t" \
    "s_nop 1\n\t" \
    "v_cndmask_b32 v110, v110, v239, vcc\n\t" \
    "v_cmp_gt_i32 vcc, 27, %[dq]\n\t" \
    "s_nop 1\n\t" \
    "v_cndmask_b32 v111, v111, v239, vcc\n\t" \
    "s_nop 1\n\t" \
    "s_waitcnt lgkmcnt(0)\n\t" \
    "v_mfma_f32_32x32x16_bf16 v[128:143], v[168:171], v[64:67], v[96:111]\n\t" \
    "ds_read_b128 v[200:203], v234 offset:0\n\t" \
    "ds_read_b128 v[204:207], v234 offset:512\n\t" \
    "v_add_f32 v237, v144, v145\n\t" \
    "v_cvt_pk_bf16_f32 v160, v144, v145\n\t" \
    "v_mfma_f32_32x32x16_bf16 v[128:143], v[172:175], v[68:71], v[128:143]\n\t" \
    "ds_read_b128 v[208:211], v234 offset:1024\n\t" \
    "ds_read_b128 v[212:215], v234 offset:1536\n\t" \
    "v_add_f32 v237, v237, v146\n\t" \
    "v_add_f32 v237, v237, v147\n\t" \
    "v_cvt_pk_bf16_f32 v161, v146, v147\n\t" \
    "v_mfma_f32_32x32x16_bf16 v[128:143], v[176:179], v[72:75], v[128:143]\n\t" \
    "ds_read_b128 v[216:219], v234 offset:4096\n\t" \
    "ds_read_b128 v[220:223], v234 offset:4608\n\t" \
    "v_add_f32 v237, v237, v148\n\t" \
    "v_add_f32 v237, v237, v149\n\t" \
    "v_cvt_pk_bf16_f32 v162, v148, v149\n\t" \
    "v_mfma_f32_32x32x16_bf16 v[128:143], v[180:183], v[76:79], v[128:143]\n\t" \
    "ds_read_b128 v[224:227], v234 offset:5120\n\t" \
    "ds_read_b128 v[228:231], v234 offset:5632\n\t" \
    "v_add_f32 v237, v237, v150\n\t" \
    "v_add_f32 v237, v237, v151\n\t" \
    "v_cvt_pk_bf16_f32 v163, v150, v151\n\t" \
    "v_mfma_f32_32x32x16_bf16 v[128:143], v[184:187], v[80:83], v[128:143]\n\t" \
    "v_add_f32 v237, v237, v152\n\t" \
    "v_add_f32 v237, v237, v153\n\t" \
    "v_cvt_pk_bf16_f32 v164, v152, v153\n\t" \
    "v_mfma_f32_32x32x16_bf16 v[128:143], v[188:191], v[84:87], v[128:143]\n\t" \
    "v_add_f32 v237, v237, v154\n\t" \
    "v_add_f32 v237, v237, v155\n\t" \
    "v_cvt_pk_bf16_f32 v165, v154, v155\n\t" \
    "v_mfma_f32_32x32x16_bf16 v[128:143], v[192:195], v[88:91], v[128:143]\n\t" \
    "v_add_f32 v237, v237, v156\n\t" \
    "v_add_f32 v237, v237, v157\n\t" \
    "v_cvt_pk_bf16_f32 v166, v156, v157\n\t" \
    "v_mfma_f32_32x32x16_bf16 v[128:143], v[196:199], v[92:95], v[128:143]\n\t" \
    "v_add_f32 v237, v237, v158\n\t" \
    "v_add_f32 v237, v237, v159\n\t" \
    "v_cvt_pk_bf16_f32 v167, v158, v159\n\t" \
    "v_cmp_lt_f32 vcc, %[thr], v237\n\t" \
    "s_cbranch_vccnz RARED_%=\n\t" \
    "BACKD_%=:\n\t" \
    "s_waitcnt lgkmcnt(0)\n\t" \
    "v_mfma_f32_32x32x16_bf16 v[0:15], v[200:203], v[160:163], v[0:15]\n\t" \
    "ds_read_b128 v[168:171], v233 offset:0\n\t" \
    "ds_read_b128 v[172:175], v233 offset:2048\n\t" \
    "v_add_f32 v236, v236, v237\n\t" \
    "v_mfma_f32_32x32x16_bf16 v[16:31], v[204:207], v[160:163], v[16:31]\n\t" \
    "ds_read_b128 v[176:179], v233 offset:4096\n\t" \
    "ds_read_b128 v[180:183], v233 offset:6144\n\t" \
    "v_exp_f32 v144, v128\n\t" \
    "v_exp_f32 v145, v129\n\t" \
    "v_mfma_f32_32x32x16_bf16 v[32:47], v[208:211], v[160:163], v[32:47]\n\t" \
    "ds_read_b128 v[184:187], v233 offset:8192\n\t" \
    "ds_read_b128 v[188:191], v233 offset:10240\n\t" \
    "v_exp_f32 v146, v130\n\t" \
    "v_exp_f32 v147, v131\n\t" \
    "v_mfma_f32_32x32x16_bf16 v[48:63], v[212:215], v[160:163], v[48:63]\n\t" \
    "ds_read_b128 v[192:195], v233 offset:12288\n\t" \
    "ds_read_b128 v[196:199], v233 offset:14336\n\t" \
    "v_exp_f32 v148, v132\n\t" \
    "v_exp_f32 v149, v133\n\t" \
    "v_mfma_f32_32x32x16_bf16 v[0:15], v[216:219], v[164:167], v[0:15]\n\t" \
    "v_exp_f32 v150, v134\n\t" \
    "v_exp_f32 v151, v135\n\t" \
    "v_exp_f32 v152, v136\n\t" \
    "v_mfma_f32_32x32x16_bf16 v[16:31], v[220:223], v[164:167], v[16:31]\n\t" \
    "v_exp_f32 v153, v137\n\t" \
    "v_exp_f32 v154, v138\n\t" \
    "v_exp_f32 v155, v139\n\t" \
    "v_mfma_f32_32x32x16_bf16 v[32:47], v[224:227], v[164:167], v[32:47]\n\t" \
    "v_exp_f32 v156, v140\n\t" \
    "v_exp_f32 v157, v141\n\t" \
    "v_mfma_f32_32x32x16_bf16 v[48:63], v[228:231], v[164:167], v[48:63]\n\t" \
    "v_exp_f32 v158, v142\n\t" \
    "v_exp_f32 v159, v143\n\t" \
    "s_waitcnt lgkmcnt(0)\n\t" \
    "ds_read_b128 v[200:203], v234 offset:8192\n\t" \
    "ds_read_b128 v[204:207], v234 offset:8704\n\t" \
    "ds_read_b128 v[208:211], v234 offset:9216\n\t" \
    "ds_read_b128 v[212:215], v234 offset:9728\n\t" \
    "ds_read_b128 v[216:219], v234 offset:12288\n\t" \
    "ds_read_b128 v[220:223], v234 offset:12800\n\t" \
    "ds_read_b128 v[224:227], v234 offset:13312\n\t" \
    "ds_read_b128 v[228:231], v234 offset:13824\n\t" \
    "v_add_f32 v237, v144, v145\n\t" \
    "v_cvt_pk_bf16_f32 v160, v144, v145\n\t" \
    "v_add_f32 v237, v237, v146\n\t" \
    "v_add_f32 v237, v237, v147\n\t" \
    "v_cvt_pk_bf16_f32 v161, v146, v147\n\t" \
    "v_add_f32 v237, v237, v148\n\t" \
    "v_add_f32 v237, v237, v149\n\t" \
    "v_cvt_pk_bf16_f32 v162, v148, v149\n\t" \
    "v_add_f32 v237, v237, v150\n\t" \
    "v_add_f32 v237, v237, v151\n\t" \
    "v_cvt_pk_bf16_f32 v163, v150, v151\n\t" \
    "v_add_f32 v237, v237, v152\n\t" \
    "v_add_f32 v237, v237, v153\n\t" \
    "v_cvt_pk_bf16_f32 v164, v152, v153\n\t" \
    "v_add_f32 v237, v237, v154\n\t" \
    "v_add_f32 v237, v237, v155\n\t" \
    "v_cvt_pk_bf16_f32 v165, v154, v155\n\t" \
    "v_add_f32 v237, v237, v156\n\t" \
    "v_add_f32 v237, v237, v157\n\t" \
    "v_cvt_pk_bf16_f32 v166, v156, v157\n\t" \
    "v_add_f32 v237, v237, v158\n\t" \
    "v_add_f32 v237, v237, v159\n\t" \
    "v_cvt_pk_bf16_f32 v167, v158, v159\n\t" \
    "v_cmp_lt_f32 vcc, %[thr], v237\n\t" \
    "s_cbranch_vccnz RAREE_%=\n\t" \
    "BACKE_%=:\n\t" \
    "s_waitcnt lgkmcnt(0)\n\t" \
    "v_add_f32 v236, v236, v237\n\t" \
    "v_mfma_f32_32x32x16_bf16 v[0:15], v[200:203], v[160:163], v[0:15]\n\t" \
    "v_mfma_f32_32x32x16_bf16 v[16:31], v[204:207], v[160:163], v[16:31]\n\t" \
    "v_mfma_f32_32x32x16_bf16 v[32:47], v[208:211], v[160:163], v[32:47]\n\t" \
    "v_mfma_f32_32x32x16_bf16 v[48:63], v[212:215], v[160:163], v[48:63]\n\t" \
    "v_mfma_f32_32x32x16_bf16 v[0:15], v[216:219], v[164:167], v[0:15]\n\t" \
    "v_mfma_f32_32x32x16_bf16 v[16:31], v[220:223], v[164:167], v[16:31]\n\t" \
    "v_mfma_f32_32x32x16_bf16 v[32:47], v[224:227], v[164:167], v[32:47]\n\t" \
    "v_mfma_f32_32x32x16_bf16 v[48:63], v[228:231], v[164:167], v[48:63]\n\t" \
    "s_branch END_%=\n\t" \
    "EVEN_%=:\n\t" \
    "v_mov_b32 v239, 0xff800000\n\t" \
    "v_cmp_gt_i32 vcc, 0, %[dq]\n\t" \
    "s_nop 1\n\t" \
    "v_cndmask_b32 v96, v96, v239, vcc\n\t" \
    "v_cmp_gt_i32 vcc, 1, %[dq]\n\t" \
    "s_nop 1\n\t" \
    "v_cndmask_b32 v97, v97, v239, vcc\n\t" \
    "v_cmp_gt_i32 vcc, 2, %[dq]\n\t" \
    "s_nop 1\n\t" \
    "v_cndmask_b32 v98, v98, v239, vcc\n\t" \
    "v_cmp_gt_i32 vcc, 3, %[dq]\n\t" \
    "s_nop 1\n\t" \
    "v_cndmask_b32 v99, v99, v239, vcc\n\t" \
    "v_cmp_gt_i32 vcc, 8, %[dq]\n\t" \
    "s_nop 1\n\t" \
    "v_cndmask_b32 v100, v100, v239, vcc\n\t" \
    "v_cmp_gt_i32 vcc, 9, %[dq]\n\t" \
    "s_nop 1\n\t" \
    "v_cndmask_b32 v101, v101, v239, vcc\n\t" \
    "v_cmp_gt_i32 vcc, 10, %[dq]\n\t" \
    "s_nop 1\n\t" \
    "v_cndmask_b32 v102, v102, v239, vcc\n\t" \
    "v_cmp_gt_i32 vcc, 11, %[dq]\n\t" \
    "s_nop 1\n\t" \
    "v_cndmask_b32 v103, v103, v239, vcc\n\t" \
    "v_cmp_gt_i32 vcc, 16, %[dq]\n\t" \
    "s_nop 1\n\t" \
    "v_cndmask_b32 v104, v104, v239, vcc\n\t" \
    "v_cmp_gt_i32 vcc, 17, %[dq]\n\t" \
    "s_nop 1\n\t" \
    "v_cndmask_b32 v105, v105, v239, vcc\n\t" \
    "v_cmp_gt_i32 vcc, 18, %[dq]\n\t" \
    "s_nop 1\n\t" \
    "v_cndmask_b32 v106, v106, v239, vcc\n\t" \
    "v_cmp_gt_i32 vcc, 19, %[dq]\n\t" \
    "s_nop 1\n\t" \
    "v_cndmask_b32 v107, v107, v239, vcc\n\t" \
    "v_cmp_gt_i32 vcc, 24, %[dq]\n\t" \
    "s_nop 1\n\t" \
    "v_cndmask_b32 v108, v108, v239, vcc\n\t" \
    "v_cmp_gt_i32 vcc, 25, %[dq]\n\t" \
    "s_nop 1\n\t" \
    "v_cndmask_b32 v109, v109, v239, vcc\n\t" \
    "v_cmp_gt_i32 vcc, 26, %[dq]\n\t" \
    "s_nop 1\n\t" \
    "v_cndmask_b32 v110, v110, v239, vcc\n\t" \
    "v_cmp_gt_i32 vcc, 27, %[dq]\n\t" \
    "s_nop 1\n\t" \
    "v_cndmask_b32 v111, v111, v239, vcc\n\t" \
    "s_nop 1\n\t" \
    "s_waitcnt lgkmcnt(0)\n\t" \
    "v_mfma_f32_32x32x16_bf16 v[112:127], v[168:171], v[64:67], v[96:111]\n\t" \
    "ds_read_b128 v[200:203], v235 offset:8192\n\t" \
    "ds_read_b128 v[204:207], v235 offset:8704\n\t" \
    "v_add_f32 v237, v144, v145\n\t" \
    "v_cvt_pk_bf16_f32 v160, v144, v145\n\t" \
    "v_mfma_f32_32x32x16_bf16 v[112:127], v[172:175], v[68:71], v[112:127]\n\t" \
    "ds_read_b128 v[208:211], v235 offset:9216\n\t" \
    "ds_read_b128 v[212:215], v235 offset:9728\n\t" \
    "v_add_f32 v237, v237, v146\n\t" \
    "v_add_f32 v237, v237, v147\n\t" \
    "v_cvt_pk_bf16_f32 v161, v146, v147\n\t" \
    "v_mfma_f32_32x32x16_bf16 v[112:127], v[176:179], v[72:75], v[112:127]\n\t" \
    "ds_read_b128 v[216:219], v235 offset:12288\n\t" \
    "ds_read_b128 v[220:223], v235 offset:12800\n\t" \
    "v_add_f32 v237, v237, v148\n\t" \
    "v_add_f32 v237, v237, v149\n\t" \
    "v_cvt_pk_bf16_f32 v162, v148, v149\n\t" \
    "v_mfma_f32_32x32x16_bf16 v[112:127], v[180:183], v[76:79], v[112:127]\n\t" \
    "ds_read_b128 v[224:227], v235 offset:13312\n\t" \
    "ds_read_b128 v[228:231], v235 offset:13824\n\t" \
    "v_add_f32 v237, v237, v150\n\t" \
    "v_add_f32 v237, v237, v151\n\t" \
    "v_cvt_pk_bf16_f32 v163, v150, v151\n\t" \
    "v_mfma_f32_32x32x16_bf16 v[112:127], v[184:187], v[80:83], v[112:127]\n\t" \
    "v_add_f32 v237, v237, v152\n\t" \
    "v_add_f32 v237, v237, v153\n\t" \
    "v_cvt_pk_bf16_f32 v164, v152, v153\n\t" \
    "v_mfma_f32_32x32x16_bf16 v[112:127], v[188:191], v[84:87], v[112:127]\n\t" \
    "v_add_f32 v237, v237, v154\n\t" \
    "v_add_f32 v237, v237, v155\n\t" \
    "v_cvt_pk_bf16_f32 v165, v154, v155\n\t" \
    "v_mfma_f32_32x32x16_bf16 v[112:127], v[192:195], v[88:91], v[112:127]\n\t" \
    "v_add_f32 v237, v237, v156\n\t" \
    "v_add_f32 v237, v237, v157\n\t" \
    "v_cvt_pk_bf16_f32 v166, v156, v157\n\t" \
    "v_mfma_f32_32x32x16_bf16 v[112:127], v[196:199], v[92:95], v[112:127]\n\t" \
    "v_add_f32 v237, v237, v158\n\t" \
    "v_add_f32 v237, v237, v159\n\t" \
    "v_cvt_pk_bf16_f32 v167, v158, v159\n\t" \
    "v_cmp_lt_f32 vcc, %[thr], v237\n\t" \
    "s_cbranch_vccnz RAREF_%=\n\t" \
    "BACKF_%=:\n\t" \
    "s_waitcnt lgkmcnt(0)\n\t" \
    "v_mfma_f32_32x32x16_bf16 v[0:15], v[200:203], v[160:163], v[0:15]\n\t" \
    "ds_read_b128 v[168:171], v232 offset:512\n\t" \
    "ds_read_b128 v[172:175], v232 offset:2560\n\t" \
    "v_add_f32 v236, v236, v237\n\t" \
    "v_mfma_f32_32x32x16_bf16 v[16:31], v[204:207], v[160:163], v[16:31]\n\t" \
    "ds_read_b128 v[176:179], v232 offset:4608\n\t" \
    "ds_read_b128 v[180:183], v232 offset:6656\n\t" \
    "v_exp_f32 v144, v112\n\t" \
    "v_exp_f32 v145, v113\n\t" \
    "v_mfma_f32_32x32x16_bf16 v[32:47], v[208:211], v[160:163], v[32:47]\n\t" \
    "ds_read_b128 v[184:187], v232 offset:8704\n\t" \
    "ds_read_b128 v[188:191], v232 offset:10752\n\t" \
    "v_exp_f32 v146, v114\n\t" \
    "v_exp_f32 v147, v115\n\t" \
    "v_mfma_f32_32x32x16_bf16 v[48:63], v[212:215], v[160:163], v[48:63]\n\t" \
    "ds_read_b128 v[192:195], v232 offset:12800\n\t" \
    "ds_read_b128 v[196:199], v232 offset:14848\n\t" \
    "v_exp_f32 v148, v116\n\t" \
    "v_exp_f32 v149, v117\n\t" \
    "v_mfma_f32_32x32x16_bf16 v[0:15], v[216:219], v[164:167], v[0:15]\n\t" \
    "v_exp_f32 v150, v118\n\t" \
    "v_exp_f32 v151, v119\n\t" \
    "v_exp_f32 v152, v120\n\t" \
    "v_mfma_f32_32x32x16_bf16 v[16:31], v[220:223], v[164:167], v[16:31]\n\t" \
    "v_exp_f32 v153, v121\n\t" \
    "v_exp_f32 v154, v122\n\t" \
    "v_exp_f32 v155, v123\n\t" \
    "v_mfma_f32_32x32x16_bf16 v[32:47], v[224:227], v[164:167], v[32:47]\n\t" \
    "v_exp_f32 v156, v124\n\t" \
    "v_exp_f32 v157, v125\n\t" \
    "v_mfma_f32_32x32x16_bf16 v[48:63], v[228:231], v[164:167], v[48:63]\n\t" \
    "v_exp_f32 v158, v126\n\t" \
    "v_exp_f32 v159, v127\n\t" \
    "s_waitcnt lgkmcnt(0)\n\t" \
    "ds_read_b128 v[200:203], v234 offset:0\n\t" \
    "ds_read_b128 v[204:207], v234 offset:512\n\t" \
    "ds_read_b128 v[208:211], v234 offset:1024\n\t" \
    "ds_read_b128 v[212:215], v234 offset:1536\n\t" \
    "ds_read_b128 v[216:219], v234 offset:4096\n\t" \
    "ds_read_b128 v[220:223], v234 offset:4608\n\t" \
    "ds_read_b128 v[224:227], v234 offset:5120\n\t" \
    "ds_read_b128 v[228:231], v234 offset:5632\n\t" \
    "v_add_f32 v237, v144, v145\n\t" \
    "v_cvt_pk_bf16_f32 v160, v144, v145\n\t" \
    "v_add_f32 v237, v237, v146\n\t" \
    "v_add_f32 v237, v237, v147\n\t" \
    "v_cvt_pk_bf16_f32 v161, v146, v147\n\t" \
    "v_add_f32 v237, v237, v148\n\t" \
    "v_add_f32 v237, v237, v149\n\t" \
    "v_cvt_pk_bf16_f32 v162, v148, v149\n\t" \
    "v_add_f32 v237, v237, v150\n\t" \
    "v_add_f32 v237, v237, v151\n\t" \
    "v_cvt_pk_bf16_f32 v163, v150, v151\n\t" \
    "v_add_f32 v237, v237, v152\n\t" \
    "v_add_f32 v237, v237, v153\n\t" \
    "v_cvt_pk_bf16_f32 v164, v152, v153\n\t" \
    "v_add_f32 v237, v237, v154\n\t" \
    "v_add_f32 v237, v237, v155\n\t" \
    "v_cvt_pk_bf16_f32 v165, v154, v155\n\t" \
    "v_add_f32 v237, v237, v156\n\t" \
    "v_add_f32 v237, v237, v157\n\t" \
    "v_cvt_pk_bf16_f32 v166, v156, v157\n\t" \
    "v_add_f32 v237, v237, v158\n\t" \
    "v_add_f32 v237, v237, v159\n\t" \
    "v_cvt_pk_bf16_f32 v167, v158, v159\n\t" \
    "v_cmp_lt_f32 vcc, %[thr], v237\n\t" \
    "s_cbranch_vccnz RAREG_%=\n\t" \
    "BACKG_%=:\n\t" \
    "s_waitcnt lgkmcnt(0)\n\t" \
    "v_add_f32 v236, v236, v237\n\t" \
    "v_mfma_f32_32x32x16_bf16 v[0:15], v[200:203], v[160:163], v[0:15]\n\t" \
    "v_mfma_f32_32x32x16_bf16 v[16:31], v[204:207], v[160:163], v[16:31]\n\t" \
    "v_mfma_f32_32x32x16_bf16 v[32:47], v[208:211], v[160:163], v[32:47]\n\t" \
    "v_mfma_f32_32x32x16_bf16 v[48:63], v[212:215], v[160:163], v[48:63]\n\t" \
    "v_mfma_f32_32x32x16_bf16 v[0:15], v[216:219], v[164:167], v[0:15]\n\t" \
    "v_mfma_f32_32x32x16_bf16 v[16:31], v[220:223], v[164:167], v[16:31]\n\t" \
    "v_mfma_f32_32x32x16_bf16 v[32:47], v[224:227], v[164:167], v[32:47]\n\t" \
    "v_mfma_f32_32x32x16_bf16 v[48:63], v[228:231], v[164:167], v[48:63]\n\t" \
    "s_branch END_%=\n\t" \
    "DMAL_%=:\n\t" \
    "s_add_u32 s60, %[t], 3\n\t" \
    "s_min_u32 s60, s60, %[ntm1]\n\t" \
    "s_lshl_b32 s60, s60, 14\n\t" \
    "s_add_u32 s62, %[khl], s60\n\t" \
    "s_addc_u32 s63, %[khh], 0\n\t" \
    "s_add_u32 s61, %[soff], 0xc000\n\t" \
    "s_and_b32 s61, s61, 0xffff\n\t" \
    "s_add_u32 m0, s61, %[kdst]\n\t" \
    "s_nop 0\n\t" \
    "global_load_lds_dwordx4 %[dvo], s[62:63] offset:0\n\t" \
    "global_load_lds_dwordx4 %[dvo], s[62:63] offset:1024\n\t" \
    "global_load_lds_dwordx4 %[dvo], s[62:63] offset:2048\n\t" \
    "global_load_lds_dwordx4 %[dvo], s[62:63] offset:3072\n\t" \
    "s_add_u32 s60, %[t], 2\n\t" \
    "s_min_u32 s60, s60, %[ntm1]\n\t" \
    "s_lshl_b32 s60, s60, 14\n\t" \
    "s_add_u32 s62, %[vhl], s60\n\t" \
    "s_addc_u32 s63, %[vhh], 0\n\t" \
    "s_add_u32 s61, %[soff], 0x8000\n\t" \
    "s_and_b32 s61, s61, 0xffff\n\t" \
    "s_add_u32 m0, s61, %[vdst]\n\t" \
    "s_nop 0\n\t" \
    "global_load_lds_dwordx4 %[dvo], s[62:63] offset:0\n\t" \
    "global_load_lds_dwordx4 %[dvo], s[62:63] offset:1024\n\t" \
    "global_load_lds_dwordx4 %[dvo], s[62:63] offset:2048\n\t" \
    "global_load_lds_dwordx4 %[dvo], s[62:63] offset:3072\n\t" \
    "s_branch DMABL_%=\n\t" \
    "DMAT_%=:\n\t" \
    "s_add_u32 s60, %[t], 3\n\t" \
    "s_min_u32 s60, s60, %[ntm1]\n\t" \
    "s_lshl_b32 s60, s60, 14\n\t" \
    "s_add_u32 s62, %[khl], s60\n\t" \
    "s_addc_u32 s63, %[khh], 0\n\t" \
    "s_add_u32 s61, %[soff], 0xc000\n\t" \
    "s_and_b32 s61, s61, 0xffff\n\t" \
    "s_add_u32 m0, s61, %[kdst]\n\t" \
    "s_nop 0\n\t" \
    "global_load_lds_dwordx4 %[dvo], s[62:63] offset:0\n\t" \
    "global_load_lds_dwordx4 %[dvo], s[62:63] offset:1024\n\t" \
    "global_load_lds_dwordx4 %[dvo], s[62:63] offset:2048\n\t" \
    "global_load_lds_dwordx4 %[dvo], s[62:63] offset:3072\n\t" \
    "s_add_u32 s60, %[t], 2\n\t" \
    "s_min_u32 s60, s60, %[ntm1]\n\t" \
    "s_lshl_b32 s60, s60, 14\n\t" \
    "s_add_u32 s62, %[vhl], s60\n\t" \
    "s_addc_u32 s63, %[vhh], 0\n\t" \
    "s_add_u32 s61, %[soff], 0x8000\n\t" \
    "s_and_b32 s61, s61, 0xffff\n\t" \
    "s_add_u32 m0, s61, %[vdst]\n\t" \
    "s_nop 0\n\t" \
    "global_load_lds_dwordx4 %[dvo], s[62:63] offset:0\n\t" \
    "global_load_lds_dwordx4 %[dvo], s[62:63] offset:1024\n\t" \
    "global_load_lds_dwordx4 %[dvo], s[62:63] offset:2048\n\t" \
    "global_load_lds_dwordx4 %[dvo], s[62:63] offset:3072\n\t" \
    "s_branch DMABT_%=\n\t" \
    "RAREA_%=:\n\t" \
    "s_nop 15\n\t" \
    "v_max3_f32 v239, v128, v129, v130\n\t" \
    "v_max3_f32 v239, v239, v131, v132\n\t" \
    "v_max3_f32 v239, v239, v133, v134\n\t" \
    "v_max3_f32 v239, v239, v135, v136\n\t" \
    "v_max3_f32 v239, v239, v137, v138\n\t" \
    "v_max3_f32 v239, v239, v139, v140\n\t" \
    "v_max3_f32 v239, v239, v141, v142\n\t" \
    "v_max_f32 v239, v239, v143\n\t" \
    "v_max_f32 v239, 0, v239\n\t" \
    "v_mov_b32 v240, v239\n\t" \
    "s_nop 1\n\t" \
    "v_permlane32_swap_b32 v239, v240\n\t" \
    "s_nop 1\n\t" \
    "v_max_f32 v239, v239, v240\n\t" \
    "v_add_f32 v238, v238, v239\n\t" \
    "v_exp_f32 v240, -v239\n\t" \
    "v_sub_f32 v241, 0, v238\n\t" \
    "v_sub_f32 v128, v128, v239\n\t" \
    "v_sub_f32 v112, v112, v239\n\t" \
    "v_mov_b32 v96, v241\n\t" \
    "v_sub_f32 v129, v129, v239\n\t" \
    "v_sub_f32 v113, v113, v239\n\t" \
    "v_mov_b32 v97, v241\n\t" \
    "v_sub_f32 v130, v130, v239\n\t" \
    "v_sub_f32 v114, v114, v239\n\t" \
    "v_mov_b32 v98, v241\n\t" \
    "v_sub_f32 v131, v131, v239\n\t" \
    "v_sub_f32 v115, v115, v239\n\t" \
    "v_mov_b32 v99, v241\n\t" \
    "v_sub_f32 v132, v132, v239\n\t" \
    "v_sub_f32 v116, v116, v239\n\t" \
    "v_mov_b32 v100, v241\n\t" \
    "v_sub_f32 v133, v133, v239\n\t" \
    "v_sub_f32 v117, v117, v239\n\t" \
    "v_mov_b32 v101, v241\n\t" \
    "v_sub_f32 v134, v134, v239\n\t" \
    "v_sub_f32 v118, v118, v239\n\t" \
    "v_mov_b32 v102, v241\n\t" \
    "v_sub_f32 v135, v135, v239\n\t" \
    "v_sub_f32 v119, v119, v239\n\t" \
    "v_mov_b32 v103, v241\n\t" \
    "v_sub_f32 v136, v136, v239\n\t" \
    "v_sub_f32 v120, v120, v239\n\t" \
    "v_mov_b32 v104, v241\n\t" \
    "v_sub_f32 v137, v137, v239\n\t" \
    "v_sub_f32 v121, v121, v239\n\t" \
    "v_mov_b32 v105, v241\n\t" \
    "v_sub_f32 v138, v138, v239\n\t" \
    "v_sub_f32 v122, v122, v239\n\t" \
    "v_mov_b32 v106, v241\n\t" \
    "v_sub_f32 v139, v139, v239\n\t" \
    "v_sub_f32 v123, v123, v239\n\t" \
    "v_mov_b32 v107, v241\n\t" \
    "v_sub_f32 v140, v140, v239\n\t" \
    "v_sub_f32 v124, v124, v239\n\t" \
    "v_mov_b32 v108, v241\n\t" \
    "v_sub_f32 v141, v141, v239\n\t" \
    "v_sub_f32 v125, v125, v239\n\t" \
    "v_mov_b32 v109, v241\n\t" \
    "v_sub_f32 v142, v142, v239\n\t" \
    "v_sub_f32 v126, v126, v239\n\t" \
    "v_mov_b32 v110, v241\n\t" \
    "v_sub_f32 v143, v143, v239\n\t" \
    "v_sub_f32 v127, v127, v239\n\t" \
    "v_mov_b32 v111, v241\n\t" \
    "v_exp_f32 v144, v128\n\t" \
    "v_exp_f32 v145, v129\n\t" \
    "v_exp_f32 v146, v130\n\t" \
    "v_exp_f32 v147, v131\n\t" \
    "v_exp_f32 v148, v132\n\t" \
    "v_exp_f32 v149, v133\n\t" \
    "v_exp_f32 v150, v134\n\t" \
    "v_exp_f32 v151, v135\n\t" \
    "v_exp_f32 v152, v136\n\t" \
    "v_exp_f32 v153, v137\n\t" \
    "v_exp_f32 v154, v138\n\t" \
    "v_exp_f32 v155, v139\n\t" \
    "v_exp_f32 v156, v140\n\t" \
    "v_exp_f32 v157, v141\n\t" \
    "v_exp_f32 v158, v142\n\t" \
    "v_exp_f32 v159, v143\n\t" \
    "v_mul_f32 v236, v236, v240\n\t" \
    "v_mul_f32 v0, v0, v240\n\t" \
    "v_mul_f32 v1, v1, v240\n\t" \
    "v_mul_f32 v2, v2, v240\n\t" \
    "v_mul_f32 v3, v3, v240\n\t" \
    "v_mul_f32 v4, v4, v240\n\t" \
    "v_mul_f32 v5, v5, v240\n\t" \
    "v_mul_f32 v6, v6, v240\n\t" \
    "v_mul_f32 v7, v7, v240\n\t" \
    "v_mul_f32 v8, v8, v240\n\t" \
    "v_mul_f32 v9, v9, v240\n\t" \
    "v_mul_f32 v10, v10, v240\n\t" \
    "v_mul_f32 v11, v11, v240\n\t" \
    "v_mul_f32 v12, v12, v240\n\t" \
    "v_mul_f32 v13, v13, v240\n\t" \
    "v_mul_f32 v14, v14, v240\n\t" \
    "v_mul_f32 v15, v15, v240\n\t" \
    "v_mul_f32 v16, v16, v240\n\t" \
    "v_mul_f32 v17, v17, v240\n\t" \
    "v_mul_f32 v18, v18, v240\n\t" \
    "v_mul_f32 v19, v19, v240\n\t" \
    "v_mul_f32 v20, v20, v240\n\t" \
    "v_mul_f32 v21, v21, v240\n\t" \
    "v_mul_f32 v22, v22, v240\n\t" \
    "v_mul_f32 v23, v23, v240\n\t" \
    "v_mul_f32 v24, v24, v240\n\t" \
    "v_mul_f32 v25, v25, v240\n\t" \
    "v_mul_f32 v26, v26, v240\n\t" \
    "v_mul_f32 v27, v27, v240\n\t" \
    "v_mul_f32 v28, v28, v240\n\t" \
    "v_mul_f32 v29, v29, v240\n\t" \
    "v_mul_f32 v30, v30, v240\n\t" \
    "v_mul_f32 v31, v31, v240\n\t" \
    "v_mul_f32 v32, v32, v240\n\t" \
    "v_mul_f32 v33, v33, v240\n\t" \
    "v_mul_f32 v34, v34, v240\n\t" \
    "v_mul_f32 v35, v35, v240\n\t" \
    "v_mul_f32 v36, v36, v240\n\t" \
    "v_mul_f32 v37, v37, v240\n\t" \
    "v_mul_f32 v38, v38, v240\n\t" \
    "v_mul_f32 v39, v39, v240\n\t" \
    "v_mul_f32 v40, v40, v240\n\t" \
    "v_mul_f32 v41, v41, v240\n\t" \
    "v_mul_f32 v42, v42, v240\n\t" \
    "v_mul_f32 v43, v43, v240\n\t" \
    "v_mul_f32 v44, v44, v240\n\t" \
    "v_mul_f32 v45, v45, v240\n\t" \
    "v_mul_f32 v46, v46, v240\n\t" \
    "v_mul_f32 v47, v47, v240\n\t" \
    "v_mul_f32 v48, v48, v240\n\t" \
    "v_mul_f32 v49, v49, v240\n\t" \
    "v_mul_f32 v50, v50, v240\n\t" \
    "v_mul_f32 v51, v51, v240\n\t" \
    "v_mul_f32 v52, v52, v240\n\t" \
    "v_mul_f32 v53, v53, v240\n\t" \
    "v_mul_f32 v54, v54, v240\n\t" \
    "v_mul_f32 v55, v55, v240\n\t" \
    "v_mul_f32 v56, v56, v240\n\t" \
    "v_mul_f32 v57, v57, v240\n\t" \
    "v_mul_f32 v58, v58, v240\n\t" \
    "v_mul_f32 v59, v59, v240\n\t" \
    "v_mul_f32 v60, v60, v240\n\t" \
    "v_mul_f32 v61, v61, v240\n\t" \
    "v_mul_f32 v62, v62, v240\n\t" \
    "v_mul_f32 v63, v63, v240\n\t" \
    "v_add_f32 v237, v144, v145\n\t" \
    "v_add_f32 v237, v237, v146\n\t" \
    "v_add_f32 v237, v237, v147\n\t" \
    "v_add_f32 v237, v237, v148\n\t" \
    "v_add_f32 v237, v237, v149\n\t" \
    "v_add_f32 v237, v237, v150\n\t" \
    "v_add_f32 v237, v237, v151\n\t" \
    "v_add_f32 v237, v237, v152\n\t" \
    "v_add_f32 v237, v237, v153\n\t" \
    "v_add_f32 v237, v237, v154\n\t" \
    "v_add_f32 v237, v237, v155\n\t" \
    "v_add_f32 v237, v237, v156\n\t" \
    "v_add_f32 v237, v237, v157\n\t" \
    "v_add_f32 v237, v237, v158\n\t" \
    "v_add_f32 v237, v237, v159\n\t" \
    "v_cvt_pk_bf16_f32 v160, v144, v145\n\t" \
    "v_cvt_pk_bf16_f32 v161, v146, v147\n\t" \
    "v_cvt_pk_bf16_f32 v162, v148, v149\n\t" \
    "v_cvt_pk_bf16_f32 v163, v150, v151\n\t" \
    "v_cvt_pk_bf16_f32 v164, v152, v153\n\t" \
    "v_cvt_pk_bf16_f32 v165, v154, v155\n\t" \
    "v_cvt_pk_bf16_f32 v166, v156, v157\n\t" \
    "v_cvt_pk_bf16_f32 v167, v158, v159\n\t" \
    "s_nop 1\n\t" \
    "s_branch BACKA_%=\n\t" \
    "RAREB_%=:\n\t" \
    "s_nop 15\n\t" \
    "v_max3_f32 v239, v112, v113, v114\n\t" \
    "v_max3_f32 v239, v239, v115, v116\n\t" \
    "v_max3_f32 v239, v239, v117, v118\n\t" \
    "v_max3_f32 v239, v239, v119, v120\n\t" \
    "v_max3_f32 v239, v239, v121, v122\n\t" \
    "v_max3_f32 v239, v239, v123, v124\n\t" \
    "v_max3_f32 v239, v239, v125, v126\n\t" \
    "v_max_f32 v239, v239, v127\n\t" \
    "v_max_f32 v239, 0, v239\n\t" \
    "v_mov_b32 v240, v239\n\t" \
    "s_nop 1\n\t" \
    "v_permlane32_swap_b32 v239, v240\n\t" \
    "s_nop 1\n\t" \
    "v_max_f32 v239, v239, v240\n\t" \
    "v_add_f32 v238, v238, v239\n\t" \
    "v_exp_f32 v240, -v239\n\t" \
    "v_sub_f32 v241, 0, v238\n\t" \
    "v_sub_f32 v112, v112, v239\n\t" \
    "v_sub_f32 v128, v128, v239\n\t" \
    "v_mov_b32 v96, v241\n\t" \
    "v_sub_f32 v113, v113, v239\n\t" \
    "v_sub_f32 v129, v129, v239\n\t" \
    "v_mov_b32 v97, v241\n\t" \
    "v_sub_f32 v114, v114, v239\n\t" \
    "v_sub_f32 v130, v130, v239\n\t" \
    "v_mov_b32 v98, v241\n\t" \
    "v_sub_f32 v115, v115, v239\n\t" \
    "v_sub_f32 v131, v131, v239\n\t" \
    "v_mov_b32 v99, v241\n\t" \
    "v_sub_f32 v116, v116, v239\n\t" \
    "v_sub_f32 v132, v132, v239\n\t" \
    "v_mov_b32 v100, v241\n\t" \
    "v_sub_f32 v117, v117, v239\n\t" \
    "v_sub_f32 v133, v133, v239\n\t" \
    "v_mov_b32 v101, v241\n\t" \
    "v_sub_f32 v118, v118, v239\n\t" \
    "v_sub_f32 v134, v134, v239\n\t" \
    "v_mov_b32 v102, v241\n\t" \
    "v_sub_f32 v119, v119, v239\n\t" \
    "v_sub_f32 v135, v135, v239\n\t" \
    "v_mov_b32 v103, v241\n\t" \
    "v_sub_f32 v120, v120, v239\n\t" \
    "v_sub_f32 v136, v136, v239\n\t" \
    "v_mov_b32 v104, v241\n\t" \
    "v_sub_f32 v121, v121, v239\n\t" \
    "v_sub_f32 v137, v137, v239\n\t" \
    "v_mov_b32 v105, v241\n\t" \
    "v_sub_f32 v122, v122, v239\n\t" \
    "v_sub_f32 v138, v138, v239\n\t" \
    "v_mov_b32 v106, v241\n\t" \
    "v_sub_f32 v123, v123, v239\n\t" \
    "v_sub_f32 v139, v139, v239\n\t" \
    "v_mov_b32 v107, v241\n\t" \
    "v_sub_f32 v124, v124, v239\n\t" \
    "v_sub_f32 v140, v140, v239\n\t" \
    "v_mov_b32 v108, v241\n\t" \
    "v_sub_f32 v125, v125, v239\n\t" \
    "v_sub_f32 v141, v141, v239\n\t" \
    "v_mov_b32 v109, v241\n\t" \
    "v_sub_f32 v126, v126, v239\n\t" \
    "v_sub_f32 v142, v142, v239\n\t" \
    "v_mov_b32 v110, v241\n\t" \
    "v_sub_f32 v127, v127, v239\n\t" \
    "v_sub_f32 v143, v143, v239\n\t" \
    "v_mov_b32 v111, v241\n\t" \
    "v_exp_f32 v144, v112\n\t" \
    "v_exp_f32 v145, v113\n\t" \
    "v_exp_f32 v146, v114\n\t" \
    "v_exp_f32 v147, v115\n\t" \
    "v_exp_f32 v148, v116\n\t" \
    "v_exp_f32 v149, v117\n\t" \
    "v_exp_f32 v150, v118\n\t" \
    "v_exp_f32 v151, v119\n\t" \
    "v_exp_f32 v152, v120\n\t" \
    "v_exp_f32 v153, v121\n\t" \
    "v_exp_f32 v154, v122\n\t" \
    "v_exp_f32 v155, v123\n\t" \
    "v_exp_f32 v156, v124\n\t" \
    "v_exp_f32 v157, v125\n\t" \
    "v_exp_f32 v158, v126\n\t" \
    "v_exp_f32 v159, v127\n\t" \
    "v_mul_f32 v236, v236, v240\n\t" \
    "v_mul_f32 v0, v0, v240\n\t" \
    "v_mul_f32 v1, v1, v240\n\t" \
    "v_mul_f32 v2, v2, v240\n\t" \
    "v_mul_f32 v3, v3, v240\n\t" \
    "v_mul_f32 v4, v4, v240\n\t" \
    "v_mul_f32 v5, v5, v240\n\t" \
    "v_mul_f32 v6, v6, v240\n\t" \
    "v_mul_f32 v7, v7, v240\n\t" \
    "v_mul_f32 v8, v8, v240\n\t" \
    "v_mul_f32 v9, v9, v240\n\t" \
    "v_mul_f32 v10, v10, v240\n\t" \
    "v_mul_f32 v11, v11, v240\n\t" \
    "v_mul_f32 v12, v12, v240\n\t" \
    "v_mul_f32 v13, v13, v240\n\t" \
    "v_mul_f32 v14, v14, v240\n\t" \
    "v_mul_f32 v15, v15, v240\n\t" \
    "v_mul_f32 v16, v16, v240\n\t" \
    "v_mul_f32 v17, v17, v240\n\t" \
    "v_mul_f32 v18, v18, v240\n\t" \
    "v_mul_f32 v19, v19, v240\n\t" \
    "v_mul_f32 v20, v20, v240\n\t" \
    "v_mul_f32 v21, v21, v240\n\t" \
    "v_mul_f32 v22, v22, v240\n\t" \
    "v_mul_f32 v23, v23, v240\n\t" \
    "v_mul_f32 v24, v24, v240\n\t" \
    "v_mul_f32 v25, v25, v240\n\t" \
    "v_mul_f32 v26, v26, v240\n\t" \
    "v_mul_f32 v27, v27, v240\n\t" \
    "v_mul_f32 v28, v28, v240\n\t" \
    "v_mul_f32 v29, v29, v240\n\t" \
    "v_mul_f32 v30, v30, v240\n\t" \
    "v_mul_f32 v31, v31, v240\n\t" \
    "v_mul_f32 v32, v32, v240\n\t" \
    "v_mul_f32 v33, v33, v240\n\t" \
    "v_mul_f32 v34, v34, v240\n\t" \
    "v_mul_f32 v35, v35, v240\n\t" \
    "v_mul_f32 v36, v36, v240\n\t" \
    "v_mul_f32 v37, v37, v240\n\t" \
    "v_mul_f32 v38, v38, v240\n\t" \
    "v_mul_f32 v39, v39, v240\n\t" \
    "v_mul_f32 v40, v40, v240\n\t" \
    "v_mul_f32 v41, v41, v240\n\t" \
    "v_mul_f32 v42, v42, v240\n\t" \
    "v_mul_f32 v43, v43, v240\n\t" \
    "v_mul_f32 v44, v44, v240\n\t" \
    "v_mul_f32 v45, v45, v240\n\t" \
    "v_mul_f32 v46, v46, v240\n\t" \
    "v_mul_f32 v47, v47, v240\n\t" \
    "v_mul_f32 v48, v48, v240\n\t" \
    "v_mul_f32 v49, v49, v240\n\t" \
    "v_mul_f32 v50, v50, v240\n\t" \
    "v_mul_f32 v51, v51, v240\n\t" \
    "v_mul_f32 v52, v52, v240\n\t" \
    "v_mul_f32 v53, v53, v240\n\t" \
    "v_mul_f32 v54, v54, v240\n\t" \
    "v_mul_f32 v55, v55, v240\n\t" \
    "v_mul_f32 v56, v56, v240\n\t" \
    "v_mul_f32 v57, v57, v240\n\t" \
    "v_mul_f32 v58, v58, v240\n\t" \
    "v_mul_f32 v59, v59, v240\n\t" \
    "v_mul_f32 v60, v60, v240\n\t" \
    "v_mul_f32 v61, v61, v240\n\t" \
    "v_mul_f32 v62, v62, v240\n\t" \
    "v_mul_f32 v63, v63, v240\n\t" \
    "v_add_f32 v237, v144, v145\n\t" \
    "v_add_f32 v237, v237, v146\n\t" \
    "v_add_f32 v237, v237, v147\n\t" \
    "v_add_f32 v237, v237, v148\n\t" \
    "v_add_f32 v237, v237, v149\n\t" \
    "v_add_f32 v237, v237, v150\n\t" \
    "v_add_f32 v237, v237, v151\n\t" \
    "v_add_f32 v237, v237, v152\n\t" \
    "v_add_f32 v237, v237, v153\n\t" \
    "v_add_f32 v237, v237, v154\n\t" \
    "v_add_f32 v237, v237, v155\n\t" \
    "v_add_f32 v237, v237, v156\n\t" \
    "v_add_f32 v237, v237, v157\n\t" \
    "v_add_f32 v237, v237, v158\n\t" \
    "v_add_f32 v237, v237, v159\n\t" \
    "v_cvt_pk_bf16_f32 v160, v144, v145\n\t" \
    "v_cvt_pk_bf16_f32 v161, v146, v147\n\t" \
    "v_cvt_pk_bf16_f32 v162, v148, v149\n\t" \
    "v_cvt_pk_bf16_f32 v163, v150, v151\n\t" \
    "v_cvt_pk_bf16_f32 v164, v152, v153\n\t" \
    "v_cvt_pk_bf16_f32 v165, v154, v155\n\t" \
    "v_cvt_pk_bf16_f32 v166, v156, v157\n\t" \
    "v_cvt_pk_bf16_f32 v167, v158, v159\n\t" \
    "s_nop 1\n\t" \
    "s_branch BACKB_%=\n\t" \
    "RAREC_%=:\n\t" \
    "s_nop 15\n\t" \
    "v_max3_f32 v239, v128, v129, v130\n\t" \
    "v_max3_f32 v239, v239, v131, v132\n\t" \
    "v_max3_f32 v239, v239, v133, v134\n\t" \
    "v_max3_f32 v239, v239, v135, v136\n\t" \
    "v_max3_f32 v239, v239, v137, v138\n\t" \
    "v_max3_f32 v239, v239, v139, v140\n\t" \
    "v_max3_f32 v239, v239, v141, v142\n\t" \
    "v_max_f32 v239, v239, v143\n\t" \
    "v_max_f32 v239, 0, v239\n\t" \
    "v_mov_b32 v240, v239\n\t" \
    "s_nop 1\n\t" \
    "v_permlane32_swap_b32 v239, v240\n\t" \
    "s_nop 1\n\t" \
    "v_max_f32 v239, v239, v240\n\t" \
    "v_add_f32 v238, v238, v239\n\t" \
    "v_exp_f32 v240, -v239\n\t" \
    "v_sub_f32 v241, 0, v238\n\t" \
    "v_sub_f32 v128, v128, v239\n\t" \
    "v_sub_f32 v112, v112, v239\n\t" \
    "v_mov_b32 v96, v241\n\t" \
    "v_sub_f32 v129, v129, v239\n\t" \
    "v_sub_f32 v113, v113, v239\n\t" \
    "v_mov_b32 v97, v241\n\t" \
    "v_sub_f32 v130, v130, v239\n\t" \
    "v_sub_f32 v114, v114, v239\n\t" \
    "v_mov_b32 v98, v241\n\t" \
    "v_sub_f32 v131, v131, v239\n\t" \
    "v_sub_f32 v115, v115, v239\n\t" \
    "v_mov_b32 v99, v241\n\t" \
    "v_sub_f32 v132, v132, v239\n\t" \
    "v_sub_f32 v116, v116, v239\n\t" \
    "v_mov_b32 v100, v241\n\t" \
    "v_sub_f32 v133, v133, v239\n\t" \
    "v_sub_f32 v117, v117, v239\n\t" \
    "v_mov_b32 v101, v241\n\t" \
    "v_sub_f32 v134, v134, v239\n\t" \
    "v_sub_f32 v118, v118, v239\n\t" \
    "v_mov_b32 v102, v241\n\t" \
    "v_sub_f32 v135, v135, v239\n\t" \
    "v_sub_f32 v119, v119, v239\n\t" \
    "v_mov_b32 v103, v241\n\t" \
    "v_sub_f32 v136, v136, v239\n\t" \
    "v_sub_f32 v120, v120, v239\n\t" \
    "v_mov_b32 v104, v241\n\t" \
    "v_sub_f32 v137, v137, v239\n\t" \
    "v_sub_f32 v121, v121, v239\n\t" \
    "v_mov_b32 v105, v241\n\t" \
    "v_sub_f32 v138, v138, v239\n\t" \
    "v_sub_f32 v122, v122, v239\n\t" \
    "v_mov_b32 v106, v241\n\t" \
    "v_sub_f32 v139, v139, v239\n\t" \
    "v_sub_f32 v123, v123, v239\n\t" \
    "v_mov_b32 v107, v241\n\t" \
    "v_sub_f32 v140, v140, v239\n\t" \
    "v_sub_f32 v124, v124, v239\n\t" \
    "v_mov_b32 v108, v241\n\t" \
    "v_sub_f32 v141, v141, v239\n\t" \
    "v_sub_f32 v125, v125, v239\n\t" \
    "v_mov_b32 v109, v241\n\t" \
    "v_sub_f32 v142, v142, v239\n\t" \
    "v_sub_f32 v126, v126, v239\n\t" \
    "v_mov_b32 v110, v241\n\t" \
    "v_sub_f32 v143, v143, v239\n\t" \
    "v_sub_f32 v127, v127, v239\n\t" \
    "v_mov_b32 v111, v241\n\t" \
    "v_exp_f32 v144, v128\n\t" \
    "v_exp_f32 v145, v129\n\t" \
    "v_exp_f32 v146, v130\n\t" \
    "v_exp_f32 v147, v131\n\t" \
    "v_exp_f32 v148, v132\n\t" \
    "v_exp_f32 v149, v133\n\t" \
    "v_exp_f32 v150, v134\n\t" \
    "v_exp_f32 v151, v135\n\t" \
    "v_exp_f32 v152, v136\n\t" \
    "v_exp_f32 v153, v137\n\t" \
    "v_exp_f32 v154, v138\n\t" \
    "v_exp_f32 v155, v139\n\t" \
    "v_exp_f32 v156, v140\n\t" \
    "v_exp_f32 v157, v141\n\t" \
    "v_exp_f32 v158, v142\n\t" \
    "v_exp_f32 v159, v143\n\t" \
    "v_mul_f32 v236, v236, v240\n\t" \
    "v_mul_f32 v0, v0, v240\n\t" \
    "v_mul_f32 v1, v1, v240\n\t" \
    "v_mul_f32 v2, v2, v240\n\t" \
    "v_mul_f32 v3, v3, v240\n\t" \
    "v_mul_f32 v4, v4, v240\n\t" \
    "v_mul_f32 v5, v5, v240\n\t" \
    "v_mul_f32 v6, v6, v240\n\t" \
    "v_mul_f32 v7, v7, v240\n\t" \
    "v_mul_f32 v8, v8, v240\n\t" \
    "v_mul_f32 v9, v9, v240\n\t" \
    "v_mul_f32 v10, v10, v240\n\t" \
    "v_mul_f32 v11, v11, v240\n\t" \
    "v_mul_f32 v12, v12, v240\n\t" \
    "v_mul_f32 v13, v13, v240\n\t" \
    "v_mul_f32 v14, v14, v240\n\t" \
    "v_mul_f32 v15, v15, v240\n\t" \
    "v_mul_f32 v16, v16, v240\n\t" \
    "v_mul_f32 v17, v17, v240\n\t" \
    "v_mul_f32 v18, v18, v240\n\t" \
    "v_mul_f32 v19, v19, v240\n\t" \
    "v_mul_f32 v20, v20, v240\n\t" \
    "v_mul_f32 v21, v21, v240\n\t" \
    "v_mul_f32 v22, v22, v240\n\t" \
    "v_mul_f32 v23, v23, v240\n\t" \
    "v_mul_f32 v24, v24, v240\n\t" \
    "v_mul_f32 v25, v25, v240\n\t" \
    "v_mul_f32 v26, v26, v240\n\t" \
    "v_mul_f32 v27, v27, v240\n\t" \
    "v_mul_f32 v28, v28, v240\n\t" \
    "v_mul_f32 v29, v29, v240\n\t" \
    "v_mul_f32 v30, v30, v240\n\t" \
    "v_mul_f32 v31, v31, v240\n\t" \
    "v_mul_f32 v32, v32, v240\n\t" \
    "v_mul_f32 v33, v33, v240\n\t" \
    "v_mul_f32 v34, v34, v240\n\t" \
    "v_mul_f32 v35, v35, v240\n\t" \
    "v_mul_f32 v36, v36, v240\n\t" \
    "v_mul_f32 v37, v37, v240\n\t" \
    "v_mul_f32 v38, v38, v240\n\t" \
    "v_mul_f32 v39, v39, v240\n\t" \
    "v_mul_f32 v40, v40, v240\n\t" \
    "v_mul_f32 v41, v41, v240\n\t" \
    "v_mul_f32 v42, v42, v240\n\t" \
    "v_mul_f32 v43, v43, v240\n\t" \
    "v_mul_f32 v44, v44, v240\n\t" \
    "v_mul_f32 v45, v45, v240\n\t" \
    "v_mul_f32 v46, v46, v240\n\t" \
    "v_mul_f32 v47, v47, v240\n\t" \
    "v_mul_f32 v48, v48, v240\n\t" \
    "v_mul_f32 v49, v49, v240\n\t" \
    "v_mul_f32 v50, v50, v240\n\t" \
    "v_mul_f32 v51, v51, v240\n\t" \
    "v_mul_f32 v52, v52, v240\n\t" \
    "v_mul_f32 v53, v53, v240\n\t" \
    "v_mul_f32 v54, v54, v240\n\t" \
    "v_mul_f32 v55, v55, v240\n\t" \
    "v_mul_f32 v56, v56, v240\n\t" \
    "v_mul_f32 v57, v57, v240\n\t" \
    "v_mul_f32 v58, v58, v240\n\t" \
    "v_mul_f32 v59, v59, v240\n\t" \
    "v_mul_f32 v60, v60, v240\n\t" \
    "v_mul_f32 v61, v61, v240\n\t" \
    "v_mul_f32 v62, v62, v240\n\t" \
    "v_mul_f32 v63, v63, v240\n\t" \
    "v_add_f32 v237, v144, v145\n\t" \
    "v_add_f32 v237, v237, v146\n\t" \
    "v_add_f32 v237, v237, v147\n\t" \
    "v_add_f32 v237, v237, v148\n\t" \
    "v_add_f32 v237, v237, v149\n\t" \
    "v_add_f32 v237, v237, v150\n\t" \
    "v_add_f32 v237, v237, v151\n\t" \
    "v_add_f32 v237, v237, v152\n\t" \
    "v_add_f32 v237, v237, v153\n\t" \
    "v_add_f32 v237, v237, v154\n\t" \
    "v_add_f32 v237, v237, v155\n\t" \
    "v_add_f32 v237, v237, v156\n\t" \
    "v_add_f32 v237, v237, v157\n\t" \
    "v_add_f32 v237, v237, v158\n\t" \
    "v_add_f32 v237, v237, v159\n\t" \
    "v_cvt_pk_bf16_f32 v160, v144, v145\n\t" \
    "v_cvt_pk_bf16_f32 v161, v146, v147\n\t" \
    "v_cvt_pk_bf16_f32 v162, v148, v149\n\t" \
    "v_cvt_pk_bf16_f32 v163, v150, v151\n\t" \
    "v_cvt_pk_bf16_f32 v164, v152, v153\n\t" \
    "v_cvt_pk_bf16_f32 v165, v154, v155\n\t" \
    "v_cvt_pk_bf16_f32 v166, v156, v157\n\t" \
    "v_cvt_pk_bf16_f32 v167, v158, v159\n\t" \
    "s_nop 1\n\t" \
    "s_branch BACKC_%=\n\t" \
    "RARED_%=:\n\t" \
    "s_nop 15\n\t" \
    "v_max3_f32 v239, v112, v113, v114\n\t" \
    "v_max3_f32 v239, v239, v115, v116\n\t" \
    "v_max3_f32 v239, v239, v117, v118\n\t" \
    "v_max3_f32 v239, v239, v119, v120\n\t" \
    "v_max3_f32 v239, v239, v121, v122\n\t" \
    "v_max3_f32 v239, v239, v123, v124\n\t" \
    "v_max3_f32 v239, v239, v125, v126\n\t" \
    "v_max_f32 v239, v239, v127\n\t" \
    "v_max_f32 v239, 0, v239\n\t" \
    "v_mov_b32 v240, v239\n\t" \
    "s_nop 1\n\t" \
    "v_permlane32_swap_b32 v239, v240\n\t" \
    "s_nop 1\n\t" \
    "v_max_f32 v239, v239, v240\n\t" \
    "v_add_f32 v238, v238, v239\n\t" \
    "v_exp_f32 v240, -v239\n\t" \
    "v_sub_f32 v241, 0, v238\n\t" \
    "v_sub_f32 v112, v112, v239\n\t" \
    "v_sub_f32 v128, v128, v239\n\t" \
    "v_mov_b32 v96, v241\n\t" \
    "v_sub_f32 v113, v113, v239\n\t" \
    "v_sub_f32 v129, v129, v239\n\t" \
    "v_mov_b32 v97, v241\n\t" \
    "v_sub_f32 v114, v114, v239\n\t" \
    "v_sub_f32 v130, v130, v239\n\t" \
    "v_mov_b32 v98, v241\n\t" \
    "v_sub_f32 v115, v115, v239\n\t" \
    "v_sub_f32 v131, v131, v239\n\t" \
    "v_mov_b32 v99, v241\n\t" \
    "v_sub_f32 v116, v116, v239\n\t" \
    "v_sub_f32 v132, v132, v239\n\t" \
    "v_mov_b32 v100, v241\n\t" \
    "v_sub_f32 v117, v117, v239\n\t" \
    "v_sub_f32 v133, v133, v239\n\t" \
    "v_mov_b32 v101, v241\n\t" \
    "v_sub_f32 v118, v118, v239\n\t" \
    "v_sub_f32 v134, v134, v239\n\t" \
    "v_mov_b32 v102, v241\n\t" \
    "v_sub_f32 v119, v119, v239\n\t" \
    "v_sub_f32 v135, v135, v239\n\t" \
    "v_mov_b32 v103, v241\n\t" \
    "v_sub_f32 v120, v120, v239\n\t" \
    "v_sub_f32 v136, v136, v239\n\t" \
    "v_mov_b32 v104, v241\n\t" \
    "v_sub_f32 v121, v121, v239\n\t" \
    "v_sub_f32 v137, v137, v239\n\t" \
    "v_mov_b32 v105, v241\n\t" \
    "v_sub_f32 v122, v122, v239\n\t" \
    "v_sub_f32 v138, v138, v239\n\t" \
    "v_mov_b32 v106, v241\n\t" \
    "v_sub_f32 v123, v123, v239\n\t" \
    "v_sub_f32 v139, v139, v239\n\t" \
    "v_mov_b32 v107, v241\n\t" \
    "v_sub_f32 v124, v124, v239\n\t" \
    "v_sub_f32 v140, v140, v239\n\t" \
    "v_mov_b32 v108, v241\n\t" \
    "v_sub_f32 v125, v125, v239\n\t" \
    "v_sub_f32 v141, v141, v239\n\t" \
    "v_mov_b32 v109, v241\n\t" \
    "v_sub_f32 v126, v126, v239\n\t" \
    "v_sub_f32 v142, v142, v239\n\t" \
    "v_mov_b32 v110, v241\n\t" \
    "v_sub_f32 v127, v127, v239\n\t" \
    "v_sub_f32 v143, v143, v239\n\t" \
    "v_mov_b32 v111, v241\n\t" \
    "v_exp_f32 v144, v112\n\t" \
    "v_exp_f32 v145, v113\n\t" \
    "v_exp_f32 v146, v114\n\t" \
    "v_exp_f32 v147, v115\n\t" \
    "v_exp_f32 v148, v116\n\t" \
    "v_exp_f32 v149, v117\n\t" \
    "v_exp_f32 v150, v118\n\t" \
    "v_exp_f32 v151, v119\n\t" \
    "v_exp_f32 v152, v120\n\t" \
    "v_exp_f32 v153, v121\n\t" \
    "v_exp_f32 v154, v122\n\t" \
    "v_exp_f32 v155, v123\n\t" \
    "v_exp_f32 v156, v124\n\t" \
    "v_exp_f32 v157, v125\n\t" \
    "v_exp_f32 v158, v126\n\t" \
    "v_exp_f32 v159, v127\n\t" \
    "v_mul_f32 v236, v236, v240\n\t" \
    "v_mul_f32 v0, v0, v240\n\t" \
    "v_mul_f32 v1, v1, v240\n\t" \
    "v_mul_f32 v2, v2, v240\n\t" \
    "v_mul_f32 v3, v3, v240\n\t" \
    "v_mul_f32 v4, v4, v240\n\t" \
    "v_mul_f32 v5, v5, v240\n\t" \
    "v_mul_f32 v6, v6, v240\n\t" \
    "v_mul_f32 v7, v7, v240\n\t" \
    "v_mul_f32 v8, v8, v240\n\t" \
    "v_mul_f32 v9, v9, v240\n\t" \
    "v_mul_f32 v10, v10, v240\n\t" \
    "v_mul_f32 v11, v11, v240\n\t" \
    "v_mul_f32 v12, v12, v240\n\t" \
    "v_mul_f32 v13, v13, v240\n\t" \
    "v_mul_f32 v14, v14, v240\n\t" \
    "v_mul_f32 v15, v15, v240\n\t" \
    "v_mul_f32 v16, v16, v240\n\t" \
    "v_mul_f32 v17, v17, v240\n\t" \
    "v_mul_f32 v18, v18, v240\n\t" \
    "v_mul_f32 v19, v19, v240\n\t" \
    "v_mul_f32 v20, v20, v240\n\t" \
    "v_mul_f32 v21, v21, v240\n\t" \
    "v_mul_f32 v22, v22, v240\n\t" \
    "v_mul_f32 v23, v23, v240\n\t" \
    "v_mul_f32 v24, v24, v240\n\t" \
    "v_mul_f32 v25, v25, v240\n\t" \
    "v_mul_f32 v26, v26, v240\n\t" \
    "v_mul_f32 v27, v27, v240\n\t" \
    "v_mul_f32 v28, v28, v240\n\t" \
    "v_mul_f32 v29, v29, v240\n\t" \
    "v_mul_f32 v30, v30, v240\n\t" \
    "v_mul_f32 v31, v31, v240\n\t" \
    "v_mul_f32 v32, v32, v240\n\t" \
    "v_mul_f32 v33, v33, v240\n\t" \
    "v_mul_f32 v34, v34, v240\n\t" \
    "v_mul_f32 v35, v35, v240\n\t" \
    "v_mul_f32 v36, v36, v240\n\t" \
    "v_mul_f32 v37, v37, v240\n\t" \
    "v_mul_f32 v38, v38, v240\n\t" \
    "v_mul_f32 v39, v39, v240\n\t" \
    "v_mul_f32 v40, v40, v240\n\t" \
    "v_mul_f32 v41, v41, v240\n\t" \
    "v_mul_f32 v42, v42, v240\n\t" \
    "v_mul_f32 v43, v43, v240\n\t" \
    "v_mul_f32 v44, v44, v240\n\t" \
    "v_mul_f32 v45, v45, v240\n\t" \
    "v_mul_f32 v46, v46, v240\n\t" \
    "v_mul_f32 v47, v47, v240\n\t" \
    "v_mul_f32 v48, v48, v240\n\t" \
    "v_mul_f32 v49, v49, v240\n\t" \
    "v_mul_f32 v50, v50, v240\n\t" \
    "v_mul_f32 v51, v51, v240\n\t" \
    "v_mul_f32 v52, v52, v240\n\t" \
    "v_mul_f32 v53, v53, v240\n\t" \
    "v_mul_f32 v54, v54, v240\n\t" \
    "v_mul_f32 v55, v55, v240\n\t" \
    "v_mul_f32 v56, v56, v240\n\t" \
    "v_mul_f32 v57, v57, v240\n\t" \
    "v_mul_f32 v58, v58, v240\n\t" \
    "v_mul_f32 v59, v59, v240\n\t" \
    "v_mul_f32 v60, v60, v240\n\t" \
    "v_mul_f32 v61, v61, v240\n\t" \
    "v_mul_f32 v62, v62, v240\n\t" \
    "v_mul_f32 v63, v63, v240\n\t" \
    "v_add_f32 v237, v144, v145\n\t" \
    "v_add_f32 v237, v237, v146\n\t" \
    "v_add_f32 v237, v237, v147\n\t" \
    "v_add_f32 v237, v237, v148\n\t" \
    "v_add_f32 v237, v237, v149\n\t" \
    "v_add_f32 v237, v237, v150\n\t" \
    "v_add_f32 v237, v237, v151\n\t" \
    "v_add_f32 v237, v237, v152\n\t" \
    "v_add_f32 v237, v237, v153\n\t" \
    "v_add_f32 v237, v237, v154\n\t" \
    "v_add_f32 v237, v237, v155\n\t" \
    "v_add_f32 v237, v237, v156\n\t" \
    "v_add_f32 v237, v237, v157\n\t" \
    "v_add_f32 v237, v237, v158\n\t" \
    "v_add_f32 v237, v237, v159\n\t" \
    "v_cvt_pk_bf16_f32 v160, v144, v145\n\t" \
    "v_cvt_pk_bf16_f32 v161, v146, v147\n\t" \
    "v_cvt_pk_bf16_f32 v162, v148, v149\n\t" \
    "v_cvt_pk_bf16_f32 v163, v150, v151\n\t" \
    "v_cvt_pk_bf16_f32 v164, v152, v153\n\t" \
    "v_cvt_pk_bf16_f32 v165, v154, v155\n\t" \
    "v_cvt_pk_bf16_f32 v166, v156, v157\n\t" \
    "v_cvt_pk_bf16_f32 v167, v158, v159\n\t" \
    "s_nop 1\n\t" \
    "s_branch BACKD_%=\n\t" \
    "RAREE_%=:\n\t" \
    "s_nop 15\n\t" \
    "v_max3_f32 v239, v128, v129, v130\n\t" \
    "v_max3_f32 v239, v239, v131, v132\n\t" \
    "v_max3_f32 v239, v239, v133, v134\n\t" \
    "v_max3_f32 v239, v239, v135, v136\n\t" \
    "v_max3_f32 v239, v239, v137, v138\n\t" \
    "v_max3_f32 v239, v239, v139, v140\n\t" \
    "v_max3_f32 v239, v239, v141, v142\n\t" \
    "v_max_f32 v239, v239, v143\n\t" \
    "v_max_f32 v239, 0, v239\n\t" \
    "v_mov_b32 v240, v239\n\t" \
    "s_nop 1\n\t" \
    "v_permlane32_swap_b32 v239, v240\n\t" \
    "s_nop 1\n\t" \
    "v_max_f32 v239, v239, v240\n\t" \
    "v_add_f32 v238, v238, v239\n\t" \
    "v_exp_f32 v240, -v239\n\t" \
    "v_sub_f32 v241, 0, v238\n\t" \
    "v_sub_f32 v128, v128, v239\n\t" \
    "v_mov_b32 v96, v241\n\t" \
    "v_sub_f32 v129, v129, v239\n\t" \
    "v_mov_b32 v97, v241\n\t" \
    "v_sub_f32 v130, v130, v239\n\t" \
    "v_mov_b32 v98, v241\n\t" \
    "v_sub_f32 v131, v131, v239\n\t" \
    "v_mov_b32 v99, v241\n\t" \
    "v_sub_f32 v132, v132, v239\n\t" \
    "v_mov_b32 v100, v241\n\t" \
    "v_sub_f32 v133, v133, v239\n\t" \
    "v_mov_b32 v101, v241\n\t" \
    "v_sub_f32 v134, v134, v239\n\t" \
    "v_mov_b32 v102, v241\n\t" \
    "v_sub_f32 v135, v135, v239\n\t" \
    "v_mov_b32 v103, v241\n\t" \
    "v_sub_f32 v136, v136, v239\n\t" \
    "v_mov_b32 v104, v241\n\t" \
    "v_sub_f32 v137, v137, v239\n\t" \
    "v_mov_b32 v105, v241\n\t" \
    "v_sub_f32 v138, v138, v239\n\t" \
    "v_mov_b32 v106, v241\n\t" \
    "v_sub_f32 v139, v139, v239\n\t" \
    "v_mov_b32 v107, v241\n\t" \
    "v_sub_f32 v140, v140, v239\n\t" \
    "v_mov_b32 v108, v241\n\t" \
    "v_sub_f32 v141, v141, v239\n\t" \
    "v_mov_b32 v109, v241\n\t" \
    "v_sub_f32 v142, v142, v239\n\t" \
    "v_mov_b32 v110, v241\n\t" \
    "v_sub_f32 v143, v143, v239\n\t" \
    "v_mov_b32 v111, v241\n\t" \
    "v_exp_f32 v144, v128\n\t" \
    "v_exp_f32 v145, v129\n\t" \
    "v_exp_f32 v146, v130\n\t" \
    "v_exp_f32 v147, v131\n\t" \
    "v_exp_f32 v148, v132\n\t" \
    "v_exp_f32 v149, v133\n\t" \
    "v_exp_f32 v150, v134\n\t" \
    "v_exp_f32 v151, v135\n\t" \
    "v_exp_f32 v152, v136\n\t" \
    "v_exp_f32 v153, v137\n\t" \
    "v_exp_f32 v154, v138\n\t" \
    "v_exp_f32 v155, v139\n\t" \
    "v_exp_f32 v156, v140\n\t" \
    "v_exp_f32 v157, v141\n\t" \
    "v_exp_f32 v158, v142\n\t" \
    "v_exp_f32 v159, v143\n\t" \
    "v_mul_f32 v236, v236, v240\n\t" \
    "v_mul_f32 v0, v0, v240\n\t" \
    "v_mul_f32 v1, v1, v240\n\t" \
    "v_mul_f32 v2, v2, v240\n\t" \
    "v_mul_f32 v3, v3, v240\n\t" \
    "v_mul_f32 v4, v4, v240\n\t" \
    "v_mul_f32 v5, v5, v240\n\t" \
    "v_mul_f32 v6, v6, v240\n\t" \
    "v_mul_f32 v7, v7, v240\n\t" \
    "v_mul_f32 v8, v8, v240\n\t" \
    "v_mul_f32 v9, v9, v240\n\t" \
    "v_mul_f32 v10, v10, v240\n\t" \
    "v_mul_f32 v11, v11, v240\n\t" \
    "v_mul_f32 v12, v12, v240\n\t" \
    "v_mul_f32 v13, v13, v240\n\t" \
    "v_mul_f32 v14, v14, v240\n\t" \
    "v_mul_f32 v15, v15, v240\n\t" \
    "v_mul_f32 v16, v16, v240\n\t" \
    "v_mul_f32 v17, v17, v240\n\t" \
    "v_mul_f32 v18, v18, v240\n\t" \
    "v_mul_f32 v19, v19, v240\n\t" \
    "v_mul_f32 v20, v20, v240\n\t" \
    "v_mul_f32 v21, v21, v240\n\t" \
    "v_mul_f32 v22, v22, v240\n\t" \
    "v_mul_f32 v23, v23, v240\n\t" \
    "v_mul_f32 v24, v24, v240\n\t" \
    "v_mul_f32 v25, v25, v240\n\t" \
    "v_mul_f32 v26, v26, v240\n\t" \
    "v_mul_f32 v27, v27, v240\n\t" \
    "v_mul_f32 v28, v28, v240\n\t" \
    "v_mul_f32 v29, v29, v240\n\t" \
    "v_mul_f32 v30, v30, v240\n\t" \
    "v_mul_f32 v31, v31, v240\n\t" \
    "v_mul_f32 v32, v32, v240\n\t" \
    "v_mul_f32 v33, v33, v240\n\t" \
    "v_mul_f32 v34, v34, v240\n\t" \
    "v_mul_f32 v35, v35, v240\n\t" \
    "v_mul_f32 v36, v36, v240\n\t" \
    "v_mul_f32 v37, v37, v240\n\t" \
    "v_mul_f32 v38, v38, v240\n\t" \
    "v_mul_f32 v39, v39, v240\n\t" \
    "v_mul_f32 v40, v40, v240\n\t" \
    "v_mul_f32 v41, v41, v240\n\t" \
    "v_mul_f32 v42, v42, v240\n\t" \
    "v_mul_f32 v43, v43, v240\n\t" \
    "v_mul_f32 v44, v44, v240\n\t" \
    "v_mul_f32 v45, v45, v240\n\t" \
    "v_mul_f32 v46, v46, v240\n\t" \
    "v_mul_f32 v47, v47, v240\n\t" \
    "v_mul_f32 v48, v48, v240\n\t" \
    "v_mul_f32 v49, v49, v240\n\t" \
    "v_mul_f32 v50, v50, v240\n\t" \
    "v_mul_f32 v51, v51, v240\n\t" \
    "v_mul_f32 v52, v52, v240\n\t" \
    "v_mul_f32 v53, v53, v240\n\t" \
    "v_mul_f32 v54, v54, v240\n\t" \
    "v_mul_f32 v55, v55, v240\n\t" \
    "v_mul_f32 v56, v56, v240\n\t" \
    "v_mul_f32 v57, v57, v240\n\t" \
    "v_mul_f32 v58, v58, v240\n\t" \
    "v_mul_f32 v59, v59, v240\n\t" \
    "v_mul_f32 v60, v60, v240\n\t" \
    "v_mul_f32 v61, v61, v240\n\t" \
    "v_mul_f32 v62, v62, v240\n\t" \
    "v_mul_f32 v63, v63, v240\n\t" \
    "v_add_f32 v237, v144, v145\n\t" \
    "v_add_f32 v237, v237, v146\n\t" \
    "v_add_f32 v237, v237, v147\n\t" \
    "v_add_f32 v237, v237, v148\n\t" \
    "v_add_f32 v237, v237, v149\n\t" \
    "v_add_f32 v237, v237, v150\n\t" \
    "v_add_f32 v237, v237, v151\n\t" \
    "v_add_f32 v237, v237, v152\n\t" \
    "v_add_f32 v237, v237, v153\n\t" \
    "v_add_f32 v237, v237, v154\n\t" \
    "v_add_f32 v237, v237, v155\n\t" \
    "v_add_f32 v237, v237, v156\n\t" \
    "v_add_f32 v237, v237, v157\n\t" \
    "v_add_f32 v237, v237, v158\n\t" \
    "v_add_f32 v237, v237, v159\n\t" \
    "v_cvt_pk_bf16_f32 v160, v144, v145\n\t" \
    "v_cvt_pk_bf16_f32 v161, v146, v147\n\t" \
    "v_cvt_pk_bf16_f32 v162, v148, v149\n\t" \
    "v_cvt_pk_bf16_f32 v163, v150, v151\n\t" \
    "v_cvt_pk_bf16_f32 v164, v152, v153\n\t" \
    "v_cvt_pk_bf16_f32 v165, v154, v155\n\t" \
    "v_cvt_pk_bf16_f32 v166, v156, v157\n\t" \
    "v_cvt_pk_bf16_f32 v167, v158, v159\n\t" \
    "s_nop 1\n\t" \
    "s_branch BACKE_%=\n\t" \
    "RAREF_%=:\n\t" \
    "s_nop 15\n\t" \
    "v_max3_f32 v239, v128, v129, v130\n\t" \
    "v_max3_f32 v239, v239, v131, v132\n\t" \
    "v_max3_f32 v239, v239, v133, v134\n\t" \
    "v_max3_f32 v239, v239, v135, v136\n\t" \
    "v_max3_f32 v239, v239, v137, v138\n\t" \
    "v_max3_f32 v239, v239, v139, v140\n\t" \
    "v_max3_f32 v239, v239, v141, v142\n\t" \
    "v_max_f32 v239, v239, v143\n\t" \
    "v_max_f32 v239, 0, v239\n\t" \
    "v_mov_b32 v240, v239\n\t" \
    "s_nop 1\n\t" \
    "v_permlane32_swap_b32 v239, v240\n\t" \
    "s_nop 1\n\t" \
    "v_max_f32 v239, v239, v240\n\t" \
    "v_add_f32 v238, v238, v239\n\t" \
    "v_exp_f32 v240, -v239\n\t" \
    "v_sub_f32 v241, 0, v238\n\t" \
    "v_sub_f32 v128, v128, v239\n\t" \
    "v_sub_f32 v112, v112, v239\n\t" \
    "v_mov_b32 v96, v241\n\t" \
    "v_sub_f32 v129, v129, v239\n\t" \
    "v_sub_f32 v113, v113, v239\n\t" \
    "v_mov_b32 v97, v241\n\t" \
    "v_sub_f32 v130, v130, v239\n\t" \
    "v_sub_f32 v114, v114, v239\n\t" \
    "v_mov_b32 v98, v241\n\t" \
    "v_sub_f32 v131, v131, v239\n\t" \
    "v_sub_f32 v115, v115, v239\n\t" \
    "v_mov_b32 v99, v241\n\t" \
    "v_sub_f32 v132, v132, v239\n\t" \
    "v_sub_f32 v116, v116, v239\n\t" \
    "v_mov_b32 v100, v241\n\t" \
    "v_sub_f32 v133, v133, v239\n\t" \
    "v_sub_f32 v117, v117, v239\n\t" \
    "v_mov_b32 v101, v241\n\t" \
    "v_sub_f32 v134, v134, v239\n\t" \
    "v_sub_f32 v118, v118, v239\n\t" \
    "v_mov_b32 v102, v241\n\t" \
    "v_sub_f32 v135, v135, v239\n\t" \
    "v_sub_f32 v119, v119, v239\n\t" \
    "v_mov_b32 v103, v241\n\t" \
    "v_sub_f32 v136, v136, v239\n\t" \
    "v_sub_f32 v120, v120, v239\n\t" \
    "v_mov_b32 v104, v241\n\t" \
    "v_sub_f32 v137, v137, v239\n\t" \
    "v_sub_f32 v121, v121, v239\n\t" \
    "v_mov_b32 v105, v241\n\t" \
    "v_sub_f32 v138, v138, v239\n\t" \
    "v_sub_f32 v122, v122, v239\n\t" \
    "v_mov_b32 v106, v241\n\t" \
    "v_sub_f32 v139, v139, v239\n\t" \
    "v_sub_f32 v123, v123, v239\n\t" \
    "v_mov_b32 v107, v241\n\t" \
    "v_sub_f32 v140, v140, v239\n\t" \
    "v_sub_f32 v124, v124, v239\n\t" \
    "v_mov_b32 v108, v241\n\t" \
    "v_sub_f32 v141, v141, v239\n\t" \
    "v_sub_f32 v125, v125, v239\n\t" \
    "v_mov_b32 v109, v241\n\t" \
    "v_sub_f32 v142, v142, v239\n\t" \
    "v_sub_f32 v126, v126, v239\n\t" \
    "v_mov_b32 v110, v241\n\t" \
    "v_sub_f32 v143, v143, v239\n\t" \
    "v_sub_f32 v127, v127, v239\n\t" \
    "v_mov_b32 v111, v241\n\t" \
    "v_exp_f32 v144, v128\n\t" \
    "v_exp_f32 v145, v129\n\t" \
    "v_exp_f32 v146, v130\n\t" \
    "v_exp_f32 v147, v131\n\t" \
    "v_exp_f32 v148, v132\n\t" \
    "v_exp_f32 v149, v133\n\t" \
    "v_exp_f32 v150, v134\n\t" \
    "v_exp_f32 v151, v135\n\t" \
    "v_exp_f32 v152, v136\n\t" \
    "v_exp_f32 v153, v137\n\t" \
    "v_exp_f32 v154, v138\n\t" \
    "v_exp_f32 v155, v139\n\t" \
    "v_exp_f32 v156, v140\n\t" \
    "v_exp_f32 v157, v141\n\t" \
    "v_exp_f32 v158, v142\n\t" \
    "v_exp_f32 v159, v143\n\t" \
    "v_mul_f32 v236, v236, v240\n\t" \
    "v_mul_f32 v0, v0, v240\n\t" \
    "v_mul_f32 v1, v1, v240\n\t" \
    "v_mul_f32 v2, v2, v240\n\t" \
    "v_mul_f32 v3, v3, v240\n\t" \
    "v_mul_f32 v4, v4, v240\n\t" \
    "v_mul_f32 v5, v5, v240\n\t" \
    "v_mul_f32 v6, v6, v240\n\t" \
    "v_mul_f32 v7, v7, v240\n\t" \
    "v_mul_f32 v8, v8, v240\n\t" \
    "v_mul_f32 v9, v9, v240\n\t" \
    "v_mul_f32 v10, v10, v240\n\t" \
    "v_mul_f32 v11, v11, v240\n\t" \
    "v_mul_f32 v12, v12, v240\n\t" \
    "v_mul_f32 v13, v13, v240\n\t" \
    "v_mul_f32 v14, v14, v240\n\t" \
    "v_mul_f32 v15, v15, v240\n\t" \
    "v_mul_f32 v16, v16, v240\n\t" \
    "v_mul_f32 v17, v17, v240\n\t" \
    "v_mul_f32 v18, v18, v240\n\t" \
    "v_mul_f32 v19, v19, v240\n\t" \
    "v_mul_f32 v20, v20, v240\n\t" \
    "v_mul_f32 v21, v21, v240\n\t" \
    "v_mul_f32 v22, v22, v240\n\t" \
    "v_mul_f32 v23, v23, v240\n\t" \
    "v_mul_f32 v24, v24, v240\n\t" \
    "v_mul_f32 v25, v25, v240\n\t" \
    "v_mul_f32 v26, v26, v240\n\t" \
    "v_mul_f32 v27, v27, v240\n\t" \
    "v_mul_f32 v28, v28, v240\n\t" \
    "v_mul_f32 v29, v29, v240\n\t" \
    "v_mul_f32 v30, v30, v240\n\t" \
    "v_mul_f32 v31, v31, v240\n\t" \
    "v_mul_f32 v32, v32, v240\n\t" \
    "v_mul_f32 v33, v33, v240\n\t" \
    "v_mul_f32 v34, v34, v240\n\t" \
    "v_mul_f32 v35, v35, v240\n\t" \
    "v_mul_f32 v36, v36, v240\n\t" \
    "v_mul_f32 v37, v37, v240\n\t" \
    "v_mul_f32 v38, v38, v240\n\t" \
    "v_mul_f32 v39, v39, v240\n\t" \
    "v_mul_f32 v40, v40, v240\n\t" \
    "v_mul_f32 v41, v41, v240\n\t" \
    "v_mul_f32 v42, v42, v240\n\t" \
    "v_mul_f32 v43, v43, v240\n\t" \
    "v_mul_f32 v44, v44, v240\n\t" \
    "v_mul_f32 v45, v45, v240\n\t" \
    "v_mul_f32 v46, v46, v240\n\t" \
    "v_mul_f32 v47, v47, v240\n\t" \
    "v_mul_f32 v48, v48, v240\n\t" \
    "v_mul_f32 v49, v49, v240\n\t" \
    "v_mul_f32 v50, v50, v240\n\t" \
    "v_mul_f32 v51, v51, v240\n\t" \
    "v_mul_f32 v52, v52, v240\n\t" \
    "v_mul_f32 v53, v53, v240\n\t" \
    "v_mul_f32 v54, v54, v240\n\t" \
    "v_mul_f32 v55, v55, v240\n\t" \
    "v_mul_f32 v56, v56, v240\n\t" \
    "v_mul_f32 v57, v57, v240\n\t" \
    "v_mul_f32 v58, v58, v240\n\t" \
    "v_mul_f32 v59, v59, v240\n\t" \
    "v_mul_f32 v60, v60, v240\n\t" \
    "v_mul_f32 v61, v61, v240\n\t" \
    "v_mul_f32 v62, v62, v240\n\t" \
    "v_mul_f32 v63, v63, v240\n\t" \
    "v_add_f32 v237, v144, v145\n\t" \
    "v_add_f32 v237, v237, v146\n\t" \
    "v_add_f32 v237, v237, v147\n\t" \
    "v_add_f32 v237, v237, v148\n\t" \
    "v_add_f32 v237, v237, v149\n\t" \
    "v_add_f32 v237, v237, v150\n\t" \
    "v_add_f32 v237, v237, v151\n\t" \
    "v_add_f32 v237, v237, v152\n\t" \
    "v_add_f32 v237, v237, v153\n\t" \
    "v_add_f32 v237, v237, v154\n\t" \
    "v_add_f32 v237, v237, v155\n\t" \
    "v_add_f32 v237, v237, v156\n\t" \
    "v_add_f32 v237, v237, v157\n\t" \
    "v_add_f32 v237, v237, v158\n\t" \
    "v_add_f32 v237, v237, v159\n\t" \
    "v_cvt_pk_bf16_f32 v160, v144, v145\n\t" \
    "v_cvt_pk_bf16_f32 v161, v146, v147\n\t" \
    "v_cvt_pk_bf16_f32 v162, v148, v149\n\t" \
    "v_cvt_pk_bf16_f32 v163, v150, v151\n\t" \
    "v_cvt_pk_bf16_f32 v164, v152, v153\n\t" \
    "v_cvt_pk_bf16_f32 v165, v154, v155\n\t" \
    "v_cvt_pk_bf16_f32 v166, v156, v157\n\t" \
    "v_cvt_pk_bf16_f32 v167, v158, v159\n\t" \
    "s_nop 1\n\t" \
    "s_branch BACKF_%=\n\t" \
    "RAREG_%=:\n\t" \
    "s_nop 15\n\t" \
    "v_max3_f32 v239, v112, v113, v114\n\t" \
    "v_max3_f32 v239, v239, v115, v116\n\t" \
    "v_max3_f32 v239, v239, v117, v118\n\t" \
    "v_max3_f32 v239, v239, v119, v120\n\t" \
    "v_max3_f32 v239, v239, v121, v122\n\t" \
    "v_max3_f32 v239, v239, v123, v124\n\t" \
    "v_max3_f32 v239, v239, v125, v126\n\t" \
    "v_max_f32 v239, v239, v127\n\t" \
    "v_max_f32 v239, 0, v239\n\t" \
    "v_mov_b32 v240, v239\n\t" \
    "s_nop 1\n\t" \
    "v_permlane32_swap_b32 v239, v240\n\t" \
    "s_nop 1\n\t" \
    "v_max_f32 v239, v239, v240\n\t" \
    "v_add_f32 v238, v238, v239\n\t" \
    "v_exp_f32 v240, -v239\n\t" \
    "v_sub_f32 v241, 0, v238\n\t" \
    "v_sub_f32 v112, v112, v239\n\t" \
    "v_mov_b32 v96, v241\n\t" \
    "v_sub_f32 v113, v113, v239\n\t" \
    "v_mov_b32 v97, v241\n\t" \
    "v_sub_f32 v114, v114, v239\n\t" \
    "v_mov_b32 v98, v241\n\t" \
    "v_sub_f32 v115, v115, v239\n\t" \
    "v_mov_b32 v99, v241\n\t" \
    "v_sub_f32 v116, v116, v239\n\t" \
    "v_mov_b32 v100, v241\n\t" \
    "v_sub_f32 v117, v117, v239\n\t" \
    "v_mov_b32 v101, v241\n\t" \
    "v_sub_f32 v118, v118, v239\n\t" \
    "v_mov_b32 v102, v241\n\t" \
    "v_sub_f32 v119, v119, v239\n\t" \
    "v_mov_b32 v103, v241\n\t" \
    "v_sub_f32 v120, v120, v239\n\t" \
    "v_mov_b32 v104, v241\n\t" \
    "v_sub_f32 v121, v121, v239\n\t" \
    "v_mov_b32 v105, v241\n\t" \
    "v_sub_f32 v122, v122, v239\n\t" \
    "v_mov_b32 v106, v241\n\t" \
    "v_sub_f32 v123, v123, v239\n\t" \
    "v_mov_b32 v107, v241\n\t" \
    "v_sub_f32 v124, v124, v239\n\t" \
    "v_mov_b32 v108, v241\n\t" \
    "v_sub_f32 v125, v125, v239\n\t" \
    "v_mov_b32 v109, v241\n\t" \
    "v_sub_f32 v126, v126, v239\n\t" \
    "v_mov_b32 v110, v241\n\t" \
    "v_sub_f32 v127, v127, v239\n\t" \
    "v_mov_b32 v111, v241\n\t" \
    "v_exp_f32 v144, v112\n\t" \
    "v_exp_f32 v145, v113\n\t" \
    "v_exp_f32 v146, v114\n\t" \
    "v_exp_f32 v147, v115\n\t" \
    "v_exp_f32 v148, v116\n\t" \
    "v_exp_f32 v149, v117\n\t" \
    "v_exp_f32 v150, v118\n\t" \
    "v_exp_f32 v151, v119\n\t" \
    "v_exp_f32 v152, v120\n\t" \
    "v_exp_f32 v153, v121\n\t" \
    "v_exp_f32 v154, v122\n\t" \
    "v_exp_f32 v155, v123\n\t" \
    "v_exp_f32 v156, v124\n\t" \
    "v_exp_f32 v157, v125\n\t" \
    "v_exp_f32 v158, v126\n\t" \
    "v_exp_f32 v159, v127\n\t" \
    "v_mul_f32 v236, v236, v240\n\t" \
    "v_mul_f32 v0, v0, v240\n\t" \
    "v_mul_f32 v1, v1, v240\n\t" \
    "v_mul_f32 v2, v2, v240\n\t" \
    "v_mul_f32 v3, v3, v240\n\t" \
    "v_mul_f32 v4, v4, v240\n\t" \
    "v_mul_f32 v5, v5, v240\n\t" \
    "v_mul_f32 v6, v6, v240\n\t" \
    "v_mul_f32 v7, v7, v240\n\t" \
    "v_mul_f32 v8, v8, v240\n\t" \
    "v_mul_f32 v9, v9, v240\n\t" \
    "v_mul_f32 v10, v10, v240\n\t" \
    "v_mul_f32 v11, v11, v240\n\t" \
    "v_mul_f32 v12, v12, v240\n\t" \
    "v_mul_f32 v13, v13, v240\n\t" \
    "v_mul_f32 v14, v14, v240\n\t" \
    "v_mul_f32 v15, v15, v240\n\t" \
    "v_mul_f32 v16, v16, v240\n\t" \
    "v_mul_f32 v17, v17, v240\n\t" \
    "v_mul_f32 v18, v18, v240\n\t" \
    "v_mul_f32 v19, v19, v240\n\t" \
    "v_mul_f32 v20, v20, v240\n\t" \
    "v_mul_f32 v21, v21, v240\n\t" \
    "v_mul_f32 v22, v22, v240\n\t" \
    "v_mul_f32 v23, v23, v240\n\t" \
    "v_mul_f32 v24, v24, v240\n\t" \
    "v_mul_f32 v25, v25, v240\n\t" \
    "v_mul_f32 v26, v26, v240\n\t" \
    "v_mul_f32 v27, v27, v240\n\t" \
    "v_mul_f32 v28, v28, v240\n\t" \
    "v_mul_f32 v29, v29, v240\n\t" \
    "v_mul_f32 v30, v30, v240\n\t" \
    "v_mul_f32 v31, v31, v240\n\t" \
    "v_mul_f32 v32, v32, v240\n\t" \
    "v_mul_f32 v33, v33, v240\n\t" \
    "v_mul_f32 v34, v34, v240\n\t" \
    "v_mul_f32 v35, v35, v240\n\t" \
    "v_mul_f32 v36, v36, v240\n\t" \
    "v_mul_f32 v37, v37, v240\n\t" \
    "v_mul_f32 v38, v38, v240\n\t" \
    "v_mul_f32 v39, v39, v240\n\t" \
    "v_mul_f32 v40, v40, v240\n\t" \
    "v_mul_f32 v41, v41, v240\n\t" \
    "v_mul_f32 v42, v42, v240\n\t" \
    "v_mul_f32 v43, v43, v240\n\t" \
    "v_mul_f32 v44, v44, v240\n\t" \
    "v_mul_f32 v45, v45, v240\n\t" \
    "v_mul_f32 v46, v46, v240\n\t" \
    "v_mul_f32 v47, v47, v240\n\t" \
    "v_mul_f32 v48, v48, v240\n\t" \
    "v_mul_f32 v49, v49, v240\n\t" \
    "v_mul_f32 v50, v50, v240\n\t" \
    "v_mul_f32 v51, v51, v240\n\t" \
    "v_mul_f32 v52, v52, v240\n\t" \
    "v_mul_f32 v53, v53, v240\n\t" \
    "v_mul_f32 v54, v54, v240\n\t" \
    "v_mul_f32 v55, v55, v240\n\t" \
    "v_mul_f32 v56, v56, v240\n\t" \
    "v_mul_f32 v57, v57, v240\n\t" \
    "v_mul_f32 v58, v58, v240\n\t" \
    "v_mul_f32 v59, v59, v240\n\t" \
    "v_mul_f32 v60, v60, v240\n\t" \
    "v_mul_f32 v61, v61, v240\n\t" \
    "v_mul_f32 v62, v62, v240\n\t" \
    "v_mul_f32 v63, v63, v240\n\t" \
    "v_add_f32 v237, v144, v145\n\t" \
    "v_add_f32 v237, v237, v146\n\t" \
    "v_add_f32 v237, v237, v147\n\t" \
    "v_add_f32 v237, v237, v148\n\t" \
    "v_add_f32 v237, v237, v149\n\t" \
    "v_add_f32 v237, v237, v150\n\t" \
    "v_add_f32 v237, v237, v151\n\t" \
    "v_add_f32 v237, v237, v152\n\t" \
    "v_add_f32 v237, v237, v153\n\t" \
    "v_add_f32 v237, v237, v154\n\t" \
    "v_add_f32 v237, v237, v155\n\t" \
    "v_add_f32 v237, v237, v156\n\t" \
    "v_add_f32 v237, v237, v157\n\t" \
    "v_add_f32 v237, v237, v158\n\t" \
    "v_add_f32 v237, v237, v159\n\t" \
    "v_cvt_pk_bf16_f32 v160, v144, v145\n\t" \
    "v_cvt_pk_bf16_f32 v161, v146, v147\n\t" \
    "v_cvt_pk_bf16_f32 v162, v148, v149\n\t" \
    "v_cvt_pk_bf16_f32 v163, v150, v151\n\t" \
    "v_cvt_pk_bf16_f32 v164, v152, v153\n\t" \
    "v_cvt_pk_bf16_f32 v165, v154, v155\n\t" \
    "v_cvt_pk_bf16_f32 v166, v156, v157\n\t" \
    "v_cvt_pk_bf16_f32 v167, v158, v159\n\t" \
    "s_nop 1\n\t" \
    "s_branch BACKG_%=\n\t" \
    "END_%=:\n\t" \
    "s_waitcnt lgkmcnt(0)\n\t" \
    "s_nop 15\n\t"
#define MX3(a, b, c) __builtin_fmaxf(__builtin_fmaxf((a), (b)), (c))
DI float max16(const f32x16& p) {
    float a = MX3(p[0], p[1], p[2]), b = MX3(p[3], p[4], p[5]);
    a = MX3(a, p[6], p[7]); b = MX3(b, p[8], p[9]); a = MX3(a, p[10], p[11]); b = MX3(b, p[12], p[13]); a = MX3(a, p[14], p[15]);
    return __builtin_fmaxf(a, b);
}
DI float halfmax(float m) { auto rr = __builtin_amdgcn_permlane32_swap(__float_as_uint(m), __float_as_uint(m), false, false); return __builtin_fmaxf(__uint_as_float(rr[0]), __uint_as_float(rr[1])); }
DI void cmask(f32x16& p, int r32, int hi) {
#pragma unroll
    for (int r = 0; r < 16; ++r) { const int kv = 4 * hi + (r & 3) + 8 * (r >> 2); if (kv > r32) p[r] = -INFINITY; }
}
#define WAITBAR(N) asm volatile("s_waitcnt vmcnt(" #N ")\n\ts_barrier" ::: "memory")
typedef unsigned u32x16 __attribute__((ext_vector_type(16)));

__global__ __launch_bounds__(512, 2) void k2_attn(const char* __restrict__ Q, const char* __restrict__ K, const char* __restrict__ VT, char* __restrict__ CTX) {
    extern __shared__ __attribute__((aligned(16))) unsigned char smem[];
    const lds_cptr lds = (lds_cptr)smem;
    const int tid = threadIdx.x, lane = tid & 63, r32 = lane & 31, hi = lane >> 5, wid = __builtin_amdgcn_readfirstlane(tid >> 6);
    const int xcd = blockIdx.x & 7, idx = blockIdx.x >> 3, h = xcd * 4 + (idx >> 3), ib = idx & 7;
    const int G = wid < 4 ? 4 * ib + wid : 63 - 4 * ib - (wid - 4);
    const int NT = 32 - 2 * ib;
    const char* Kh = K + (size_t)h * 32 * SLOT; const char* Vh = VT + (size_t)h * 32 * SLOT;
    const unsigned lds0 = (unsigned)(uintptr_t)smem;
    const unsigned dmaw = (unsigned)(wid - 4) >> 31;
    const unsigned dvo = (wid & 3) * 4096 + lane * 16, kdst = lds0 + LDS_K + (wid & 3) * 4096, vdst = lds0 + LDS_V + (wid & 3) * 4096;
#define DMA_T(base, dst, t, sl) do { const char* g_ = (base) + (size_t)(t) * SLOT; const unsigned l_ = (unsigned)__builtin_amdgcn_readfirstlane((dst) + ((sl) & 3) * SLOT); glds2(g_, dvo, l_); glds2(g_ + 2048, dvo, l_ + 2048); } while (0)
#define DMA_K(t, sl) DMA_T(Kh, kdst, t, sl)
#define DMA_V(t, sl) DMA_T(Vh, vdst, t, sl)
#define DMA_STEP(s) do { if (dmaw) { DMA_K(min((s) + 3, NT - 1), (s) + 3); DMA_V(min((s) + 2, NT - 1), (s) + 2); } } while (0)
    STAMP(2, 0);
    u32x16 qa, qb;
    { const char* qp = Q + ((size_t)h * 32 + (G >> 1)) * SLOT; const unsigned qo = hi * 1024 + ((G & 1) * 32 + r32) * 16;
      u32x4 q[8];
#define QLD(d) asm volatile("global_load_dwordx4 %0, %1, %2 offset:%c3" : "=&v"(q[d]) : "v"(qo + ((d) >> 1) * 4096), "s"(qp), "i"(((d) & 1) * 2048) : "memory")
      QLD(0); QLD(1); QLD(2); QLD(3); QLD(4); QLD(5); QLD(6); QLD(7);
#undef QLD
      if (dmaw) { DMA_K(0, 0); DMA_V(0, 0); DMA_K(1, 1); DMA_K(2, 2); DMA_V(1, 1); } else WAIT_V(0);
      asm volatile("s_waitcnt vmcnt(8)\n\ts_barrier" : "+v"(q[0]), "+v"(q[1]), "+v"(q[2]), "+v"(q[3]), "+v"(q[4]), "+v"(q[5]), "+v"(q[6]), "+v"(q[7]) :: "memory");
#pragma unroll
      for (int i = 0; i < 4; ++i)
#pragma unroll
          for (int e = 0; e < 4; ++e) { qa[4 * i + e] = q[i][e]; qb[4 * i + e] = q[4 + i][e]; }
    }
    STAMP(2, 1);
    const unsigned kp0 = lds0 + LDS_K + hi * 1024 + r32 * 16, vp0 = lds0 + LDS_V + hi * 2048 + r32 * 16;
    f32x16 o0 = f32x16{}, o1 = f32x16{}, o2 = f32x16{}, o3 = f32x16{}, negm, cA, cB, P = f32x16{};
    u32x16 kfa, kfb;
    float l_reg = 0.f, mhat;
    {
        f32x16 s = f32x16{};
#pragma unroll
        for (int j = 0; j < 8; ++j) {
            const bf16x8 kf = lds_rd(lds + LDS_K + hi * 1024 + r32 * 16 + j * 2048); const u32x4 kw = __builtin_bit_cast(u32x4, kf);
#pragma unroll
            for (int e = 0; e < 4; ++e) { if (j < 4) kfa[4 * j + e] = kw[e]; else kfb[4 * (j - 4) + e] = kw[e]; }
            const u32x4 qw = j < 4 ? (u32x4){qa[4 * j], qa[4 * j + 1], qa[4 * j + 2], qa[4 * j + 3]} : (u32x4){qb[4 * j - 16], qb[4 * j - 15], qb[4 * j - 14], qb[4 * j - 13]};
            s = MFMA32(kf, __builtin_bit_cast(bf16x8, qw), s);
        }
        if (G == 0) cmask(s, r32, hi);
        mhat = halfmax(max16(s));
#pragma unroll
        for (int r = 0; r < 16; ++r) { negm[r] = -mhat; cB[r] = -INFINITY; cA[r] = -INFINITY; }
    }
    const float thr = 65536.0f;
    int t = 0; unsigned soff = 0;
    unsigned vk = kp0, vkn = kp0 + SLOT, vv = vp0, vvp = vp0;
#define K2_STATE [o0] "+&{v[0:15]}"(o0), [o1] "+&{v[16:31]}"(o1), [o2] "+&{v[32:47]}"(o2), [o3] "+&{v[48:63]}"(o3), [negm] "+&{v[96:111]}"(negm), [ca] "+&{v[112:127]}"(cA), [cb] "+&{v[128:143]}"(cB), \
        [p] "+&{v[144:159]}"(P), [kfa] "+&{v[168:183]}"(kfa), [kfb] "+&{v[184:199]}"(kfb), [vk] "+&{v232}"(vk), [vkn] "+&{v233}"(vkn), [vv] "+&{v234}"(vv), [vvp] "+&{v235}"(vvp), [l] "+&{v236}"(l_reg), [mh] "+&{v238}"(mhat)
#define K2_IN [qa] "{v[64:79]}"(qa), [qb] "{v[80:95]}"(qb), [thr] "s"(thr)
#define K2_CLOB "v160", "v161", "v162", "v163", "v164", "v165", "v166", "v167", "v200", "v201", "v202", "v203", "v204", "v205", "v206", "v207", "v208", "v209", "v210", "v211", "v212", "v213", "v214", "v215", \
        "v216", "v217", "v218", "v219", "v220", "v221", "v222", "v223", "v224", "v225", "v226", "v227", "v228", "v229", "v230", "v231", "v237", "v239", "v240", "v241", "vcc", "scc", "memory"
    const int nloop = G >> 1;
    asm volatile(K2_ASM : K2_STATE, [t] "+&s"(t), [soff] "+&s"(soff)
                 : K2_IN, [n] "s"(nloop), [odd] "s"(G & 1), [dmaw] "s"(dmaw), [ntm1] "s"(NT - 1), [khl] "s"((unsigned)(uintptr_t)Kh), [khh] "s"((unsigned)((uintptr_t)Kh >> 32)),
                   [vhl] "s"((unsigned)(uintptr_t)Vh), [vhh] "s"((unsigned)((uintptr_t)Vh >> 32)), [kdst] "s"(kdst), [vdst] "s"(vdst), [dvo] "v"(dvo), [kp0] "v"(kp0), [vp0] "v"(vp0), [dq] "v"(r32 - 4 * hi)
                 : K2_CLOB, "s60", "s61", "s62", "s63");
    STAMP(2, 2);
    {
        auto rr = __builtin_amdgcn_permlane32_swap(__float_as_uint(l_reg), __float_as_uint(l_reg), false, false);
        const float inv = 1.0f / (__uint_as_float(rr[0]) + __uint_as_float(rr[1]));
        char* op = CTX + ((size_t)((G >> 2) * 64 + 2 * h) * 8 + hi) * 2048 + ((G & 3) * 32 + r32) * 16;
#define K2_ST(o, db) do { _Pragma("unroll") for (int g = 0; g < 4; g += 2) { \
            const unsigned x0 = cvtpk(o[4 * g] * inv, o[4 * g + 1] * inv), x1 = cvtpk(o[4 * g + 2] * inv, o[4 * g + 3] * inv); \
            const unsigned y0 = cvtpk(o[4 * g + 4] * inv, o[4 * g + 5] * inv), y1 = cvtpk(o[4 * g + 6] * inv, o[4 * g + 7] * inv); \
            const auto r0 = __builtin_amdgcn_permlane32_swap(x0, y0, false, false), r1 = __builtin_amdgcn_permlane32_swap(x1, y1, false, false); \
            *(u32x4*)(op + (size_t)(((db) >> 1) * 8 + 4 * ((db) & 1) + g) * 2048) = (u32x4){r0[0], r1[0], r0[1], r1[1]}; } } while (0)
        K2_ST(o0, 0); K2_ST(o1, 1); K2_ST(o2, 2); K2_ST(o3, 3);
    }
    WAITBAR(16); ++t;
    if (t < NT) { DMA_STEP(t); WAITBAR(16); ++t;
        for (; t < NT; ++t) { DMA_STEP(t); WAITBAR(8); } }
    STAMP(2, 3);
    WAIT_V(0);
    STAMP(2, 4);
}
}

namespace k3 {
constexpr int NS = 4, STG = 32768, LDS_BYTES = NS * STG;
typedef __attribute__((address_space(1))) unsigned gu32;
struct Fr { bf16x8 a[4][2], b[2][2]; };

__global__ __launch_bounds__(512, 2) void k3_out(const char* __restrict__ ctx, const char* __restrict__ H, const bf16_t* __restrict__ wo, const bf16_t* __restrict__ w2,
                                                 const bf16_t* __restrict__ b2, const bf16_t* __restrict__ x, float* part, unsigned* flags, bf16_t* __restrict__ out) {
    extern __shared__ __attribute__((aligned(16))) unsigned char smem[];
    lds_ptr lds = (lds_ptr)smem;
    const int tid = threadIdx.x, wid = __builtin_amdgcn_readfirstlane(tid >> 6), lane = tid & 63, wr = wid >> 2, wc = wid & 3, fr = lane & 15, fq = lane >> 4;
    const int b = blockIdx.x, idx = b >> 3, cons = (b >> 2) & 1, mt = 4 * (b & 3) + (idx >> 3), ntile = idx & 7, tile = mt * 8 + ntile;
    const int nt = cons ? 64 : 56;
    const char* abase = cons ? ctx + (size_t)mt * 64 * 16384 : H + (size_t)mt * 56 * 16384;
    const char* bbase = (const char*)(cons ? wo : w2) + ntile * 256;
    const unsigned va = wid * 2048 + lane * 16, lds0 = (unsigned)(uintptr_t)smem;
    const unsigned vb = (8 * (wid >> 1) + ((lane & 31) >> 2)) * (DM * 2) + (64 * (wid & 1) + 32 * (lane >> 5) + 8 * ((lane & 3) ^ (wid & 2))) * 2;
    const int aoff = fq * 2048 + (wr * 64 + fr) * 16;
    const int boff0 = 16384 + fq * 2048 + wc * 512 + (fr >> 2) * 64 + (fq & 1) * 32 + (fr & 3) * 8, boff1 = boff0 ^ 32;
#define K3_STAGE(t) do { const unsigned sl_ = lds0 + ((t) & 3) * STG; const char* b_ = bbase + (size_t)(t) * (64 * DM * 2); \
        glds2(abase + (size_t)(t) * 16384, va, sl_ + wid * 2048); \
        glds(b_, vb, sl_ + 16384 + wid * 1024); glds(b_ + 32 * DM * 2, vb, sl_ + 16384 + wid * 1024 + 8192); } while (0)
#define K3_RDA(F, j) F.a[(j) >> 1][(j) & 1] = lds_rd(lds + sl_ + aoff + ((j) & 1) * 8192 + ((j) >> 1) * 256)
#define K3_RDB(F, j) F.b[(j) >> 1][(j) & 1] = cat8(lds_tr(lds + sl_ + (((j) >> 1) ? boff1 : boff0) + ((j) & 1) * 8192), lds_tr(lds + sl_ + (((j) >> 1) ? boff1 : boff0) + ((j) & 1) * 8192 + 256))
#define K3_MF(F, j) acc[((j) >> 1) & 3][(j) & 1] = MFMA16(F.b[(j) & 1][(j) >> 3], F.a[((j) >> 1) & 3][(j) >> 3], acc[((j) >> 1) & 3][(j) & 1])
#define K3_STEP(t, C, N) do { \
        if ((t) + 3 < nt) { WAIT_V(8); } else if ((t) + 2 < nt) { WAIT_V(4); } else { WAIT_V(0); } \
        __builtin_amdgcn_s_waitcnt(0xC07F); BAR(); SBAR(); \
        if ((t) + 4 < nt) K3_STAGE((t) + 4); \
        const unsigned sl_ = (((t) + 1) & 3) * STG; \
        SBAR(); \
        K3_MF(C, 0); K3_RDB(N, 0); SBAR(); K3_MF(C, 1); K3_RDA(N, 0); SBAR(); K3_MF(C, 2); K3_RDA(N, 2); SBAR(); K3_MF(C, 3); K3_RDB(N, 2); SBAR(); \
        K3_MF(C, 4); K3_RDA(N, 4); SBAR(); K3_MF(C, 5); K3_RDA(N, 6); SBAR(); K3_MF(C, 6); K3_RDB(N, 1); SBAR(); K3_MF(C, 7); K3_RDA(N, 1); SBAR(); \
        K3_MF(C, 8); K3_RDA(N, 3); SBAR(); K3_MF(C, 9); K3_RDB(N, 3); SBAR(); K3_MF(C, 10); K3_RDA(N, 5); SBAR(); K3_MF(C, 11); K3_RDA(N, 7); SBAR(); \
        K3_MF(C, 12); K3_MF(C, 13); K3_MF(C, 14); K3_MF(C, 15); SBAR(); \
        } while (0)
    f32x4 acc[4][2];
#pragma unroll
    for (int m = 0; m < 4; ++m) { acc[m][0] = (f32x4){0.f, 0.f, 0.f, 0.f}; acc[m][1] = (f32x4){0.f, 0.f, 0.f, 0.f}; }
    Fr FA, FB;
    STAMP(3, 0);
    K3_STAGE(0); K3_STAGE(1); K3_STAGE(2); K3_STAGE(3);
    WAIT_V(12); BAR(); SBAR();
    STAMP(3, 1);
    { const unsigned sl_ = 0;
#pragma unroll
      for (int j = 0; j < 4; ++j) { K3_RDB(FA, j); }
#pragma unroll
      for (int j = 0; j < 8; ++j) { K3_RDA(FA, j); } }
    for (int t = 0; t < nt; t += 2) {
        K3_STEP(t, FA, FB);
        K3_STEP(t + 1, FB, FA);
    }
    STAMP(3, 2);
    const auto rs = __builtin_amdgcn_make_buffer_rsrc((void*)((char*)part + (size_t)tile * 65536), 0, 65536, 0x00020000);
    gu32* flag = (gu32*)(flags + tile * 64);
    if (!cons) {
#pragma unroll
        for (int m = 0; m < 4; ++m)
#pragma unroll
            for (int n = 0; n < 2; ++n) {
                const int row = mt * 128 + wr * 64 + m * 16 + fr, col = ntile * 128 + wc * 32 + n * 16 + fq * 4;
                const u32x2 xb = *(const u32x2*)(x + (size_t)row * DM + col), bb = *(const u32x2*)(b2 + col);
                const f32x4 v = acc[m][n] + (f32x4){bflo(xb[0]) + bflo(bb[0]), bfhi(xb[0]) + bfhi(bb[0]), bflo(xb[1]) + bflo(bb[1]), bfhi(xb[1]) + bfhi(bb[1])};
                __builtin_amdgcn_raw_buffer_store_b128(__builtin_bit_cast(u32x4, v), rs, (m * 2 + n) * 8192 + tid * 16, 0, 16);
            }
        WAIT_V(0);
        __syncthreads();
        if (tid == 0) __hip_atomic_store(flag, 1u, __ATOMIC_RELAXED, __HIP_MEMORY_SCOPE_AGENT);
    } else {
        if (wid == 0) {
            for (int spin = 0; spin < (1 << 20); ++spin) {
                if (__builtin_amdgcn_readfirstlane(__hip_atomic_load(flag, __ATOMIC_RELAXED, __HIP_MEMORY_SCOPE_AGENT)) == 1u) break;
                __builtin_amdgcn_s_sleep(1);
            }
            __builtin_amdgcn_fence(__ATOMIC_ACQUIRE, "agent");
            WAIT_V(0);
        }
        __syncthreads();
        u32x4 pv[8];
#pragma unroll
        for (int j = 0; j < 8; ++j) pv[j] = __builtin_amdgcn_raw_buffer_load_b128(rs, j * 8192 + tid * 16, 0, 16);
        const int cg = ((fq & 1) << 1) | (fq >> 1);
#pragma unroll
        for (int m = 0; m < 4; ++m) {
            const f32x4 v0 = acc[m][0] + __builtin_bit_cast(f32x4, pv[m * 2]), v1 = acc[m][1] + __builtin_bit_cast(f32x4, pv[m * 2 + 1]);
            const int row = mt * 128 + wr * 64 + m * 16 + fr;
            *(u32x4*)(out + (size_t)row * DM + ntile * 128 + wc * 32 + cg * 8) = k1::pack_swap16(v0, v1);
        }
    }
    STAMP(3, 3);
}
}

extern "C" void kernel_launch(void* const* d_in, const int* in_sizes, int n_in, void* d_out, int out_size, void* d_ws, size_t ws_size, hipStream_t stream) {
    const bf16_t* x = (const bf16_t*)d_in[0]; const bf16_t* ckv = (const bf16_t*)d_in[1];
    const bf16_t* wq = (const bf16_t*)d_in[2]; const bf16_t* wuk = (const bf16_t*)d_in[3]; const bf16_t* wuv = (const bf16_t*)d_in[4]; const bf16_t* wo = (const bf16_t*)d_in[5];
    const bf16_t* w1 = (const bf16_t*)d_in[7]; const bf16_t* b1 = (const bf16_t*)d_in[8]; const bf16_t* w2 = (const bf16_t*)d_in[9]; const bf16_t* b2 = (const bf16_t*)d_in[10];
    char* ws = (char*)d_ws;
    (void)hipFuncSetAttribute((const void*)k1::k1_proj, hipFuncAttributeMaxDynamicSharedMemorySize, k1::LDS_BYTES);
    (void)hipFuncSetAttribute((const void*)k2::k2_attn, hipFuncAttributeMaxDynamicSharedMemorySize, k2::LDS_BYTES);
    (void)hipFuncSetAttribute((const void*)k3::k3_out, hipFuncAttributeMaxDynamicSharedMemorySize, k3::LDS_BYTES);
    k0_ln<<<SEQ / 4, 256, 0, stream>>>(x, (bf16_t*)(ws + WS_XN), (unsigned*)(ws + WS_FLAG));
    k1::k1_proj<<<256, 512, k1::LDS_BYTES, stream>>>(ckv, ws + WS_XN, wuk, wuv, wq, w1, b1, ws + WS_Q, ws + WS_K, ws + WS_VT, ws + WS_H);
    k2::k2_attn<<<256, 512, k2::LDS_BYTES, stream>>>(ws + WS_Q, ws + WS_K, ws + WS_VT, ws + WS_CTX);
    k3::k3_out<<<256, 512, k3::LDS_BYTES, stream>>>(ws + WS_CTX, ws + WS_H, wo, w2, b2, x, (float*)(ws + WS_PART), (unsigned*)(ws + WS_FLAG), (bf16_t*)d_out);
}
```

```cpp
#include <hip/hip_runtime.h>
#include <cstdint>

#define STAMP(k, i) do {} while (0)
#define LAS __attribute__((address_space(3)))
#define DI __device__ __forceinline__
typedef unsigned short bf16_t;
typedef short bf16x8 __attribute__((ext_vector_type(8)));
typedef short s16x4 __attribute__((ext_vector_type(4)));
typedef short v4i16_t __attribute__((ext_vector_type(4)));
typedef float f32x2 __attribute__((ext_vector_type(2)));
typedef float f32x4 __attribute__((ext_vector_type(4)));
typedef float f32x16 __attribute__((ext_vector_type(16)));
typedef unsigned u32x2 __attribute__((ext_vector_type(2)));
typedef unsigned u32x4 __attribute__((ext_vector_type(4)));
typedef __bf16 bf16x2_t __attribute__((ext_vector_type(2)));
typedef LAS unsigned char* lds_ptr;
typedef LAS const unsigned char* lds_cptr;

constexpr int SEQ = 2048, DM = 1024, NH = 32, HD = 128, DC = 512, FF = 3584, NQ = NH * HD;
constexpr size_t MiB = 1u << 20;
constexpr size_t WS_FLAG = 0;
constexpr size_t WS_XN = 1 * MiB;
constexpr size_t WS_Q = 8 * MiB;
constexpr size_t WS_K = 24 * MiB;
constexpr size_t WS_VT = 40 * MiB;
constexpr size_t WS_CTX = 56 * MiB;
constexpr size_t WS_H = 72 * MiB;
constexpr size_t WS_PART = 88 * MiB;

DI unsigned cvtpk(float lo, float hi) { f32x2 v = {lo, hi}; bf16x2_t b = __builtin_convertvector(v, bf16x2_t); return __builtin_bit_cast(unsigned, b); }
DI float bflo(unsigned w) { return __uint_as_float(w << 16); }
DI float bfhi(unsigned w) { return __uint_as_float(w & 0xffff0000u); }
DI s16x4 lds_tr(lds_cptr p) { return __builtin_bit_cast(s16x4, __builtin_amdgcn_ds_read_tr16_b64_v4i16((LAS v4i16_t*)p)); }
DI bf16x8 lds_rd(lds_cptr p) { return *(const LAS bf16x8*)p; }
DI bf16x8 lds_rdv(lds_cptr p) { return *(const volatile LAS bf16x8*)p; }
DI bf16x8 cat8(s16x4 a, s16x4 b) { return __builtin_shufflevector(a, b, 0, 1, 2, 3, 4, 5, 6, 7); }
DI void st16_sc1(void* p, u32x4 v) { asm volatile("global_store_dwordx4 %0, %1, off sc1\n\ts_nop 1" :: "v"(p), "v"(v) : "memory"); }
#define ST16(p, v) st16_sc1((void*)(p), v)
#define SBAR() __builtin_amdgcn_sched_barrier(0)
#define PIN(x) asm volatile("" : "+v"(x))
#define WAIT_V(n) asm volatile("s_waitcnt vmcnt(" #n ")" ::: "memory")
#define WAIT_L(n) asm volatile("s_waitcnt lgkmcnt(" #n ")" ::: "memory")
#define BAR() __builtin_amdgcn_s_barrier()
#define MFMA16(a, b, c) __builtin_amdgcn_mfma_f32_16x16x32_bf16(a, b, c, 0, 0, 0)
#define MFMA32(a, b, c) __builtin_amdgcn_mfma_f32_32x32x16_bf16(a, b, c, 0, 0, 0)
DI void glds(const char* sbase, unsigned voff, unsigned ldsaddr) {
    asm volatile("s_mov_b32 m0, %2\n\ts_nop 0\n\tglobal_load_lds_dwordx4 %0, %1" :: "v"(voff), "s"(sbase), "s"(ldsaddr) : "memory");
}
DI void glds2(const char* sbase, unsigned voff, unsigned ldsaddr) {
    asm volatile("s_mov_b32 m0, %2\n\ts_nop 0\n\tglobal_load_lds_dwordx4 %0, %1\n\tglobal_load_lds_dwordx4 %0, %1 offset:1024" :: "v"(voff), "s"(sbase), "s"(ldsaddr) : "memory");
}

__global__ __launch_bounds__(256) void k0_ln(const bf16_t* __restrict__ x, bf16_t* __restrict__ xn, unsigned* __restrict__ flags) {
    const int lane = threadIdx.x & 63, row = blockIdx.x * 4 + (threadIdx.x >> 6);
    const u32x4* src = (const u32x4*)(x + (size_t)row * DM);
    const u32x4 v0 = src[lane], v1 = src[lane + 64];
    float f[16];
#pragma unroll
    for (int i = 0; i < 4; ++i) { f[2 * i] = bflo(v0[i]); f[2 * i + 1] = bfhi(v0[i]); f[8 + 2 * i] = bflo(v1[i]); f[8 + 2 * i + 1] = bfhi(v1[i]); }
    float s = 0.f;
#pragma unroll
    for (int i = 0; i < 16; ++i) s += f[i];
#pragma unroll
    for (int o = 32; o >= 1; o >>= 1) s += __shfl_xor(s, o);
    const float mean = s * (1.0f / DM);
    float q = 0.f;
#pragma unroll
    for (int i = 0; i < 16; ++i) { f[i] -= mean; q += f[i] * f[i]; }
#pragma unroll
    for (int o = 32; o >= 1; o >>= 1) q += __shfl_xor(q, o);
    const float rstd = 1.0f / sqrtf(q * (1.0f / DM) + 1e-5f);
    u32x4 w0, w1;
#pragma unroll
    for (int i = 0; i < 4; ++i) { w0[i] = cvtpk(f[2 * i] * rstd, f[2 * i + 1] * rstd); w1[i] = cvtpk(f[8 + 2 * i] * rstd, f[8 + 2 * i + 1] * rstd); }
    const int mh = row >> 7, r = row & 127, c = lane & 7, kt = lane >> 3, sw = (c ^ ((r >> 1) & 7)) * 16;
    char* base = (char*)xn + (size_t)mh * 16 * 16384 + r * 128 + sw;
    *(u32x4*)(base + (size_t)kt * 16384) = w0;
    *(u32x4*)(base + (size_t)(kt + 8) * 16384) = w1;
    if (blockIdx.x == 0) flags[threadIdx.x * 32] = 0u;
}

namespace k1 {
constexpr int HTB = 16384, LDS_BYTES = 8 * HTB;
enum { T_K = 0, T_V = 1, T_Q = 2, T_H = 3 };
struct Src {
    const char* a; const char* b;
    unsigned aK, aH, aD, bK, bD;
    unsigned va, vb;
};
struct Frag { bf16x8 At[4][2], B0[2][2], B1[2][2]; };

DI f32x2 gelu_tanh2(f32x2 v) {
    const f32x2 z = v * (v * v * -0.10294324f + -2.3022082f);
    f32x2 e; e.x = __builtin_amdgcn_exp2f(z.x); e.y = __builtin_amdgcn_exp2f(z.y);
    e = e + 1.0f;
    f32x2 r; r.x = __builtin_amdgcn_rcpf(e.x); r.y = __builtin_amdgcn_rcpf(e.y);
    return v * r;
}

#define K1_SA(b, h) (((b) * 2 + (h)) * HTB)
#define K1_SB(b, h) ((4 + (b) * 2 + (h)) * HTB)
#define K1_STAGE(bufoff, gbase, voff, d2) do { glds((const char*)(gbase), voff, ldsw + (bufoff)); glds((const char*)(gbase) + (d2), voff, ldsw + (bufoff) + 8192); } while (0)
#define K1_LDA(dst, b, h) do { _Pragma("unroll") for (int m = 0; m < 4; ++m) { dst[m][0] = lds_rd(lds + K1_SA(b, h) + aoff0 + m * 2048); dst[m][1] = lds_rd(lds + K1_SA(b, h) + aoff1 + m * 2048); } } while (0)
#define K1_LDB(dst, b, h) do { _Pragma("unroll") for (int k = 0; k < 2; ++k) { \
        dst[0][k] = cat8(lds_tr(lds + K1_SB(b, h) + boff0 + k * 8192), lds_tr(lds + K1_SB(b, h) + boff0 + k * 8192 + 256)); \
        dst[1][k] = cat8(lds_tr(lds + K1_SB(b, h) + boff1 + k * 8192), lds_tr(lds + K1_SB(b, h) + boff1 + k * 8192 + 256)); } } while (0)
#define K1_MMA(ai, bj, At, Bt) do { __builtin_amdgcn_s_setprio(1); _Pragma("unroll") for (int m = 0; m < 4; ++m) _Pragma("unroll") for (int n = 0; n < 2; ++n) _Pragma("unroll") for (int k = 0; k < 2; ++k) \
        acc[ai][bj][m][n] = SWAP ? MFMA16(At[m][k], Bt[n][k], acc[ai][bj][m][n]) : MFMA16(Bt[n][k], At[m][k], acc[ai][bj][m][n]); __builtin_amdgcn_s_setprio(0); } while (0)

template <bool SWAP>
DI void kloop(lds_ptr lds, f32x4 (&acc)[2][2][4][2], const Src& cur, const Src& nxt, int nt, unsigned ldsw, int aoff0, int aoff1, int boff0, int boff1) {
    bf16x8 At[4][2], B0[2][2], B1[2][2];
    for (int t = 0; t < nt; t += 2) {
        const bool last = (t == nt - 2);
        const char* a1h = cur.a + (size_t)(t + 1) * cur.aK + cur.aH;
        const char* a2 = last ? nxt.a : cur.a + (size_t)(t + 2) * cur.aK; const char* b2 = last ? nxt.b : cur.b + (size_t)(t + 2) * cur.bK;
        const unsigned aK = last ? nxt.aK : cur.aK, aH = last ? nxt.aH : cur.aH, aD = last ? nxt.aD : cur.aD, bK = last ? nxt.bK : cur.bK, bD = last ? nxt.bD : cur.bD;
        const unsigned va = last ? nxt.va : cur.va, vb = last ? nxt.vb : cur.vb;
        const char* a3 = a2 + aK; const char* b3 = b2 + bK;
        K1_LDB(B0, 0, 0); K1_LDB(B1, 0, 1); SBAR(); K1_LDA(At, 0, 0); K1_STAGE(K1_SA(1, 1), a1h, cur.va, cur.aD);
        WAIT_V(8); WAIT_L(0); BAR(); K1_MMA(0, 0, At, B0); K1_MMA(0, 1, At, B1); BAR(); SBAR();
        K1_LDA(At, 0, 1); K1_STAGE(K1_SB(0, 0), b2, vb, bD); K1_STAGE(K1_SB(0, 1), b2 + 256, vb, bD); K1_STAGE(K1_SA(0, 0), a2, va, aD);
        WAIT_V(8); WAIT_L(0); BAR(); K1_MMA(1, 0, At, B0); K1_MMA(1, 1, At, B1); BAR(); SBAR();
        K1_LDB(B0, 1, 0); K1_LDB(B1, 1, 1); SBAR(); K1_LDA(At, 1, 0); K1_STAGE(K1_SA(0, 1), a2 + aH, va, aD);
        WAIT_V(8); WAIT_L(0); BAR(); K1_MMA(0, 0, At, B0); K1_MMA(0, 1, At, B1); BAR(); SBAR();
        K1_LDA(At, 1, 1); K1_STAGE(K1_SB(1, 0), b3, vb, bD); K1_STAGE(K1_SB(1, 1), b3 + 256, vb, bD); K1_STAGE(K1_SA(1, 0), a3, va, aD);
        WAIT_V(8); WAIT_L(0); BAR(); K1_MMA(1, 0, At, B0); K1_MMA(1, 1, At, B1); BAR(); SBAR();
    }
}

DI void zero_acc(f32x4 (&acc)[2][2][4][2]) {
#pragma unroll
    for (int a = 0; a < 2; ++a)
#pragma unroll
        for (int b = 0; b < 2; ++b)
#pragma unroll
            for (int m = 0; m < 4; ++m)
#pragma unroll
                for (int n = 0; n < 2; ++n) acc[a][b][m][n] = (f32x4){0.f, 0.f, 0.f, 0.f};
}
DI u32x4 pack_swap16(f32x4 v0, f32x4 v1) {
    const unsigned x0 = cvtpk(v0[0], v0[1]), x1 = cvtpk(v0[2], v0[3]), y0 = cvtpk(v1[0], v1[1]), y1 = cvtpk(v1[2], v1[3]);
    const auto r0 = __builtin_amdgcn_permlane16_swap(x0, y0, false, false), r1 = __builtin_amdgcn_permlane16_swap(x1, y1, false, false);
    return (u32x4){r0[0], r1[0], r0[1], r1[1]};
}
DI u32x4 pack_swap32(f32x4 v0, f32x4 v1) {
    const unsigned x0 = cvtpk(v0[0], v0[1]), x1 = cvtpk(v0[2], v0[3]), y0 = cvtpk(v1[0], v1[1]), y1 = cvtpk(v1[2], v1[3]);
    const auto r0 = __builtin_amdgcn_permlane32_swap(x0, y0, false, false), r1 = __builtin_amdgcn_permlane32_swap(x1, y1, false, false);
    return (u32x4){r0[0], r1[0], r0[1], r1[1]};
}
DI void epi_qk(const f32x4 (&acc)[2][2][4][2], char* dst, int pm, int pn, float sc, int wr, int wc, int fr, int fq) {
    const int cg = ((fq & 1) << 1) | (fq >> 1);
    char* p = dst + (size_t)(2 * pn * 32 + 4 * pm + wr) * 16384 + (4 * wc + cg) * 1024 + fr * 16;
#pragma unroll
    for (int ai = 0; ai < 2; ++ai)
#pragma unroll
        for (int bj = 0; bj < 2; ++bj)
#pragma unroll
            for (int m = 0; m < 4; ++m)
                ST16(p + (size_t)(bj * 32 + ai * 2) * 16384 + m * 256, pack_swap16(acc[ai][bj][m][0] * sc, acc[ai][bj][m][1] * sc));
}
DI void epi_v(const f32x4 (&acc)[2][2][4][2], char* dst, int pm, int pn, int wr, int wc, int fr, int fq) {
    char* p = dst + (size_t)(2 * pn * 32 + 4 * pm + wr) * 16384 + fq * 2048 + (32 * wc + fr) * 16;
#pragma unroll
    for (int ai = 0; ai < 2; ++ai)
#pragma unroll
        for (int bj = 0; bj < 2; ++bj)
#pragma unroll
            for (int m = 0; m < 4; m += 2)
#pragma unroll
                for (int n = 0; n < 2; ++n)
                    ST16(p + (size_t)(bj * 32 + ai * 2) * 16384 + m * 4096 + n * 256, pack_swap32(acc[ai][bj][m][n], acc[ai][bj][m + 1][n]));
}
DI void epi_h(const f32x4 (&acc)[2][2][4][2], char* dst, const bf16_t* b1, int pm, int pn, int wr, int wc, int fr, int fq) {
    const int cg = ((fq & 1) << 1) | (fq >> 1);
    f32x4 bv[2][2];
#pragma unroll
    for (int bj = 0; bj < 2; ++bj)
#pragma unroll
        for (int n = 0; n < 2; ++n) { const u32x2 w = *(const u32x2*)(b1 + pn * 256 + bj * 128 + wc * 32 + n * 16 + fq * 4); bv[bj][n] = (f32x4){bflo(w[0]), bfhi(w[0]), bflo(w[1]), bfhi(w[1])}; }
    char* p = dst + ((size_t)(2 * pm * 56 + 4 * pn + (wc >> 1)) * 8 + 4 * (wc & 1) + cg) * 2048 + (64 * wr + fr) * 16;
#pragma unroll
    for (int ai = 0; ai < 2; ++ai)
#pragma unroll
        for (int bj = 0; bj < 2; ++bj)
#pragma unroll
            for (int m = 0; m < 4; ++m) {
                f32x4 v0 = acc[ai][bj][m][0] + bv[bj][0], v1 = acc[ai][bj][m][1] + bv[bj][1];
                { const f32x2 a = gelu_tanh2((f32x2){v0[0], v0[1]}), b = gelu_tanh2((f32x2){v0[2], v0[3]}), c = gelu_tanh2((f32x2){v1[0], v1[1]}), d = gelu_tanh2((f32x2){v1[2], v1[3]});
                  v0 = (f32x4){a.x, a.y, b.x, b.y}; v1 = (f32x4){c.x, c.y, d.x, d.y}; }
                ST16(p + (size_t)(ai * 56 + bj * 2) * 16384 + m * 256, pack_swap16(v0, v1));
            }
}

__global__ __launch_bounds__(512, 2) void k1_proj(const bf16_t* __restrict__ ckv, const char* __restrict__ xn, const bf16_t* __restrict__ wuk, const bf16_t* __restrict__ wuv,
                                                  const bf16_t* __restrict__ wq, const bf16_t* __restrict__ w1, const bf16_t* __restrict__ b1,
                                                  char* __restrict__ Qo, char* __restrict__ Ko, char* __restrict__ VTo, char* __restrict__ Ho) {
    extern __shared__ __attribute__((aligned(16))) unsigned char smem[];
    lds_ptr lds = (lds_ptr)smem;
    const int tid = threadIdx.x, wid = __builtin_amdgcn_readfirstlane(tid >> 6), lane = tid & 63, wr = wid >> 2, wc = wid & 3, fr = lane & 15, fq = lane >> 4;
    const int xcd = blockIdx.x & 7, idx = blockIdx.x >> 3, pm = idx & 7, pj = xcd + 8 * (idx >> 3);
    const bool isV = pj >= 16, has2 = pj < 30, isH = pj >= 16;
    const unsigned ldsw = (unsigned)(uintptr_t)smem + (unsigned)wid * 1024u;
    const int aoff0 = (wr * 64 + fr) * 128 + ((fq ^ (fr >> 1)) * 16), aoff1 = aoff0 ^ 64;
    const int boff0 = fq * 2048 + wc * 512 + (fr >> 2) * 64 + (fq & 1) * 32 + (fr & 3) * 8, boff1 = boff0 ^ 32;
    const unsigned brow = 8 * (wid >> 1) + ((lane & 31) >> 2), bcol = (64 * (wid & 1) + 32 * (lane >> 5) + 8 * ((lane & 3) ^ (wid & 2))) * 2;
    Src u0, u1;
    u0.a = (const char*)ckv + (size_t)pm * 256 * DC * 2; u0.aK = 128; u0.aH = 128 * DC * 2; u0.aD = 64 * DC * 2;
    u0.va = (tid >> 3) * (DC * 2) + (((tid & 7) ^ ((tid >> 4) & 7)) * 16);
    u0.b = (const char*)(isV ? wuv : wuk) + (pj & 15) * 512; u0.bK = 64 * NQ * 2; u0.bD = 32 * NQ * 2; u0.vb = brow * (NQ * 2) + bcol;
    if (has2) {
        const unsigned ldb2 = isH ? FF * 2 : NQ * 2;
        u1.a = xn + (size_t)pm * 2 * 16 * 16384; u1.aK = 16384; u1.aH = 16 * 16384; u1.aD = 8192; u1.va = tid * 16;
        u1.b = (const char*)(isH ? w1 : wq) + (pj & 15) * 512; u1.bK = 64 * ldb2; u1.bD = 32 * ldb2; u1.vb = brow * ldb2 + bcol;
    } else u1 = u0;
    f32x4 acc[2][2][4][2];
    zero_acc(acc);
    STAMP(1, 0);
    K1_STAGE(K1_SB(0, 0), u0.b, u0.vb, u0.bD); K1_STAGE(K1_SB(0, 1), u0.b + 256, u0.vb, u0.bD); K1_STAGE(K1_SA(0, 0), u0.a, u0.va, u0.aD); K1_STAGE(K1_SA(0, 1), u0.a + u0.aH, u0.va, u0.aD);
    if (wr == 1) BAR();
    WAIT_V(2); BAR();
    K1_STAGE(K1_SB(1, 0), u0.b + u0.bK, u0.vb, u0.bD); K1_STAGE(K1_SA(1, 0), u0.a + u0.aK, u0.va, u0.aD); K1_STAGE(K1_SB(1, 1), u0.b + u0.bK + 256, u0.vb, u0.bD);
    WAIT_V(6); BAR();
    STAMP(1, 1);
    if (isV) kloop<true>(lds, acc, u0, u1, DC / 64, ldsw, aoff0, aoff1, boff0, boff1);
    else     kloop<false>(lds, acc, u0, u1, DC / 64, ldsw, aoff0, aoff1, boff0, boff1);
    STAMP(1, 2);
    if (wr == 0) BAR();
    if (isV) epi_v(acc, VTo, pm, pj & 15, wr, wc, fr, fq); else epi_qk(acc, Ko, pm, pj & 15, 1.0f, wr, wc, fr, fq);
    if (has2) {
        zero_acc(acc);
        STAMP(1, 3);
        if (wr == 1) BAR();
        kloop<false>(lds, acc, u1, u1, DM / 64, ldsw, aoff0, aoff1, boff0, boff1);
        STAMP(1, 4);
        if (wr == 0) BAR();
        if (isH) epi_h(acc, Ho, b1, pm, pj & 15, wr, wc, fr, fq); else epi_qk(acc, Qo, pm, pj & 15, 0.12751743082459868f, wr, wc, fr, fq);
    }
    STAMP(1, 5);
    WAIT_V(0);
    STAMP(1, 6);
}
#undef K1_SA
#undef K1_SB
#undef K1_STAGE
#undef K1_LDA
#undef K1_LDB
#undef K1_MMA
}

namespace k2 {
constexpr int SLOT = 16384, LDS_K = 0, LDS_V = 4 * SLOT, LDS_BYTES = 8 * SLOT;
#define K2_ASM \
    "s_nop 1\n\t" \
    "s_cmp_eq_u32 %[n], 0\n\t" \
    "s_cbranch_scc1 TAIL_%=\n\t" \
    "LOOP_%=:\n\t" \
    "s_cmp_lg_u32 %[dmaw], 0\n\t" \
    "s_cbranch_scc1 DMAL_%=\n\t" \
    "DMABL_%=:\n\t" \
    "s_waitcnt lgkmcnt(0)\n\t" \
    "v_mfma_f32_32x32x16_bf16 v[112:127], v[168:171], v[64:67], v[96:111]\n\t" \
    "ds_read_b128 v[200:203], v235 offset:8192\n\t" \
    "ds_read_b128 v[204:207], v235 offset:8704\n\t" \
    "v_add_f32 v237, v144, v145\n\t" \
    "v_cvt_pk_bf16_f32 v160, v144, v145\n\t" \
    "v_mfma_f32_32x32x16_bf16 v[112:127], v[172:175], v[68:71], v[112:127]\n\t" \
    "ds_read_b128 v[208:211], v235 offset:9216\n\t" \
    "ds_read_b128 v[212:215], v235 offset:9728\n\t" \
    "v_add_f32 v237, v237, v146\n\t" \
    "v_add_f32 v237, v237, v147\n\t" \
    "v_cvt_pk_bf16_f32 v161, v146, v147\n\t" \
    "v_mfma_f32_32x32x16_bf16 v[112:127], v[176:179], v[72:75], v[112:127]\n\t" \
    "ds_read_b128 v[216:219], v235 offset:12288\n\t" \
    "ds_read_b128 v[220:223], v235 offset:12800\n\t" \
    "v_add_f32 v237, v237, v148\n\t" \
    "v_add_f32 v237, v237, v149\n\t" \
    "v_cvt_pk_bf16_f32 v162, v148, v149\n\t" \
    "v_mfma_f32_32x32x16_bf16 v[112:127], v[180:183], v[76:79], v[112:127]\n\t" \
    "ds_read_b128 v[224:227], v235 offset:13312\n\t" \
    "ds_read_b128 v[228:231], v235 offset:13824\n\t" \
    "v_add_f32 v237, v237, v150\n\t" \
    "v_add_f32 v237, v237, v151\n\t" \
    "v_cvt_pk_bf16_f32 v163, v150, v151\n\t" \
    "v_mfma_f32_32x32x16_bf16 v[112:127], v[184:187], v[80:83], v[112:127]\n\t" \
    "v_add_f32 v237, v237, v152\n\t" \
    "v_add_f32 v237, v237, v153\n\t" \
    "v_cvt_pk_bf16_f32 v164, v152, v153\n\t" \
    "v_mfma_f32_32x32x16_bf16 v[112:127], v[188:191], v[84:87], v[112:127]\n\t" \
    "v_add_f32 v237, v237, v154\n\t" \
    "v_add_f32 v237, v237, v155\n\t" \
    "v_cvt_pk_bf16_f32 v165, v154, v155\n\t" \
    "v_mfma_f32_32x32x16_bf16 v[112:127], v[192:195], v[88:91], v[112:127]\n\t" \
    "v_add_f32 v237, v237, v156\n\t" \
    "v_add_f32 v237, v237, v157\n\t" \
    "v_cvt_pk_bf16_f32 v166, v156, v157\n\t" \
    "v_mfma_f32_32x32x16_bf16 v[112:127], v[196:199], v[92:95], v[112:127]\n\t" \
    "v_add_f32 v237, v237, v158\n\t" \
    "v_add_f32 v237, v237, v159\n\t" \
    "v_cvt_pk_bf16_f32 v167, v158, v159\n\t" \
    "v_cmp_lt_f32 vcc, %[thr], v237\n\t" \
    "s_cbranch_vccnz RAREA_%=\n\t" \
    "BACKA_%=:\n\t" \
    "s_waitcnt lgkmcnt(0)\n\t" \
    "v_mfma_f32_32x32x16_bf16 v[0:15], v[200:203], v[160:163], v[0:15]\n\t" \
    "ds_read_b128 v[168:171], v232 offset:512\n\t" \
    "ds_read_b128 v[172:175], v232 offset:2560\n\t" \
    "v_add_f32 v236, v236, v237\n\t" \
    "v_mfma_f32_32x32x16_bf16 v[16:31], v[204:207], v[160:163], v[16:31]\n\t" \
    "ds_read_b128 v[176:179], v232 offset:4608\n\t" \
    "ds_read_b128 v[180:183], v232 offset:6656\n\t" \
    "v_exp_f32 v144, v112\n\t" \
    "v_exp_f32 v145, v113\n\t" \
    "v_mfma_f32_32x32x16_bf16 v[32:47], v[208:211], v[160:163], v[32:47]\n\t" \
    "ds_read_b128 v[184:187], v232 offset:8704\n\t" \
    "ds_read_b128 v[188:191], v232 offset:10752\n\t" \
    "v_exp_f32 v146, v114\n\t" \
    "v_exp_f32 v147, v115\n\t" \
    "v_mfma_f32_32x32x16_bf16 v[48:63], v[212:215], v[160:163], v[48:63]\n\t" \
    "ds_read_b128 v[192:195], v232 offset:12800\n\t" \
    "ds_read_b128 v[196:199], v232 offset:14848\n\t" \
    "v_exp_f32 v148, v116\n\t" \
    "v_exp_f32 v149, v117\n\t" \
    "v_mfma_f32_32x32x16_bf16 v[0:15], v[216:219], v[164:167], v[0:15]\n\t" \
    "v_exp_f32 v150, v118\n\t" \
    "v_exp_f32 v151, v119\n\t" \
    "v_exp_f32 v152, v120\n\t" \
    "v_mfma_f32_32x32x16_bf16 v[16:31], v[220:223], v[164:167], v[16:31]\n\t" \
    "v_exp_f32 v153, v121\n\t" \
    "v_exp_f32 v154, v122\n\t" \
    "v_exp_f32 v155, v123\n\t" \
    "v_mfma_f32_32x32x16_bf16 v[32:47], v[224:227], v[164:167], v[32:47]\n\t" \
    "v_exp_f32 v156, v124\n\t" \
    "v_exp_f32 v157, v125\n\t" \
    "v_mfma_f32_32x32x16_bf16 v[48:63], v[228:231], v[164:167], v[48:63]\n\t" \
    "v_exp_f32 v158, v126\n\t" \
    "v_exp_f32 v159, v127\n\t" \
    "s_waitcnt lgkmcnt(0)\n\t" \
    "v_mfma_f32_32x32x16_bf16 v[128:143], v[168:171], v[64:67], v[96:111]\n\t" \
    "ds_read_b128 v[200:203], v234 offset:0\n\t" \
    "ds_read_b128 v[204:207], v234 offset:512\n\t" \
    "v_add_f32 v237, v144, v145\n\t" \
    "v_cvt_pk_bf16_f32 v160, v144, v145\n\t" \
    "v_mfma_f32_32x32x16_bf16 v[128:143], v[172:175], v[68:71], v[128:143]\n\t" \
    "ds_read_b128 v[208:211], v234 offset:1024\n\t" \
    "ds_read_b128 v[212:215], v234 offset:1536\n\t" \
    "v_add_f32 v237, v237, v146\n\t" \
    "v_add_f32 v237, v237, v147\n\t" \
    "v_cvt_pk_bf16_f32 v161, v146, v147\n\t" \
    "v_mfma_f32_32x32x16_bf16 v[128:143], v[176:179], v[72:75], v[128:143]\n\t" \
    "ds_read_b128 v[216:219], v234 offset:4096\n\t" \
    "ds_read_b128 v[220:223], v234 offset:4608\n\t" \
    "v_add_f32 v237, v237, v148\n\t" \
    "v_add_f32 v237, v237, v149\n\t" \
    "v_cvt_pk_bf16_f32 v162, v148, v149\n\t" \
    "v_mfma_f32_32x32x16_bf16 v[128:143], v[180:183], v[76:79], v[128:143]\n\t" \
    "ds_read_b128 v[224:227], v234 offset:5120\n\t" \
    "ds_read_b128 v[228:231], v234 offset:5632\n\t" \
    "v_add_f32 v237, v237, v150\n\t" \
    "v_add_f32 v237, v237, v151\n\t" \
    "v_cvt_pk_bf16_f32 v163, v150, v151\n\t" \
    "v_mfma_f32_32x32x16_bf16 v[128:143], v[184:187], v[80:83], v[128:143]\n\t" \
    "v_add_f32 v237, v237, v152\n\t" \
    "v_add_f32 v237, v237, v153\n\t" \
    "v_cvt_pk_bf16_f32 v164, v152, v153\n\t" \
    "v_mfma_f32_32x32x16_bf16 v[128:143], v[188:191], v[84:87], v[128:143]\n\t" \
    "v_add_f32 v237, v237, v154\n\t" \
    "v_add_f32 v237, v237, v155\n\t" \
    "v_cvt_pk_bf16_f32 v165, v154, v155\n\t" \
    "v_mfma_f32_32x32x16_bf16 v[128:143], v[192:195], v[88:91], v[128:143]\n\t" \
    "v_add_f32 v237, v237, v156\n\t" \
    "v_add_f32 v237, v237, v157\n\t" \
    "v_cvt_pk_bf16_f32 v166, v156, v157\n\t" \
    "v_mfma_f32_32x32x16_bf16 v[128:143], v[196:199], v[92:95], v[128:143]\n\t" \
    "v_add_f32 v237, v237, v158\n\t" \
    "v_add_f32 v237, v237, v159\n\t" \
    "v_cvt_pk_bf16_f32 v167, v158, v159\n\t" \
    "v_cmp_lt_f32 vcc, %[thr], v237\n\t" \
    "s_cbranch_vccnz RAREB_%=\n\t" \
    "BACKB_%=:\n\t" \
    "s_waitcnt lgkmcnt(0)\n\t" \
    "v_mfma_f32_32x32x16_bf16 v[0:15], v[200:203], v[160:163], v[0:15]\n\t" \
    "ds_read_b128 v[168:171], v233 offset:0\n\t" \
    "ds_read_b128 v[172:175], v233 offset:2048\n\t" \
    "v_add_f32 v236, v236, v237\n\t" \
    "v_mfma_f32_32x32x16_bf16 v[16:31], v[204:207], v[160:163], v[16:31]\n\t" \
    "ds_read_b128 v[176:179], v233 offset:4096\n\t" \
    "ds_read_b128 v[180:183], v233 offset:6144\n\t" \
    "v_exp_f32 v144, v128\n\t" \
    "v_exp_f32 v145, v129\n\t" \
    "v_mfma_f32_32x32x16_bf16 v[32:47], v[208:211], v[160:163], v[32:47]\n\t" \
    "ds_read_b128 v[184:187], v233 offset:8192\n\t" \
    "ds_read_b128 v[188:191], v233 offset:10240\n\t" \
    "v_exp_f32 v146, v130\n\t" \
    "v_exp_f32 v147, v131\n\t" \
    "v_mfma_f32_32x32x16_bf16 v[48:63], v[212:215], v[160:163], v[48:63]\n\t" \
    "ds_read_b128 v[192:195], v233 offset:12288\n\t" \
    "ds_read_b128 v[196:199], v233 offset:14336\n\t" \
    "v_exp_f32 v148, v132\n\t" \
    "v_exp_f32 v149, v133\n\t" \
    "v_mfma_f32_32x32x16_bf16 v[0:15], v[216:219], v[164:167], v[0:15]\n\t" \
    "v_exp_f32 v150, v134\n\t" \
    "v_exp_f32 v151, v135\n\t" \
    "v_exp_f32 v152, v136\n\t" \
    "v_mfma_f32_32x32x16_bf16 v[16:31], v[220:223], v[164:167], v[16:31]\n\t" \
    "v_exp_f32 v153, v137\n\t" \
    "v_exp_f32 v154, v138\n\t" \
    "v_exp_f32 v155, v139\n\t" \
    "s_add_u32 %[soff], %[soff], 0x4000\n\t" \
    "s_and_b32 %[soff], %[soff], 0xffff\n\t" \
    "v_mfma_f32_32x32x16_bf16 v[32:47], v[224:227], v[164:167], v[32:47]\n\t" \
    "v_exp_f32 v156, v140\n\t" \
    "v_exp_f32 v157, v141\n\t" \
    "v_mov_b32 v235, v234\n\t" \
    "v_mov_b32 v232, v233\n\t" \
    "v_mfma_f32_32x32x16_bf16 v[48:63], v[228:231], v[164:167], v[48:63]\n\t" \
    "v_exp_f32 v158, v142\n\t" \
    "v_exp_f32 v159, v143\n\t" \
    "v_add_u32 v234, %[soff], %[vp0]\n\t" \
    "s_add_u32 s60, %[soff], 0x4000\n\t" \
    "s_and_b32 s60, s60, 0xffff\n\t" \
    "v_add_u32 v233, s60, %[kp0]\n\t" \
    "s_waitcnt vmcnt(8)\n\t" \
    "s_barrier\n\t" \
    "s_add_u32 %[t], %[t], 1\n\t" \
    "s_cmp_lt_u32 %[t], %[n]\n\t" \
    "s_cbranch_scc1 LOOP_%=\n\t" \
    "TAIL_%=:\n\t" \
    "s_cmp_lg_u32 %[dmaw], 0\n\t" \
    "s_cbranch_scc1 DMAT_%=\n\t" \
    "DMABT_%=:\n\t" \
    "s_cmp_eq_u32 %[odd], 0\n\t" \
    "s_cbranch_scc1 EVEN_%=\n\t" \
    "s_waitcnt lgkmcnt(0)\n\t" \
    "v_mfma_f32_32x32x16_bf16 v[112:127], v[168:171], v[64:67], v[96:111]\n\t" \
    "ds_read_b128 v[200:203], v235 offset:8192\n\t" \
    "ds_read_b128 v[204:207], v235 offset:8704\n\t" \
    "v_add_f32 v237, v144, v145\n\t" \
    "v_cvt_pk_bf16_f32 v160, v144, v145\n\t" \
    "v_mfma_f32_32x32x16_bf16 v[112:127], v[172:175], v[68:71], v[112:127]\n\t" \
    "ds_read_b128 v[208:211], v235 offset:9216\n\t" \
    "ds_read_b128 v[212:215], v235 offset:9728\n\t" \
    "v_add_f32 v237, v237, v146\n\t" \
    "v_add_f32 v237, v237, v147\n\t" \
    "v_cvt_pk_bf16_f32 v161, v146, v147\n\t" \
    "v_mfma_f32_32x32x16_bf16 v[112:127], v[176:179], v[72:75], v[112:127]\n\t" \
    "ds_read_b128 v[216:219], v235 offset:12288\n\t" \
    "ds_read_b128 v[220:223], v235 offset:12800\n\t" \
    "v_add_f32 v237, v237, v148\n\t" \
    "v_add_f32 v237, v237, v149\n\t" \
    "v_cvt_pk_bf16_f32 v162, v148, v149\n\t" \
    "v_mfma_f32_32x32x16_bf16 v[112:127], v[180:183], v[76:79], v[112:127]\n\t" \
    "ds_read_b128 v[224:227], v235 offset:13312\n\t" \
    "ds_read_b128 v[228:231], v235 offset:13824\n\t" \
    "v_add_f32 v237, v237, v150\n\t" \
    "v_add_f32 v237, v237, v151\n\t" \
    "v_cvt_pk_bf16_f32 v163, v150, v151\n\t" \
    "v_mfma_f32_32x32x16_bf16 v[112:127], v[184:187], v[80:83], v[112:127]\n\t" \
    "v_add_f32 v237, v237, v152\n\t" \
    "v_add_f32 v237, v237, v153\n\t" \
    "v_cvt_pk_bf16_f32 v164, v152, v153\n\t" \
    "v_mfma_f32_32x32x16_bf16 v[112:127], v[188:191], v[84:87], v[112:127]\n\t" \
    "v_add_f32 v237, v237, v154\n\t" \
    "v_add_f32 v237, v237, v155\n\t" \
    "v_cvt_pk_bf16_f32 v165, v154, v155\n\t" \
    "v_mfma_f32_32x32x16_bf16 v[112:127], v[192:195], v[88:91], v[112:127]\n\t" \
    "v_add_f32 v237, v237, v156\n\t" \
    "v_add_f32 v237, v237, v157\n\t" \
    "v_cvt_pk_bf16_f32 v166, v156, v157\n\t" \
    "v_mfma_f32_32x32x16_bf16 v[112:127], v[196:199], v[92:95], v[112:127]\n\t" \
    "v_add_f32 v237, v237, v158\n\t" \
    "v_add_f32 v237, v237, v159\n\t" \
    "v_cvt_pk_bf16_f32 v167, v158, v159\n\t" \
    "v_cmp_lt_f32 vcc, %[thr], v237\n\t" \
    "s_cbranch_vccnz RAREC_%=\n\t" \
    "BACKC_%=:\n\t" \
    "s_waitcnt lgkmcnt(0)\n\t" \
    "v_mfma_f32_32x32x16_bf16 v[0:15], v[200:203], v[160:163], v[0:15]\n\t" \
    "ds_read_b128 v[168:171], v232 offset:512\n\t" \
    "ds_read_b128 v[172:175], v232 offset:2560\n\t" \
    "v_add_f32 v236, v236, v237\n\t" \
    "v_mfma_f32_32x32x16_bf16 v[16:31], v[204:207], v[160:163], v[16:31]\n\t" \
    "ds_read_b128 v[176:179], v232 offset:4608\n\t" \
    "ds_read_b128 v[180:183], v232 offset:6656\n\t" \
    "v_exp_f32 v144, v112\n\t" \
    "v_exp_f32 v145, v113\n\t" \
    "v_mfma_f32_32x32x16_bf16 v[32:47], v[208:211], v[160:163], v[32:47]\n\t" \
    "ds_read_b128 v[184:187], v232 offset:8704\n\t" \
    "ds_read_b128 v[188:191], v232 offset:10752\n\t" \
    "v_exp_f32 v146, v114\n\t" \
    "v_exp_f32 v147, v115\n\t" \
    "v_mfma_f32_32x32x16_bf16 v[48:63], v[212:215], v[160:163], v[48:63]\n\t" \
    "ds_read_b128 v[192:195], v232 offset:12800\n\t" \
    "ds_read_b128 v[196:199], v232 offset:14848\n\t" \
    "v_exp_f32 v148, v116\n\t" \
    "v_exp_f32 v149, v117\n\t" \
    "v_mfma_f32_32x32x16_bf16 v[0:15], v[216:219], v[164:167], v[0:15]\n\t" \
    "v_exp_f32 v150, v118\n\t" \
    "v_exp_f32 v151, v119\n\t" \
    "v_exp_f32 v152, v120\n\t" \
    "v_mfma_f32_32x32x16_bf16 v[16:31], v[220:223], v[164:167], v[16:31]\n\t" \
    "v_exp_f32 v153, v121\n\t" \
    "v_exp_f32 v154, v122\n\t" \
    "v_exp_f32 v155, v123\n\t" \
    "v_mfma_f32_32x32x16_bf16 v[32:47], v[224:227], v[164:167], v[32:47]\n\t" \
    "v_exp_f32 v156, v124\n\t" \
    "v_exp_f32 v157, v125\n\t" \
    "v_mfma_f32_32x32x16_bf16 v[48:63], v[228:231], v[164:167], v[48:63]\n\t" \
    "v_exp_f32 v158, v126\n\t" \
    "v_exp_f32 v159, v127\n\t" \
    "v_mov_b32 v239, 0xff800000\n\t" \
    "v_cmp_gt_i32 vcc, 0, %[dq]\n\t" \
    "s_nop 1\n\t" \
    "v_cndmask_b32 v96, v96, v239, vcc\n\t" \
    "v_cmp_gt_i32 vcc, 1, %[dq]\n\t" \
    "s_nop 1\n\t" \
    "v_cndmask_b32 v97, v97, v239, vcc\n\t" \
    "v_cmp_gt_i32 vcc, 2, %[dq]\n\t" \
    "s_nop 1\n\t" \
    "v_cndmask_b32 v98, v98, v239, vcc\n\t" \
    "v_cmp_gt_i32 vcc, 3, %[dq]\n\t" \
    "s_nop 1\n\t" \
    "v_cndmask_b32 v99, v99, v239, vcc\n\t" \
    "v_cmp_gt_i32 vcc, 8, %[dq]\n\t" \
    "s_nop 1\n\t" \
    "v_cndmask_b32 v100, v100, v239, vcc\n\t" \
    "v_cmp_gt_i32 vcc, 9, %[dq]\n\t" \
    "s_nop 1\n\t" \
    "v_cndmask_b32 v101, v101, v239, vcc\n\t" \
    "v_cmp_gt_i32 vcc, 10, %[dq]\n\t" \
    "s_nop 1\n\t" \
    "v_cndmask_b32 v102, v102, v239, vcc\n\t" \
    "v_cmp_gt_i32 vcc, 11, %[dq]\n\t" \
    "s_nop 1\n\t" \
    "v_cndmask_b32 v103, v103, v239, vcc\n\t" \
    "v_cmp_gt_i32 vcc, 16, %[dq]\n\t" \
    "s_nop 1\n\t" \
    "v_cndmask_b32 v104, v104, v239, vcc\n\t" \
    "v_cmp_gt_i32 vcc, 17, %[dq]\n\t" \
    "s_nop 1\n\t" \
    "v_cndmask_b32 v105, v105, v239, vcc\n\t" \
    "v_cmp_gt_i32 vcc, 18, %[dq]\n\t" \
    "s_nop 1\n\t" \
    "v_cndmask_b32 v106, v106, v239, vcc\n\t" \
    "v_cmp_gt_i32 vcc, 19, %[dq]\n\t" \
    "s_nop 1\n\t" \
    "v_cndmask_b32 v107, v107, v239, vcc\n\t" \
    "v_cmp_gt_i32 vcc, 24, %[dq]\n\t" \
    "s_nop 1\n\t" \
    "v_cndmask_b32 v108, v108, v239, vcc\n\t" \
    "v_cmp_gt_i32 vcc, 25, %[dq]\n\t" \
    "s_nop 1\n\t" \
    "v_cndmask_b32 v109, v109, v239, vcc\n\t" \
    "v_cmp_gt_i32 vcc, 26, %[dq]\n\t" \
    "s_nop 1\n\t" \
    "v_cndmask_b32 v110, v110, v239, vcc\n\t" \
    "v_cmp_gt_i32 vcc, 27, %[dq]\n\t" \
    "s_nop 1\n\t" \
    "v_cndmask_b32 v111, v111, v239, vcc\n\t" \
    "s_nop 1\n\t" \
    "s_waitcnt lgkmcnt(0)\n\t" \
    "v_mfma_f32_32x32x16_bf16 v[128:143], v[168:171], v[64:67], v[96:111]\n\t" \
    "ds_read_b128 v[200:203], v234 offset:0\n\t" \
    "ds_read_b128 v[204:207], v234 offset:512\n\t" \
    "v_add_f32 v237, v144, v145\n\t" \
    "v_cvt_pk_bf16_f32 v160, v144, v145\n\t" \
    "v_mfma_f32_32x32x16_bf16 v[128:143], v[172:175], v[68:71], v[128:143]\n\t" \
    "ds_read_b128 v[208:211], v234 offset:1024\n\t" \
    "ds_read_b128 v[212:215], v234 offset:1536\n\t" \
    "v_add_f32 v237, v237, v146\n\t" \
    "v_add_f32 v237, v237, v147\n\t" \
    "v_cvt_pk_bf16_f32 v161, v146, v147\n\t" \
    "v_mfma_f32_32x32x16_bf16 v[128:143], v[176:179], v[72:75], v[128:143]\n\t" \
    "ds_read_b128 v[216:219], v234 offset:4096\n\t" \
    "ds_read_b128 v[220:223], v234 offset:4608\n\t" \
    "v_add_f32 v237, v237, v148\n\t" \
    "v_add_f32 v237, v237, v149\n\t" \
    "v_cvt_pk_bf16_f32 v162, v148, v149\n\t" \
    "v_mfma_f32_32x32x16_bf16 v[128:143], v[180:183], v[76:79], v[128:143]\n\t" \
    "ds_read_b128 v[224:227], v234 offset:5120\n\t" \
    "ds_read_b128 v[228:231], v234 offset:5632\n\t" \
    "v_add_f32 v237, v237, v150\n\t" \
    "v_add_f32 v237, v237, v151\n\t" \
    "v_cvt_pk_bf16_f32 v163, v150, v151\n\t" \
    "v_mfma_f32_32x32x16_bf16 v[128:143], v[184:187], v[80:83], v[128:143]\n\t" \
    "v_add_f32 v237, v237, v152\n\t" \
    "v_add_f32 v237, v237, v153\n\t" \
    "v_cvt_pk_bf16_f32 v164, v152, v153\n\t" \
    "v_mfma_f32_32x32x16_bf16 v[128:143], v[188:191], v[84:87], v[128:143]\n\t" \
    "v_add_f32 v237, v237, v154\n\t" \
    "v_add_f32 v237, v237, v155\n\t" \
    "v_cvt_pk_bf16_f32 v165, v154, v155\n\t" \
    "v_mfma_f32_32x32x16_bf16 v[128:143], v[192:195], v[88:91], v[128:143]\n\t" \
    "v_add_f32 v237, v237, v156\n\t" \
    "v_add_f32 v237, v237, v157\n\t" \
    "v_cvt_pk_bf16_f32 v166, v156, v157\n\t" \
    "v_mfma_f32_32x32x16_bf16 v[128:143], v[196:199], v[92:95], v[128:143]\n\t" \
    "v_add_f32 v237, v237, v158\n\t" \
    "v_add_f32 v237, v237, v159\n\t" \
    "v_cvt_pk_bf16_f32 v167, v158, v159\n\t" \
    "v_cmp_lt_f32 vcc, %[thr], v237\n\t" \
    "s_cbranch_vccnz RARED_%=\n\t" \
    "BACKD_%=:\n\t" \
    "s_waitcnt lgkmcnt(0)\n\t" \
    "v_mfma_f32_32x32x16_bf16 v[0:15], v[200:203], v[160:163], v[0:15]\n\t" \
    "ds_read_b128 v[168:171], v233 offset:0\n\t" \
    "ds_read_b128 v[172:175], v233 offset:2048\n\t" \
    "v_add_f32 v236, v236, v237\n\t" \
    "v_mfma_f32_32x32x16_bf16 v[16:31], v[204:207], v[160:163], v[16:31]\n\t" \
    "ds_read_b128 v[176:179], v233 offset:4096\n\t" \
    "ds_read_b128 v[180:183], v233 offset:6144\n\t" \
    "v_exp_f32 v144, v128\n\t" \
    "v_exp_f32 v145, v129\n\t" \
    "v_mfma_f32_32x32x16_bf16 v[32:47], v[208:211], v[160:163], v[32:47]\n\t" \
    "ds_read_b128 v[184:187], v233 offset:8192\n\t" \
    "ds_read_b128 v[188:191], v233 offset:10240\n\t" \
    "v_exp_f32 v146, v130\n\t" \
    "v_exp_f32 v147, v131\n\t" \
    "v_mfma_f32_32x32x16_bf16 v[48:63], v[212:215], v[160:163], v[48:63]\n\t" \
    "ds_read_b128 v[192:195], v233 offset:12288\n\t" \
    "ds_read_b128 v[196:199], v233 offset:14336\n\t" \
    "v_exp_f32 v148, v132\n\t" \
    "v_exp_f32 v149, v133\n\t" \
    "v_mfma_f32_32x32x16_bf16 v[0:15], v[216:219], v[164:167], v[0:15]\n\t" \
    "v_exp_f32 v150, v134\n\t" \
    "v_exp_f32 v151, v135\n\t" \
    "v_exp_f32 v152, v136\n\t" \
    "v_mfma_f32_32x32x16_bf16 v[16:31], v[220:223], v[164:167], v[16:31]\n\t" \
    "v_exp_f32 v153, v137\n\t" \
    "v_exp_f32 v154, v138\n\t" \
    "v_exp_f32 v155, v139\n\t" \
    "v_mfma_f32_32x32x16_bf16 v[32:47], v[224:227], v[164:167], v[32:47]\n\t" \
    "v_exp_f32 v156, v140\n\t" \
    "v_exp_f32 v157, v141\n\t" \
    "v_mfma_f32_32x32x16_bf16 v[48:63], v[228:231], v[164:167], v[48:63]\n\t" \
    "v_exp_f32 v158, v142\n\t" \
    "v_exp_f32 v159, v143\n\t" \
    "s_waitcnt lgkmcnt(0)\n\t" \
    "ds_read_b128 v[200:203], v234 offset:8192\n\t" \
    "ds_read_b128 v[204:207], v234 offset:8704\n\t" \
    "ds_read_b128 v[208:211], v234 offset:9216\n\t" \
    "ds_read_b128 v[212:215], v234 offset:9728\n\t" \
    "ds_read_b128 v[216:219], v234 offset:12288\n\t" \
    "ds_read_b128 v[220:223], v234 offset:12800\n\t" \
    "ds_read_b128 v[224:227], v234 offset:13312\n\t" \
    "ds_read_b128 v[228:231], v234 offset:13824\n\t" \
    "v_add_f32 v237, v144, v145\n\t" \
    "v_cvt_pk_bf16_f32 v160, v144, v145\n\t" \
    "v_add_f32 v237, v237, v146\n\t" \
    "v_add_f32 v237, v237, v147\n\t" \
    "v_cvt_pk_bf16_f32 v161, v146, v147\n\t" \
    "v_add_f32 v237, v237, v148\n\t" \
    "v_add_f32 v237, v237, v149\n\t" \
    "v_cvt_pk_bf16_f32 v162, v148, v149\n\t" \
    "v_add_f32 v237, v237, v150\n\t" \
    "v_add_f32 v237, v237, v151\n\t" \
    "v_cvt_pk_bf16_f32 v163, v150, v151\n\t" \
    "v_add_f32 v237, v237, v152\n\t" \
    "v_add_f32 v237, v237, v153\n\t" \
    "v_cvt_pk_bf16_f32 v164, v152, v153\n\t" \
    "v_add_f32 v237, v237, v154\n\t" \
    "v_add_f32 v237, v237, v155\n\t" \
    "v_cvt_pk_bf16_f32 v165, v154, v155\n\t" \
    "v_add_f32 v237, v237, v156\n\t" \
    "v_add_f32 v237, v237, v157\n\t" \
    "v_cvt_pk_bf16_f32 v166, v156, v157\n\t" \
    "v_add_f32 v237, v237, v158\n\t" \
    "v_add_f32 v237, v237, v159\n\t" \
    "v_cvt_pk_bf16_f32 v167, v158, v159\n\t" \
    "v_cmp_lt_f32 vcc, %[thr], v237\n\t" \
    "s_cbranch_vccnz RAREE_%=\n\t" \
    "BACKE_%=:\n\t" \
    "s_waitcnt lgkmcnt(0)\n\t" \
    "v_add_f32 v236, v236, v237\n\t" \
    "v_mfma_f32_32x32x16_bf16 v[0:15], v[200:203], v[160:163], v[0:15]\n\t" \
    "v_mfma_f32_32x32x16_bf16 v[16:31], v[204:207], v[160:163], v[16:31]\n\t" \
    "v_mfma_f32_32x32x16_bf16 v[32:47], v[208:211], v[160:163], v[32:47]\n\t" \
    "v_mfma_f32_32x32x16_bf16 v[48:63], v[212:215], v[160:163], v[48:63]\n\t" \
    "v_mfma_f32_32x32x16_bf16 v[0:15], v[216:219], v[164:167], v[0:15]\n\t" \
    "v_mfma_f32_32x32x16_bf16 v[16:31], v[220:223], v[164:167], v[16:31]\n\t" \
    "v_mfma_f32_32x32x16_bf16 v[32:47], v[224:227], v[164:167], v[32:47]\n\t" \
    "v_mfma_f32_32x32x16_bf16 v[48:63], v[228:231], v[164:167], v[48:63]\n\t" \
    "s_branch END_%=\n\t" \
    "EVEN_%=:\n\t" \
    "v_mov_b32 v239, 0xff800000\n\t" \
    "v_cmp_gt_i32 vcc, 0, %[dq]\n\t" \
    "s_nop 1\n\t" \
    "v_cndmask_b32 v96, v96, v239, vcc\n\t" \
    "v_cmp_gt_i32 vcc, 1, %[dq]\n\t" \
    "s_nop 1\n\t" \
    "v_cndmask_b32 v97, v97, v239, vcc\n\t" \
    "v_cmp_gt_i32 vcc, 2, %[dq]\n\t" \
    "s_nop 1\n\t" \
    "v_cndmask_b32 v98, v98, v239, vcc\n\t" \
    "v_cmp_gt_i32 vcc, 3, %[dq]\n\t" \
    "s_nop 1\n\t" \
    "v_cndmask_b32 v99, v99, v239, vcc\n\t" \
    "v_cmp_gt_i32 vcc, 8, %[dq]\n\t" \
    "s_nop 1\n\t" \
    "v_cndmask_b32 v100, v100, v239, vcc\n\t" \
    "v_cmp_gt_i32 vcc, 9, %[dq]\n\t" \
    "s_nop 1\n\t" \
    "v_cndmask_b32 v101, v101, v239, vcc\n\t" \
    "v_cmp_gt_i32 vcc, 10, %[dq]\n\t" \
    "s_nop 1\n\t" \
    "v_cndmask_b32 v102, v102, v239, vcc\n\t" \
    "v_cmp_gt_i32 vcc, 11, %[dq]\n\t" \
    "s_nop 1\n\t" \
    "v_cndmask_b32 v103, v103, v239, vcc\n\t" \
    "v_cmp_gt_i32 vcc, 16, %[dq]\n\t" \
    "s_nop 1\n\t" \
    "v_cndmask_b32 v104, v104, v239, vcc\n\t" \
    "v_cmp_gt_i32 vcc, 17, %[dq]\n\t" \
    "s_nop 1\n\t" \
    "v_cndmask_b32 v105, v105, v239, vcc\n\t" \
    "v_cmp_gt_i32 vcc, 18, %[dq]\n\t" \
    "s_nop 1\n\t" \
    "v_cndmask_b32 v106, v106, v239, vcc\n\t" \
    "v_cmp_gt_i32 vcc, 19, %[dq]\n\t" \
    "s_nop 1\n\t" \
    "v_cndmask_b32 v107, v107, v239, vcc\n\t" \
    "v_cmp_gt_i32 vcc, 24, %[dq]\n\t" \
    "s_nop 1\n\t" \
    "v_cndmask_b32 v108, v108, v239, vcc\n\t" \
    "v_cmp_gt_i32 vcc, 25, %[dq]\n\t" \
    "s_nop 1\n\t" \
    "v_cndmask_b32 v109, v109, v239, vcc\n\t" \
    "v_cmp_gt_i32 vcc, 26, %[dq]\n\t" \
    "s_nop 1\n\t" \
    "v_cndmask_b32 v110, v110, v239, vcc\n\t" \
    "v_cmp_gt_i32 vcc, 27, %[dq]\n\t" \
    "s_nop 1\n\t" \
    "v_cndmask_b32 v111, v111, v239, vcc\n\t" \
    "s_nop 1\n\t" \
    "s_waitcnt lgkmcnt(0)\n\t" \
    "v_mfma_f32_32x32x16_bf16 v[112:127], v[168:171], v[64:67], v[96:111]\n\t" \
    "ds_read_b128 v[200:203], v235 offset:8192\n\t" \
    "ds_read_b128 v[204:207], v235 offset:8704\n\t" \
    "v_add_f32 v237, v144, v145\n\t" \
    "v_cvt_pk_bf16_f32 v160, v144, v145\n\t" \
    "v_mfma_f32_32x32x16_bf16 v[112:127], v[172:175], v[68:71], v[112:127]\n\t" \
    "ds_read_b128 v[208:211], v235 offset:9216\n\t" \
    "ds_read_b128 v[212:215], v235 offset:9728\n\t" \
    "v_add_f32 v237, v237, v146\n\t" \
    "v_add_f32 v237, v237, v147\n\t" \
    "v_cvt_pk_bf16_f32 v161, v146, v147\n\t" \
    "v_mfma_f32_32x32x16_bf16 v[112:127], v[176:179], v[72:75], v[112:127]\n\t" \
    "ds_read_b128 v[216:219], v235 offset:12288\n\t" \
    "ds_read_b128 v[220:223], v235 offset:12800\n\t" \
    "v_add_f32 v237, v237, v148\n\t" \
    "v_add_f32 v237, v237, v149\n\t" \
    "v_cvt_pk_bf16_f32 v162, v148, v149\n\t" \
    "v_mfma_f32_32x32x16_bf16 v[112:127], v[180:183], v[76:79], v[112:127]\n\t" \
    "ds_read_b128 v[224:227], v235 offset:13312\n\t" \
    "ds_read_b128 v[228:231], v235 offset:13824\n\t" \
    "v_add_f32 v237, v237, v150\n\t" \
    "v_add_f32 v237, v237, v151\n\t" \
    "v_cvt_pk_bf16_f32 v163, v150, v151\n\t" \
    "v_mfma_f32_32x32x16_bf16 v[112:127], v[184:187], v[80:83], v[112:127]\n\t" \
    "v_add_f32 v237, v237, v152\n\t" \
    "v_add_f32 v237, v237, v153\n\t" \
    "v_cvt_pk_bf16_f32 v164, v152, v153\n\t" \
    "v_mfma_f32_32x32x16_bf16 v[112:127], v[188:191], v[84:87], v[112:127]\n\t" \
    "v_add_f32 v237, v237, v154\n\t" \
    "v_add_f32 v237, v237, v155\n\t" \
    "v_cvt_pk_bf16_f32 v165, v154, v155\n\t" \
    "v_mfma_f32_32x32x16_bf16 v[112:127], v[192:195], v[88:91], v[112:127]\n\t" \
    "v_add_f32 v237, v237, v156\n\t" \
    "v_add_f32 v237, v237, v157\n\t" \
    "v_cvt_pk_bf16_f32 v166, v156, v157\n\t" \
    "v_mfma_f32_32x32x16_bf16 v[112:127], v[196:199], v[92:95], v[112:127]\n\t" \
    "v_add_f32 v237, v237, v158\n\t" \
    "v_add_f32 v237, v237, v159\n\t" \
    "v_cvt_pk_bf16_f32 v167, v158, v159\n\t" \
    "v_cmp_lt_f32 vcc, %[thr], v237\n\t" \
    "s_cbranch_vccnz RAREF_%=\n\t" \
    "BACKF_%=:\n\t" \
    "s_waitcnt lgkmcnt(0)\n\t" \
    "v_mfma_f32_32x32x16_bf16 v[0:15], v[200:203], v[160:163], v[0:15]\n\t" \
    "ds_read_b128 v[168:171], v232 offset:512\n\t" \
    "ds_read_b128 v[172:175], v232 offset:2560\n\t" \
    "v_add_f32 v236, v236, v237\n\t" \
    "v_mfma_f32_32x32x16_bf16 v[16:31], v[204:207], v[160:163], v[16:31]\n\t" \
    "ds_read_b128 v[176:179], v232 offset:4608\n\t" \
    "ds_read_b128 v[180:183], v232 offset:6656\n\t" \
    "v_exp_f32 v144, v112\n\t" \
    "v_exp_f32 v145, v113\n\t" \
    "v_mfma_f32_32x32x16_bf16 v[32:47], v[208:211], v[160:163], v[32:47]\n\t" \
    "ds_read_b128 v[184:187], v232 offset:8704\n\t" \
    "ds_read_b128 v[188:191], v232 offset:10752\n\t" \
    "v_exp_f32 v146, v114\n\t" \
    "v_exp_f32 v147, v115\n\t" \
    "v_mfma_f32_32x32x16_bf16 v[48:63], v[212:215], v[160:163], v[48:63]\n\t" \
    "ds_read_b128 v[192:195], v232 offset:12800\n\t" \
    "ds_read_b128 v[196:199], v232 offset:14848\n\t" \
    "v_exp_f32 v148, v116\n\t" \
    "v_exp_f32 v149, v117\n\t" \
    "v_mfma_f32_32x32x16_bf16 v[0:15], v[216:219], v[164:167], v[0:15]\n\t" \
    "v_exp_f32 v150, v118\n\t" \
    "v_exp_f32 v151, v119\n\t" \
    "v_exp_f32 v152, v120\n\t" \
    "v_mfma_f32_32x32x16_bf16 v[16:31], v[220:223], v[164:167], v[16:31]\n\t" \
    "v_exp_f32 v153, v121\n\t" \
    "v_exp_f32 v154, v122\n\t" \
    "v_exp_f32 v155, v123\n\t" \
    "v_mfma_f32_32x32x16_bf16 v[32:47], v[224:227], v[164:167], v[32:47]\n\t" \
    "v_exp_f32 v156, v124\n\t" \
    "v_exp_f32 v157, v125\n\t" \
    "v_mfma_f32_32x32x16_bf16 v[48:63], v[228:231], v[164:167], v[48:63]\n\t" \
    "v_exp_f32 v158, v126\n\t" \
    "v_exp_f32 v159, v127\n\t" \
    "s_waitcnt lgkmcnt(0)\n\t" \
    "ds_read_b128 v[200:203], v234 offset:0\n\t" \
    "ds_read_b128 v[204:207], v234 offset:512\n\t" \
    "ds_read_b128 v[208:211], v234 offset:1024\n\t" \
    "ds_read_b128 v[212:215], v234 offset:1536\n\t" \
    "ds_read_b128 v[216:219], v234 offset:4096\n\t" \
    "ds_read_b128 v[220:223], v234 offset:4608\n\t" \
    "ds_read_b128 v[224:227], v234 offset:5120\n\t" \
    "ds_read_b128 v[228:231], v234 offset:5632\n\t" \
    "v_add_f32 v237, v144, v145\n\t" \
    "v_cvt_pk_bf16_f32 v160, v144, v145\n\t" \
    "v_add_f32 v237, v237, v146\n\t" \
    "v_add_f32 v237, v237, v147\n\t" \
    "v_cvt_pk_bf16_f32 v161, v146, v147\n\t" \
    "v_add_f32 v237, v237, v148\n\t" \
    "v_add_f32 v237, v237, v149\n\t" \
    "v_cvt_pk_bf16_f32 v162, v148, v149\n\t" \
    "v_add_f32 v237, v237, v150\n\t" \
    "v_add_f32 v237, v237, v151\n\t" \
    "v_cvt_pk_bf16_f32 v163, v150, v151\n\t" \
    "v_add_f32 v237, v237, v152\n\t" \
    "v_add_f32 v237, v237, v153\n\t" \
    "v_cvt_pk_bf16_f32 v164, v152, v153\n\t" \
    "v_add_f32 v237, v237, v154\n\t" \
    "v_add_f32 v237, v237, v155\n\t" \
    "v_cvt_pk_bf16_f32 v165, v154, v155\n\t" \
    "v_add_f32 v237, v237, v156\n\t" \
    "v_add_f32 v237, v237, v157\n\t" \
    "v_cvt_pk_bf16_f32 v166, v156, v157\n\t" \
    "v_add_f32 v237, v237, v158\n\t" \
    "v_add_f32 v237, v237, v159\n\t" \
    "v_cvt_pk_bf16_f32 v167, v158, v159\n\t" \
    "v_cmp_lt_f32 vcc, %[thr], v237\n\t" \
    "s_cbranch_vccnz RAREG_%=\n\t" \
    "BACKG_%=:\n\t" \
    "s_waitcnt lgkmcnt(0)\n\t" \
    "v_add_f32 v236, v236, v237\n\t" \
    "v_mfma_f32_32x32x16_bf16 v[0:15], v[200:203], v[160:163], v[0:15]\n\t" \
    "v_mfma_f32_32x32x16_bf16 v[16:31], v[204:207], v[160:163], v[16:31]\n\t" \
    "v_mfma_f32_32x32x16_bf16 v[32:47], v[208:211], v[160:163], v[32:47]\n\t" \
    "v_mfma_f32_32x32x16_bf16 v[48:63], v[212:215], v[160:163], v[48:63]\n\t" \
    "v_mfma_f32_32x32x16_bf16 v[0:15], v[216:219], v[164:167], v[0:15]\n\t" \
    "v_mfma_f32_32x32x16_bf16 v[16:31], v[220:223], v[164:167], v[16:31]\n\t" \
    "v_mfma_f32_32x32x16_bf16 v[32:47], v[224:227], v[164:167], v[32:47]\n\t" \
    "v_mfma_f32_32x32x16_bf16 v[48:63], v[228:231], v[164:167], v[48:63]\n\t" \
    "s_branch END_%=\n\t" \
    "DMAL_%=:\n\t" \
    "s_add_u32 s60, %[t], 3\n\t" \
    "s_min_u32 s60, s60, %[ntm1]\n\t" \
    "s_lshl_b32 s60, s60, 14\n\t" \
    "s_add_u32 s62, %[khl], s60\n\t" \
    "s_addc_u32 s63, %[khh], 0\n\t" \
    "s_add_u32 s61, %[soff], 0xc000\n\t" \
    "s_and_b32 s61, s61, 0xffff\n\t" \
    "s_add_u32 m0, s61, %[kdst]\n\t" \
    "s_nop 0\n\t" \
    "global_load_lds_dwordx4 %[dvo], s[62:63] offset:0\n\t" \
    "global_load_lds_dwordx4 %[dvo], s[62:63] offset:1024\n\t" \
    "global_load_lds_dwordx4 %[dvo], s[62:63] offset:2048\n\t" \
    "global_load_lds_dwordx4 %[dvo], s[62:63] offset:3072\n\t" \
    "s_add_u32 s60, %[t], 2\n\t" \
    "s_min_u32 s60, s60, %[ntm1]\n\t" \
    "s_lshl_b32 s60, s60, 14\n\t" \
    "s_add_u32 s62, %[vhl], s60\n\t" \
    "s_addc_u32 s63, %[vhh], 0\n\t" \
    "s_add_u32 s61, %[soff], 0x8000\n\t" \
    "s_and_b32 s61, s61, 0xffff\n\t" \
    "s_add_u32 m0, s61, %[vdst]\n\t" \
    "s_nop 0\n\t" \
    "global_load_lds_dwordx4 %[dvo], s[62:63] offset:0\n\t" \
    "global_load_lds_dwordx4 %[dvo], s[62:63] offset:1024\n\t" \
    "global_load_lds_dwordx4 %[dvo], s[62:63] offset:2048\n\t" \
    "global_load_lds_dwordx4 %[dvo], s[62:63] offset:3072\n\t" \
    "s_branch DMABL_%=\n\t" \
    "DMAT_%=:\n\t" \
    "s_add_u32 s60, %[t], 3\n\t" \
    "s_min_u32 s60, s60, %[ntm1]\n\t" \
    "s_lshl_b32 s60, s60, 14\n\t" \
    "s_add_u32 s62, %[khl], s60\n\t" \
    "s_addc_u32 s63, %[khh], 0\n\t" \
    "s_add_u32 s61, %[soff], 0xc000\n\t" \
    "s_and_b32 s61, s61, 0xffff\n\t" \
    "s_add_u32 m0, s61, %[kdst]\n\t" \
    "s_nop 0\n\t" \
    "global_load_lds_dwordx4 %[dvo], s[62:63] offset:0\n\t" \
    "global_load_lds_dwordx4 %[dvo], s[62:63] offset:1024\n\t" \
    "global_load_lds_dwordx4 %[dvo], s[62:63] offset:2048\n\t" \
    "global_load_lds_dwordx4 %[dvo], s[62:63] offset:3072\n\t" \
    "s_add_u32 s60, %[t], 2\n\t" \
    "s_min_u32 s60, s60, %[ntm1]\n\t" \
    "s_lshl_b32 s60, s60, 14\n\t" \
    "s_add_u32 s62, %[vhl], s60\n\t" \
    "s_addc_u32 s63, %[vhh], 0\n\t" \
    "s_add_u32 s61, %[soff], 0x8000\n\t" \
    "s_and_b32 s61, s61, 0xffff\n\t" \
    "s_add_u32 m0, s61, %[vdst]\n\t" \
    "s_nop 0\n\t" \
    "global_load_lds_dwordx4 %[dvo], s[62:63] offset:0\n\t" \
    "global_load_lds_dwordx4 %[dvo], s[62:63] offset:1024\n\t" \
    "global_load_lds_dwordx4 %[dvo], s[62:63] offset:2048\n\t" \
    "global_load_lds_dwordx4 %[dvo], s[62:63] offset:3072\n\t" \
    "s_branch DMABT_%=\n\t" \
    "RAREA_%=:\n\t" \
    "s_nop 15\n\t" \
    "v_max3_f32 v239, v128, v129, v130\n\t" \
    "v_max3_f32 v239, v239, v131, v132\n\t" \
    "v_max3_f32 v239, v239, v133, v134\n\t" \
    "v_max3_f32 v239, v239, v135, v136\n\t" \
    "v_max3_f32 v239, v239, v137, v138\n\t" \
    "v_max3_f32 v239, v239, v139, v140\n\t" \
    "v_max3_f32 v239, v239, v141, v142\n\t" \
    "v_max_f32 v239, v239, v143\n\t" \
    "v_max_f32 v239, 0, v239\n\t" \
    "v_mov_b32 v240, v239\n\t" \
    "s_nop 1\n\t" \
    "v_permlane32_swap_b32 v239, v240\n\t" \
    "s_nop 1\n\t" \
    "v_max_f32 v239, v239, v240\n\t" \
    "v_add_f32 v238, v238, v239\n\t" \
    "v_exp_f32 v240, -v239\n\t" \
    "v_sub_f32 v241, 0, v238\n\t" \
    "v_sub_f32 v128, v128, v239\n\t" \
    "v_sub_f32 v112, v112, v239\n\t" \
    "v_mov_b32 v96, v241\n\t" \
    "v_sub_f32 v129, v129, v239\n\t" \
    "v_sub_f32 v113, v113, v239\n\t" \
    "v_mov_b32 v97, v241\n\t" \
    "v_sub_f32 v130, v130, v239\n\t" \
    "v_sub_f32 v114, v114, v239\n\t" \
    "v_mov_b32 v98, v241\n\t" \
    "v_sub_f32 v131, v131, v239\n\t" \
    "v_sub_f32 v115, v115, v239\n\t" \
    "v_mov_b32 v99, v241\n\t" \
    "v_sub_f32 v132, v132, v239\n\t" \
    "v_sub_f32 v116, v116, v239\n\t" \
    "v_mov_b32 v100, v241\n\t" \
    "v_sub_f32 v133, v133, v239\n\t" \
    "v_sub_f32 v117, v117, v239\n\t" \
    "v_mov_b32 v101, v241\n\t" \
    "v_sub_f32 v134, v134, v239\n\t" \
    "v_sub_f32 v118, v118, v239\n\t" \
    "v_mov_b32 v102, v241\n\t" \
    "v_sub_f32 v135, v135, v239\n\t" \
    "v_sub_f32 v119, v119, v239\n\t" \
    "v_mov_b32 v103, v241\n\t" \
    "v_sub_f32 v136, v136, v239\n\t" \
    "v_sub_f32 v120, v120, v239\n\t" \
    "v_mov_b32 v104, v241\n\t" \
    "v_sub_f32 v137, v137, v239\n\t" \
    "v_sub_f32 v121, v121, v239\n\t" \
    "v_mov_b32 v105, v241\n\t" \
    "v_sub_f32 v138, v138, v239\n\t" \
    "v_sub_f32 v122, v122, v239\n\t" \
    "v_mov_b32 v106, v241\n\t" \
    "v_sub_f32 v139, v139, v239\n\t" \
    "v_sub_f32 v123, v123, v239\n\t" \
    "v_mov_b32 v107, v241\n\t" \
    "v_sub_f32 v140, v140, v239\n\t" \
    "v_sub_f32 v124, v124, v239\n\t" \
    "v_mov_b32 v108, v241\n\t" \
    "v_sub_f32 v141, v141, v239\n\t" \
    "v_sub_f32 v125, v125, v239\n\t" \
    "v_mov_b32 v109, v241\n\t" \
    "v_sub_f32 v142, v142, v239\n\t" \
    "v_sub_f32 v126, v126, v239\n\t" \
    "v_mov_b32 v110, v241\n\t" \
    "v_sub_f32 v143, v143, v239\n\t" \
    "v_sub_f32 v127, v127, v239\n\t" \
    "v_mov_b32 v111, v241\n\t" \
    "v_exp_f32 v144, v128\n\t" \
    "v_exp_f32 v145, v129\n\t" \
    "v_exp_f32 v146, v130\n\t" \
    "v_exp_f32 v147, v131\n\t" \
    "v_exp_f32 v148, v132\n\t" \
    "v_exp_f32 v149, v133\n\t" \
    "v_exp_f32 v150, v134\n\t" \
    "v_exp_f32 v151, v135\n\t" \
    "v_exp_f32 v152, v136\n\t" \
    "v_exp_f32 v153, v137\n\t" \
    "v_exp_f32 v154, v138\n\t" \
    "v_exp_f32 v155, v139\n\t" \
    "v_exp_f32 v156, v140\n\t" \
    "v_exp_f32 v157, v141\n\t" \
    "v_exp_f32 v158, v142\n\t" \
    "v_exp_f32 v159, v143\n\t" \
    "v_mul_f32 v236, v236, v240\n\t" \
    "v_mul_f32 v0, v0, v240\n\t" \
    "v_mul_f32 v1, v1, v240\n\t" \
    "v_mul_f32 v2, v2, v240\n\t" \
    "v_mul_f32 v3, v3, v240\n\t" \
    "v_mul_f32 v4, v4, v240\n\t" \
    "v_mul_f32 v5, v5, v240\n\t" \
    "v_mul_f32 v6, v6, v240\n\t" \
    "v_mul_f32 v7, v7, v240\n\t" \
    "v_mul_f32 v8, v8, v240\n\t" \
    "v_mul_f32 v9, v9, v240\n\t" \
    "v_mul_f32 v10, v10, v240\n\t" \
    "v_mul_f32 v11, v11, v240\n\t" \
    "v_mul_f32 v12, v12, v240\n\t" \
    "v_mul_f32 v13, v13, v240\n\t" \
    "v_mul_f32 v14, v14, v240\n\t" \
    "v_mul_f32 v15, v15, v240\n\t" \
    "v_mul_f32 v16, v16, v240\n\t" \
    "v_mul_f32 v17, v17, v240\n\t" \
    "v_mul_f32 v18, v18, v240\n\t" \
    "v_mul_f32 v19, v19, v240\n\t" \
    "v_mul_f32 v20, v20, v240\n\t" \
    "v_mul_f32 v21, v21, v240\n\t" \
    "v_mul_f32 v22, v22, v240\n\t" \
    "v_mul_f32 v23, v23, v240\n\t" \
    "v_mul_f32 v24, v24, v240\n\t" \
    "v_mul_f32 v25, v25, v240\n\t" \
    "v_mul_f32 v26, v26, v240\n\t" \
    "v_mul_f32 v27, v27, v240\n\t" \
    "v_mul_f32 v28, v28, v240\n\t" \
    "v_mul_f32 v29, v29, v240\n\t" \
    "v_mul_f32 v30, v30, v240\n\t" \
    "v_mul_f32 v31, v31, v240\n\t" \
    "v_mul_f32 v32, v32, v240\n\t" \
    "v_mul_f32 v33, v33, v240\n\t" \
    "v_mul_f32 v34, v34, v240\n\t" \
    "v_mul_f32 v35, v35, v240\n\t" \
    "v_mul_f32 v36, v36, v240\n\t" \
    "v_mul_f32 v37, v37, v240\n\t" \
    "v_mul_f32 v38, v38, v240\n\t" \
    "v_mul_f32 v39, v39, v240\n\t" \
    "v_mul_f32 v40, v40, v240\n\t" \
    "v_mul_f32 v41, v41, v240\n\t" \
    "v_mul_f32 v42, v42, v240\n\t" \
    "v_mul_f32 v43, v43, v240\n\t" \
    "v_mul_f32 v44, v44, v240\n\t" \
    "v_mul_f32 v45, v45, v240\n\t" \
    "v_mul_f32 v46, v46, v240\n\t" \
    "v_mul_f32 v47, v47, v240\n\t" \
    "v_mul_f32 v48, v48, v240\n\t" \
    "v_mul_f32 v49, v49, v240\n\t" \
    "v_mul_f32 v50, v50, v240\n\t" \
    "v_mul_f32 v51, v51, v240\n\t" \
    "v_mul_f32 v52, v52, v240\n\t" \
    "v_mul_f32 v53, v53, v240\n\t" \
    "v_mul_f32 v54, v54, v240\n\t" \
    "v_mul_f32 v55, v55, v240\n\t" \
    "v_mul_f32 v56, v56, v240\n\t" \
    "v_mul_f32 v57, v57, v240\n\t" \
    "v_mul_f32 v58, v58, v240\n\t" \
    "v_mul_f32 v59, v59, v240\n\t" \
    "v_mul_f32 v60, v60, v240\n\t" \
    "v_mul_f32 v61, v61, v240\n\t" \
    "v_mul_f32 v62, v62, v240\n\t" \
    "v_mul_f32 v63, v63, v240\n\t" \
    "v_add_f32 v237, v144, v145\n\t" \
    "v_add_f32 v237, v237, v146\n\t" \
    "v_add_f32 v237, v237, v147\n\t" \
    "v_add_f32 v237, v237, v148\n\t" \
    "v_add_f32 v237, v237, v149\n\t" \
    "v_add_f32 v237, v237, v150\n\t" \
    "v_add_f32 v237, v237, v151\n\t" \
    "v_add_f32 v237, v237, v152\n\t" \
    "v_add_f32 v237, v237, v153\n\t" \
    "v_add_f32 v237, v237, v154\n\t" \
    "v_add_f32 v237, v237, v155\n\t" \
    "v_add_f32 v237, v237, v156\n\t" \
    "v_add_f32 v237, v237, v157\n\t" \
    "v_add_f32 v237, v237, v158\n\t" \
    "v_add_f32 v237, v237, v159\n\t" \
    "v_cvt_pk_bf16_f32 v160, v144, v145\n\t" \
    "v_cvt_pk_bf16_f32 v161, v146, v147\n\t" \
    "v_cvt_pk_bf16_f32 v162, v148, v149\n\t" \
    "v_cvt_pk_bf16_f32 v163, v150, v151\n\t" \
    "v_cvt_pk_bf16_f32 v164, v152, v153\n\t" \
    "v_cvt_pk_bf16_f32 v165, v154, v155\n\t" \
    "v_cvt_pk_bf16_f32 v166, v156, v157\n\t" \
    "v_cvt_pk_bf16_f32 v167, v158, v159\n\t" \
    "s_nop 1\n\t" \
    "s_branch BACKA_%=\n\t" \
    "RAREB_%=:\n\t" \
    "s_nop 15\n\t" \
    "v_max3_f32 v239, v112, v113, v114\n\t" \
    "v_max3_f32 v239, v239, v115, v116\n\t" \
    "v_max3_f32 v239, v239, v117, v118\n\t" \
    "v_max3_f32 v239, v239, v119, v120\n\t" \
    "v_max3_f32 v239, v239, v121, v122\n\t" \
    "v_max3_f32 v239, v239, v123, v124\n\t" \
    "v_max3_f32 v239, v239, v125, v126\n\t" \
    "v_max_f32 v239, v239, v127\n\t" \
    "v_max_f32 v239, 0, v239\n\t" \
    "v_mov_b32 v240, v239\n\t" \
    "s_nop 1\n\t" \
    "v_permlane32_swap_b32 v239, v240\n\t" \
    "s_nop 1\n\t" \
    "v_max_f32 v239, v239, v240\n\t" \
    "v_add_f32 v238, v238, v239\n\t" \
    "v_exp_f32 v240, -v239\n\t" \
    "v_sub_f32 v241, 0, v238\n\t" \
    "v_sub_f32 v112, v112, v239\n\t" \
    "v_sub_f32 v128, v128, v239\n\t" \
    "v_mov_b32 v96, v241\n\t" \
    "v_sub_f32 v113, v113, v239\n\t" \
    "v_sub_f32 v129, v129, v239\n\t" \
    "v_mov_b32 v97, v241\n\t" \
    "v_sub_f32 v114, v114, v239\n\t" \
    "v_sub_f32 v130, v130, v239\n\t" \
    "v_mov_b32 v98, v241\n\t" \
    "v_sub_f32 v115, v115, v239\n\t" \
    "v_sub_f32 v131, v131, v239\n\t" \
    "v_mov_b32 v99, v241\n\t" \
    "v_sub_f32 v116, v116, v239\n\t" \
    "v_sub_f32 v132, v132, v239\n\t" \
    "v_mov_b32 v100, v241\n\t" \
    "v_sub_f32 v117, v117, v239\n\t" \
    "v_sub_f32 v133, v133, v239\n\t" \
    "v_mov_b32 v101, v241\n\t" \
    "v_sub_f32 v118, v118, v239\n\t" \
    "v_sub_f32 v134, v134, v239\n\t" \
    "v_mov_b32 v102, v241\n\t" \
    "v_sub_f32 v119, v119, v239\n\t" \
    "v_sub_f32 v135, v135, v239\n\t" \
    "v_mov_b32 v103, v241\n\t" \
    "v_sub_f32 v120, v120, v239\n\t" \
    "v_sub_f32 v136, v136, v239\n\t" \
    "v_mov_b32 v104, v241\n\t" \
    "v_sub_f32 v121, v121, v239\n\t" \
    "v_sub_f32 v137, v137, v239\n\t" \
    "v_mov_b32 v105, v241\n\t" \
    "v_sub_f32 v122, v122, v239\n\t" \
    "v_sub_f32 v138, v138, v239\n\t" \
    "v_mov_b32 v106, v241\n\t" \
    "v_sub_f32 v123, v123, v239\n\t" \
    "v_sub_f32 v139, v139, v239\n\t" \
    "v_mov_b32 v107, v241\n\t" \
    "v_sub_f32 v124, v124, v239\n\t" \
    "v_sub_f32 v140, v140, v239\n\t" \
    "v_mov_b32 v108, v241\n\t" \
    "v_sub_f32 v125, v125, v239\n\t" \
    "v_sub_f32 v141, v141, v239\n\t" \
    "v_mov_b32 v109, v241\n\t" \
    "v_sub_f32 v126, v126, v239\n\t" \
    "v_sub_f32 v142, v142, v239\n\t" \
    "v_mov_b32 v110, v241\n\t" \
    "v_sub_f32 v127, v127, v239\n\t" \
    "v_sub_f32 v143, v143, v239\n\t" \
    "v_mov_b32 v111, v241\n\t" \
    "v_exp_f32 v144, v112\n\t" \
    "v_exp_f32 v145, v113\n\t" \
    "v_exp_f32 v146, v114\n\t" \
    "v_exp_f32 v147, v115\n\t" \
    "v_exp_f32 v148, v116\n\t" \
    "v_exp_f32 v149, v117\n\t" \
    "v_exp_f32 v150, v118\n\t" \
    "v_exp_f32 v151, v119\n\t" \
    "v_exp_f32 v152, v120\n\t" \
    "v_exp_f32 v153, v121\n\t" \
    "v_exp_f32 v154, v122\n\t" \
    "v_exp_f32 v155, v123\n\t" \
    "v_exp_f32 v156, v124\n\t" \
    "v_exp_f32 v157, v125\n\t" \
    "v_exp_f32 v158, v126\n\t" \
    "v_exp_f32 v159, v127\n\t" \
    "v_mul_f32 v236, v236, v240\n\t" \
    "v_mul_f32 v0, v0, v240\n\t" \
    "v_mul_f32 v1, v1, v240\n\t" \
    "v_mul_f32 v2, v2, v240\n\t" \
    "v_mul_f32 v3, v3, v240\n\t" \
    "v_mul_f32 v4, v4, v240\n\t" \
    "v_mul_f32 v5, v5, v240\n\t" \
    "v_mul_f32 v6, v6, v240\n\t" \
    "v_mul_f32 v7, v7, v240\n\t" \
    "v_mul_f32 v8, v8, v240\n\t" \
    "v_mul_f32 v9, v9, v240\n\t" \
    "v_mul_f32 v10, v10, v240\n\t" \
    "v_mul_f32 v11, v11, v240\n\t" \
    "v_mul_f32 v12, v12, v240\n\t" \
    "v_mul_f32 v13, v13, v240\n\t" \
    "v_mul_f32 v14, v14, v240\n\t" \
    "v_mul_f32 v15, v15, v240\n\t" \
    "v_mul_f32 v16, v16, v240\n\t" \
    "v_mul_f32 v17, v17, v240\n\t" \
    "v_mul_f32 v18, v18, v240\n\t" \
    "v_mul_f32 v19, v19, v240\n\t" \
    "v_mul_f32 v20, v20, v240\n\t" \
    "v_mul_f32 v21, v21, v240\n\t" \
    "v_mul_f32 v22, v22, v240\n\t" \
    "v_mul_f32 v23, v23, v240\n\t" \
    "v_mul_f32 v24, v24, v240\n\t" \
    "v_mul_f32 v25, v25, v240\n\t" \
    "v_mul_f32 v26, v26, v240\n\t" \
    "v_mul_f32 v27, v27, v240\n\t" \
    "v_mul_f32 v28, v28, v240\n\t" \
    "v_mul_f32 v29, v29, v240\n\t" \
    "v_mul_f32 v30, v30, v240\n\t" \
    "v_mul_f32 v31, v31, v240\n\t" \
    "v_mul_f32 v32, v32, v240\n\t" \
    "v_mul_f32 v33, v33, v240\n\t" \
    "v_mul_f32 v34, v34, v240\n\t" \
    "v_mul_f32 v35, v35, v240\n\t" \
    "v_mul_f32 v36, v36, v240\n\t" \
    "v_mul_f32 v37, v37, v240\n\t" \
    "v_mul_f32 v38, v38, v240\n\t" \
    "v_mul_f32 v39, v39, v240\n\t" \
    "v_mul_f32 v40, v40, v240\n\t" \
    "v_mul_f32 v41, v41, v240\n\t" \
    "v_mul_f32 v42, v42, v240\n\t" \
    "v_mul_f32 v43, v43, v240\n\t" \
    "v_mul_f32 v44, v44, v240\n\t" \
    "v_mul_f32 v45, v45, v240\n\t" \
    "v_mul_f32 v46, v46, v240\n\t" \
    "v_mul_f32 v47, v47, v240\n\t" \
    "v_mul_f32 v48, v48, v240\n\t" \
    "v_mul_f32 v49, v49, v240\n\t" \
    "v_mul_f32 v50, v50, v240\n\t" \
    "v_mul_f32 v51, v51, v240\n\t" \
    "v_mul_f32 v52, v52, v240\n\t" \
    "v_mul_f32 v53, v53, v240\n\t" \
    "v_mul_f32 v54, v54, v240\n\t" \
    "v_mul_f32 v55, v55, v240\n\t" \
    "v_mul_f32 v56, v56, v240\n\t" \
    "v_mul_f32 v57, v57, v240\n\t" \
    "v_mul_f32 v58, v58, v240\n\t" \
    "v_mul_f32 v59, v59, v240\n\t" \
    "v_mul_f32 v60, v60, v240\n\t" \
    "v_mul_f32 v61, v61, v240\n\t" \
    "v_mul_f32 v62, v62, v240\n\t" \
    "v_mul_f32 v63, v63, v240\n\t" \
    "v_add_f32 v237, v144, v145\n\t" \
    "v_add_f32 v237, v237, v146\n\t" \
    "v_add_f32 v237, v237, v147\n\t" \
    "v_add_f32 v237, v237, v148\n\t" \
    "v_add_f32 v237, v237, v149\n\t" \
    "v_add_f32 v237, v237, v150\n\t" \
    "v_add_f32 v237, v237, v151\n\t" \
    "v_add_f32 v237, v237, v152\n\t" \
    "v_add_f32 v237, v237, v153\n\t" \
    "v_add_f32 v237, v237, v154\n\t" \
    "v_add_f32 v237, v237, v155\n\t" \
    "v_add_f32 v237, v237, v156\n\t" \
    "v_add_f32 v237, v237, v157\n\t" \
    "v_add_f32 v237, v237, v158\n\t" \
    "v_add_f32 v237, v237, v159\n\t" \
    "v_cvt_pk_bf16_f32 v160, v144, v145\n\t" \
    "v_cvt_pk_bf16_f32 v161, v146, v147\n\t" \
    "v_cvt_pk_bf16_f32 v162, v148, v149\n\t" \
    "v_cvt_pk_bf16_f32 v163, v150, v151\n\t" \
    "v_cvt_pk_bf16_f32 v164, v152, v153\n\t" \
    "v_cvt_pk_bf16_f32 v165, v154, v155\n\t" \
    "v_cvt_pk_bf16_f32 v166, v156, v157\n\t" \
    "v_cvt_pk_bf16_f32 v167, v158, v159\n\t" \
    "s_nop 1\n\t" \
    "s_branch BACKB_%=\n\t" \
    "RAREC_%=:\n\t" \
    "s_nop 15\n\t" \
    "v_max3_f32 v239, v128, v129, v130\n\t" \
    "v_max3_f32 v239, v239, v131, v132\n\t" \
    "v_max3_f32 v239, v239, v133, v134\n\t" \
    "v_max3_f32 v239, v239, v135, v136\n\t" \
    "v_max3_f32 v239, v239, v137, v138\n\t" \
    "v_max3_f32 v239, v239, v139, v140\n\t" \
    "v_max3_f32 v239, v239, v141, v142\n\t" \
    "v_max_f32 v239, v239, v143\n\t" \
    "v_max_f32 v239, 0, v239\n\t" \
    "v_mov_b32 v240, v239\n\t" \
    "s_nop 1\n\t" \
    "v_permlane32_swap_b32 v239, v240\n\t" \
    "s_nop 1\n\t" \
    "v_max_f32 v239, v239, v240\n\t" \
    "v_add_f32 v238, v238, v239\n\t" \
    "v_exp_f32 v240, -v239\n\t" \
    "v_sub_f32 v241, 0, v238\n\t" \
    "v_sub_f32 v128, v128, v239\n\t" \
    "v_sub_f32 v112, v112, v239\n\t" \
    "v_mov_b32 v96, v241\n\t" \
    "v_sub_f32 v129, v129, v239\n\t" \
    "v_sub_f32 v113, v113, v239\n\t" \
    "v_mov_b32 v97, v241\n\t" \
    "v_sub_f32 v130, v130, v239\n\t" \
    "v_sub_f32 v114, v114, v239\n\t" \
    "v_mov_b32 v98, v241\n\t" \
    "v_sub_f32 v131, v131, v239\n\t" \
    "v_sub_f32 v115, v115, v239\n\t" \
    "v_mov_b32 v99, v241\n\t" \
    "v_sub_f32 v132, v132, v239\n\t" \
    "v_sub_f32 v116, v116, v239\n\t" \
    "v_mov_b32 v100, v241\n\t" \
    "v_sub_f32 v133, v133, v239\n\t" \
    "v_sub_f32 v117, v117, v239\n\t" \
    "v_mov_b32 v101, v241\n\t" \
    "v_sub_f32 v134, v134, v239\n\t" \
    "v_sub_f32 v118, v118, v239\n\t" \
    "v_mov_b32 v102, v241\n\t" \
    "v_sub_f32 v135, v135, v239\n\t" \
    "v_sub_f32 v119, v119, v239\n\t" \
    "v_mov_b32 v103, v241\n\t" \
    "v_sub_f32 v136, v136, v239\n\t" \
    "v_sub_f32 v120, v120, v239\n\t" \
    "v_mov_b32 v104, v241\n\t" \
    "v_sub_f32 v137, v137, v239\n\t" \
    "v_sub_f32 v121, v121, v239\n\t" \
    "v_mov_b32 v105, v241\n\t" \
    "v_sub_f32 v138, v138, v239\n\t" \
    "v_sub_f32 v122, v122, v239\n\t" \
    "v_mov_b32 v106, v241\n\t" \
    "v_sub_f32 v139, v139, v239\n\t" \
    "v_sub_f32 v123, v123, v239\n\t" \
    "v_mov_b32 v107, v241\n\t" \
    "v_sub_f32 v140, v140, v239\n\t" \
    "v_sub_f32 v124, v124, v239\n\t" \
    "v_mov_b32 v108, v241\n\t" \
    "v_sub_f32 v141, v141, v239\n\t" \
    "v_sub_f32 v125, v125, v239\n\t" \
    "v_mov_b32 v109, v241\n\t" \
    "v_sub_f32 v142, v142, v239\n\t" \
    "v_sub_f32 v126, v126, v239\n\t" \
    "v_mov_b32 v110, v241\n\t" \
    "v_sub_f32 v143, v143, v239\n\t" \
    "v_sub_f32 v127, v127, v239\n\t" \
    "v_mov_b32 v111, v241\n\t" \
    "v_exp_f32 v144, v128\n\t" \
    "v_exp_f32 v145, v129\n\t" \
    "v_exp_f32 v146, v130\n\t" \
    "v_exp_f32 v147, v131\n\t" \
    "v_exp_f32 v148, v132\n\t" \
    "v_exp_f32 v149, v133\n\t" \
    "v_exp_f32 v150, v134\n\t" \
    "v_exp_f32 v151, v135\n\t" \
    "v_exp_f32 v152, v136\n\t" \
    "v_exp_f32 v153, v137\n\t" \
    "v_exp_f32 v154, v138\n\t" \
    "v_exp_f32 v155, v139\n\t" \
    "v_exp_f32 v156, v140\n\t" \
    "v_exp_f32 v157, v141\n\t" \
    "v_exp_f32 v158, v142\n\t" \
    "v_exp_f32 v159, v143\n\t" \
    "v_mul_f32 v236, v236, v240\n\t" \
    "v_mul_f32 v0, v0, v240\n\t" \
    "v_mul_f32 v1, v1, v240\n\t" \
    "v_mul_f32 v2, v2, v240\n\t" \
    "v_mul_f32 v3, v3, v240\n\t" \
    "v_mul_f32 v4, v4, v240\n\t" \
    "v_mul_f32 v5, v5, v240\n\t" \
    "v_mul_f32 v6, v6, v240\n\t" \
    "v_mul_f32 v7, v7, v240\n\t" \
    "v_mul_f32 v8, v8, v240\n\t" \
    "v_mul_f32 v9, v9, v240\n\t" \
    "v_mul_f32 v10, v10, v240\n\t" \
    "v_mul_f32 v11, v11, v240\n\t" \
    "v_mul_f32 v12, v12, v240\n\t" \
    "v_mul_f32 v13, v13, v240\n\t" \
    "v_mul_f32 v14, v14, v240\n\t" \
    "v_mul_f32 v15, v15, v240\n\t" \
    "v_mul_f32 v16, v16, v240\n\t" \
    "v_mul_f32 v17, v17, v240\n\t" \
    "v_mul_f32 v18, v18, v240\n\t" \
    "v_mul_f32 v19, v19, v240\n\t" \
    "v_mul_f32 v20, v20, v240\n\t" \
    "v_mul_f32 v21, v21, v240\n\t" \
    "v_mul_f32 v22, v22, v240\n\t" \
    "v_mul_f32 v23, v23, v240\n\t" \
    "v_mul_f32 v24, v24, v240\n\t" \
    "v_mul_f32 v25, v25, v240\n\t" \
    "v_mul_f32 v26, v26, v240\n\t" \
    "v_mul_f32 v27, v27, v240\n\t" \
    "v_mul_f32 v28, v28, v240\n\t" \
    "v_mul_f32 v29, v29, v240\n\t" \
    "v_mul_f32 v30, v30, v240\n\t" \
    "v_mul_f32 v31, v31, v240\n\t" \
    "v_mul_f32 v32, v32, v240\n\t" \
    "v_mul_f32 v33, v33, v240\n\t" \
    "v_mul_f32 v34, v34, v240\n\t" \
    "v_mul_f32 v35, v35, v240\n\t" \
    "v_mul_f32 v36, v36, v240\n\t" \
    "v_mul_f32 v37, v37, v240\n\t" \
    "v_mul_f32 v38, v38, v240\n\t" \
    "v_mul_f32 v39, v39, v240\n\t" \
    "v_mul_f32 v40, v40, v240\n\t" \
    "v_mul_f32 v41, v41, v240\n\t" \
    "v_mul_f32 v42, v42, v240\n\t" \
    "v_mul_f32 v43, v43, v240\n\t" \
    "v_mul_f32 v44, v44, v240\n\t" \
    "v_mul_f32 v45, v45, v240\n\t" \
    "v_mul_f32 v46, v46, v240\n\t" \
    "v_mul_f32 v47, v47, v240\n\t" \
    "v_mul_f32 v48, v48, v240\n\t" \
    "v_mul_f32 v49, v49, v240\n\t" \
    "v_mul_f32 v50, v50, v240\n\t" \
    "v_mul_f32 v51, v51, v240\n\t" \
    "v_mul_f32 v52, v52, v240\n\t" \
    "v_mul_f32 v53, v53, v240\n\t" \
    "v_mul_f32 v54, v54, v240\n\t" \
    "v_mul_f32 v55, v55, v240\n\t" \
    "v_mul_f32 v56, v56, v240\n\t" \
    "v_mul_f32 v57, v57, v240\n\t" \
    "v_mul_f32 v58, v58, v240\n\t" \
    "v_mul_f32 v59, v59, v240\n\t" \
    "v_mul_f32 v60, v60, v240\n\t" \
    "v_mul_f32 v61, v61, v240\n\t" \
    "v_mul_f32 v62, v62, v240\n\t" \
    "v_mul_f32 v63, v63, v240\n\t" \
    "v_add_f32 v237, v144, v145\n\t" \
    "v_add_f32 v237, v237, v146\n\t" \
    "v_add_f32 v237, v237, v147\n\t" \
    "v_add_f32 v237, v237, v148\n\t" \
    "v_add_f32 v237, v237, v149\n\t" \
    "v_add_f32 v237, v237, v150\n\t" \
    "v_add_f32 v237, v237, v151\n\t" \
    "v_add_f32 v237, v237, v152\n\t" \
    "v_add_f32 v237, v237, v153\n\t" \
    "v_add_f32 v237, v237, v154\n\t" \
    "v_add_f32 v237, v237, v155\n\t" \
    "v_add_f32 v237, v237, v156\n\t" \
    "v_add_f32 v237, v237, v157\n\t" \
    "v_add_f32 v237, v237, v158\n\t" \
    "v_add_f32 v237, v237, v159\n\t" \
    "v_cvt_pk_bf16_f32 v160, v144, v145\n\t" \
    "v_cvt_pk_bf16_f32 v161, v146, v147\n\t" \
    "v_cvt_pk_bf16_f32 v162, v148, v149\n\t" \
    "v_cvt_pk_bf16_f32 v163, v150, v151\n\t" \
    "v_cvt_pk_bf16_f32 v164, v152, v153\n\t" \
    "v_cvt_pk_bf16_f32 v165, v154, v155\n\t" \
    "v_cvt_pk_bf16_f32 v166, v156, v157\n\t" \
    "v_cvt_pk_bf16_f32 v167, v158, v159\n\t" \
    "s_nop 1\n\t" \
    "s_branch BACKC_%=\n\t" \
    "RARED_%=:\n\t" \
    "s_nop 15\n\t" \
    "v_max3_f32 v239, v112, v113, v114\n\t" \
    "v_max3_f32 v239, v239, v115, v116\n\t" \
    "v_max3_f32 v239, v239, v117, v118\n\t" \
    "v_max3_f32 v239, v239, v119, v120\n\t" \
    "v_max3_f32 v239, v239, v121, v122\n\t" \
    "v_max3_f32 v239, v239, v123, v124\n\t" \
    "v_max3_f32 v239, v239, v125, v126\n\t" \
    "v_max_f32 v239, v239, v127\n\t" \
    "v_max_f32 v239, 0, v239\n\t" \
    "v_mov_b32 v240, v239\n\t" \
    "s_nop 1\n\t" \
    "v_permlane32_swap_b32 v239, v240\n\t" \
    "s_nop 1\n\t" \
    "v_max_f32 v239, v239, v240\n\t" \
    "v_add_f32 v238, v238, v239\n\t" \
    "v_exp_f32 v240, -v239\n\t" \
    "v_sub_f32 v241, 0, v238\n\t" \
    "v_sub_f32 v112, v112, v239\n\t" \
    "v_sub_f32 v128, v128, v239\n\t" \
    "v_mov_b32 v96, v241\n\t" \
    "v_sub_f32 v113, v113, v239\n\t" \
    "v_sub_f32 v129, v129, v239\n\t" \
    "v_mov_b32 v97, v241\n\t" \
    "v_sub_f32 v114, v114, v239\n\t" \
    "v_sub_f32 v130, v130, v239\n\t" \
    "v_mov_b32 v98, v241\n\t" \
    "v_sub_f32 v115, v115, v239\n\t" \
    "v_sub_f32 v131, v131, v239\n\t" \
    "v_mov_b32 v99, v241\n\t" \
    "v_sub_f32 v116, v116, v239\n\t" \
    "v_sub_f32 v132, v132, v239\n\t" \
    "v_mov_b32 v100, v241\n\t" \
    "v_sub_f32 v117, v117, v239\n\t" \
    "v_sub_f32 v133, v133, v239\n\t" \
    "v_mov_b32 v101, v241\n\t" \
    "v_sub_f32 v118, v118, v239\n\t" \
    "v_sub_f32 v134, v134, v239\n\t" \
    "v_mov_b32 v102, v241\n\t" \
    "v_sub_f32 v119, v119, v239\n\t" \
    "v_sub_f32 v135, v135, v239\n\t" \
    "v_mov_b32 v103, v241\n\t" \
    "v_sub_f32 v120, v120, v239\n\t" \
    "v_sub_f32 v136, v136, v239\n\t" \
    "v_mov_b32 v104, v241\n\t" \
    "v_sub_f32 v121, v121, v239\n\t" \
    "v_sub_f32 v137, v137, v239\n\t" \
    "v_mov_b32 v105, v241\n\t" \
    "v_sub_f32 v122, v122, v239\n\t" \
    "v_sub_f32 v138, v138, v239\n\t" \
    "v_mov_b32 v106, v241\n\t" \
    "v_sub_f32 v123, v123, v239\n\t" \
    "v_sub_f32 v139, v139, v239\n\t" \
    "v_mov_b32 v107, v241\n\t" \
    "v_sub_f32 v124, v124, v239\n\t" \
    "v_sub_f32 v140, v140, v239\n\t" \
    "v_mov_b32 v108, v241\n\t" \
    "v_sub_f32 v125, v125, v239\n\t" \
    "v_sub_f32 v141, v141, v239\n\t" \
    "v_mov_b32 v109, v241\n\t" \
    "v_sub_f32 v126, v126, v239\n\t" \
    "v_sub_f32 v142, v142, v239\n\t" \
    "v_mov_b32 v110, v241\n\t" \
    "v_sub_f32 v127, v127, v239\n\t" \
    "v_sub_f32 v143, v143, v239\n\t" \
    "v_mov_b32 v111, v241\n\t" \
    "v_exp_f32 v144, v112\n\t" \
    "v_exp_f32 v145, v113\n\t" \
    "v_exp_f32 v146, v114\n\t" \
    "v_exp_f32 v147, v115\n\t" \
    "v_exp_f32 v148, v116\n\t" \
    "v_exp_f32 v149, v117\n\t" \
    "v_exp_f32 v150, v118\n\t" \
    "v_exp_f32 v151, v119\n\t" \
    "v_exp_f32 v152, v120\n\t" \
    "v_exp_f32 v153, v121\n\t" \
    "v_exp_f32 v154, v122\n\t" \
    "v_exp_f32 v155, v123\n\t" \
    "v_exp_f32 v156, v124\n\t" \
    "v_exp_f32 v157, v125\n\t" \
    "v_exp_f32 v158, v126\n\t" \
    "v_exp_f32 v159, v127\n\t" \
    "v_mul_f32 v236, v236, v240\n\t" \
    "v_mul_f32 v0, v0, v240\n\t" \
    "v_mul_f32 v1, v1, v240\n\t" \
    "v_mul_f32 v2, v2, v240\n\t" \
    "v_mul_f32 v3, v3, v240\n\t" \
    "v_mul_f32 v4, v4, v240\n\t" \
    "v_mul_f32 v5, v5, v240\n\t" \
    "v_mul_f32 v6, v6, v240\n\t" \
    "v_mul_f32 v7, v7, v240\n\t" \
    "v_mul_f32 v8, v8, v240\n\t" \
    "v_mul_f32 v9, v9, v240\n\t" \
    "v_mul_f32 v10, v10, v240\n\t" \
    "v_mul_f32 v11, v11, v240\n\t" \
    "v_mul_f32 v12, v12, v240\n\t" \
    "v_mul_f32 v13, v13, v240\n\t" \
    "v_mul_f32 v14, v14, v240\n\t" \
    "v_mul_f32 v15, v15, v240\n\t" \
    "v_mul_f32 v16, v16, v240\n\t" \
    "v_mul_f32 v17, v17, v240\n\t" \
    "v_mul_f32 v18, v18, v240\n\t" \
    "v_mul_f32 v19, v19, v240\n\t" \
    "v_mul_f32 v20, v20, v240\n\t" \
    "v_mul_f32 v21, v21, v240\n\t" \
    "v_mul_f32 v22, v22, v240\n\t" \
    "v_mul_f32 v23, v23, v240\n\t" \
    "v_mul_f32 v24, v24, v240\n\t" \
    "v_mul_f32 v25, v25, v240\n\t" \
    "v_mul_f32 v26, v26, v240\n\t" \
    "v_mul_f32 v27, v27, v240\n\t" \
    "v_mul_f32 v28, v28, v240\n\t" \
    "v_mul_f32 v29, v29, v240\n\t" \
    "v_mul_f32 v30, v30, v240\n\t" \
    "v_mul_f32 v31, v31, v240\n\t" \
    "v_mul_f32 v32, v32, v240\n\t" \
    "v_mul_f32 v33, v33, v240\n\t" \
    "v_mul_f32 v34, v34, v240\n\t" \
    "v_mul_f32 v35, v35, v240\n\t" \
    "v_mul_f32 v36, v36, v240\n\t" \
    "v_mul_f32 v37, v37, v240\n\t" \
    "v_mul_f32 v38, v38, v240\n\t" \
    "v_mul_f32 v39, v39, v240\n\t" \
    "v_mul_f32 v40, v40, v240\n\t" \
    "v_mul_f32 v41, v41, v240\n\t" \
    "v_mul_f32 v42, v42, v240\n\t" \
    "v_mul_f32 v43, v43, v240\n\t" \
    "v_mul_f32 v44, v44, v240\n\t" \
    "v_mul_f32 v45, v45, v240\n\t" \
    "v_mul_f32 v46, v46, v240\n\t" \
    "v_mul_f32 v47, v47, v240\n\t" \
    "v_mul_f32 v48, v48, v240\n\t" \
    "v_mul_f32 v49, v49, v240\n\t" \
    "v_mul_f32 v50, v50, v240\n\t" \
    "v_mul_f32 v51, v51, v240\n\t" \
    "v_mul_f32 v52, v52, v240\n\t" \
    "v_mul_f32 v53, v53, v240\n\t" \
    "v_mul_f32 v54, v54, v240\n\t" \
    "v_mul_f32 v55, v55, v240\n\t" \
    "v_mul_f32 v56, v56, v240\n\t" \
    "v_mul_f32 v57, v57, v240\n\t" \
    "v_mul_f32 v58, v58, v240\n\t" \
    "v_mul_f32 v59, v59, v240\n\t" \
    "v_mul_f32 v60, v60, v240\n\t" \
    "v_mul_f32 v61, v61, v240\n\t" \
    "v_mul_f32 v62, v62, v240\n\t" \
    "v_mul_f32 v63, v63, v240\n\t" \
    "v_add_f32 v237, v144, v145\n\t" \
    "v_add_f32 v237, v237, v146\n\t" \
    "v_add_f32 v237, v237, v147\n\t" \
    "v_add_f32 v237, v237, v148\n\t" \
    "v_add_f32 v237, v237, v149\n\t" \
    "v_add_f32 v237, v237, v150\n\t" \
    "v_add_f32 v237, v237, v151\n\t" \
    "v_add_f32 v237, v237, v152\n\t" \
    "v_add_f32 v237, v237, v153\n\t" \
    "v_add_f32 v237, v237, v154\n\t" \
    "v_add_f32 v237, v237, v155\n\t" \
    "v_add_f32 v237, v237, v156\n\t" \
    "v_add_f32 v237, v237, v157\n\t" \
    "v_add_f32 v237, v237, v158\n\t" \
    "v_add_f32 v237, v237, v159\n\t" \
    "v_cvt_pk_bf16_f32 v160, v144, v145\n\t" \
    "v_cvt_pk_bf16_f32 v161, v146, v147\n\t" \
    "v_cvt_pk_bf16_f32 v162, v148, v149\n\t" \
    "v_cvt_pk_bf16_f32 v163, v150, v151\n\t" \
    "v_cvt_pk_bf16_f32 v164, v152, v153\n\t" \
    "v_cvt_pk_bf16_f32 v165, v154, v155\n\t" \
    "v_cvt_pk_bf16_f32 v166, v156, v157\n\t" \
    "v_cvt_pk_bf16_f32 v167, v158, v159\n\t" \
    "s_nop 1\n\t" \
    "s_branch BACKD_%=\n\t" \
    "RAREE_%=:\n\t" \
    "s_nop 15\n\t" \
    "v_max3_f32 v239, v128, v129, v130\n\t" \
    "v_max3_f32 v239, v239, v131, v132\n\t" \
    "v_max3_f32 v239, v239, v133, v134\n\t" \
    "v_max3_f32 v239, v239, v135, v136\n\t" \
    "v_max3_f32 v239, v239, v137, v138\n\t" \
    "v_max3_f32 v239, v239, v139, v140\n\t" \
    "v_max3_f32 v239, v239, v141, v142\n\t" \
    "v_max_f32 v239, v239, v143\n\t" \
    "v_max_f32 v239, 0, v239\n\t" \
    "v_mov_b32 v240, v239\n\t" \
    "s_nop 1\n\t" \
    "v_permlane32_swap_b32 v239, v240\n\t" \
    "s_nop 1\n\t" \
    "v_max_f32 v239, v239, v240\n\t" \
    "v_add_f32 v238, v238, v239\n\t" \
    "v_exp_f32 v240, -v239\n\t" \
    "v_sub_f32 v241, 0, v238\n\t" \
    "v_sub_f32 v128, v128, v239\n\t" \
    "v_mov_b32 v96, v241\n\t" \
    "v_sub_f32 v129, v129, v239\n\t" \
    "v_mov_b32 v97, v241\n\t" \
    "v_sub_f32 v130, v130, v239\n\t" \
    "v_mov_b32 v98, v241\n\t" \
    "v_sub_f32 v131, v131, v239\n\t" \
    "v_mov_b32 v99, v241\n\t" \
    "v_sub_f32 v132, v132, v239\n\t" \
    "v_mov_b32 v100, v241\n\t" \
    "v_sub_f32 v133, v133, v239\n\t" \
    "v_mov_b32 v101, v241\n\t" \
    "v_sub_f32 v134, v134, v239\n\t" \
    "v_mov_b32 v102, v241\n\t" \
    "v_sub_f32 v135, v135, v239\n\t" \
    "v_mov_b32 v103, v241\n\t" \
    "v_sub_f32 v136, v136, v239\n\t" \
    "v_mov_b32 v104, v241\n\t" \
    "v_sub_f32 v137, v137, v239\n\t" \
    "v_mov_b32 v105, v241\n\t" \
    "v_sub_f32 v138, v138, v239\n\t" \
    "v_mov_b32 v106, v241\n\t" \
    "v_sub_f32 v139, v139, v239\n\t" \
    "v_mov_b32 v107, v241\n\t" \
    "v_sub_f32 v140, v140, v239\n\t" \
    "v_mov_b32 v108, v241\n\t" \
    "v_sub_f32 v141, v141, v239\n\t" \
    "v_mov_b32 v109, v241\n\t" \
    "v_sub_f32 v142, v142, v239\n\t" \
    "v_mov_b32 v110, v241\n\t" \
    "v_sub_f32 v143, v143, v239\n\t" \
    "v_mov_b32 v111, v241\n\t" \
    "v_exp_f32 v144, v128\n\t" \
    "v_exp_f32 v145, v129\n\t" \
    "v_exp_f32 v146, v130\n\t" \
    "v_exp_f32 v147, v131\n\t" \
    "v_exp_f32 v148, v132\n\t" \
    "v_exp_f32 v149, v133\n\t" \
    "v_exp_f32 v150, v134\n\t" \
    "v_exp_f32 v151, v135\n\t" \
    "v_exp_f32 v152, v136\n\t" \
    "v_exp_f32 v153, v137\n\t" \
    "v_exp_f32 v154, v138\n\t" \
    "v_exp_f32 v155, v139\n\t" \
    "v_exp_f32 v156, v140\n\t" \
    "v_exp_f32 v157, v141\n\t" \
    "v_exp_f32 v158, v142\n\t" \
    "v_exp_f32 v159, v143\n\t" \
    "v_mul_f32 v236, v236, v240\n\t" \
    "v_mul_f32 v0, v0, v240\n\t" \
    "v_mul_f32 v1, v1, v240\n\t" \
    "v_mul_f32 v2, v2, v240\n\t" \
    "v_mul_f32 v3, v3, v240\n\t" \
    "v_mul_f32 v4, v4, v240\n\t" \
    "v_mul_f32 v5, v5, v240\n\t" \
    "v_mul_f32 v6, v6, v240\n\t" \
    "v_mul_f32 v7, v7, v240\n\t" \
    "v_mul_f32 v8, v8, v240\n\t" \
    "v_mul_f32 v9, v9, v240\n\t" \
    "v_mul_f32 v10, v10, v240\n\t" \
    "v_mul_f32 v11, v11, v240\n\t" \
    "v_mul_f32 v12, v12, v240\n\t" \
    "v_mul_f32 v13, v13, v240\n\t" \
    "v_mul_f32 v14, v14, v240\n\t" \
    "v_mul_f32 v15, v15, v240\n\t" \
    "v_mul_f32 v16, v16, v240\n\t" \
    "v_mul_f32 v17, v17, v240\n\t" \
    "v_mul_f32 v18, v18, v240\n\t" \
    "v_mul_f32 v19, v19, v240\n\t" \
    "v_mul_f32 v20, v20, v240\n\t" \
    "v_mul_f32 v21, v21, v240\n\t" \
    "v_mul_f32 v22, v22, v240\n\t" \
    "v_mul_f32 v23, v23, v240\n\t" \
    "v_mul_f32 v24, v24, v240\n\t" \
    "v_mul_f32 v25, v25, v240\n\t" \
    "v_mul_f32 v26, v26, v240\n\t" \
    "v_mul_f32 v27, v27, v240\n\t" \
    "v_mul_f32 v28, v28, v240\n\t" \
    "v_mul_f32 v29, v29, v240\n\t" \
    "v_mul_f32 v30, v30, v240\n\t" \
    "v_mul_f32 v31, v31, v240\n\t" \
    "v_mul_f32 v32, v32, v240\n\t" \
    "v_mul_f32 v33, v33, v240\n\t" \
    "v_mul_f32 v34, v34, v240\n\t" \
    "v_mul_f32 v35, v35, v240\n\t" \
    "v_mul_f32 v36, v36, v240\n\t" \
    "v_mul_f32 v37, v37, v240\n\t" \
    "v_mul_f32 v38, v38, v240\n\t" \
    "v_mul_f32 v39, v39, v240\n\t" \
    "v_mul_f32 v40, v40, v240\n\t" \
    "v_mul_f32 v41, v41, v240\n\t" \
    "v_mul_f32 v42, v42, v240\n\t" \
    "v_mul_f32 v43, v43, v240\n\t" \
    "v_mul_f32 v44, v44, v240\n\t" \
    "v_mul_f32 v45, v45, v240\n\t" \
    "v_mul_f32 v46, v46, v240\n\t" \
    "v_mul_f32 v47, v47, v240\n\t" \
    "v_mul_f32 v48, v48, v240\n\t" \
    "v_mul_f32 v49, v49, v240\n\t" \
    "v_mul_f32 v50, v50, v240\n\t" \
    "v_mul_f32 v51, v51, v240\n\t" \
    "v_mul_f32 v52, v52, v240\n\t" \
    "v_mul_f32 v53, v53, v240\n\t" \
    "v_mul_f32 v54, v54, v240\n\t" \
    "v_mul_f32 v55, v55, v240\n\t" \
    "v_mul_f32 v56, v56, v240\n\t" \
    "v_mul_f32 v57, v57, v240\n\t" \
    "v_mul_f32 v58, v58, v240\n\t" \
    "v_mul_f32 v59, v59, v240\n\t" \
    "v_mul_f32 v60, v60, v240\n\t" \
    "v_mul_f32 v61, v61, v240\n\t" \
    "v_mul_f32 v62, v62, v240\n\t" \
    "v_mul_f32 v63, v63, v240\n\t" \
    "v_add_f32 v237, v144, v145\n\t" \
    "v_add_f32 v237, v237, v146\n\t" \
    "v_add_f32 v237, v237, v147\n\t" \
    "v_add_f32 v237, v237, v148\n\t" \
    "v_add_f32 v237, v237, v149\n\t" \
    "v_add_f32 v237, v237, v150\n\t" \
    "v_add_f32 v237, v237, v151\n\t" \
    "v_add_f32 v237, v237, v152\n\t" \
    "v_add_f32 v237, v237, v153\n\t" \
    "v_add_f32 v237, v237, v154\n\t" \
    "v_add_f32 v237, v237, v155\n\t" \
    "v_add_f32 v237, v237, v156\n\t" \
    "v_add_f32 v237, v237, v157\n\t" \
    "v_add_f32 v237, v237, v158\n\t" \
    "v_add_f32 v237, v237, v159\n\t" \
    "v_cvt_pk_bf16_f32 v160, v144, v145\n\t" \
    "v_cvt_pk_bf16_f32 v161, v146, v147\n\t" \
    "v_cvt_pk_bf16_f32 v162, v148, v149\n\t" \
    "v_cvt_pk_bf16_f32 v163, v150, v151\n\t" \
    "v_cvt_pk_bf16_f32 v164, v152, v153\n\t" \
    "v_cvt_pk_bf16_f32 v165, v154, v155\n\t" \
    "v_cvt_pk_bf16_f32 v166, v156, v157\n\t" \
    "v_cvt_pk_bf16_f32 v167, v158, v159\n\t" \
    "s_nop 1\n\t" \
    "s_branch BACKE_%=\n\t" \
    "RAREF_%=:\n\t" \
    "s_nop 15\n\t" \
    "v_max3_f32 v239, v128, v129, v130\n\t" \
    "v_max3_f32 v239, v239, v131, v132\n\t" \
    "v_max3_f32 v239, v239, v133, v134\n\t" \
    "v_max3_f32 v239, v239, v135, v136\n\t" \
    "v_max3_f32 v239, v239, v137, v138\n\t" \
    "v_max3_f32 v239, v239, v139, v140\n\t" \
    "v_max3_f32 v239, v239, v141, v142\n\t" \
    "v_max_f32 v239, v239, v143\n\t" \
    "v_max_f32 v239, 0, v239\n\t" \
    "v_mov_b32 v240, v239\n\t" \
    "s_nop 1\n\t" \
    "v_permlane32_swap_b32 v239, v240\n\t" \
    "s_nop 1\n\t" \
    "v_max_f32 v239, v239, v240\n\t" \
    "v_add_f32 v238, v238, v239\n\t" \
    "v_exp_f32 v240, -v239\n\t" \
    "v_sub_f32 v241, 0, v238\n\t" \
    "v_sub_f32 v128, v128, v239\n\t" \
    "v_sub_f32 v112, v112, v239\n\t" \
    "v_mov_b32 v96, v241\n\t" \
    "v_sub_f32 v129, v129, v239\n\t" \
    "v_sub_f32 v113, v113, v239\n\t" \
    "v_mov_b32 v97, v241\n\t" \
    "v_sub_f32 v130, v130, v239\n\t" \
    "v_sub_f32 v114, v114, v239\n\t" \
    "v_mov_b32 v98, v241\n\t" \
    "v_sub_f32 v131, v131, v239\n\t" \
    "v_sub_f32 v115, v115, v239\n\t" \
    "v_mov_b32 v99, v241\n\t" \
    "v_sub_f32 v132, v132, v239\n\t" \
    "v_sub_f32 v116, v116, v239\n\t" \
    "v_mov_b32 v100, v241\n\t" \
    "v_sub_f32 v133, v133, v239\n\t" \
    "v_sub_f32 v117, v117, v239\n\t" \
    "v_mov_b32 v101, v241\n\t" \
    "v_sub_f32 v134, v134, v239\n\t" \
    "v_sub_f32 v118, v118, v239\n\t" \
    "v_mov_b32 v102, v241\n\t" \
    "v_sub_f32 v135, v135, v239\n\t" \
    "v_sub_f32 v119, v119, v239\n\t" \
    "v_mov_b32 v103, v241\n\t" \
    "v_sub_f32 v136, v136, v239\n\t" \
    "v_sub_f32 v120, v120, v239\n\t" \
    "v_mov_b32 v104, v241\n\t" \
    "v_sub_f32 v137, v137, v239\n\t" \
    "v_sub_f32 v121, v121, v239\n\t" \
    "v_mov_b32 v105, v241\n\t" \
    "v_sub_f32 v138, v138, v239\n\t" \
    "v_sub_f32 v122, v122, v239\n\t" \
    "v_mov_b32 v106, v241\n\t" \
    "v_sub_f32 v139, v139, v239\n\t" \
    "v_sub_f32 v123, v123, v239\n\t" \
    "v_mov_b32 v107, v241\n\t" \
    "v_sub_f32 v140, v140, v239\n\t" \
    "v_sub_f32 v124, v124, v239\n\t" \
    "v_mov_b32 v108, v241\n\t" \
    "v_sub_f32 v141, v141, v239\n\t" \
    "v_sub_f32 v125, v125, v239\n\t" \
    "v_mov_b32 v109, v241\n\t" \
    "v_sub_f32 v142, v142, v239\n\t" \
    "v_sub_f32 v126, v126, v239\n\t" \
    "v_mov_b32 v110, v241\n\t" \
    "v_sub_f32 v143, v143, v239\n\t" \
    "v_sub_f32 v127, v127, v239\n\t" \
    "v_mov_b32 v111, v241\n\t" \
    "v_exp_f32 v144, v128\n\t" \
    "v_exp_f32 v145, v129\n\t" \
    "v_exp_f32 v146, v130\n\t" \
    "v_exp_f32 v147, v131\n\t" \
    "v_exp_f32 v148, v132\n\t" \
    "v_exp_f32 v149, v133\n\t" \
    "v_exp_f32 v150, v134\n\t" \
    "v_exp_f32 v151, v135\n\t" \
    "v_exp_f32 v152, v136\n\t" \
    "v_exp_f32 v153, v137\n\t" \
    "v_exp_f32 v154, v138\n\t" \
    "v_exp_f32 v155, v139\n\t" \
    "v_exp_f32 v156, v140\n\t" \
    "v_exp_f32 v157, v141\n\t" \
    "v_exp_f32 v158, v142\n\t" \
    "v_exp_f32 v159, v143\n\t" \
    "v_mul_f32 v236, v236, v240\n\t" \
    "v_mul_f32 v0, v0, v240\n\t" \
    "v_mul_f32 v1, v1, v240\n\t" \
    "v_mul_f32 v2, v2, v240\n\t" \
    "v_mul_f32 v3, v3, v240\n\t" \
    "v_mul_f32 v4, v4, v240\n\t" \
    "v_mul_f32 v5, v5, v240\n\t" \
    "v_mul_f32 v6, v6, v240\n\t" \
    "v_mul_f32 v7, v7, v240\n\t" \
    "v_mul_f32 v8, v8, v240\n\t" \
    "v_mul_f32 v9, v9, v240\n\t" \
    "v_mul_f32 v10, v10, v240\n\t" \
    "v_mul_f32 v11, v11, v240\n\t" \
    "v_mul_f32 v12, v12, v240\n\t" \
    "v_mul_f32 v13, v13, v240\n\t" \
    "v_mul_f32 v14, v14, v240\n\t" \
    "v_mul_f32 v15, v15, v240\n\t" \
    "v_mul_f32 v16, v16, v240\n\t" \
    "v_mul_f32 v17, v17, v240\n\t" \
    "v_mul_f32 v18, v18, v240\n\t" \
    "v_mul_f32 v19, v19, v240\n\t" \
    "v_mul_f32 v20, v20, v240\n\t" \
    "v_mul_f32 v21, v21, v240\n\t" \
    "v_mul_f32 v22, v22, v240\n\t" \
    "v_mul_f32 v23, v23, v240\n\t" \
    "v_mul_f32 v24, v24, v240\n\t" \
    "v_mul_f32 v25, v25, v240\n\t" \
    "v_mul_f32 v26, v26, v240\n\t" \
    "v_mul_f32 v27, v27, v240\n\t" \
    "v_mul_f32 v28, v28, v240\n\t" \
    "v_mul_f32 v29, v29, v240\n\t" \
    "v_mul_f32 v30, v30, v240\n\t" \
    "v_mul_f32 v31, v31, v240\n\t" \
    "v_mul_f32 v32, v32, v240\n\t" \
    "v_mul_f32 v33, v33, v240\n\t" \
    "v_mul_f32 v34, v34, v240\n\t" \
    "v_mul_f32 v35, v35, v240\n\t" \
    "v_mul_f32 v36, v36, v240\n\t" \
    "v_mul_f32 v37, v37, v240\n\t" \
    "v_mul_f32 v38, v38, v240\n\t" \
    "v_mul_f32 v39, v39, v240\n\t" \
    "v_mul_f32 v40, v40, v240\n\t" \
    "v_mul_f32 v41, v41, v240\n\t" \
    "v_mul_f32 v42, v42, v240\n\t" \
    "v_mul_f32 v43, v43, v240\n\t" \
    "v_mul_f32 v44, v44, v240\n\t" \
    "v_mul_f32 v45, v45, v240\n\t" \
    "v_mul_f32 v46, v46, v240\n\t" \
    "v_mul_f32 v47, v47, v240\n\t" \
    "v_mul_f32 v48, v48, v240\n\t" \
    "v_mul_f32 v49, v49, v240\n\t" \
    "v_mul_f32 v50, v50, v240\n\t" \
    "v_mul_f32 v51, v51, v240\n\t" \
    "v_mul_f32 v52, v52, v240\n\t" \
    "v_mul_f32 v53, v53, v240\n\t" \
    "v_mul_f32 v54, v54, v240\n\t" \
    "v_mul_f32 v55, v55, v240\n\t" \
    "v_mul_f32 v56, v56, v240\n\t" \
    "v_mul_f32 v57, v57, v240\n\t" \
    "v_mul_f32 v58, v58, v240\n\t" \
    "v_mul_f32 v59, v59, v240\n\t" \
    "v_mul_f32 v60, v60, v240\n\t" \
    "v_mul_f32 v61, v61, v240\n\t" \
    "v_mul_f32 v62, v62, v240\n\t" \
    "v_mul_f32 v63, v63, v240\n\t" \
    "v_add_f32 v237, v144, v145\n\t" \
    "v_add_f32 v237, v237, v146\n\t" \
    "v_add_f32 v237, v237, v147\n\t" \
    "v_add_f32 v237, v237, v148\n\t" \
    "v_add_f32 v237, v237, v149\n\t" \
    "v_add_f32 v237, v237, v150\n\t" \
    "v_add_f32 v237, v237, v151\n\t" \
    "v_add_f32 v237, v237, v152\n\t" \
    "v_add_f32 v237, v237, v153\n\t" \
    "v_add_f32 v237, v237, v154\n\t" \
    "v_add_f32 v237, v237, v155\n\t" \
    "v_add_f32 v237, v237, v156\n\t" \
    "v_add_f32 v237, v237, v157\n\t" \
    "v_add_f32 v237, v237, v158\n\t" \
    "v_add_f32 v237, v237, v159\n\t" \
    "v_cvt_pk_bf16_f32 v160, v144, v145\n\t" \
    "v_cvt_pk_bf16_f32 v161, v146, v147\n\t" \
    "v_cvt_pk_bf16_f32 v162, v148, v149\n\t" \
    "v_cvt_pk_bf16_f32 v163, v150, v151\n\t" \
    "v_cvt_pk_bf16_f32 v164, v152, v153\n\t" \
    "v_cvt_pk_bf16_f32 v165, v154, v155\n\t" \
    "v_cvt_pk_bf16_f32 v166, v156, v157\n\t" \
    "v_cvt_pk_bf16_f32 v167, v158, v159\n\t" \
    "s_nop 1\n\t" \
    "s_branch BACKF_%=\n\t" \
    "RAREG_%=:\n\t" \
    "s_nop 15\n\t" \
    "v_max3_f32 v239, v112, v113, v114\n\t" \
    "v_max3_f32 v239, v239, v115, v116\n\t" \
    "v_max3_f32 v239, v239, v117, v118\n\t" \
    "v_max3_f32 v239, v239, v119, v120\n\t" \
    "v_max3_f32 v239, v239, v121, v122\n\t" \
    "v_max3_f32 v239, v239, v123, v124\n\t" \
    "v_max3_f32 v239, v239, v125, v126\n\t" \
    "v_max_f32 v239, v239, v127\n\t" \
    "v_max_f32 v239, 0, v239\n\t" \
    "v_mov_b32 v240, v239\n\t" \
    "s_nop 1\n\t" \
    "v_permlane32_swap_b32 v239, v240\n\t" \
    "s_nop 1\n\t" \
    "v_max_f32 v239, v239, v240\n\t" \
    "v_add_f32 v238, v238, v239\n\t" \
    "v_exp_f32 v240, -v239\n\t" \
    "v_sub_f32 v241, 0, v238\n\t" \
    "v_sub_f32 v112, v112, v239\n\t" \
    "v_mov_b32 v96, v241\n\t" \
    "v_sub_f32 v113, v113, v239\n\t" \
    "v_mov_b32 v97, v241\n\t" \
    "v_sub_f32 v114, v114, v239\n\t" \
    "v_mov_b32 v98, v241\n\t" \
    "v_sub_f32 v115, v115, v239\n\t" \
    "v_mov_b32 v99, v241\n\t" \
    "v_sub_f32 v116, v116, v239\n\t" \
    "v_mov_b32 v100, v241\n\t" \
    "v_sub_f32 v117, v117, v239\n\t" \
    "v_mov_b32 v101, v241\n\t" \
    "v_sub_f32 v118, v118, v239\n\t" \
    "v_mov_b32 v102, v241\n\t" \
    "v_sub_f32 v119, v119, v239\n\t" \
    "v_mov_b32 v103, v241\n\t" \
    "v_sub_f32 v120, v120, v239\n\t" \
    "v_mov_b32 v104, v241\n\t" \
    "v_sub_f32 v121, v121, v239\n\t" \
    "v_mov_b32 v105, v241\n\t" \
    "v_sub_f32 v122, v122, v239\n\t" \
    "v_mov_b32 v106, v241\n\t" \
    "v_sub_f32 v123, v123, v239\n\t" \
    "v_mov_b32 v107, v241\n\t" \
    "v_sub_f32 v124, v124, v239\n\t" \
    "v_mov_b32 v108, v241\n\t" \
    "v_sub_f32 v125, v125, v239\n\t" \
    "v_mov_b32 v109, v241\n\t" \
    "v_sub_f32 v126, v126, v239\n\t" \
    "v_mov_b32 v110, v241\n\t" \
    "v_sub_f32 v127, v127, v239\n\t" \
    "v_mov_b32 v111, v241\n\t" \
    "v_exp_f32 v144, v112\n\t" \
    "v_exp_f32 v145, v113\n\t" \
    "v_exp_f32 v146, v114\n\t" \
    "v_exp_f32 v147, v115\n\t" \
    "v_exp_f32 v148, v116\n\t" \
    "v_exp_f32 v149, v117\n\t" \
    "v_exp_f32 v150, v118\n\t" \
    "v_exp_f32 v151, v119\n\t" \
    "v_exp_f32 v152, v120\n\t" \
    "v_exp_f32 v153, v121\n\t" \
    "v_exp_f32 v154, v122\n\t" \
    "v_exp_f32 v155, v123\n\t" \
    "v_exp_f32 v156, v124\n\t" \
    "v_exp_f32 v157, v125\n\t" \
    "v_exp_f32 v158, v126\n\t" \
    "v_exp_f32 v159, v127\n\t" \
    "v_mul_f32 v236, v236, v240\n\t" \
    "v_mul_f32 v0, v0, v240\n\t" \
    "v_mul_f32 v1, v1, v240\n\t" \
    "v_mul_f32 v2, v2, v240\n\t" \
    "v_mul_f32 v3, v3, v240\n\t" \
    "v_mul_f32 v4, v4, v240\n\t" \
    "v_mul_f32 v5, v5, v240\n\t" \
    "v_mul_f32 v6, v6, v240\n\t" \
    "v_mul_f32 v7, v7, v240\n\t" \
    "v_mul_f32 v8, v8, v240\n\t" \
    "v_mul_f32 v9, v9, v240\n\t" \
    "v_mul_f32 v10, v10, v240\n\t" \
    "v_mul_f32 v11, v11, v240\n\t" \
    "v_mul_f32 v12, v12, v240\n\t" \
    "v_mul_f32 v13, v13, v240\n\t" \
    "v_mul_f32 v14, v14, v240\n\t" \
    "v_mul_f32 v15, v15, v240\n\t" \
    "v_mul_f32 v16, v16, v240\n\t" \
    "v_mul_f32 v17, v17, v240\n\t" \
    "v_mul_f32 v18, v18, v240\n\t" \
    "v_mul_f32 v19, v19, v240\n\t" \
    "v_mul_f32 v20, v20, v240\n\t" \
    "v_mul_f32 v21, v21, v240\n\t" \
    "v_mul_f32 v22, v22, v240\n\t" \
    "v_mul_f32 v23, v23, v240\n\t" \
    "v_mul_f32 v24, v24, v240\n\t" \
    "v_mul_f32 v25, v25, v240\n\t" \
    "v_mul_f32 v26, v26, v240\n\t" \
    "v_mul_f32 v27, v27, v240\n\t" \
    "v_mul_f32 v28, v28, v240\n\t" \
    "v_mul_f32 v29, v29, v240\n\t" \
    "v_mul_f32 v30, v30, v240\n\t" \
    "v_mul_f32 v31, v31, v240\n\t" \
    "v_mul_f32 v32, v32, v240\n\t" \
    "v_mul_f32 v33, v33, v240\n\t" \
    "v_mul_f32 v34, v34, v240\n\t" \
    "v_mul_f32 v35, v35, v240\n\t" \
    "v_mul_f32 v36, v36, v240\n\t" \
    "v_mul_f32 v37, v37, v240\n\t" \
    "v_mul_f32 v38, v38, v240\n\t" \
    "v_mul_f32 v39, v39, v240\n\t" \
    "v_mul_f32 v40, v40, v240\n\t" \
    "v_mul_f32 v41, v41, v240\n\t" \
    "v_mul_f32 v42, v42, v240\n\t" \
    "v_mul_f32 v43, v43, v240\n\t" \
    "v_mul_f32 v44, v44, v240\n\t" \
    "v_mul_f32 v45, v45, v240\n\t" \
    "v_mul_f32 v46, v46, v240\n\t" \
    "v_mul_f32 v47, v47, v240\n\t" \
    "v_mul_f32 v48, v48, v240\n\t" \
    "v_mul_f32 v49, v49, v240\n\t" \
    "v_mul_f32 v50, v50, v240\n\t" \
    "v_mul_f32 v51, v51, v240\n\t" \
    "v_mul_f32 v52, v52, v240\n\t" \
    "v_mul_f32 v53, v53, v240\n\t" \
    "v_mul_f32 v54, v54, v240\n\t" \
    "v_mul_f32 v55, v55, v240\n\t" \
    "v_mul_f32 v56, v56, v240\n\t" \
    "v_mul_f32 v57, v57, v240\n\t" \
    "v_mul_f32 v58, v58, v240\n\t" \
    "v_mul_f32 v59, v59, v240\n\t" \
    "v_mul_f32 v60, v60, v240\n\t" \
    "v_mul_f32 v61, v61, v240\n\t" \
    "v_mul_f32 v62, v62, v240\n\t" \
    "v_mul_f32 v63, v63, v240\n\t" \
    "v_add_f32 v237, v144, v145\n\t" \
    "v_add_f32 v237, v237, v146\n\t" \
    "v_add_f32 v237, v237, v147\n\t" \
    "v_add_f32 v237, v237, v148\n\t" \
    "v_add_f32 v237, v237, v149\n\t" \
    "v_add_f32 v237, v237, v150\n\t" \
    "v_add_f32 v237, v237, v151\n\t" \
    "v_add_f32 v237, v237, v152\n\t" \
    "v_add_f32 v237, v237, v153\n\t" \
    "v_add_f32 v237, v237, v154\n\t" \
    "v_add_f32 v237, v237, v155\n\t" \
    "v_add_f32 v237, v237, v156\n\t" \
    "v_add_f32 v237, v237, v157\n\t" \
    "v_add_f32 v237, v237, v158\n\t" \
    "v_add_f32 v237, v237, v159\n\t" \
    "v_cvt_pk_bf16_f32 v160, v144, v145\n\t" \
    "v_cvt_pk_bf16_f32 v161, v146, v147\n\t" \
    "v_cvt_pk_bf16_f32 v162, v148, v149\n\t" \
    "v_cvt_pk_bf16_f32 v163, v150, v151\n\t" \
    "v_cvt_pk_bf16_f32 v164, v152, v153\n\t" \
    "v_cvt_pk_bf16_f32 v165, v154, v155\n\t" \
    "v_cvt_pk_bf16_f32 v166, v156, v157\n\t" \
    "v_cvt_pk_bf16_f32 v167, v158, v159\n\t" \
    "s_nop 1\n\t" \
    "s_branch BACKG_%=\n\t" \
    "END_%=:\n\t" \
    "s_waitcnt lgkmcnt(0)\n\t" \
    "s_nop 15\n\t"
#define MX3(a, b, c) __builtin_fmaxf(__builtin_fmaxf((a), (b)), (c))
DI float max16(const f32x16& p) {
    float a = MX3(p[0], p[1], p[2]), b = MX3(p[3], p[4], p[5]);
    a = MX3(a, p[6], p[7]); b = MX3(b, p[8], p[9]); a = MX3(a, p[10], p[11]); b = MX3(b, p[12], p[13]); a = MX3(a, p[14], p[15]);
    return __builtin_fmaxf(a, b);
}
DI float halfmax(float m) { auto rr = __builtin_amdgcn_permlane32_swap(__float_as_uint(m), __float_as_uint(m), false, false); return __builtin_fmaxf(__uint_as_float(rr[0]), __uint_as_float(rr[1])); }
DI void cmask(f32x16& p, int r32, int hi) {
#pragma unroll
    for (int r = 0; r < 16; ++r) { const int kv = 4 * hi + (r & 3) + 8 * (r >> 2); if (kv > r32) p[r] = -INFINITY; }
}
#define WAITBAR(N) asm volatile("s_waitcnt vmcnt(" #N ")\n\ts_barrier" ::: "memory")
typedef unsigned u32x16 __attribute__((ext_vector_type(16)));

__global__ __launch_bounds__(512, 2) void k2_attn(const char* __restrict__ Q, const char* __restrict__ K, const char* __restrict__ VT, char* __restrict__ CTX) {
    extern __shared__ __attribute__((aligned(16))) unsigned char smem[];
    const lds_cptr lds = (lds_cptr)smem;
    const int tid = threadIdx.x, lane = tid & 63, r32 = lane & 31, hi = lane >> 5, wid = __builtin_amdgcn_readfirstlane(tid >> 6);
    const int xcd = blockIdx.x & 7, idx = blockIdx.x >> 3, h = xcd * 4 + (idx >> 3), ib = idx & 7;
    const int G = wid < 4 ? 4 * ib + wid : 63 - 4 * ib - (wid - 4);
    const int NT = 32 - 2 * ib;
    const char* Kh = K + (size_t)h * 32 * SLOT; const char* Vh = VT + (size_t)h * 32 * SLOT;
    const unsigned lds0 = (unsigned)(uintptr_t)smem;
    const unsigned dmaw = (unsigned)(wid - 4) >> 31;
    const unsigned dvo = (wid & 3) * 4096 + lane * 16, kdst = lds0 + LDS_K + (wid & 3) * 4096, vdst = lds0 + LDS_V + (wid & 3) * 4096;
#define DMA_T(base, dst, t, sl) do { const char* g_ = (base) + (size_t)(t) * SLOT; const unsigned l_ = (unsigned)__builtin_amdgcn_readfirstlane((dst) + ((sl) & 3) * SLOT); glds2(g_, dvo, l_); glds2(g_ + 2048, dvo, l_ + 2048); } while (0)
#define DMA_K(t, sl) DMA_T(Kh, kdst, t, sl)
#define DMA_V(t, sl) DMA_T(Vh, vdst, t, sl)
#define DMA_STEP(s) do { if (dmaw) { DMA_K(min((s) + 3, NT - 1), (s) + 3); DMA_V(min((s) + 2, NT - 1), (s) + 2); } } while (0)
    STAMP(2, 0);
    u32x16 qa, qb;
    { const char* qp = Q + ((size_t)h * 32 + (G >> 1)) * SLOT; const unsigned qo = hi * 1024 + ((G & 1) * 32 + r32) * 16;
      u32x4 q[8];
#define QLD(d) asm volatile("global_load_dwordx4 %0, %1, %2 offset:%c3" : "=&v"(q[d]) : "v"(qo + ((d) >> 1) * 4096), "s"(qp), "i"(((d) & 1) * 2048) : "memory")
      QLD(0); QLD(1); QLD(2); QLD(3); QLD(4); QLD(5); QLD(6); QLD(7);
#undef QLD
      if (dmaw) { DMA_K(0, 0); DMA_V(0, 0); DMA_K(1, 1); DMA_K(2, 2); DMA_V(1, 1); } else WAIT_V(0);
      asm volatile("s_waitcnt vmcnt(8)\n\ts_barrier" : "+v"(q[0]), "+v"(q[1]), "+v"(q[2]), "+v"(q[3]), "+v"(q[4]), "+v"(q[5]), "+v"(q[6]), "+v"(q[7]) :: "memory");
#pragma unroll
      for (int i = 0; i < 4; ++i)
#pragma unroll
          for (int e = 0; e < 4; ++e) { qa[4 * i + e] = q[i][e]; qb[4 * i + e] = q[4 + i][e]; }
    }
    STAMP(2, 1);
    const unsigned kp0 = lds0 + LDS_K + hi * 1024 + r32 * 16, vp0 = lds0 + LDS_V + hi * 2048 + r32 * 16;
    f32x16 o0 = f32x16{}, o1 = f32x16{}, o2 = f32x16{}, o3 = f32x16{}, negm, cA, cB, P = f32x16{};
    u32x16 kfa, kfb;
    float l_reg = 0.f, mhat;
    {
        f32x16 s = f32x16{};
#pragma unroll
        for (int j = 0; j < 8; ++j) {
            const bf16x8 kf = lds_rd(lds + LDS_K + hi * 1024 + r32 * 16 + j * 2048); const u32x4 kw = __builtin_bit_cast(u32x4, kf);
#pragma unroll
            for (int e = 0; e < 4; ++e) { if (j < 4) kfa[4 * j + e] = kw[e]; else kfb[4 * (j - 4) + e] = kw[e]; }
            const u32x4 qw = j < 4 ? (u32x4){qa[4 * j], qa[4 * j + 1], qa[4 * j + 2], qa[4 * j + 3]} : (u32x4){qb[4 * j - 16], qb[4 * j - 15], qb[4 * j - 14], qb[4 * j - 13]};
            s = MFMA32(kf, __builtin_bit_cast(bf16x8, qw), s);
        }
        if (G == 0) cmask(s, r32, hi);
        mhat = halfmax(max16(s));
#pragma unroll
        for (int r = 0; r < 16; ++r) { negm[r] = -mhat; cB[r] = -INFINITY; cA[r] = -INFINITY; }
    }
    const float thr = 65536.0f;
    int t = 0; unsigned soff = 0;
    unsigned vk = kp0, vkn = kp0 + SLOT, vv = vp0, vvp = vp0;
#define K2_STATE [o0] "+&{v[0:15]}"(o0), [o1] "+&{v[16:31]}"(o1), [o2] "+&{v[32:47]}"(o2), [o3] "+&{v[48:63]}"(o3), [negm] "+&{v[96:111]}"(negm), [ca] "+&{v[112:127]}"(cA), [cb] "+&{v[128:143]}"(cB), \
        [p] "+&{v[144:159]}"(P), [kfa] "+&{v[168:183]}"(kfa), [kfb] "+&{v[184:199]}"(kfb), [vk] "+&{v232}"(vk), [vkn] "+&{v233}"(vkn), [vv] "+&{v234}"(vv), [vvp] "+&{v235}"(vvp), [l] "+&{v236}"(l_reg), [mh] "+&{v238}"(mhat)
#define K2_IN [qa] "{v[64:79]}"(qa), [qb] "{v[80:95]}"(qb), [thr] "s"(thr)
#define K2_CLOB "v160", "v161", "v162", "v163", "v164", "v165", "v166", "v167", "v200", "v201", "v202", "v203", "v204", "v205", "v206", "v207", "v208", "v209", "v210", "v211", "v212", "v213", "v214", "v215", \
        "v216", "v217", "v218", "v219", "v220", "v221", "v222", "v223", "v224", "v225", "v226", "v227", "v228", "v229", "v230", "v231", "v237", "v239", "v240", "v241", "vcc", "scc", "memory"
    const int nloop = G >> 1;
    asm volatile(K2_ASM : K2_STATE, [t] "+&s"(t), [soff] "+&s"(soff)
                 : K2_IN, [n] "s"(nloop), [odd] "s"(G & 1), [dmaw] "s"(dmaw), [ntm1] "s"(NT - 1), [khl] "s"((unsigned)(uintptr_t)Kh), [khh] "s"((unsigned)((uintptr_t)Kh >> 32)),
                   [vhl] "s"((unsigned)(uintptr_t)Vh), [vhh] "s"((unsigned)((uintptr_t)Vh >> 32)), [kdst] "s"(kdst), [vdst] "s"(vdst), [dvo] "v"(dvo), [kp0] "v"(kp0), [vp0] "v"(vp0), [dq] "v"(r32 - 4 * hi)
                 : K2_CLOB, "s60", "s61", "s62", "s63");
    STAMP(2, 2);
    {
        auto rr = __builtin_amdgcn_permlane32_swap(__float_as_uint(l_reg), __float_as_uint(l_reg), false, false);
        const float inv = 1.0f / (__uint_as_float(rr[0]) + __uint_as_float(rr[1]));
        char* op = CTX + ((size_t)((G >> 2) * 64 + 2 * h) * 8 + hi) * 2048 + ((G & 3) * 32 + r32) * 16;
#define K2_ST(o, db) do { _Pragma("unroll") for (int g = 0; g < 4; g += 2) { \
            const unsigned x0 = cvtpk(o[4 * g] * inv, o[4 * g + 1] * inv), x1 = cvtpk(o[4 * g + 2] * inv, o[4 * g + 3] * inv); \
            const unsigned y0 = cvtpk(o[4 * g + 4] * inv, o[4 * g + 5] * inv), y1 = cvtpk(o[4 * g + 6] * inv, o[4 * g + 7] * inv); \
            const auto r0 = __builtin_amdgcn_permlane32_swap(x0, y0, false, false), r1 = __builtin_amdgcn_permlane32_swap(x1, y1, false, false); \
            ST16(op + (size_t)(((db) >> 1) * 8 + 4 * ((db) & 1) + g) * 2048, ((u32x4){r0[0], r1[0], r0[1], r1[1]})); } } while (0)
        K2_ST(o0, 0); K2_ST(o1, 1); K2_ST(o2, 2); K2_ST(o3, 3);
    }
    WAITBAR(16); ++t;
    if (t < NT) { DMA_STEP(t); WAITBAR(16); ++t;
        for (; t < NT; ++t) { DMA_STEP(t); WAITBAR(8); } }
    STAMP(2, 3);
    WAIT_V(0);
    STAMP(2, 4);
}
}

namespace k3 {
constexpr int STG = 49152, LDS_BYTES = 3 * STG;
typedef __attribute__((address_space(1))) unsigned gu32;
#define K3_ASM \
    "s_mov_b64 s[60:61], %[a1]\n\t" \
    "s_mov_b64 s[62:63], %[b1]\n\t" \
    "s_mov_b32 s67, %[nl]\n\t" \
    "s_mov_b32 s68, 0\n\t" \
    "s_add_u32 m0, %[mA], s68\n\t" \
    "s_add_u32 s66, %[mB], s68\n\t" \
    "global_load_lds_dwordx4 %[va], s[60:61] offset:0\n\t" \
    "global_load_lds_dwordx4 %[va], s[60:61] offset:1024\n\t" \
    "global_load_lds_dwordx4 %[va], s[60:61] offset:2048\n\t" \
    "global_load_lds_dwordx4 %[va], s[60:61] offset:3072\n\t" \
    "s_mov_b32 m0, s66\n\t" \
    "s_add_u32 s64, s62, 0x10000\n\t" \
    "s_addc_u32 s65, s63, 0\n\t" \
    "global_load_lds_dwordx4 %[vb], s[62:63]\n\t" \
    "s_add_u32 m0, s66, 8192\n\t" \
    "s_add_u32 s60, s60, 0x4000\n\t" \
    "global_load_lds_dwordx4 %[vb], s[64:65]\n\t" \
    "s_addc_u32 s61, s61, 0\n\t" \
    "s_add_u32 s62, s62, 0x20000\n\t" \
    "s_addc_u32 s63, s63, 0\n\t" \
    "s_mov_b32 s68, 49152\n\t" \
    "s_add_u32 m0, %[mA], s68\n\t" \
    "s_add_u32 s66, %[mB], s68\n\t" \
    "global_load_lds_dwordx4 %[va], s[60:61] offset:0\n\t" \
    "global_load_lds_dwordx4 %[va], s[60:61] offset:1024\n\t" \
    "global_load_lds_dwordx4 %[va], s[60:61] offset:2048\n\t" \
    "global_load_lds_dwordx4 %[va], s[60:61] offset:3072\n\t" \
    "s_mov_b32 m0, s66\n\t" \
    "s_add_u32 s64, s62, 0x10000\n\t" \
    "s_addc_u32 s65, s63, 0\n\t" \
    "global_load_lds_dwordx4 %[vb], s[62:63]\n\t" \
    "s_add_u32 m0, s66, 8192\n\t" \
    "s_add_u32 s60, s60, 0x4000\n\t" \
    "global_load_lds_dwordx4 %[vb], s[64:65]\n\t" \
    "s_addc_u32 s61, s61, 0\n\t" \
    "s_add_u32 s62, s62, 0x20000\n\t" \
    "s_addc_u32 s63, s63, 0\n\t" \
    "s_mov_b32 s68, 98304\n\t" \
    "s_add_u32 m0, %[mA], s68\n\t" \
    "s_add_u32 s66, %[mB], s68\n\t" \
    "global_load_lds_dwordx4 %[va], s[60:61] offset:0\n\t" \
    "global_load_lds_dwordx4 %[va], s[60:61] offset:1024\n\t" \
    "global_load_lds_dwordx4 %[va], s[60:61] offset:2048\n\t" \
    "global_load_lds_dwordx4 %[va], s[60:61] offset:3072\n\t" \
    "s_mov_b32 m0, s66\n\t" \
    "s_add_u32 s64, s62, 0x10000\n\t" \
    "s_addc_u32 s65, s63, 0\n\t" \
    "global_load_lds_dwordx4 %[vb], s[62:63]\n\t" \
    "s_add_u32 m0, s66, 8192\n\t" \
    "s_add_u32 s60, s60, 0x4000\n\t" \
    "global_load_lds_dwordx4 %[vb], s[64:65]\n\t" \
    "s_addc_u32 s61, s61, 0\n\t" \
    "s_add_u32 s62, s62, 0x20000\n\t" \
    "s_addc_u32 s63, s63, 0\n\t" \
    "s_mov_b32 s68, 0\n\t" \
    "s_mov_b32 s69, 98304\n\t" \
    "v_mov_b32 v128, v134\n\t" \
    "v_mov_b32 v129, v135\n\t" \
    "v_mov_b32 v130, v136\n\t" \
    "v_add_u32 v131, 49152, v134\n\t" \
    "v_add_u32 v132, 49152, v135\n\t" \
    "v_add_u32 v133, 49152, v136\n\t" \
    "s_waitcnt vmcnt(12)\n\t" \
    "s_barrier\n\t" \
    "ds_read_b128 v[64:67], v128 offset:0\n\t" \
    "ds_read_b64_tr_b16 v[80:81], v129 offset:0\n\t" \
    "ds_read_b64_tr_b16 v[82:83], v129 offset:256\n\t" \
    "ds_read_b64_tr_b16 v[84:85], v130 offset:0\n\t" \
    "ds_read_b64_tr_b16 v[86:87], v130 offset:256\n\t" \
    "ds_read_b128 v[68:71], v128 offset:256\n\t" \
    "ds_read_b64_tr_b16 v[88:89], v129 offset:512\n\t" \
    "ds_read_b64_tr_b16 v[90:91], v129 offset:768\n\t" \
    "ds_read_b64_tr_b16 v[92:93], v130 offset:512\n\t" \
    "ds_read_b64_tr_b16 v[94:95], v130 offset:768\n\t" \
    "ds_read_b128 v[72:75], v128 offset:512\n\t" \
    "ds_read_b128 v[76:79], v128 offset:768\n\t" \
    "LOOP_%=:\n\t" \
    "s_add_u32 m0, %[mA], s68\n\t" \
    "s_add_u32 s66, %[mB], s68\n\t" \
    "global_load_lds_dwordx4 %[va], s[60:61] offset:0\n\t" \
    "global_load_lds_dwordx4 %[va], s[60:61] offset:1024\n\t" \
    "global_load_lds_dwordx4 %[va], s[60:61] offset:2048\n\t" \
    "global_load_lds_dwordx4 %[va], s[60:61] offset:3072\n\t" \
    "s_mov_b32 m0, s66\n\t" \
    "s_add_u32 s64, s62, 0x10000\n\t" \
    "s_addc_u32 s65, s63, 0\n\t" \
    "global_load_lds_dwordx4 %[vb], s[62:63]\n\t" \
    "s_add_u32 m0, s66, 8192\n\t" \
    "s_add_u32 s60, s60, 0x4000\n\t" \
    "global_load_lds_dwordx4 %[vb], s[64:65]\n\t" \
    "s_addc_u32 s61, s61, 0\n\t" \
    "s_add_u32 s62, s62, 0x20000\n\t" \
    "s_addc_u32 s63, s63, 0\n\t" \
    "v_mfma_f32_16x16x32_bf16 v[0:3], v[112:115], v[96:99], v[0:3]\n\t" \
    "ds_read_b128 v[64:67], v131 offset:0\n\t" \
    "ds_read_b64_tr_b16 v[80:81], v132 offset:0\n\t" \
    "v_mfma_f32_16x16x32_bf16 v[4:7], v[116:119], v[96:99], v[4:7]\n\t" \
    "ds_read_b64_tr_b16 v[82:83], v132 offset:256\n\t" \
    "ds_read_b64_tr_b16 v[84:85], v133 offset:0\n\t" \
    "v_mfma_f32_16x16x32_bf16 v[8:11], v[120:123], v[96:99], v[8:11]\n\t" \
    "ds_read_b64_tr_b16 v[86:87], v133 offset:256\n\t" \
    "ds_read_b128 v[68:71], v131 offset:256\n\t" \
    "v_mfma_f32_16x16x32_bf16 v[12:15], v[124:127], v[96:99], v[12:15]\n\t" \
    "ds_read_b64_tr_b16 v[88:89], v132 offset:512\n\t" \
    "ds_read_b64_tr_b16 v[90:91], v132 offset:768\n\t" \
    "v_mfma_f32_16x16x32_bf16 v[16:19], v[112:115], v[100:103], v[16:19]\n\t" \
    "ds_read_b64_tr_b16 v[92:93], v133 offset:512\n\t" \
    "ds_read_b64_tr_b16 v[94:95], v133 offset:768\n\t" \
    "v_mfma_f32_16x16x32_bf16 v[20:23], v[116:119], v[100:103], v[20:23]\n\t" \
    "ds_read_b128 v[72:75], v131 offset:512\n\t" \
    "ds_read_b128 v[76:79], v131 offset:768\n\t" \
    "v_mfma_f32_16x16x32_bf16 v[24:27], v[120:123], v[100:103], v[24:27]\n\t" \
    "s_add_u32 m0, %[mA], s68\n\t" \
    "s_add_u32 s66, %[mB], s68\n\t" \
    "global_load_lds_dwordx4 %[va], s[60:61] offset:0\n\t" \
    "v_mfma_f32_16x16x32_bf16 v[28:31], v[124:127], v[100:103], v[28:31]\n\t" \
    "global_load_lds_dwordx4 %[va], s[60:61] offset:1024\n\t" \
    "global_load_lds_dwordx4 %[va], s[60:61] offset:2048\n\t" \
    "global_load_lds_dwordx4 %[va], s[60:61] offset:3072\n\t" \
    "v_mfma_f32_16x16x32_bf16 v[32:35], v[112:115], v[104:107], v[32:35]\n\t" \
    "s_mov_b32 m0, s66\n\t" \
    "s_add_u32 s64, s62, 0x10000\n\t" \
    "s_addc_u32 s65, s63, 0\n\t" \
    "v_mfma_f32_16x16x32_bf16 v[36:39], v[116:119], v[104:107], v[36:39]\n\t" \
    "global_load_lds_dwordx4 %[vb], s[62:63]\n\t" \
    "s_add_u32 m0, s66, 8192\n\t" \
    "s_add_u32 s60, s60, 0x4000\n\t" \
    "v_mfma_f32_16x16x32_bf16 v[40:43], v[120:123], v[104:107], v[40:43]\n\t" \
    "global_load_lds_dwordx4 %[vb], s[64:65]\n\t" \
    "s_addc_u32 s61, s61, 0\n\t" \
    "s_add_u32 s62, s62, 0x20000\n\t" \
    "v_mfma_f32_16x16x32_bf16 v[44:47], v[124:127], v[104:107], v[44:47]\n\t" \
    "s_addc_u32 s63, s63, 0\n\t" \
    "v_mov_b32 v128, v131\n\t" \
    "v_mov_b32 v129, v132\n\t" \
    "v_mfma_f32_16x16x32_bf16 v[48:51], v[112:115], v[108:111], v[48:51]\n\t" \
    "v_mov_b32 v130, v133\n\t" \
    "v_add_u32 v131, s69, v134\n\t" \
    "v_add_u32 v132, s69, v135\n\t" \
    "v_add_u32 v133, s69, v136\n\t" \
    "v_mfma_f32_16x16x32_bf16 v[52:55], v[116:119], v[108:111], v[52:55]\n\t" \
    "s_add_u32 s68, s68, 49152\n\t" \
    "s_cmp_eq_u32 s68, 147456\n\t" \
    "s_cselect_b32 s68, 0, s68\n\t" \
    "s_add_u32 s69, s69, 49152\n\t" \
    "v_mfma_f32_16x16x32_bf16 v[56:59], v[120:123], v[108:111], v[56:59]\n\t" \
    "s_cmp_eq_u32 s69, 147456\n\t" \
    "s_cselect_b32 s69, 0, s69\n\t" \
    "v_mfma_f32_16x16x32_bf16 v[60:63], v[124:127], v[108:111], v[60:63]\n\t" \
    "s_sub_u32 s67, s67, 1\n\t" \
    "s_cmp_lg_u32 s67, 0\n\t" \
    "s_cbranch_scc1 LOOP_%=\n\t" \
    "s_waitcnt lgkmcnt(0)\n\t" \
    "v_mfma_f32_16x16x32_bf16 v[0:3], v[80:83], v[64:67], v[0:3]\n\t" \
    "ds_read_b128 v[96:99], v128 offset:8192\n\t" \
    "ds_read_b64_tr_b16 v[112:113], v129 offset:8192\n\t" \
    "v_mfma_f32_16x16x32_bf16 v[4:7], v[84:87], v[64:67], v[4:7]\n\t" \
    "ds_read_b64_tr_b16 v[114:115], v129 offset:8448\n\t" \
    "ds_read_b64_tr_b16 v[116:117], v130 offset:8192\n\t" \
    "v_mfma_f32_16x16x32_bf16 v[8:11], v[88:91], v[64:67], v[8:11]\n\t" \
    "ds_read_b64_tr_b16 v[118:119], v130 offset:8448\n\t" \
    "ds_read_b128 v[100:103], v128 offset:8448\n\t" \
    "v_mfma_f32_16x16x32_bf16 v[12:15], v[92:95], v[64:67], v[12:15]\n\t" \
    "ds_read_b64_tr_b16 v[120:121], v129 offset:8704\n\t" \
    "ds_read_b64_tr_b16 v[122:123], v129 offset:8960\n\t" \
    "v_mfma_f32_16x16x32_bf16 v[16:19], v[80:83], v[68:71], v[16:19]\n\t" \
    "ds_read_b64_tr_b16 v[124:125], v130 offset:8704\n\t" \
    "ds_read_b64_tr_b16 v[126:127], v130 offset:8960\n\t" \
    "v_mfma_f32_16x16x32_bf16 v[20:23], v[84:87], v[68:71], v[20:23]\n\t" \
    "ds_read_b128 v[104:107], v128 offset:8704\n\t" \
    "ds_read_b128 v[108:111], v128 offset:8960\n\t" \
    "v_mfma_f32_16x16x32_bf16 v[24:27], v[88:91], v[68:71], v[24:27]\n\t" \
    "v_mfma_f32_16x16x32_bf16 v[28:31], v[92:95], v[68:71], v[28:31]\n\t" \
    "v_mfma_f32_16x16x32_bf16 v[32:35], v[80:83], v[72:75], v[32:35]\n\t" \
    "v_mfma_f32_16x16x32_bf16 v[36:39], v[84:87], v[72:75], v[36:39]\n\t" \
    "v_mfma_f32_16x16x32_bf16 v[40:43], v[88:91], v[72:75], v[40:43]\n\t" \
    "v_mfma_f32_16x16x32_bf16 v[44:47], v[92:95], v[72:75], v[44:47]\n\t" \
    "v_mfma_f32_16x16x32_bf16 v[48:51], v[80:83], v[76:79], v[48:51]\n\t" \
    "v_mfma_f32_16x16x32_bf16 v[52:55], v[84:87], v[76:79], v[52:55]\n\t" \
    "v_mfma_f32_16x16x32_bf16 v[56:59], v[88:91], v[76:79], v[56:59]\n\t" \
    "v_mfma_f32_16x16x32_bf16 v[60:63], v[92:95], v[76:79], v[60:63]\n\t" \
    "s_waitcnt vmcnt(6) lgkmcnt(0)\n\t" \
    "s_barrier\n\t" \
    "v_mfma_f32_16x16x32_bf16 v[0:3], v[112:115], v[96:99], v[0:3]\n\t" \
    "ds_read_b128 v[64:67], v131 offset:0\n\t" \
    "ds_read_b64_tr_b16 v[80:81], v132 offset:0\n\t" \
    "v_mfma_f32_16x16x32_bf16 v[4:7], v[116:119], v[96:99], v[4:7]\n\t" \
    "ds_read_b64_tr_b16 v[82:83], v132 offset:256\n\t" \
    "ds_read_b64_tr_b16 v[84:85], v133 offset:0\n\t" \
    "v_mfma_f32_16x16x32_bf16 v[8:11], v[120:123], v[96:99], v[8:11]\n\t" \
    "ds_read_b64_tr_b16 v[86:87], v133 offset:256\n\t" \
    "ds_read_b128 v[68:71], v131 offset:256\n\t" \
    "v_mfma_f32_16x16x32_bf16 v[12:15], v[124:127], v[96:99], v[12:15]\n\t" \
    "ds_read_b64_tr_b16 v[88:89], v132 offset:512\n\t" \
    "ds_read_b64_tr_b16 v[90:91], v132 offset:768\n\t" \
    "v_mfma_f32_16x16x32_bf16 v[16:19], v[112:115], v[100:103], v[16:19]\n\t" \
    "ds_read_b64_tr_b16 v[92:93], v133 offset:512\n\t" \
    "ds_read_b64_tr_b16 v[94:95], v133 offset:768\n\t" \
    "v_mfma_f32_16x16x32_bf16 v[20:23], v[116:119], v[100:103], v[20:23]\n\t" \
    "ds_read_b128 v[72:75], v131 offset:512\n\t" \
    "ds_read_b128 v[76:79], v131 offset:768\n\t" \
    "v_mfma_f32_16x16x32_bf16 v[24:27], v[120:123], v[100:103], v[24:27]\n\t" \
    "v_mov_b32 v128, v131\n\t" \
    "v_mov_b32 v129, v132\n\t" \
    "v_mov_b32 v130, v133\n\t" \
    "v_mfma_f32_16x16x32_bf16 v[28:31], v[124:127], v[100:103], v[28:31]\n\t" \
    "v_add_u32 v131, s69, v134\n\t" \
    "v_add_u32 v132, s69, v135\n\t" \
    "v_add_u32 v133, s69, v136\n\t" \
    "v_mfma_f32_16x16x32_bf16 v[32:35], v[112:115], v[104:107], v[32:35]\n\t" \
    "s_add_u32 s68, s68, 49152\n\t" \
    "s_cmp_eq_u32 s68, 147456\n\t" \
    "s_cselect_b32 s68, 0, s68\n\t" \
    "v_mfma_f32_16x16x32_bf16 v[36:39], v[116:119], v[104:107], v[36:39]\n\t" \
    "s_add_u32 s69, s69, 49152\n\t" \
    "s_cmp_eq_u32 s69, 147456\n\t" \
    "s_cselect_b32 s69, 0, s69\n\t" \
    "v_mfma_f32_16x16x32_bf16 v[40:43], v[120:123], v[104:107], v[40:43]\n\t" \
    "v_mfma_f32_16x16x32_bf16 v[44:47], v[124:127], v[104:107], v[44:47]\n\t" \
    "v_mfma_f32_16x16x32_bf16 v[48:51], v[112:115], v[108:111], v[48:51]\n\t" \
    "v_mfma_f32_16x16x32_bf16 v[52:55], v[116:119], v[108:111], v[52:55]\n\t" \
    "v_mfma_f32_16x16x32_bf16 v[56:59], v[120:123], v[108:111], v[56:59]\n\t" \
    "v_mfma_f32_16x16x32_bf16 v[60:63], v[124:127], v[108:111], v[60:63]\n\t" \
    "s_waitcnt lgkmcnt(0)\n\t" \
    "v_mfma_f32_16x16x32_bf16 v[0:3], v[80:83], v[64:67], v[0:3]\n\t" \
    "ds_read_b128 v[96:99], v128 offset:8192\n\t" \
    "ds_read_b64_tr_b16 v[112:113], v129 offset:8192\n\t" \
    "v_mfma_f32_16x16x32_bf16 v[4:7], v[84:87], v[64:67], v[4:7]\n\t" \
    "ds_read_b64_tr_b16 v[114:115], v129 offset:8448\n\t" \
    "ds_read_b64_tr_b16 v[116:117], v130 offset:8192\n\t" \
    "v_mfma_f32_16x16x32_bf16 v[8:11], v[88:91], v[64:67], v[8:11]\n\t" \
    "ds_read_b64_tr_b16 v[118:119], v130 offset:8448\n\t" \
    "ds_read_b128 v[100:103], v128 offset:8448\n\t" \
    "v_mfma_f32_16x16x32_bf16 v[12:15], v[92:95], v[64:67], v[12:15]\n\t" \
    "ds_read_b64_tr_b16 v[120:121], v129 offset:8704\n\t" \
    "ds_read_b64_tr_b16 v[122:123], v129 offset:8960\n\t" \
    "v_mfma_f32_16x16x32_bf16 v[16:19], v[80:83], v[68:71], v[16:19]\n\t" \
    "ds_read_b64_tr_b16 v[124:125], v130 offset:8704\n\t" \
    "ds_read_b64_tr_b16 v[126:127], v130 offset:8960\n\t" \
    "v_mfma_f32_16x16x32_bf16 v[20:23], v[84:87], v[68:71], v[20:23]\n\t" \
    "ds_read_b128 v[104:107], v128 offset:8704\n\t" \
    "ds_read_b128 v[108:111], v128 offset:8960\n\t" \
    "v_mfma_f32_16x16x32_bf16 v[24:27], v[88:91], v[68:71], v[24:27]\n\t" \
    "v_mfma_f32_16x16x32_bf16 v[28:31], v[92:95], v[68:71], v[28:31]\n\t" \
    "v_mfma_f32_16x16x32_bf16 v[32:35], v[80:83], v[72:75], v[32:35]\n\t" \
    "v_mfma_f32_16x16x32_bf16 v[36:39], v[84:87], v[72:75], v[36:39]\n\t" \
    "v_mfma_f32_16x16x32_bf16 v[40:43], v[88:91], v[72:75], v[40:43]\n\t" \
    "v_mfma_f32_16x16x32_bf16 v[44:47], v[92:95], v[72:75], v[44:47]\n\t" \
    "v_mfma_f32_16x16x32_bf16 v[48:51], v[80:83], v[76:79], v[48:51]\n\t" \
    "v_mfma_f32_16x16x32_bf16 v[52:55], v[84:87], v[76:79], v[52:55]\n\t" \
    "v_mfma_f32_16x16x32_bf16 v[56:59], v[88:91], v[76:79], v[56:59]\n\t" \
    "v_mfma_f32_16x16x32_bf16 v[60:63], v[92:95], v[76:79], v[60:63]\n\t" \
    "s_waitcnt vmcnt(0) lgkmcnt(0)\n\t" \
    "s_barrier\n\t" \
    "v_mfma_f32_16x16x32_bf16 v[0:3], v[112:115], v[96:99], v[0:3]\n\t" \
    "ds_read_b128 v[64:67], v131 offset:0\n\t" \
    "ds_read_b64_tr_b16 v[80:81], v132 offset:0\n\t" \
    "v_mfma_f32_16x16x32_bf16 v[4:7], v[116:119], v[96:99], v[4:7]\n\t" \
    "ds_read_b64_tr_b16 v[82:83], v132 offset:256\n\t" \
    "ds_read_b64_tr_b16 v[84:85], v133 offset:0\n\t" \
    "v_mfma_f32_16x16x32_bf16 v[8:11], v[120:123], v[96:99], v[8:11]\n\t" \
    "ds_read_b64_tr_b16 v[86:87], v133 offset:256\n\t" \
    "ds_read_b128 v[68:71], v131 offset:256\n\t" \
    "v_mfma_f32_16x16x32_bf16 v[12:15], v[124:127], v[96:99], v[12:15]\n\t" \
    "ds_read_b64_tr_b16 v[88:89], v132 offset:512\n\t" \
    "ds_read_b64_tr_b16 v[90:91], v132 offset:768\n\t" \
    "v_mfma_f32_16x16x32_bf16 v[16:19], v[112:115], v[100:103], v[16:19]\n\t" \
    "ds_read_b64_tr_b16 v[92:93], v133 offset:512\n\t" \
    "ds_read_b64_tr_b16 v[94:95], v133 offset:768\n\t" \
    "v_mfma_f32_16x16x32_bf16 v[20:23], v[116:119], v[100:103], v[20:23]\n\t" \
    "ds_read_b128 v[72:75], v131 offset:512\n\t" \
    "ds_read_b128 v[76:79], v131 offset:768\n\t" \
    "v_mfma_f32_16x16x32_bf16 v[24:27], v[120:123], v[100:103], v[24:27]\n\t" \
    "v_mov_b32 v128, v131\n\t" \
    "v_mov_b32 v129, v132\n\t" \
    "v_mov_b32 v130, v133\n\t" \
    "v_mfma_f32_16x16x32_bf16 v[28:31], v[124:127], v[100:103], v[28:31]\n\t" \
    "v_add_u32 v131, s69, v134\n\t" \
    "v_add_u32 v132, s69, v135\n\t" \
    "v_add_u32 v133, s69, v136\n\t" \
    "v_mfma_f32_16x16x32_bf16 v[32:35], v[112:115], v[104:107], v[32:35]\n\t" \
    "s_add_u32 s68, s68, 49152\n\t" \
    "s_cmp_eq_u32 s68, 147456\n\t" \
    "s_cselect_b32 s68, 0, s68\n\t" \
    "v_mfma_f32_16x16x32_bf16 v[36:39], v[116:119], v[104:107], v[36:39]\n\t" \
    "s_add_u32 s69, s69, 49152\n\t" \
    "s_cmp_eq_u32 s69, 147456\n\t" \
    "s_cselect_b32 s69, 0, s69\n\t" \
    "v_mfma_f32_16x16x32_bf16 v[40:43], v[120:123], v[104:107], v[40:43]\n\t" \
    "v_mfma_f32_16x16x32_bf16 v[44:47], v[124:127], v[104:107], v[44:47]\n\t" \
    "v_mfma_f32_16x16x32_bf16 v[48:51], v[112:115], v[108:111], v[48:51]\n\t" \
    "v_mfma_f32_16x16x32_bf16 v[52:55], v[116:119], v[108:111], v[52:55]\n\t" \
    "v_mfma_f32_16x16x32_bf16 v[56:59], v[120:123], v[108:111], v[56:59]\n\t" \
    "v_mfma_f32_16x16x32_bf16 v[60:63], v[124:127], v[108:111], v[60:63]\n\t" \
    "s_waitcnt lgkmcnt(0)\n\t" \
    "v_mfma_f32_16x16x32_bf16 v[0:3], v[80:83], v[64:67], v[0:3]\n\t" \
    "ds_read_b128 v[96:99], v128 offset:8192\n\t" \
    "ds_read_b64_tr_b16 v[112:113], v129 offset:8192\n\t" \
    "v_mfma_f32_16x16x32_bf16 v[4:7], v[84:87], v[64:67], v[4:7]\n\t" \
    "ds_read_b64_tr_b16 v[114:115], v129 offset:8448\n\t" \
    "ds_read_b64_tr_b16 v[116:117], v130 offset:8192\n\t" \
    "v_mfma_f32_16x16x32_bf16 v[8:11], v[88:91], v[64:67], v[8:11]\n\t" \
    "ds_read_b64_tr_b16 v[118:119], v130 offset:8448\n\t" \
    "ds_read_b128 v[100:103], v128 offset:8448\n\t" \
    "v_mfma_f32_16x16x32_bf16 v[12:15], v[92:95], v[64:67], v[12:15]\n\t" \
    "ds_read_b64_tr_b16 v[120:121], v129 offset:8704\n\t" \
    "ds_read_b64_tr_b16 v[122:123], v129 offset:8960\n\t" \
    "v_mfma_f32_16x16x32_bf16 v[16:19], v[80:83], v[68:71], v[16:19]\n\t" \
    "ds_read_b64_tr_b16 v[124:125], v130 offset:8704\n\t" \
    "ds_read_b64_tr_b16 v[126:127], v130 offset:8960\n\t" \
    "v_mfma_f32_16x16x32_bf16 v[20:23], v[84:87], v[68:71], v[20:23]\n\t" \
    "ds_read_b128 v[104:107], v128 offset:8704\n\t" \
    "ds_read_b128 v[108:111], v128 offset:8960\n\t" \
    "v_mfma_f32_16x16x32_bf16 v[24:27], v[88:91], v[68:71], v[24:27]\n\t" \
    "v_mfma_f32_16x16x32_bf16 v[28:31], v[92:95], v[68:71], v[28:31]\n\t" \
    "v_mfma_f32_16x16x32_bf16 v[32:35], v[80:83], v[72:75], v[32:35]\n\t" \
    "v_mfma_f32_16x16x32_bf16 v[36:39], v[84:87], v[72:75], v[36:39]\n\t" \
    "v_mfma_f32_16x16x32_bf16 v[40:43], v[88:91], v[72:75], v[40:43]\n\t" \
    "v_mfma_f32_16x16x32_bf16 v[44:47], v[92:95], v[72:75], v[44:47]\n\t" \
    "v_mfma_f32_16x16x32_bf16 v[48:51], v[80:83], v[76:79], v[48:51]\n\t" \
    "v_mfma_f32_16x16x32_bf16 v[52:55], v[84:87], v[76:79], v[52:55]\n\t" \
    "v_mfma_f32_16x16x32_bf16 v[56:59], v[88:91], v[76:79], v[56:59]\n\t" \
    "v_mfma_f32_16x16x32_bf16 v[60:63], v[92:95], v[76:79], v[60:63]\n\t" \
    "s_waitcnt vmcnt(0) lgkmcnt(0)\n\t" \
    "s_barrier\n\t" \
    "v_mfma_f32_16x16x32_bf16 v[0:3], v[112:115], v[96:99], v[0:3]\n\t" \
    "v_mov_b32 v128, v131\n\t" \
    "v_mov_b32 v129, v132\n\t" \
    "v_mfma_f32_16x16x32_bf16 v[4:7], v[116:119], v[96:99], v[4:7]\n\t" \
    "v_mov_b32 v130, v133\n\t" \
    "v_add_u32 v131, s69, v134\n\t" \
    "v_mfma_f32_16x16x32_bf16 v[8:11], v[120:123], v[96:99], v[8:11]\n\t" \
    "v_add_u32 v132, s69, v135\n\t" \
    "v_add_u32 v133, s69, v136\n\t" \
    "v_mfma_f32_16x16x32_bf16 v[12:15], v[124:127], v[96:99], v[12:15]\n\t" \
    "s_add_u32 s68, s68, 49152\n\t" \
    "s_cmp_eq_u32 s68, 147456\n\t" \
    "v_mfma_f32_16x16x32_bf16 v[16:19], v[112:115], v[100:103], v[16:19]\n\t" \
    "s_cselect_b32 s68, 0, s68\n\t" \
    "s_add_u32 s69, s69, 49152\n\t" \
    "v_mfma_f32_16x16x32_bf16 v[20:23], v[116:119], v[100:103], v[20:23]\n\t" \
    "s_cmp_eq_u32 s69, 147456\n\t" \
    "s_cselect_b32 s69, 0, s69\n\t" \
    "v_mfma_f32_16x16x32_bf16 v[24:27], v[120:123], v[100:103], v[24:27]\n\t" \
    "v_mfma_f32_16x16x32_bf16 v[28:31], v[124:127], v[100:103], v[28:31]\n\t" \
    "v_mfma_f32_16x16x32_bf16 v[32:35], v[112:115], v[104:107], v[32:35]\n\t" \
    "v_mfma_f32_16x16x32_bf16 v[36:39], v[116:119], v[104:107], v[36:39]\n\t" \
    "v_mfma_f32_16x16x32_bf16 v[40:43], v[120:123], v[104:107], v[40:43]\n\t" \
    "v_mfma_f32_16x16x32_bf16 v[44:47], v[124:127], v[104:107], v[44:47]\n\t" \
    "v_mfma_f32_16x16x32_bf16 v[48:51], v[112:115], v[108:111], v[48:51]\n\t" \
    "v_mfma_f32_16x16x32_bf16 v[52:55], v[116:119], v[108:111], v[52:55]\n\t" \
    "v_mfma_f32_16x16x32_bf16 v[56:59], v[120:123], v[108:111], v[56:59]\n\t" \
    "v_mfma_f32_16x16x32_bf16 v[60:63], v[124:127], v[108:111], v[60:63]\n\t" \
    "s_nop 15\n\t"
DI u32x4 pack8(const f32x16& c, int n) { return (u32x4){cvtpk(c[4 * n], c[4 * n + 1]), cvtpk(c[4 * n + 2], c[4 * n + 3]), cvtpk(c[4 * n + 4], c[4 * n + 5]), cvtpk(c[4 * n + 6], c[4 * n + 7])}; }
DI void add8(f32x16& c, int n, u32x4 w) {
#pragma unroll
    for (int i = 0; i < 4; ++i) { c[4 * n + 2 * i] += bflo(w[i]); c[4 * n + 2 * i + 1] += bfhi(w[i]); }
}
DI void poll(unsigned* flags, int first, int count, int lane) {
    gu32* f = (gu32*)(flags + (first + (lane % count)) * 32);
    for (int spin = 0; spin < (1 << 20); ++spin) {
        if (__all(__hip_atomic_load(f, __ATOMIC_RELAXED, __HIP_MEMORY_SCOPE_AGENT) == 1u)) break;
        __builtin_amdgcn_s_sleep(1);
    }
}
__global__ __launch_bounds__(512, 2) void k3_out(const char* __restrict__ ctx, const char* __restrict__ H, const bf16_t* __restrict__ wo, const bf16_t* __restrict__ w2,
                                                 const bf16_t* __restrict__ b2, const bf16_t* __restrict__ x, float* part, unsigned* flags, bf16_t* __restrict__ out) {
    extern __shared__ __attribute__((aligned(16))) unsigned char smem[];
    const int tid = threadIdx.x, wid = __builtin_amdgcn_readfirstlane(tid >> 6), lane = tid & 63, wr = wid >> 1, wc = wid & 1, fr = lane & 15, fq = lane >> 4;
    const int xcd = blockIdx.x & 7, idx = blockIdx.x >> 3, sl = xcd >> 1, mt = 4 * (xcd & 1) + (idx >> 3), ntile = idx & 7, tile = mt * 8 + ntile;
    const unsigned lds0 = (unsigned)(uintptr_t)smem;
    const size_t aw = (size_t)(wid & 3) * 4096, bw = (size_t)ntile * 256 + (size_t)(8 * (wid >> 1)) * (DM * 2) + 128 * (wid & 1);
    const int kt0 = sl < 2 ? 32 * sl : 28 * (sl - 2);
    const char* a1 = (sl < 2 ? ctx + (size_t)(2 * mt + (wid >> 2)) * 64 * 16384 : H + (size_t)(2 * mt + (wid >> 2)) * 56 * 16384) + aw + (size_t)kt0 * 16384;
    const char* b1 = (const char*)(sl < 2 ? wo : w2) + bw + (size_t)kt0 * (64 * DM * 2);
    const int nl = (sl < 2 ? 32 : 28) - 3;
    const unsigned va = lane * 16, vb = ((lane & 31) >> 2) * (DM * 2) + (32 * (lane >> 5) + 8 * ((lane & 3) ^ (wid & 2))) * 2;
    const unsigned mA = lds0 + (wid >> 2) * 16384 + (wid & 3) * 4096, mB = lds0 + 32768 + wid * 1024;
    const unsigned ra = lds0 + (wr >> 1) * 16384 + fq * 2048 + ((wr & 1) * 64 + fr) * 16;
    const unsigned rb0 = lds0 + 32768 + fq * 2048 + wc * 1024 + (fr >> 2) * 64 + (fq & 1) * 32 + (fr & 3) * 8, rb1 = rb0 ^ 32;
    f32x16 c0 = f32x16{}, c1 = f32x16{}, c2 = f32x16{}, c3 = f32x16{};
    u32x2 xb[4][4], bb[4];
    if (sl == 2) {
#pragma unroll
        for (int n = 0; n < 4; ++n) { bb[n] = *(const u32x2*)(b2 + ntile * 128 + wc * 64 + fq * 4 + n * 16);
#pragma unroll
            for (int m = 0; m < 4; ++m) xb[m][n] = *(const u32x2*)(x + (size_t)(mt * 256 + wr * 64 + m * 16 + fr) * DM + ntile * 128 + wc * 64 + fq * 4 + n * 16); }
    } else {
#pragma unroll
        for (int n = 0; n < 4; ++n) { bb[n] = (u32x2){0u, 0u};
#pragma unroll
            for (int m = 0; m < 4; ++m) xb[m][n] = (u32x2){0u, 0u}; }
    }
    STAMP(3, 0);
    asm volatile(K3_ASM
                 : "+&{v[0:15]}"(c0), "+&{v[16:31]}"(c1), "+&{v[32:47]}"(c2), "+&{v[48:63]}"(c3)
                 : "{v134}"(ra), "{v135}"(rb0), "{v136}"(rb1),
                   [va] "v"(va), [vb] "v"(vb), [a1] "s"(a1), [b1] "s"(b1), [nl] "s"(nl), [mA] "s"(mA), [mB] "s"(mB)
                 : "v64", "v65", "v66", "v67", "v68", "v69", "v70", "v71", "v72", "v73", "v74", "v75", "v76", "v77", "v78", "v79", "v80", "v81", "v82", "v83", "v84", "v85", "v86", "v87", "v88", "v89", "v90", "v91", "v92", "v93", "v94", "v95", "v96", "v97", "v98", "v99", "v100", "v101", "v102", "v103", "v104", "v105", "v106", "v107", "v108", "v109", "v110", "v111", "v112", "v113", "v114", "v115", "v116", "v117", "v118", "v119", "v120", "v121", "v122", "v123", "v124", "v125", "v126", "v127", "v128", "v129", "v130", "v131", "v132", "v133", "s60", "s61", "s62", "s63", "s64", "s65", "s66", "s67", "s68", "s69", "scc", "memory");
    STAMP(3, 1);
    const auto rs = __builtin_amdgcn_make_buffer_rsrc((void*)((char*)part + (size_t)tile * 4 * 65536), 0, 4 * 65536, 0x00020000);
#define K3_SEND(c, m) do { __builtin_amdgcn_raw_buffer_store_b128(pack8(c, 0), rs, tid * 16 + (m) * 16384 + sl * 65536, 0, 16); __builtin_amdgcn_raw_buffer_store_b128(pack8(c, 2), rs, tid * 16 + (m) * 16384 + 8192 + sl * 65536, 0, 16); } while (0)
#define K3_GET(src, m, p) __builtin_amdgcn_raw_buffer_load_b128(rs, tid * 16 + (m) * 16384 + (p) * 8192, (src) * 65536, 16)
    gu32* myflag = (gu32*)(flags + (tile * 4 + sl) * 32);
    if (sl >= 2) {
#define K3_RES(c, m) do { _Pragma("unroll") for (int n = 0; n < 4; ++n) { \
            c[4 * n] += bflo(xb[m][n][0]) + bflo(bb[n][0]); c[4 * n + 1] += bfhi(xb[m][n][0]) + bfhi(bb[n][0]); c[4 * n + 2] += bflo(xb[m][n][1]) + bflo(bb[n][1]); c[4 * n + 3] += bfhi(xb[m][n][1]) + bfhi(bb[n][1]); } } while (0)
        K3_RES(c0, 0); K3_RES(c1, 1); K3_RES(c2, 2); K3_RES(c3, 3);
        K3_SEND(c0, 0); K3_SEND(c1, 1); K3_SEND(c2, 2); K3_SEND(c3, 3);
        WAIT_V(0);
        __syncthreads();
        if (tid == 0) __hip_atomic_store(myflag, 1u, __ATOMIC_RELAXED, __HIP_MEMORY_SCOPE_AGENT);
        STAMP(3, 2);
        return;
    }
    f32x16 f0, f1;
    if (sl == 0) { K3_SEND(c2, 2); K3_SEND(c3, 3); f0 = c0; f1 = c1; } else { K3_SEND(c0, 0); K3_SEND(c1, 1); f0 = c2; f1 = c3; }
    const int m0 = 2 * sl;
    WAIT_V(0);
    __syncthreads();
    if (tid == 0) __hip_atomic_store(myflag, 1u, __ATOMIC_RELAXED, __HIP_MEMORY_SCOPE_AGENT);
    STAMP(3, 2);
    if (wid == 0) poll(flags, tile * 4, 4, lane);
    asm volatile("" ::: "memory"); BAR(); asm volatile("" ::: "memory");
    STAMP(3, 3);
    u32x4 pa[3][2][2];
#pragma unroll
    for (int k = 0; k < 3; ++k)
#pragma unroll
        for (int m = 0; m < 2; ++m)
#pragma unroll
            for (int p = 0; p < 2; ++p) pa[k][m][p] = K3_GET(k == 0 ? 1 - sl : 1 + k, m0 + m, p);
#pragma unroll
    for (int k = 0; k < 3; ++k) { add8(f0, 0, pa[k][0][0]); add8(f0, 2, pa[k][0][1]); add8(f1, 0, pa[k][1][0]); add8(f1, 2, pa[k][1][1]); }
    const int row = mt * 256 + wr * 64 + m0 * 16 + fr;
    const int cg = ((fq & 1) << 1) | (fq >> 1);
    bf16_t* orow = out + (size_t)row * DM + ntile * 128 + wc * 64 + cg * 8;
#define K3_F4(c, n) (f32x4){c[4 * (n)], c[4 * (n) + 1], c[4 * (n) + 2], c[4 * (n) + 3]}
    *(u32x4*)(orow) = k1::pack_swap16(K3_F4(f0, 0), K3_F4(f0, 1)); *(u32x4*)(orow + 32) = k1::pack_swap16(K3_F4(f0, 2), K3_F4(f0, 3));
    *(u32x4*)(orow + 16 * DM) = k1::pack_swap16(K3_F4(f1, 0), K3_F4(f1, 1)); *(u32x4*)(orow + 16 * DM + 32) = k1::pack_swap16(K3_F4(f1, 2), K3_F4(f1, 3));
    STAMP(3, 4);
}
}

extern "C" void kernel_launch(void* const* d_in, const int* in_sizes, int n_in, void* d_out, int out_size, void* d_ws, size_t ws_size, hipStream_t stream) {
    const bf16_t* x = (const bf16_t*)d_in[0]; const bf16_t* ckv = (const bf16_t*)d_in[1];
    const bf16_t* wq = (const bf16_t*)d_in[2]; const bf16_t* wuk = (const bf16_t*)d_in[3]; const bf16_t* wuv = (const bf16_t*)d_in[4]; const bf16_t* wo = (const bf16_t*)d_in[5];
    const bf16_t* w1 = (const bf16_t*)d_in[7]; const bf16_t* b1 = (const bf16_t*)d_in[8]; const bf16_t* w2 = (const bf16_t*)d_in[9]; const bf16_t* b2 = (const bf16_t*)d_in[10];
    char* ws = (char*)d_ws;
    (void)hipFuncSetAttribute((const void*)k1::k1_proj, hipFuncAttributeMaxDynamicSharedMemorySize, k1::LDS_BYTES);
    (void)hipFuncSetAttribute((const void*)k2::k2_attn, hipFuncAttributeMaxDynamicSharedMemorySize, k2::LDS_BYTES);
    (void)hipFuncSetAttribute((const void*)k3::k3_out, hipFuncAttributeMaxDynamicSharedMemorySize, k3::LDS_BYTES);
    k0_ln<<<SEQ / 4, 256, 0, stream>>>(x, (bf16_t*)(ws + WS_XN), (unsigned*)(ws + WS_FLAG));
    k1::k1_proj<<<256, 512, k1::LDS_BYTES, stream>>>(ckv, ws + WS_XN, wuk, wuv, wq, w1, b1, ws + WS_Q, ws + WS_K, ws + WS_VT, ws + WS_H);
    k2::k2_attn<<<256, 512, k2::LDS_BYTES, stream>>>(ws + WS_Q, ws + WS_K, ws + WS_VT, ws + WS_CTX);
    k3::k3_out<<<256, 512, k3::LDS_BYTES, stream>>>(ws + WS_CTX, ws + WS_H, wo, w2, b2, x, (float*)(ws + WS_PART), (unsigned*)(ws + WS_FLAG), (bf16_t*)d_out);
}
```

```cpp
#define K1_PF 2
#include <hip/hip_runtime.h>
#include <cstdint>

#define STAMP(k, i) do {} while (0)
#define LAS __attribute__((address_space(3)))
#define DI __device__ __forceinline__
typedef unsigned short bf16_t;
typedef short bf16x8 __attribute__((ext_vector_type(8)));
typedef short s16x4 __attribute__((ext_vector_type(4)));
typedef short v4i16_t __attribute__((ext_vector_type(4)));
typedef float f32x2 __attribute__((ext_vector_type(2)));
typedef float f32x4 __attribute__((ext_vector_type(4)));
typedef float f32x16 __attribute__((ext_vector_type(16)));
typedef unsigned u32x2 __attribute__((ext_vector_type(2)));
typedef unsigned u32x4 __attribute__((ext_vector_type(4)));
typedef __bf16 bf16x2_t __attribute__((ext_vector_type(2)));
typedef LAS unsigned char* lds_ptr;
typedef LAS const unsigned char* lds_cptr;

constexpr int SEQ = 2048, DM = 1024, NH = 32, HD = 128, DC = 512, FF = 3584, NQ = NH * HD;
constexpr size_t MiB = 1u << 20;
constexpr size_t WS_FLAG = 0;
constexpr size_t WS_XN = 1 * MiB;
constexpr size_t WS_Q = 8 * MiB;
constexpr size_t WS_K = 24 * MiB;
constexpr size_t WS_VT = 40 * MiB;
constexpr size_t WS_CTX = 56 * MiB;
constexpr size_t WS_H = 72 * MiB;
constexpr size_t WS_PART = 88 * MiB;

DI unsigned cvtpk(float lo, float hi) { f32x2 v = {lo, hi}; bf16x2_t b = __builtin_convertvector(v, bf16x2_t); return __builtin_bit_cast(unsigned, b); }
DI float bflo(unsigned w) { return __uint_as_float(w << 16); }
DI float bfhi(unsigned w) { return __uint_as_float(w & 0xffff0000u); }
DI s16x4 lds_tr(lds_cptr p) { return __builtin_bit_cast(s16x4, __builtin_amdgcn_ds_read_tr16_b64_v4i16((LAS v4i16_t*)p)); }
DI bf16x8 lds_rd(lds_cptr p) { return *(const LAS bf16x8*)p; }
DI bf16x8 lds_rdv(lds_cptr p) { return *(const volatile LAS bf16x8*)p; }
DI bf16x8 cat8(s16x4 a, s16x4 b) { return __builtin_shufflevector(a, b, 0, 1, 2, 3, 4, 5, 6, 7); }
DI void st16_sc1(void* p, u32x4 v) { asm volatile("global_store_dwordx4 %0, %1, off sc1\n\ts_nop 1" :: "v"(p), "v"(v) : "memory"); }
#define ST16(p, v) st16_sc1((void*)(p), v)
#define SBAR() __builtin_amdgcn_sched_barrier(0)
#define PIN(x) asm volatile("" : "+v"(x))
#define WAIT_V(n) asm volatile("s_waitcnt vmcnt(" #n ")" ::: "memory")
#define WAIT_L(n) asm volatile("s_waitcnt lgkmcnt(" #n ")" ::: "memory")
#define BAR() __builtin_amdgcn_s_barrier()
#define MFMA16(a, b, c) __builtin_amdgcn_mfma_f32_16x16x32_bf16(a, b, c, 0, 0, 0)
#define MFMA32(a, b, c) __builtin_amdgcn_mfma_f32_32x32x16_bf16(a, b, c, 0, 0, 0)
DI void glds(const char* sbase, unsigned voff, unsigned ldsaddr) {
    asm volatile("s_mov_b32 m0, %2\n\ts_nop 0\n\tglobal_load_lds_dwordx4 %0, %1" :: "v"(voff), "s"(sbase), "s"(ldsaddr) : "memory");
}
DI void glds2(const char* sbase, unsigned voff, unsigned ldsaddr) {
    asm volatile("s_mov_b32 m0, %2\n\ts_nop 0\n\tglobal_load_lds_dwordx4 %0, %1\n\tglobal_load_lds_dwordx4 %0, %1 offset:1024" :: "v"(voff), "s"(sbase), "s"(ldsaddr) : "memory");
}

__global__ __launch_bounds__(256) void k0_ln(const bf16_t* __restrict__ x, bf16_t* __restrict__ xn, unsigned* __restrict__ flags,
                                             const char* __restrict__ ckv, const char* __restrict__ wuk, const char* __restrict__ wuv, const char* __restrict__ wq, const char* __restrict__ wf1) {
    const int lane = threadIdx.x & 63, row = blockIdx.x * 4 + (threadIdx.x >> 6);
    const u32x4* src = (const u32x4*)(x + (size_t)row * DM);
    const u32x4 v0 = src[lane], v1 = src[lane + 64];
    float f[16];
#pragma unroll
    for (int i = 0; i < 4; ++i) { f[2 * i] = bflo(v0[i]); f[2 * i + 1] = bfhi(v0[i]); f[8 + 2 * i] = bflo(v1[i]); f[8 + 2 * i + 1] = bfhi(v1[i]); }
    float s = 0.f;
#pragma unroll
    for (int i = 0; i < 16; ++i) s += f[i];
#pragma unroll
    for (int o = 32; o >= 1; o >>= 1) s += __shfl_xor(s, o);
    const float mean = s * (1.0f / DM);
    float q = 0.f;
#pragma unroll
    for (int i = 0; i < 16; ++i) { f[i] -= mean; q += f[i] * f[i]; }
#pragma unroll
    for (int o = 32; o >= 1; o >>= 1) q += __shfl_xor(q, o);
    const float rstd = 1.0f / sqrtf(q * (1.0f / DM) + 1e-5f);
    u32x4 w0, w1;
#pragma unroll
    for (int i = 0; i < 4; ++i) { w0[i] = cvtpk(f[2 * i] * rstd, f[2 * i + 1] * rstd); w1[i] = cvtpk(f[8 + 2 * i] * rstd, f[8 + 2 * i + 1] * rstd); }
    const int mh = row >> 7, r = row & 127, c = lane & 7, kt = lane >> 3, sw = (c ^ ((r >> 1) & 7)) * 16;
    char* base = (char*)xn + (size_t)mh * 16 * 16384 + r * 128 + sw;
    *(u32x4*)(base + (size_t)kt * 16384) = w0;
    *(u32x4*)(base + (size_t)(kt + 8) * 16384) = w1;
    if (blockIdx.x == 0) flags[threadIdx.x * 32] = 0u;
}

namespace k1 {
constexpr int HTB = 16384, LDS_BYTES = 8 * HTB;
enum { T_K = 0, T_V = 1, T_Q = 2, T_H = 3 };
struct Src {
    const char* a; const char* b;
    unsigned aK, aH, aD, bK, bD;
    unsigned va, vb;
};
struct Frag { bf16x8 At[4][2], B0[2][2], B1[2][2]; };

DI f32x2 gelu_tanh2(f32x2 v) {
    const f32x2 z = v * (v * v * -0.10294324f + -2.3022082f);
    f32x2 e; e.x = __builtin_amdgcn_exp2f(z.x); e.y = __builtin_amdgcn_exp2f(z.y);
    e = e + 1.0f;
    f32x2 r; r.x = __builtin_amdgcn_rcpf(e.x); r.y = __builtin_amdgcn_rcpf(e.y);
    return v * r;
}

#define K1_SA(b, h) (((b) * 2 + (h)) * HTB)
#define K1_SB(b, h) ((4 + (b) * 2 + (h)) * HTB)
#define K1_STAGE(bufoff, gbase, voff, d2) do { glds((const char*)(gbase), voff, ldsw + (bufoff)); glds((const char*)(gbase) + (d2), voff, ldsw + (bufoff) + 8192); } while (0)
#define K1_LDA(dst, b, h) do { _Pragma("unroll") for (int m = 0; m < 4; ++m) { dst[m][0] = lds_rd(lds + K1_SA(b, h) + aoff0 + m * 2048); dst[m][1] = lds_rd(lds + K1_SA(b, h) + aoff1 + m * 2048); } } while (0)
#define K1_LDB(dst, b, h) do { _Pragma("unroll") for (int k = 0; k < 2; ++k) { \
        dst[0][k] = cat8(lds_tr(lds + K1_SB(b, h) + boff0 + k * 8192), lds_tr(lds + K1_SB(b, h) + boff0 + k * 8192 + 256)); \
        dst[1][k] = cat8(lds_tr(lds + K1_SB(b, h) + boff1 + k * 8192), lds_tr(lds + K1_SB(b, h) + boff1 + k * 8192 + 256)); } } while (0)
#define K1_MMA(ai, bj, At, Bt) do { __builtin_amdgcn_s_setprio(1); _Pragma("unroll") for (int m = 0; m < 4; ++m) _Pragma("unroll") for (int n = 0; n < 2; ++n) _Pragma("unroll") for (int k = 0; k < 2; ++k) \
        acc[ai][bj][m][n] = SWAP ? MFMA16(At[m][k], Bt[n][k], acc[ai][bj][m][n]) : MFMA16(Bt[n][k], At[m][k], acc[ai][bj][m][n]); __builtin_amdgcn_s_setprio(0); } while (0)

template <bool SWAP>
DI void kloop(lds_ptr lds, f32x4 (&acc)[2][2][4][2], const Src& cur, const Src& nxt, int nt, unsigned ldsw, int aoff0, int aoff1, int boff0, int boff1) {
    bf16x8 At[4][2], B0[2][2], B1[2][2];
    for (int t = 0; t < nt; t += 2) {
        const bool last = (t == nt - 2);
        const char* a1h = cur.a + (size_t)(t + 1) * cur.aK + cur.aH;
        const char* a2 = last ? nxt.a : cur.a + (size_t)(t + 2) * cur.aK; const char* b2 = last ? nxt.b : cur.b + (size_t)(t + 2) * cur.bK;
        const unsigned aK = last ? nxt.aK : cur.aK, aH = last ? nxt.aH : cur.aH, aD = last ? nxt.aD : cur.aD, bK = last ? nxt.bK : cur.bK, bD = last ? nxt.bD : cur.bD;
        const unsigned va = last ? nxt.va : cur.va, vb = last ? nxt.vb : cur.vb;
        const char* a3 = a2 + aK; const char* b3 = b2 + bK;
        K1_LDB(B0, 0, 0); K1_LDB(B1, 0, 1); SBAR(); K1_LDA(At, 0, 0); K1_STAGE(K1_SA(1, 1), a1h, cur.va, cur.aD);
        WAIT_V(8); WAIT_L(0); BAR(); K1_MMA(0, 0, At, B0); K1_MMA(0, 1, At, B1); BAR(); SBAR();
        K1_LDA(At, 0, 1); K1_STAGE(K1_SB(0, 0), b2, vb, bD); K1_STAGE(K1_SB(0, 1), b2 + 256, vb, bD); K1_STAGE(K1_SA(0, 0), a2, va, aD);
        WAIT_V(8); WAIT_L(0); BAR(); K1_MMA(1, 0, At, B0); K1_MMA(1, 1, At, B1); BAR(); SBAR();
        K1_LDB(B0, 1, 0); K1_LDB(B1, 1, 1); SBAR(); K1_LDA(At, 1, 0); K1_STAGE(K1_SA(0, 1), a2 + aH, va, aD);
        WAIT_V(8); WAIT_L(0); BAR(); K1_MMA(0, 0, At, B0); K1_MMA(0, 1, At, B1); BAR(); SBAR();
        K1_LDA(At, 1, 1); K1_STAGE(K1_SB(1, 0), b3, vb, bD); K1_STAGE(K1_SB(1, 1), b3 + 256, vb, bD); K1_STAGE(K1_SA(1, 0), a3, va, aD);
        WAIT_V(8); WAIT_L(0); BAR(); K1_MMA(1, 0, At, B0); K1_MMA(1, 1, At, B1); BAR(); SBAR();
    }
}

DI void zero_acc(f32x4 (&acc)[2][2][4][2]) {
#pragma unroll
    for (int a = 0; a < 2; ++a)
#pragma unroll
        for (int b = 0; b < 2; ++b)
#pragma unroll
            for (int m = 0; m < 4; ++m)
#pragma unroll
                for (int n = 0; n < 2; ++n) acc[a][b][m][n] = (f32x4){0.f, 0.f, 0.f, 0.f};
}
DI u32x4 pack_swap16(f32x4 v0, f32x4 v1) {
    const unsigned x0 = cvtpk(v0[0], v0[1]), x1 = cvtpk(v0[2], v0[3]), y0 = cvtpk(v1[0], v1[1]), y1 = cvtpk(v1[2], v1[3]);
    const auto r0 = __builtin_amdgcn_permlane16_swap(x0, y0, false, false), r1 = __builtin_amdgcn_permlane16_swap(x1, y1, false, false);
    return (u32x4){r0[0], r1[0], r0[1], r1[1]};
}
DI u32x4 pack_swap32(f32x4 v0, f32x4 v1) {
    const unsigned x0 = cvtpk(v0[0], v0[1]), x1 = cvtpk(v0[2], v0[3]), y0 = cvtpk(v1[0], v1[1]), y1 = cvtpk(v1[2], v1[3]);
    const auto r0 = __builtin_amdgcn_permlane32_swap(x0, y0, false, false), r1 = __builtin_amdgcn_permlane32_swap(x1, y1, false, false);
    return (u32x4){r0[0], r1[0], r0[1], r1[1]};
}
DI void epi_qk(const f32x4 (&acc)[2][2][4][2], char* dst, int pm, int pn, float sc, int wr, int wc, int fr, int fq) {
    const int cg = ((fq & 1) << 1) | (fq >> 1);
    char* p = dst + (size_t)(2 * pn * 32 + 4 * pm + wr) * 16384 + (4 * wc + cg) * 1024 + fr * 16;
#pragma unroll
    for (int ai = 0; ai < 2; ++ai)
#pragma unroll
        for (int bj = 0; bj < 2; ++bj)
#pragma unroll
            for (int m = 0; m < 4; ++m)
                ST16(p + (size_t)(bj * 32 + ai * 2) * 16384 + m * 256, pack_swap16(acc[ai][bj][m][0] * sc, acc[ai][bj][m][1] * sc));
}
DI void epi_v(const f32x4 (&acc)[2][2][4][2], char* dst, int pm, int pn, int wr, int wc, int fr, int fq) {
    char* p = dst + (size_t)(2 * pn * 32 + 4 * pm + wr) * 16384 + fq * 2048 + (32 * wc + fr) * 16;
#pragma unroll
    for (int ai = 0; ai < 2; ++ai)
#pragma unroll
        for (int bj = 0; bj < 2; ++bj)
#pragma unroll
            for (int m = 0; m < 4; m += 2)
#pragma unroll
                for (int n = 0; n < 2; ++n)
                    ST16(p + (size_t)(bj * 32 + ai * 2) * 16384 + m * 4096 + n * 256, pack_swap32(acc[ai][bj][m][n], acc[ai][bj][m + 1][n]));
}
DI void epi_h(const f32x4 (&acc)[2][2][4][2], char* dst, const bf16_t* b1, int pm, int pn, int wr, int wc, int fr, int fq) {
    const int cg = ((fq & 1) << 1) | (fq >> 1);
    f32x4 bv[2][2];
#pragma unroll
    for (int bj = 0; bj < 2; ++bj)
#pragma unroll
        for (int n = 0; n < 2; ++n) { const u32x2 w = *(const u32x2*)(b1 + pn * 256 + bj * 128 + wc * 32 + n * 16 + fq * 4); bv[bj][n] = (f32x4){bflo(w[0]), bfhi(w[0]), bflo(w[1]), bfhi(w[1])}; }
    char* p = dst + ((size_t)(2 * pm * 56 + 4 * pn + (wc >> 1)) * 8 + 4 * (wc & 1) + cg) * 2048 + (64 * wr + fr) * 16;
#pragma unroll
    for (int ai = 0; ai < 2; ++ai)
#pragma unroll
        for (int bj = 0; bj < 2; ++bj)
#pragma unroll
            for (int m = 0; m < 4; ++m) {
                f32x4 v0 = acc[ai][bj][m][0] + bv[bj][0], v1 = acc[ai][bj][m][1] + bv[bj][1];
                { const f32x2 a = gelu_tanh2((f32x2){v0[0], v0[1]}), b = gelu_tanh2((f32x2){v0[2], v0[3]}), c = gelu_tanh2((f32x2){v1[0], v1[1]}), d = gelu_tanh2((f32x2){v1[2], v1[3]});
                  v0 = (f32x4){a.x, a.y, b.x, b.y}; v1 = (f32x4){c.x, c.y, d.x, d.y}; }
                ST16(p + (size_t)(ai * 56 + bj * 2) * 16384 + m * 256, pack_swap16(v0, v1));
            }
}

__global__ __launch_bounds__(512, 2) void k1_proj(const bf16_t* __restrict__ ckv, const char* __restrict__ xn, const bf16_t* __restrict__ wuk, const bf16_t* __restrict__ wuv,
                                                  const bf16_t* __restrict__ wq, const bf16_t* __restrict__ w1, const bf16_t* __restrict__ b1,
                                                  char* __restrict__ Qo, char* __restrict__ Ko, char* __restrict__ VTo, char* __restrict__ Ho) {
    extern __shared__ __attribute__((aligned(16))) unsigned char smem[];
    lds_ptr lds = (lds_ptr)smem;
    const int tid = threadIdx.x, wid = __builtin_amdgcn_readfirstlane(tid >> 6), lane = tid & 63, wr = wid >> 2, wc = wid & 3, fr = lane & 15, fq = lane >> 4;
    const int xcd = blockIdx.x & 7, idx = blockIdx.x >> 3, pm = idx & 7, pj = xcd + 8 * (idx >> 3);
    const bool isV = pj >= 16, has2 = pj < 30, isH = pj >= 16;
    const unsigned ldsw = (unsigned)(uintptr_t)smem + (unsigned)wid * 1024u;
    const int aoff0 = (wr * 64 + fr) * 128 + ((fq ^ (fr >> 1)) * 16), aoff1 = aoff0 ^ 64;
    const int boff0 = fq * 2048 + wc * 512 + (fr >> 2) * 64 + (fq & 1) * 32 + (fr & 3) * 8, boff1 = boff0 ^ 32;
    const unsigned brow = 8 * (wid >> 1) + ((lane & 31) >> 2), bcol = (64 * (wid & 1) + 32 * (lane >> 5) + 8 * ((lane & 3) ^ (wid & 2))) * 2;
    Src u0, u1;
    u0.a = (const char*)ckv + (size_t)pm * 256 * DC * 2; u0.aK = 128; u0.aH = 128 * DC * 2; u0.aD = 64 * DC * 2;
    u0.va = (tid >> 3) * (DC * 2) + (((tid & 7) ^ ((tid >> 4) & 7)) * 16);
    u0.b = (const char*)(isV ? wuv : wuk) + (pj & 15) * 512; u0.bK = 64 * NQ * 2; u0.bD = 32 * NQ * 2; u0.vb = brow * (NQ * 2) + bcol;
    if (has2) {
        const unsigned ldb2 = isH ? FF * 2 : NQ * 2;
        u1.a = xn + (size_t)pm * 2 * 16 * 16384; u1.aK = 16384; u1.aH = 16 * 16384; u1.aD = 8192; u1.va = tid * 16;
        u1.b = (const char*)(isH ? w1 : wq) + (pj & 15) * 512; u1.bK = 64 * ldb2; u1.bD = 32 * ldb2; u1.vb = brow * ldb2 + bcol;
    } else u1 = u0;
    f32x4 acc[2][2][4][2];
    zero_acc(acc);
    STAMP(1, 0);
    K1_STAGE(K1_SB(0, 0), u0.b, u0.vb, u0.bD); K1_STAGE(K1_SB(0, 1), u0.b + 256, u0.vb, u0.bD); K1_STAGE(K1_SA(0, 0), u0.a, u0.va, u0.aD); K1_STAGE(K1_SA(0, 1), u0.a + u0.aH, u0.va, u0.aD);
    if (wr == 1) BAR();
    WAIT_V(2); BAR();
    K1_STAGE(K1_SB(1, 0), u0.b + u0.bK, u0.vb, u0.bD); K1_STAGE(K1_SA(1, 0), u0.a + u0.aK, u0.va, u0.aD); K1_STAGE(K1_SB(1, 1), u0.b + u0.bK + 256, u0.vb, u0.bD);
    WAIT_V(6); BAR();
    if (has2) {
        unsigned pf; const unsigned ldb2 = isH ? FF * 2 : NQ * 2;
        asm volatile("global_load_dword %0, %1, %2" : "=v"(pf) : "v"((unsigned)(pm * 128 + (tid >> 2)) * ldb2 + (tid & 3) * 128), "s"(u1.b) : "memory");
    }
    STAMP(1, 1);
    if (isV) kloop<true>(lds, acc, u0, u1, DC / 64, ldsw, aoff0, aoff1, boff0, boff1);
    else     kloop<false>(lds, acc, u0, u1, DC / 64, ldsw, aoff0, aoff1, boff0, boff1);
    STAMP(1, 2);
    if (wr == 0) BAR();
    if (isV) epi_v(acc, VTo, pm, pj & 15, wr, wc, fr, fq); else epi_qk(acc, Ko, pm, pj & 15, 1.0f, wr, wc, fr, fq);
    if (has2) {
        zero_acc(acc);
        STAMP(1, 3);
        if (wr == 1) BAR();
        kloop<false>(lds, acc, u1, u1, DM / 64, ldsw, aoff0, aoff1, boff0, boff1);
        STAMP(1, 4);
        if (wr == 0) BAR();
        if (isH) epi_h(acc, Ho, b1, pm, pj & 15, wr, wc, fr, fq); else epi_qk(acc, Qo, pm, pj & 15, 0.12751743082459868f, wr, wc, fr, fq);
    }
    STAMP(1, 5);
    WAIT_V(0);
    STAMP(1, 6);
}
#undef K1_SA
#undef K1_SB
#undef K1_STAGE
#undef K1_LDA
#undef K1_LDB
#undef K1_MMA
}

namespace k2 {
constexpr int SLOT = 16384, LDS_K = 0, LDS_V = 4 * SLOT, LDS_BYTES = 8 * SLOT;
#define K2_ASM \
    "s_nop 1\n\t" \
    "s_cmp_eq_u32 %[n], 0\n\t" \
    "s_cbranch_scc1 TAIL_%=\n\t" \
    "LOOP_%=:\n\t" \
    "s_cmp_lg_u32 %[dmaw], 0\n\t" \
    "s_cbranch_scc1 DMAL_%=\n\t" \
    "DMABL_%=:\n\t" \
    "s_waitcnt lgkmcnt(0)\n\t" \
    "v_mfma_f32_32x32x16_bf16 v[112:127], v[168:171], v[64:67], v[96:111]\n\t" \
    "ds_read_b128 v[200:203], v235 offset:8192\n\t" \
    "ds_read_b128 v[204:207], v235 offset:8704\n\t" \
    "v_add_f32 v237, v144, v145\n\t" \
    "v_cvt_pk_bf16_f32 v160, v144, v145\n\t" \
    "v_mfma_f32_32x32x16_bf16 v[112:127], v[172:175], v[68:71], v[112:127]\n\t" \
    "ds_read_b128 v[208:211], v235 offset:9216\n\t" \
    "ds_read_b128 v[212:215], v235 offset:9728\n\t" \
    "v_add_f32 v237, v237, v146\n\t" \
    "v_add_f32 v237, v237, v147\n\t" \
    "v_cvt_pk_bf16_f32 v161, v146, v147\n\t" \
    "v_mfma_f32_32x32x16_bf16 v[112:127], v[176:179], v[72:75], v[112:127]\n\t" \
    "ds_read_b128 v[216:219], v235 offset:12288\n\t" \
    "ds_read_b128 v[220:223], v235 offset:12800\n\t" \
    "v_add_f32 v237, v237, v148\n\t" \
    "v_add_f32 v237, v237, v149\n\t" \
    "v_cvt_pk_bf16_f32 v162, v148, v149\n\t" \
    "v_mfma_f32_32x32x16_bf16 v[112:127], v[180:183], v[76:79], v[112:127]\n\t" \
    "ds_read_b128 v[224:227], v235 offset:13312\n\t" \
    "ds_read_b128 v[228:231], v235 offset:13824\n\t" \
    "v_add_f32 v237, v237, v150\n\t" \
    "v_add_f32 v237, v237, v151\n\t" \
    "v_cvt_pk_bf16_f32 v163, v150, v151\n\t" \
    "v_mfma_f32_32x32x16_bf16 v[112:127], v[184:187], v[80:83], v[112:127]\n\t" \
    "v_add_f32 v237, v237, v152\n\t" \
    "v_add_f32 v237, v237, v153\n\t" \
    "v_cvt_pk_bf16_f32 v164, v152, v153\n\t" \
    "v_mfma_f32_32x32x16_bf16 v[112:127], v[188:191], v[84:87], v[112:127]\n\t" \
    "v_add_f32 v237, v237, v154\n\t" \
    "v_add_f32 v237, v237, v155\n\t" \
    "v_cvt_pk_bf16_f32 v165, v154, v155\n\t" \
    "v_mfma_f32_32x32x16_bf16 v[112:127], v[192:195], v[88:91], v[112:127]\n\t" \
    "v_add_f32 v237, v237, v156\n\t" \
    "v_add_f32 v237, v237, v157\n\t" \
    "v_cvt_pk_bf16_f32 v166, v156, v157\n\t" \
    "v_mfma_f32_32x32x16_bf16 v[112:127], v[196:199], v[92:95], v[112:127]\n\t" \
    "v_add_f32 v237, v237, v158\n\t" \
    "v_add_f32 v237, v237, v159\n\t" \
    "v_cvt_pk_bf16_f32 v167, v158, v159\n\t" \
    "v_cmp_lt_f32 vcc, %[thr], v237\n\t" \
    "s_cbranch_vccnz RAREA_%=\n\t" \
    "BACKA_%=:\n\t" \
    "s_waitcnt lgkmcnt(0)\n\t" \
    "v_mfma_f32_32x32x16_bf16 v[0:15], v[200:203], v[160:163], v[0:15]\n\t" \
    "ds_read_b128 v[168:171], v232 offset:512\n\t" \
    "ds_read_b128 v[172:175], v232 offset:2560\n\t" \
    "v_add_f32 v236, v236, v237\n\t" \
    "v_mfma_f32_32x32x16_bf16 v[16:31], v[204:207], v[160:163], v[16:31]\n\t" \
    "ds_read_b128 v[176:179], v232 offset:4608\n\t" \
    "ds_read_b128 v[180:183], v232 offset:6656\n\t" \
    "v_exp_f32 v144, v112\n\t" \
    "v_exp_f32 v145, v113\n\t" \
    "v_mfma_f32_32x32x16_bf16 v[32:47], v[208:211], v[160:163], v[32:47]\n\t" \
    "ds_read_b128 v[184:187], v232 offset:8704\n\t" \
    "ds_read_b128 v[188:191], v232 offset:10752\n\t" \
    "v_exp_f32 v146, v114\n\t" \
    "v_exp_f32 v147, v115\n\t" \
    "v_mfma_f32_32x32x16_bf16 v[48:63], v[212:215], v[160:163], v[48:63]\n\t" \
    "ds_read_b128 v[192:195], v232 offset:12800\n\t" \
    "ds_read_b128 v[196:199], v232 offset:14848\n\t" \
    "v_exp_f32 v148, v116\n\t" \
    "v_exp_f32 v149, v117\n\t" \
    "v_mfma_f32_32x32x16_bf16 v[0:15], v[216:219], v[164:167], v[0:15]\n\t" \
    "v_exp_f32 v150, v118\n\t" \
    "v_exp_f32 v151, v119\n\t" \
    "v_exp_f32 v152, v120\n\t" \
    "v_mfma_f32_32x32x16_bf16 v[16:31], v[220:223], v[164:167], v[16:31]\n\t" \
    "v_exp_f32 v153, v121\n\t" \
    "v_exp_f32 v154, v122\n\t" \
    "v_exp_f32 v155, v123\n\t" \
    "v_mfma_f32_32x32x16_bf16 v[32:47], v[224:227], v[164:167], v[32:47]\n\t" \
    "v_exp_f32 v156, v124\n\t" \
    "v_exp_f32 v157, v125\n\t" \
    "v_mfma_f32_32x32x16_bf16 v[48:63], v[228:231], v[164:167], v[48:63]\n\t" \
    "v_exp_f32 v158, v126\n\t" \
    "v_exp_f32 v159, v127\n\t" \
    "s_waitcnt lgkmcnt(0)\n\t" \
    "v_mfma_f32_32x32x16_bf16 v[128:143], v[168:171], v[64:67], v[96:111]\n\t" \
    "ds_read_b128 v[200:203], v234 offset:0\n\t" \
    "ds_read_b128 v[204:207], v234 offset:512\n\t" \
    "v_add_f32 v237, v144, v145\n\t" \
    "v_cvt_pk_bf16_f32 v160, v144, v145\n\t" \
    "v_mfma_f32_32x32x16_bf16 v[128:143], v[172:175], v[68:71], v[128:143]\n\t" \
    "ds_read_b128 v[208:211], v234 offset:1024\n\t" \
    "ds_read_b128 v[212:215], v234 offset:1536\n\t" \
    "v_add_f32 v237, v237, v146\n\t" \
    "v_add_f32 v237, v237, v147\n\t" \
    "v_cvt_pk_bf16_f32 v161, v146, v147\n\t" \
    "v_mfma_f32_32x32x16_bf16 v[128:143], v[176:179], v[72:75], v[128:143]\n\t" \
    "ds_read_b128 v[216:219], v234 offset:4096\n\t" \
    "ds_read_b128 v[220:223], v234 offset:4608\n\t" \
    "v_add_f32 v237, v237, v148\n\t" \
    "v_add_f32 v237, v237, v149\n\t" \
    "v_cvt_pk_bf16_f32 v162, v148, v149\n\t" \
    "v_mfma_f32_32x32x16_bf16 v[128:143], v[180:183], v[76:79], v[128:143]\n\t" \
    "ds_read_b128 v[224:227], v234 offset:5120\n\t" \
    "ds_read_b128 v[228:231], v234 offset:5632\n\t" \
    "v_add_f32 v237, v237, v150\n\t" \
    "v_add_f32 v237, v237, v151\n\t" \
    "v_cvt_pk_bf16_f32 v163, v150, v151\n\t" \
    "v_mfma_f32_32x32x16_bf16 v[128:143], v[184:187], v[80:83], v[128:143]\n\t" \
    "v_add_f32 v237, v237, v152\n\t" \
    "v_add_f32 v237, v237, v153\n\t" \
    "v_cvt_pk_bf16_f32 v164, v152, v153\n\t" \
    "v_mfma_f32_32x32x16_bf16 v[128:143], v[188:191], v[84:87], v[128:143]\n\t" \
    "v_add_f32 v237, v237, v154\n\t" \
    "v_add_f32 v237, v237, v155\n\t" \
    "v_cvt_pk_bf16_f32 v165, v154, v155\n\t" \
    "v_mfma_f32_32x32x16_bf16 v[128:143], v[192:195], v[88:91], v[128:143]\n\t" \
    "v_add_f32 v237, v237, v156\n\t" \
    "v_add_f32 v237, v237, v157\n\t" \
    "v_cvt_pk_bf16_f32 v166, v156, v157\n\t" \
    "v_mfma_f32_32x32x16_bf16 v[128:143], v[196:199], v[92:95], v[128:143]\n\t" \
    "v_add_f32 v237, v237, v158\n\t" \
    "v_add_f32 v237, v237, v159\n\t" \
    "v_cvt_pk_bf16_f32 v167, v158, v159\n\t" \
    "v_cmp_lt_f32 vcc, %[thr], v237\n\t" \
    "s_cbranch_vccnz RAREB_%=\n\t" \
    "BACKB_%=:\n\t" \
    "s_waitcnt lgkmcnt(0)\n\t" \
    "v_mfma_f32_32x32x16_bf16 v[0:15], v[200:203], v[160:163], v[0:15]\n\t" \
    "ds_read_b128 v[168:171], v233 offset:0\n\t" \
    "ds_read_b128 v[172:175], v233 offset:2048\n\t" \
    "v_add_f32 v236, v236, v237\n\t" \
    "v_mfma_f32_32x32x16_bf16 v[16:31], v[204:207], v[160:163], v[16:31]\n\t" \
    "ds_read_b128 v[176:179], v233 offset:4096\n\t" \
    "ds_read_b128 v[180:183], v233 offset:6144\n\t" \
    "v_exp_f32 v144, v128\n\t" \
    "v_exp_f32 v145, v129\n\t" \
    "v_mfma_f32_32x32x16_bf16 v[32:47], v[208:211], v[160:163], v[32:47]\n\t" \
    "ds_read_b128 v[184:187], v233 offset:8192\n\t" \
    "ds_read_b128 v[188:191], v233 offset:10240\n\t" \
    "v_exp_f32 v146, v130\n\t" \
    "v_exp_f32 v147, v131\n\t" \
    "v_mfma_f32_32x32x16_bf16 v[48:63], v[212:215], v[160:163], v[48:63]\n\t" \
    "ds_read_b128 v[192:195], v233 offset:12288\n\t" \
    "ds_read_b128 v[196:199], v233 offset:14336\n\t" \
    "v_exp_f32 v148, v132\n\t" \
    "v_exp_f32 v149, v133\n\t" \
    "v_mfma_f32_32x32x16_bf16 v[0:15], v[216:219], v[164:167], v[0:15]\n\t" \
    "v_exp_f32 v150, v134\n\t" \
    "v_exp_f32 v151, v135\n\t" \
    "v_exp_f32 v152, v136\n\t" \
    "v_mfma_f32_32x32x16_bf16 v[16:31], v[220:223], v[164:167], v[16:31]\n\t" \
    "v_exp_f32 v153, v137\n\t" \
    "v_exp_f32 v154, v138\n\t" \
    "v_exp_f32 v155, v139\n\t" \
    "s_add_u32 %[soff], %[soff], 0x4000\n\t" \
    "s_and_b32 %[soff], %[soff], 0xffff\n\t" \
    "v_mfma_f32_32x32x16_bf16 v[32:47], v[224:227], v[164:167], v[32:47]\n\t" \
    "v_exp_f32 v156, v140\n\t" \
    "v_exp_f32 v157, v141\n\t" \
    "v_mov_b32 v235, v234\n\t" \
    "v_mov_b32 v232, v233\n\t" \
    "v_mfma_f32_32x32x16_bf16 v[48:63], v[228:231], v[164:167], v[48:63]\n\t" \
    "v_exp_f32 v158, v142\n\t" \
    "v_exp_f32 v159, v143\n\t" \
    "v_add_u32 v234, %[soff], %[vp0]\n\t" \
    "s_add_u32 s60, %[soff], 0x4000\n\t" \
    "s_and_b32 s60, s60, 0xffff\n\t" \
    "v_add_u32 v233, s60, %[kp0]\n\t" \
    "s_waitcnt vmcnt(8)\n\t" \
    "s_barrier\n\t" \
    "s_add_u32 %[t], %[t], 1\n\t" \
    "s_cmp_lt_u32 %[t], %[n]\n\t" \
    "s_cbranch_scc1 LOOP_%=\n\t" \
    "TAIL_%=:\n\t" \
    "s_cmp_lg_u32 %[dmaw], 0\n\t" \
    "s_cbranch_scc1 DMAT_%=\n\t" \
    "DMABT_%=:\n\t" \
    "s_cmp_eq_u32 %[odd], 0\n\t" \
    "s_cbranch_scc1 EVEN_%=\n\t" \
    "s_waitcnt lgkmcnt(0)\n\t" \
    "v_mfma_f32_32x32x16_bf16 v[112:127], v[168:171], v[64:67], v[96:111]\n\t" \
    "ds_read_b128 v[200:203], v235 offset:8192\n\t" \
    "ds_read_b128 v[204:207], v235 offset:8704\n\t" \
    "v_add_f32 v237, v144, v145\n\t" \
    "v_cvt_pk_bf16_f32 v160, v144, v145\n\t" \
    "v_mfma_f32_32x32x16_bf16 v[112:127], v[172:175], v[68:71], v[112:127]\n\t" \
    "ds_read_b128 v[208:211], v235 offset:9216\n\t" \
    "ds_read_b128 v[212:215], v235 offset:9728\n\t" \
    "v_add_f32 v237, v237, v146\n\t" \
    "v_add_f32 v237, v237, v147\n\t" \
    "v_cvt_pk_bf16_f32 v161, v146, v147\n\t" \
    "v_mfma_f32_32x32x16_bf16 v[112:127], v[176:179], v[72:75], v[112:127]\n\t" \
    "ds_read_b128 v[216:219], v235 offset:12288\n\t" \
    "ds_read_b128 v[220:223], v235 offset:12800\n\t" \
    "v_add_f32 v237, v237, v148\n\t" \
    "v_add_f32 v237, v237, v149\n\t" \
    "v_cvt_pk_bf16_f32 v162, v148, v149\n\t" \
    "v_mfma_f32_32x32x16_bf16 v[112:127], v[180:183], v[76:79], v[112:127]\n\t" \
    "ds_read_b128 v[224:227], v235 offset:13312\n\t" \
    "ds_read_b128 v[228:231], v235 offset:13824\n\t" \
    "v_add_f32 v237, v237, v150\n\t" \
    "v_add_f32 v237, v237, v151\n\t" \
    "v_cvt_pk_bf16_f32 v163, v150, v151\n\t" \
    "v_mfma_f32_32x32x16_bf16 v[112:127], v[184:187], v[80:83], v[112:127]\n\t" \
    "v_add_f32 v237, v237, v152\n\t" \
    "v_add_f32 v237, v237, v153\n\t" \
    "v_cvt_pk_bf16_f32 v164, v152, v153\n\t" \
    "v_mfma_f32_32x32x16_bf16 v[112:127], v[188:191], v[84:87], v[112:127]\n\t" \
    "v_add_f32 v237, v237, v154\n\t" \
    "v_add_f32 v237, v237, v155\n\t" \
    "v_cvt_pk_bf16_f32 v165, v154, v155\n\t" \
    "v_mfma_f32_32x32x16_bf16 v[112:127], v[192:195], v[88:91], v[112:127]\n\t" \
    "v_add_f32 v237, v237, v156\n\t" \
    "v_add_f32 v237, v237, v157\n\t" \
    "v_cvt_pk_bf16_f32 v166, v156, v157\n\t" \
    "v_mfma_f32_32x32x16_bf16 v[112:127], v[196:199], v[92:95], v[112:127]\n\t" \
    "v_add_f32 v237, v237, v158\n\t" \
    "v_add_f32 v237, v237, v159\n\t" \
    "v_cvt_pk_bf16_f32 v167, v158, v159\n\t" \
    "v_cmp_lt_f32 vcc, %[thr], v237\n\t" \
    "s_cbranch_vccnz RAREC_%=\n\t" \
    "BACKC_%=:\n\t" \
    "s_waitcnt lgkmcnt(0)\n\t" \
    "v_mfma_f32_32x32x16_bf16 v[0:15], v[200:203], v[160:163], v[0:15]\n\t" \
    "ds_read_b128 v[168:171], v232 offset:512\n\t" \
    "ds_read_b128 v[172:175], v232 offset:2560\n\t" \
    "v_add_f32 v236, v236, v237\n\t" \
    "v_mfma_f32_32x32x16_bf16 v[16:31], v[204:207], v[160:163], v[16:31]\n\t" \
    "ds_read_b128 v[176:179], v232 offset:4608\n\t" \
    "ds_read_b128 v[180:183], v232 offset:6656\n\t" \
    "v_exp_f32 v144, v112\n\t" \
    "v_exp_f32 v145, v113\n\t" \
    "v_mfma_f32_32x32x16_bf16 v[32:47], v[208:211], v[160:163], v[32:47]\n\t" \
    "ds_read_b128 v[184:187], v232 offset:8704\n\t" \
    "ds_read_b128 v[188:191], v232 offset:10752\n\t" \
    "v_exp_f32 v146, v114\n\t" \
    "v_exp_f32 v147, v115\n\t" \
    "v_mfma_f32_32x32x16_bf16 v[48:63], v[212:215], v[160:163], v[48:63]\n\t" \
    "ds_read_b128 v[192:195], v232 offset:12800\n\t" \
    "ds_read_b128 v[196:199], v232 offset:14848\n\t" \
    "v_exp_f32 v148, v116\n\t" \
    "v_exp_f32 v149, v117\n\t" \
    "v_mfma_f32_32x32x16_bf16 v[0:15], v[216:219], v[164:167], v[0:15]\n\t" \
    "v_exp_f32 v150, v118\n\t" \
    "v_exp_f32 v151, v119\n\t" \
    "v_exp_f32 v152, v120\n\t" \
    "v_mfma_f32_32x32x16_bf16 v[16:31], v[220:223], v[164:167], v[16:31]\n\t" \
    "v_exp_f32 v153, v121\n\t" \
    "v_exp_f32 v154, v122\n\t" \
    "v_exp_f32 v155, v123\n\t" \
    "v_mfma_f32_32x32x16_bf16 v[32:47], v[224:227], v[164:167], v[32:47]\n\t" \
    "v_exp_f32 v156, v124\n\t" \
    "v_exp_f32 v157, v125\n\t" \
    "v_mfma_f32_32x32x16_bf16 v[48:63], v[228:231], v[164:167], v[48:63]\n\t" \
    "v_exp_f32 v158, v126\n\t" \
    "v_exp_f32 v159, v127\n\t" \
    "v_mov_b32 v239, 0xff800000\n\t" \
    "v_cmp_gt_i32 vcc, 0, %[dq]\n\t" \
    "s_nop 1\n\t" \
    "v_cndmask_b32 v96, v96, v239, vcc\n\t" \
    "v_cmp_gt_i32 vcc, 1, %[dq]\n\t" \
    "s_nop 1\n\t" \
    "v_cndmask_b32 v97, v97, v239, vcc\n\t" \
    "v_cmp_gt_i32 vcc, 2, %[dq]\n\t" \
    "s_nop 1\n\t" \
    "v_cndmask_b32 v98, v98, v239, vcc\n\t" \
    "v_cmp_gt_i32 vcc, 3, %[dq]\n\t" \
    "s_nop 1\n\t" \
    "v_cndmask_b32 v99, v99, v239, vcc\n\t" \
    "v_cmp_gt_i32 vcc, 8, %[dq]\n\t" \
    "s_nop 1\n\t" \
    "v_cndmask_b32 v100, v100, v239, vcc\n\t" \
    "v_cmp_gt_i32 vcc, 9, %[dq]\n\t" \
    "s_nop 1\n\t" \
    "v_cndmask_b32 v101, v101, v239, vcc\n\t" \
    "v_cmp_gt_i32 vcc, 10, %[dq]\n\t" \
    "s_nop 1\n\t" \
    "v_cndmask_b32 v102, v102, v239, vcc\n\t" \
    "v_cmp_gt_i32 vcc, 11, %[dq]\n\t" \
    "s_nop 1\n\t" \
    "v_cndmask_b32 v103, v103, v239, vcc\n\t" \
    "v_cmp_gt_i32 vcc, 16, %[dq]\n\t" \
    "s_nop 1\n\t" \
    "v_cndmask_b32 v104, v104, v239, vcc\n\t" \
    "v_cmp_gt_i32 vcc, 17, %[dq]\n\t" \
    "s_nop 1\n\t" \
    "v_cndmask_b32 v105, v105, v239, vcc\n\t" \
    "v_cmp_gt_i32 vcc, 18, %[dq]\n\t" \
    "s_nop 1\n\t" \
    "v_cndmask_b32 v106, v106, v239, vcc\n\t" \
    "v_cmp_gt_i32 vcc, 19, %[dq]\n\t" \
    "s_nop 1\n\t" \
    "v_cndmask_b32 v107, v107, v239, vcc\n\t" \
    "v_cmp_gt_i32 vcc, 24, %[dq]\n\t" \
    "s_nop 1\n\t" \
    "v_cndmask_b32 v108, v108, v239, vcc\n\t" \
    "v_cmp_gt_i32 vcc, 25, %[dq]\n\t" \
    "s_nop 1\n\t" \
    "v_cndmask_b32 v109, v109, v239, vcc\n\t" \
    "v_cmp_gt_i32 vcc, 26, %[dq]\n\t" \
    "s_nop 1\n\t" \
    "v_cndmask_b32 v110, v110, v239, vcc\n\t" \
    "v_cmp_gt_i32 vcc, 27, %[dq]\n\t" \
    "s_nop 1\n\t" \
    "v_cndmask_b32 v111, v111, v239, vcc\n\t" \
    "s_nop 1\n\t" \
    "s_waitcnt lgkmcnt(0)\n\t" \
    "v_mfma_f32_32x32x16_bf16 v[128:143], v[168:171], v[64:67], v[96:111]\n\t" \
    "ds_read_b128 v[200:203], v234 offset:0\n\t" \
    "ds_read_b128 v[204:207], v234 offset:512\n\t" \
    "v_add_f32 v237, v144, v145\n\t" \
    "v_cvt_pk_bf16_f32 v160, v144, v145\n\t" \
    "v_mfma_f32_32x32x16_bf16 v[128:143], v[172:175], v[68:71], v[128:143]\n\t" \
    "ds_read_b128 v[208:211], v234 offset:1024\n\t" \
    "ds_read_b128 v[212:215], v234 offset:1536\n\t" \
    "v_add_f32 v237, v237, v146\n\t" \
    "v_add_f32 v237, v237, v147\n\t" \
    "v_cvt_pk_bf16_f32 v161, v146, v147\n\t" \
    "v_mfma_f32_32x32x16_bf16 v[128:143], v[176:179], v[72:75], v[128:143]\n\t" \
    "ds_read_b128 v[216:219], v234 offset:4096\n\t" \
    "ds_read_b128 v[220:223], v234 offset:4608\n\t" \
    "v_add_f32 v237, v237, v148\n\t" \
    "v_add_f32 v237, v237, v149\n\t" \
    "v_cvt_pk_bf16_f32 v162, v148, v149\n\t" \
    "v_mfma_f32_32x32x16_bf16 v[128:143], v[180:183], v[76:79], v[128:143]\n\t" \
    "ds_read_b128 v[224:227], v234 offset:5120\n\t" \
    "ds_read_b128 v[228:231], v234 offset:5632\n\t" \
    "v_add_f32 v237, v237, v150\n\t" \
    "v_add_f32 v237, v237, v151\n\t" \
    "v_cvt_pk_bf16_f32 v163, v150, v151\n\t" \
    "v_mfma_f32_32x32x16_bf16 v[128:143], v[184:187], v[80:83], v[128:143]\n\t" \
    "v_add_f32 v237, v237, v152\n\t" \
    "v_add_f32 v237, v237, v153\n\t" \
    "v_cvt_pk_bf16_f32 v164, v152, v153\n\t" \
    "v_mfma_f32_32x32x16_bf16 v[128:143], v[188:191], v[84:87], v[128:143]\n\t" \
    "v_add_f32 v237, v237, v154\n\t" \
    "v_add_f32 v237, v237, v155\n\t" \
    "v_cvt_pk_bf16_f32 v165, v154, v155\n\t" \
    "v_mfma_f32_32x32x16_bf16 v[128:143], v[192:195], v[88:91], v[128:143]\n\t" \
    "v_add_f32 v237, v237, v156\n\t" \
    "v_add_f32 v237, v237, v157\n\t" \
    "v_cvt_pk_bf16_f32 v166, v156, v157\n\t" \
    "v_mfma_f32_32x32x16_bf16 v[128:143], v[196:199], v[92:95], v[128:143]\n\t" \
    "v_add_f32 v237, v237, v158\n\t" \
    "v_add_f32 v237, v237, v159\n\t" \
    "v_cvt_pk_bf16_f32 v167, v158, v159\n\t" \
    "v_cmp_lt_f32 vcc, %[thr], v237\n\t" \
    "s_cbranch_vccnz RARED_%=\n\t" \
    "BACKD_%=:\n\t" \
    "s_waitcnt lgkmcnt(0)\n\t" \
    "v_mfma_f32_32x32x16_bf16 v[0:15], v[200:203], v[160:163], v[0:15]\n\t" \
    "ds_read_b128 v[168:171], v233 offset:0\n\t" \
    "ds_read_b128 v[172:175], v233 offset:2048\n\t" \
    "v_add_f32 v236, v236, v237\n\t" \
    "v_mfma_f32_32x32x16_bf16 v[16:31], v[204:207], v[160:163], v[16:31]\n\t" \
    "ds_read_b128 v[176:179], v233 offset:4096\n\t" \
    "ds_read_b128 v[180:183], v233 offset:6144\n\t" \
    "v_exp_f32 v144, v128\n\t" \
    "v_exp_f32 v145, v129\n\t" \
    "v_mfma_f32_32x32x16_bf16 v[32:47], v[208:211], v[160:163], v[32:47]\n\t" \
    "ds_read_b128 v[184:187], v233 offset:8192\n\t" \
    "ds_read_b128 v[188:191], v233 offset:10240\n\t" \
    "v_exp_f32 v146, v130\n\t" \
    "v_exp_f32 v147, v131\n\t" \
    "v_mfma_f32_32x32x16_bf16 v[48:63], v[212:215], v[160:163], v[48:63]\n\t" \
    "ds_read_b128 v[192:195], v233 offset:12288\n\t" \
    "ds_read_b128 v[196:199], v233 offset:14336\n\t" \
    "v_exp_f32 v148, v132\n\t" \
    "v_exp_f32 v149, v133\n\t" \
    "v_mfma_f32_32x32x16_bf16 v[0:15], v[216:219], v[164:167], v[0:15]\n\t" \
    "v_exp_f32 v150, v134\n\t" \
    "v_exp_f32 v151, v135\n\t" \
    "v_exp_f32 v152, v136\n\t" \
    "v_mfma_f32_32x32x16_bf16 v[16:31], v[220:223], v[164:167], v[16:31]\n\t" \
    "v_exp_f32 v153, v137\n\t" \
    "v_exp_f32 v154, v138\n\t" \
    "v_exp_f32 v155, v139\n\t" \
    "v_mfma_f32_32x32x16_bf16 v[32:47], v[224:227], v[164:167], v[32:47]\n\t" \
    "v_exp_f32 v156, v140\n\t" \
    "v_exp_f32 v157, v141\n\t" \
    "v_mfma_f32_32x32x16_bf16 v[48:63], v[228:231], v[164:167], v[48:63]\n\t" \
    "v_exp_f32 v158, v142\n\t" \
    "v_exp_f32 v159, v143\n\t" \
    "s_waitcnt lgkmcnt(0)\n\t" \
    "ds_read_b128 v[200:203], v234 offset:8192\n\t" \
    "ds_read_b128 v[204:207], v234 offset:8704\n\t" \
    "ds_read_b128 v[208:211], v234 offset:9216\n\t" \
    "ds_read_b128 v[212:215], v234 offset:9728\n\t" \
    "ds_read_b128 v[216:219], v234 offset:12288\n\t" \
    "ds_read_b128 v[220:223], v234 offset:12800\n\t" \
    "ds_read_b128 v[224:227], v234 offset:13312\n\t" \
    "ds_read_b128 v[228:231], v234 offset:13824\n\t" \
    "v_add_f32 v237, v144, v145\n\t" \
    "v_cvt_pk_bf16_f32 v160, v144, v145\n\t" \
    "v_add_f32 v237, v237, v146\n\t" \
    "v_add_f32 v237, v237, v147\n\t" \
    "v_cvt_pk_bf16_f32 v161, v146, v147\n\t" \
    "v_add_f32 v237, v237, v148\n\t" \
    "v_add_f32 v237, v237, v149\n\t" \
    "v_cvt_pk_bf16_f32 v162, v148, v149\n\t" \
    "v_add_f32 v237, v237, v150\n\t" \
    "v_add_f32 v237, v237, v151\n\t" \
    "v_cvt_pk_bf16_f32 v163, v150, v151\n\t" \
    "v_add_f32 v237, v237, v152\n\t" \
    "v_add_f32 v237, v237, v153\n\t" \
    "v_cvt_pk_bf16_f32 v164, v152, v153\n\t" \
    "v_add_f32 v237, v237, v154\n\t" \
    "v_add_f32 v237, v237, v155\n\t" \
    "v_cvt_pk_bf16_f32 v165, v154, v155\n\t" \
    "v_add_f32 v237, v237, v156\n\t" \
    "v_add_f32 v237, v237, v157\n\t" \
    "v_cvt_pk_bf16_f32 v166, v156, v157\n\t" \
    "v_add_f32 v237, v237, v158\n\t" \
    "v_add_f32 v237, v237, v159\n\t" \
    "v_cvt_pk_bf16_f32 v167, v158, v159\n\t" \
    "v_cmp_lt_f32 vcc, %[thr], v237\n\t" \
    "s_cbranch_vccnz RAREE_%=\n\t" \
    "BACKE_%=:\n\t" \
    "s_waitcnt lgkmcnt(0)\n\t" \
    "v_add_f32 v236, v236, v237\n\t" \
    "v_mfma_f32_32x32x16_bf16 v[0:15], v[200:203], v[160:163], v[0:15]\n\t" \
    "v_mfma_f32_32x32x16_bf16 v[16:31], v[204:207], v[160:163], v[16:31]\n\t" \
    "v_mfma_f32_32x32x16_bf16 v[32:47], v[208:211], v[160:163], v[32:47]\n\t" \
    "v_mfma_f32_32x32x16_bf16 v[48:63], v[212:215], v[160:163], v[48:63]\n\t" \
    "v_mfma_f32_32x32x16_bf16 v[0:15], v[216:219], v[164:167], v[0:15]\n\t" \
    "v_mfma_f32_32x32x16_bf16 v[16:31], v[220:223], v[164:167], v[16:31]\n\t" \
    "v_mfma_f32_32x32x16_bf16 v[32:47], v[224:227], v[164:167], v[32:47]\n\t" \
    "v_mfma_f32_32x32x16_bf16 v[48:63], v[228:231], v[164:167], v[48:63]\n\t" \
    "s_branch END_%=\n\t" \
    "EVEN_%=:\n\t" \
    "v_mov_b32 v239, 0xff800000\n\t" \
    "v_cmp_gt_i32 vcc, 0, %[dq]\n\t" \
    "s_nop 1\n\t" \
    "v_cndmask_b32 v96, v96, v239, vcc\n\t" \
    "v_cmp_gt_i32 vcc, 1, %[dq]\n\t" \
    "s_nop 1\n\t" \
    "v_cndmask_b32 v97, v97, v239, vcc\n\t" \
    "v_cmp_gt_i32 vcc, 2, %[dq]\n\t" \
    "s_nop 1\n\t" \
    "v_cndmask_b32 v98, v98, v239, vcc\n\t" \
    "v_cmp_gt_i32 vcc, 3, %[dq]\n\t" \
    "s_nop 1\n\t" \
    "v_cndmask_b32 v99, v99, v239, vcc\n\t" \
    "v_cmp_gt_i32 vcc, 8, %[dq]\n\t" \
    "s_nop 1\n\t" \
    "v_cndmask_b32 v100, v100, v239, vcc\n\t" \
    "v_cmp_gt_i32 vcc, 9, %[dq]\n\t" \
    "s_nop 1\n\t" \
    "v_cndmask_b32 v101, v101, v239, vcc\n\t" \
    "v_cmp_gt_i32 vcc, 10, %[dq]\n\t" \
    "s_nop 1\n\t" \
    "v_cndmask_b32 v102, v102, v239, vcc\n\t" \
    "v_cmp_gt_i32 vcc, 11, %[dq]\n\t" \
    "s_nop 1\n\t" \
    "v_cndmask_b32 v103, v103, v239, vcc\n\t" \
    "v_cmp_gt_i32 vcc, 16, %[dq]\n\t" \
    "s_nop 1\n\t" \
    "v_cndmask_b32 v104, v104, v239, vcc\n\t" \
    "v_cmp_gt_i32 vcc, 17, %[dq]\n\t" \
    "s_nop 1\n\t" \
    "v_cndmask_b32 v105, v105, v239, vcc\n\t" \
    "v_cmp_gt_i32 vcc, 18, %[dq]\n\t" \
    "s_nop 1\n\t" \
    "v_cndmask_b32 v106, v106, v239, vcc\n\t" \
    "v_cmp_gt_i32 vcc, 19, %[dq]\n\t" \
    "s_nop 1\n\t" \
    "v_cndmask_b32 v107, v107, v239, vcc\n\t" \
    "v_cmp_gt_i32 vcc, 24, %[dq]\n\t" \
    "s_nop 1\n\t" \
    "v_cndmask_b32 v108, v108, v239, vcc\n\t" \
    "v_cmp_gt_i32 vcc, 25, %[dq]\n\t" \
    "s_nop 1\n\t" \
    "v_cndmask_b32 v109, v109, v239, vcc\n\t" \
    "v_cmp_gt_i32 vcc, 26, %[dq]\n\t" \
    "s_nop 1\n\t" \
    "v_cndmask_b32 v110, v110, v239, vcc\n\t" \
    "v_cmp_gt_i32 vcc, 27, %[dq]\n\t" \
    "s_nop 1\n\t" \
    "v_cndmask_b32 v111, v111, v239, vcc\n\t" \
    "s_nop 1\n\t" \
    "s_waitcnt lgkmcnt(0)\n\t" \
    "v_mfma_f32_32x32x16_bf16 v[112:127], v[168:171], v[64:67], v[96:111]\n\t" \
    "ds_read_b128 v[200:203], v235 offset:8192\n\t" \
    "ds_read_b128 v[204:207], v235 offset:8704\n\t" \
    "v_add_f32 v237, v144, v145\n\t" \
    "v_cvt_pk_bf16_f32 v160, v144, v145\n\t" \
    "v_mfma_f32_32x32x16_bf16 v[112:127], v[172:175], v[68:71], v[112:127]\n\t" \
    "ds_read_b128 v[208:211], v235 offset:9216\n\t" \
    "ds_read_b128 v[212:215], v235 offset:9728\n\t" \
    "v_add_f32 v237, v237, v146\n\t" \
    "v_add_f32 v237, v237, v147\n\t" \
    "v_cvt_pk_bf16_f32 v161, v146, v147\n\t" \
    "v_mfma_f32_32x32x16_bf16 v[112:127], v[176:179], v[72:75], v[112:127]\n\t" \
    "ds_read_b128 v[216:219], v235 offset:12288\n\t" \
    "ds_read_b128 v[220:223], v235 offset:12800\n\t" \
    "v_add_f32 v237, v237, v148\n\t" \
    "v_add_f32 v237, v237, v149\n\t" \
    "v_cvt_pk_bf16_f32 v162, v148, v149\n\t" \
    "v_mfma_f32_32x32x16_bf16 v[112:127], v[180:183], v[76:79], v[112:127]\n\t" \
    "ds_read_b128 v[224:227], v235 offset:13312\n\t" \
    "ds_read_b128 v[228:231], v235 offset:13824\n\t" \
    "v_add_f32 v237, v237, v150\n\t" \
    "v_add_f32 v237, v237, v151\n\t" \
    "v_cvt_pk_bf16_f32 v163, v150, v151\n\t" \
    "v_mfma_f32_32x32x16_bf16 v[112:127], v[184:187], v[80:83], v[112:127]\n\t" \
    "v_add_f32 v237, v237, v152\n\t" \
    "v_add_f32 v237, v237, v153\n\t" \
    "v_cvt_pk_bf16_f32 v164, v152, v153\n\t" \
    "v_mfma_f32_32x32x16_bf16 v[112:127], v[188:191], v[84:87], v[112:127]\n\t" \
    "v_add_f32 v237, v237, v154\n\t" \
    "v_add_f32 v237, v237, v155\n\t" \
    "v_cvt_pk_bf16_f32 v165, v154, v155\n\t" \
    "v_mfma_f32_32x32x16_bf16 v[112:127], v[192:195], v[88:91], v[112:127]\n\t" \
    "v_add_f32 v237, v237, v156\n\t" \
    "v_add_f32 v237, v237, v157\n\t" \
    "v_cvt_pk_bf16_f32 v166, v156, v157\n\t" \
    "v_mfma_f32_32x32x16_bf16 v[112:127], v[196:199], v[92:95], v[112:127]\n\t" \
    "v_add_f32 v237, v237, v158\n\t" \
    "v_add_f32 v237, v237, v159\n\t" \
    "v_cvt_pk_bf16_f32 v167, v158, v159\n\t" \
    "v_cmp_lt_f32 vcc, %[thr], v237\n\t" \
    "s_cbranch_vccnz RAREF_%=\n\t" \
    "BACKF_%=:\n\t" \
    "s_waitcnt lgkmcnt(0)\n\t" \
    "v_mfma_f32_32x32x16_bf16 v[0:15], v[200:203], v[160:163], v[0:15]\n\t" \
    "ds_read_b128 v[168:171], v232 offset:512\n\t" \
    "ds_read_b128 v[172:175], v232 offset:2560\n\t" \
    "v_add_f32 v236, v236, v237\n\t" \
    "v_mfma_f32_32x32x16_bf16 v[16:31], v[204:207], v[160:163], v[16:31]\n\t" \
    "ds_read_b128 v[176:179], v232 offset:4608\n\t" \
    "ds_read_b128 v[180:183], v232 offset:6656\n\t" \
    "v_exp_f32 v144, v112\n\t" \
    "v_exp_f32 v145, v113\n\t" \
    "v_mfma_f32_32x32x16_bf16 v[32:47], v[208:211], v[160:163], v[32:47]\n\t" \
    "ds_read_b128 v[184:187], v232 offset:8704\n\t" \
    "ds_read_b128 v[188:191], v232 offset:10752\n\t" \
    "v_exp_f32 v146, v114\n\t" \
    "v_exp_f32 v147, v115\n\t" \
    "v_mfma_f32_32x32x16_bf16 v[48:63], v[212:215], v[160:163], v[48:63]\n\t" \
    "ds_read_b128 v[192:195], v232 offset:12800\n\t" \
    "ds_read_b128 v[196:199], v232 offset:14848\n\t" \
    "v_exp_f32 v148, v116\n\t" \
    "v_exp_f32 v149, v117\n\t" \
    "v_mfma_f32_32x32x16_bf16 v[0:15], v[216:219], v[164:167], v[0:15]\n\t" \
    "v_exp_f32 v150, v118\n\t" \
    "v_exp_f32 v151, v119\n\t" \
    "v_exp_f32 v152, v120\n\t" \
    "v_mfma_f32_32x32x16_bf16 v[16:31], v[220:223], v[164:167], v[16:31]\n\t" \
    "v_exp_f32 v153, v121\n\t" \
    "v_exp_f32 v154, v122\n\t" \
    "v_exp_f32 v155, v123\n\t" \
    "v_mfma_f32_32x32x16_bf16 v[32:47], v[224:227], v[164:167], v[32:47]\n\t" \
    "v_exp_f32 v156, v124\n\t" \
    "v_exp_f32 v157, v125\n\t" \
    "v_mfma_f32_32x32x16_bf16 v[48:63], v[228:231], v[164:167], v[48:63]\n\t" \
    "v_exp_f32 v158, v126\n\t" \
    "v_exp_f32 v159, v127\n\t" \
    "s_waitcnt lgkmcnt(0)\n\t" \
    "ds_read_b128 v[200:203], v234 offset:0\n\t" \
    "ds_read_b128 v[204:207], v234 offset:512\n\t" \
    "ds_read_b128 v[208:211], v234 offset:1024\n\t" \
    "ds_read_b128 v[212:215], v234 offset:1536\n\t" \
    "ds_read_b128 v[216:219], v234 offset:4096\n\t" \
    "ds_read_b128 v[220:223], v234 offset:4608\n\t" \
    "ds_read_b128 v[224:227], v234 offset:5120\n\t" \
    "ds_read_b128 v[228:231], v234 offset:5632\n\t" \
    "v_add_f32 v237, v144, v145\n\t" \
    "v_cvt_pk_bf16_f32 v160, v144, v145\n\t" \
    "v_add_f32 v237, v237, v146\n\t" \
    "v_add_f32 v237, v237, v147\n\t" \
    "v_cvt_pk_bf16_f32 v161, v146, v147\n\t" \
    "v_add_f32 v237, v237, v148\n\t" \
    "v_add_f32 v237, v237, v149\n\t" \
    "v_cvt_pk_bf16_f32 v162, v148, v149\n\t" \
    "v_add_f32 v237, v237, v150\n\t" \
    "v_add_f32 v237, v237, v151\n\t" \
    "v_cvt_pk_bf16_f32 v163, v150, v151\n\t" \
    "v_add_f32 v237, v237, v152\n\t" \
    "v_add_f32 v237, v237, v153\n\t" \
    "v_cvt_pk_bf16_f32 v164, v152, v153\n\t" \
    "v_add_f32 v237, v237, v154\n\t" \
    "v_add_f32 v237, v237, v155\n\t" \
    "v_cvt_pk_bf16_f32 v165, v154, v155\n\t" \
    "v_add_f32 v237, v237, v156\n\t" \
    "v_add_f32 v237, v237, v157\n\t" \
    "v_cvt_pk_bf16_f32 v166, v156, v157\n\t" \
    "v_add_f32 v237, v237, v158\n\t" \
    "v_add_f32 v237, v237, v159\n\t" \
    "v_cvt_pk_bf16_f32 v167, v158, v159\n\t" \
    "v_cmp_lt_f32 vcc, %[thr], v237\n\t" \
    "s_cbranch_vccnz RAREG_%=\n\t" \
    "BACKG_%=:\n\t" \
    "s_waitcnt lgkmcnt(0)\n\t" \
    "v_add_f32 v236, v236, v237\n\t" \
    "v_mfma_f32_32x32x16_bf16 v[0:15], v[200:203], v[160:163], v[0:15]\n\t" \
    "v_mfma_f32_32x32x16_bf16 v[16:31], v[204:207], v[160:163], v[16:31]\n\t" \
    "v_mfma_f32_32x32x16_bf16 v[32:47], v[208:211], v[160:163], v[32:47]\n\t" \
    "v_mfma_f32_32x32x16_bf16 v[48:63], v[212:215], v[160:163], v[48:63]\n\t" \
    "v_mfma_f32_32x32x16_bf16 v[0:15], v[216:219], v[164:167], v[0:15]\n\t" \
    "v_mfma_f32_32x32x16_bf16 v[16:31], v[220:223], v[164:167], v[16:31]\n\t" \
    "v_mfma_f32_32x32x16_bf16 v[32:47], v[224:227], v[164:167], v[32:47]\n\t" \
    "v_mfma_f32_32x32x16_bf16 v[48:63], v[228:231], v[164:167], v[48:63]\n\t" \
    "s_branch END_%=\n\t" \
    "DMAL_%=:\n\t" \
    "s_add_u32 s60, %[t], 3\n\t" \
    "s_min_u32 s60, s60, %[ntm1]\n\t" \
    "s_lshl_b32 s60, s60, 14\n\t" \
    "s_add_u32 s62, %[khl], s60\n\t" \
    "s_addc_u32 s63, %[khh], 0\n\t" \
    "s_add_u32 s61, %[soff], 0xc000\n\t" \
    "s_and_b32 s61, s61, 0xffff\n\t" \
    "s_add_u32 m0, s61, %[kdst]\n\t" \
    "s_nop 0\n\t" \
    "global_load_lds_dwordx4 %[dvo], s[62:63] offset:0\n\t" \
    "global_load_lds_dwordx4 %[dvo], s[62:63] offset:1024\n\t" \
    "global_load_lds_dwordx4 %[dvo], s[62:63] offset:2048\n\t" \
    "global_load_lds_dwordx4 %[dvo], s[62:63] offset:3072\n\t" \
    "s_add_u32 s60, %[t], 2\n\t" \
    "s_min_u32 s60, s60, %[ntm1]\n\t" \
    "s_lshl_b32 s60, s60, 14\n\t" \
    "s_add_u32 s62, %[vhl], s60\n\t" \
    "s_addc_u32 s63, %[vhh], 0\n\t" \
    "s_add_u32 s61, %[soff], 0x8000\n\t" \
    "s_and_b32 s61, s61, 0xffff\n\t" \
    "s_add_u32 m0, s61, %[vdst]\n\t" \
    "s_nop 0\n\t" \
    "global_load_lds_dwordx4 %[dvo], s[62:63] offset:0\n\t" \
    "global_load_lds_dwordx4 %[dvo], s[62:63] offset:1024\n\t" \
    "global_load_lds_dwordx4 %[dvo], s[62:63] offset:2048\n\t" \
    "global_load_lds_dwordx4 %[dvo], s[62:63] offset:3072\n\t" \
    "s_branch DMABL_%=\n\t" \
    "DMAT_%=:\n\t" \
    "s_add_u32 s60, %[t], 3\n\t" \
    "s_min_u32 s60, s60, %[ntm1]\n\t" \
    "s_lshl_b32 s60, s60, 14\n\t" \
    "s_add_u32 s62, %[khl], s60\n\t" \
    "s_addc_u32 s63, %[khh], 0\n\t" \
    "s_add_u32 s61, %[soff], 0xc000\n\t" \
    "s_and_b32 s61, s61, 0xffff\n\t" \
    "s_add_u32 m0, s61, %[kdst]\n\t" \
    "s_nop 0\n\t" \
    "global_load_lds_dwordx4 %[dvo], s[62:63] offset:0\n\t" \
    "global_load_lds_dwordx4 %[dvo], s[62:63] offset:1024\n\t" \
    "global_load_lds_dwordx4 %[dvo], s[62:63] offset:2048\n\t" \
    "global_load_lds_dwordx4 %[dvo], s[62:63] offset:3072\n\t" \
    "s_add_u32 s60, %[t], 2\n\t" \
    "s_min_u32 s60, s60, %[ntm1]\n\t" \
    "s_lshl_b32 s60, s60, 14\n\t" \
    "s_add_u32 s62, %[vhl], s60\n\t" \
    "s_addc_u32 s63, %[vhh], 0\n\t" \
    "s_add_u32 s61, %[soff], 0x8000\n\t" \
    "s_and_b32 s61, s61, 0xffff\n\t" \
    "s_add_u32 m0, s61, %[vdst]\n\t" \
    "s_nop 0\n\t" \
    "global_load_lds_dwordx4 %[dvo], s[62:63] offset:0\n\t" \
    "global_load_lds_dwordx4 %[dvo], s[62:63] offset:1024\n\t" \
    "global_load_lds_dwordx4 %[dvo], s[62:63] offset:2048\n\t" \
    "global_load_lds_dwordx4 %[dvo], s[62:63] offset:3072\n\t" \
    "s_branch DMABT_%=\n\t" \
    "RAREA_%=:\n\t" \
    "s_nop 15\n\t" \
    "v_max3_f32 v239, v128, v129, v130\n\t" \
    "v_max3_f32 v239, v239, v131, v132\n\t" \
    "v_max3_f32 v239, v239, v133, v134\n\t" \
    "v_max3_f32 v239, v239, v135, v136\n\t" \
    "v_max3_f32 v239, v239, v137, v138\n\t" \
    "v_max3_f32 v239, v239, v139, v140\n\t" \
    "v_max3_f32 v239, v239, v141, v142\n\t" \
    "v_max_f32 v239, v239, v143\n\t" \
    "v_max_f32 v239, 0, v239\n\t" \
    "v_mov_b32 v240, v239\n\t" \
    "s_nop 1\n\t" \
    "v_permlane32_swap_b32 v239, v240\n\t" \
    "s_nop 1\n\t" \
    "v_max_f32 v239, v239, v240\n\t" \
    "v_add_f32 v238, v238, v239\n\t" \
    "v_exp_f32 v240, -v239\n\t" \
    "v_sub_f32 v241, 0, v238\n\t" \
    "v_sub_f32 v128, v128, v239\n\t" \
    "v_sub_f32 v112, v112, v239\n\t" \
    "v_mov_b32 v96, v241\n\t" \
    "v_sub_f32 v129, v129, v239\n\t" \
    "v_sub_f32 v113, v113, v239\n\t" \
    "v_mov_b32 v97, v241\n\t" \
    "v_sub_f32 v130, v130, v239\n\t" \
    "v_sub_f32 v114, v114, v239\n\t" \
    "v_mov_b32 v98, v241\n\t" \
    "v_sub_f32 v131, v131, v239\n\t" \
    "v_sub_f32 v115, v115, v239\n\t" \
    "v_mov_b32 v99, v241\n\t" \
    "v_sub_f32 v132, v132, v239\n\t" \
    "v_sub_f32 v116, v116, v239\n\t" \
    "v_mov_b32 v100, v241\n\t" \
    "v_sub_f32 v133, v133, v239\n\t" \
    "v_sub_f32 v117, v117, v239\n\t" \
    "v_mov_b32 v101, v241\n\t" \
    "v_sub_f32 v134, v134, v239\n\t" \
    "v_sub_f32 v118, v118, v239\n\t" \
    "v_mov_b32 v102, v241\n\t" \
    "v_sub_f32 v135, v135, v239\n\t" \
    "v_sub_f32 v119, v119, v239\n\t" \
    "v_mov_b32 v103, v241\n\t" \
    "v_sub_f32 v136, v136, v239\n\t" \
    "v_sub_f32 v120, v120, v239\n\t" \
    "v_mov_b32 v104, v241\n\t" \
    "v_sub_f32 v137, v137, v239\n\t" \
    "v_sub_f32 v121, v121, v239\n\t" \
    "v_mov_b32 v105, v241\n\t" \
    "v_sub_f32 v138, v138, v239\n\t" \
    "v_sub_f32 v122, v122, v239\n\t" \
    "v_mov_b32 v106, v241\n\t" \
    "v_sub_f32 v139, v139, v239\n\t" \
    "v_sub_f32 v123, v123, v239\n\t" \
    "v_mov_b32 v107, v241\n\t" \
    "v_sub_f32 v140, v140, v239\n\t" \
    "v_sub_f32 v124, v124, v239\n\t" \
    "v_mov_b32 v108, v241\n\t" \
    "v_sub_f32 v141, v141, v239\n\t" \
    "v_sub_f32 v125, v125, v239\n\t" \
    "v_mov_b32 v109, v241\n\t" \
    "v_sub_f32 v142, v142, v239\n\t" \
    "v_sub_f32 v126, v126, v239\n\t" \
    "v_mov_b32 v110, v241\n\t" \
    "v_sub_f32 v143, v143, v239\n\t" \
    "v_sub_f32 v127, v127, v239\n\t" \
    "v_mov_b32 v111, v241\n\t" \
    "v_exp_f32 v144, v128\n\t" \
    "v_exp_f32 v145, v129\n\t" \
    "v_exp_f32 v146, v130\n\t" \
    "v_exp_f32 v147, v131\n\t" \
    "v_exp_f32 v148, v132\n\t" \
    "v_exp_f32 v149, v133\n\t" \
    "v_exp_f32 v150, v134\n\t" \
    "v_exp_f32 v151, v135\n\t" \
    "v_exp_f32 v152, v136\n\t" \
    "v_exp_f32 v153, v137\n\t" \
    "v_exp_f32 v154, v138\n\t" \
    "v_exp_f32 v155, v139\n\t" \
    "v_exp_f32 v156, v140\n\t" \
    "v_exp_f32 v157, v141\n\t" \
    "v_exp_f32 v158, v142\n\t" \
    "v_exp_f32 v159, v143\n\t" \
    "v_mul_f32 v236, v236, v240\n\t" \
    "v_mul_f32 v0, v0, v240\n\t" \
    "v_mul_f32 v1, v1, v240\n\t" \
    "v_mul_f32 v2, v2, v240\n\t" \
    "v_mul_f32 v3, v3, v240\n\t" \
    "v_mul_f32 v4, v4, v240\n\t" \
    "v_mul_f32 v5, v5, v240\n\t" \
    "v_mul_f32 v6, v6, v240\n\t" \
    "v_mul_f32 v7, v7, v240\n\t" \
    "v_mul_f32 v8, v8, v240\n\t" \
    "v_mul_f32 v9, v9, v240\n\t" \
    "v_mul_f32 v10, v10, v240\n\t" \
    "v_mul_f32 v11, v11, v240\n\t" \
    "v_mul_f32 v12, v12, v240\n\t" \
    "v_mul_f32 v13, v13, v240\n\t" \
    "v_mul_f32 v14, v14, v240\n\t" \
    "v_mul_f32 v15, v15, v240\n\t" \
    "v_mul_f32 v16, v16, v240\n\t" \
    "v_mul_f32 v17, v17, v240\n\t" \
    "v_mul_f32 v18, v18, v240\n\t" \
    "v_mul_f32 v19, v19, v240\n\t" \
    "v_mul_f32 v20, v20, v240\n\t" \
    "v_mul_f32 v21, v21, v240\n\t" \
    "v_mul_f32 v22, v22, v240\n\t" \
    "v_mul_f32 v23, v23, v240\n\t" \
    "v_mul_f32 v24, v24, v240\n\t" \
    "v_mul_f32 v25, v25, v240\n\t" \
    "v_mul_f32 v26, v26, v240\n\t" \
    "v_mul_f32 v27, v27, v240\n\t" \
    "v_mul_f32 v28, v28, v240\n\t" \
    "v_mul_f32 v29, v29, v240\n\t" \
    "v_mul_f32 v30, v30, v240\n\t" \
    "v_mul_f32 v31, v31, v240\n\t" \
    "v_mul_f32 v32, v32, v240\n\t" \
    "v_mul_f32 v33, v33, v240\n\t" \
    "v_mul_f32 v34, v34, v240\n\t" \
    "v_mul_f32 v35, v35, v240\n\t" \
    "v_mul_f32 v36, v36, v240\n\t" \
    "v_mul_f32 v37, v37, v240\n\t" \
    "v_mul_f32 v38, v38, v240\n\t" \
    "v_mul_f32 v39, v39, v240\n\t" \
    "v_mul_f32 v40, v40, v240\n\t" \
    "v_mul_f32 v41, v41, v240\n\t" \
    "v_mul_f32 v42, v42, v240\n\t" \
    "v_mul_f32 v43, v43, v240\n\t" \
    "v_mul_f32 v44, v44, v240\n\t" \
    "v_mul_f32 v45, v45, v240\n\t" \
    "v_mul_f32 v46, v46, v240\n\t" \
    "v_mul_f32 v47, v47, v240\n\t" \
    "v_mul_f32 v48, v48, v240\n\t" \
    "v_mul_f32 v49, v49, v240\n\t" \
    "v_mul_f32 v50, v50, v240\n\t" \
    "v_mul_f32 v51, v51, v240\n\t" \
    "v_mul_f32 v52, v52, v240\n\t" \
    "v_mul_f32 v53, v53, v240\n\t" \
    "v_mul_f32 v54, v54, v240\n\t" \
    "v_mul_f32 v55, v55, v240\n\t" \
    "v_mul_f32 v56, v56, v240\n\t" \
    "v_mul_f32 v57, v57, v240\n\t" \
    "v_mul_f32 v58, v58, v240\n\t" \
    "v_mul_f32 v59, v59, v240\n\t" \
    "v_mul_f32 v60, v60, v240\n\t" \
    "v_mul_f32 v61, v61, v240\n\t" \
    "v_mul_f32 v62, v62, v240\n\t" \
    "v_mul_f32 v63, v63, v240\n\t" \
    "v_add_f32 v237, v144, v145\n\t" \
    "v_add_f32 v237, v237, v146\n\t" \
    "v_add_f32 v237, v237, v147\n\t" \
    "v_add_f32 v237, v237, v148\n\t" \
    "v_add_f32 v237, v237, v149\n\t" \
    "v_add_f32 v237, v237, v150\n\t" \
    "v_add_f32 v237, v237, v151\n\t" \
    "v_add_f32 v237, v237, v152\n\t" \
    "v_add_f32 v237, v237, v153\n\t" \
    "v_add_f32 v237, v237, v154\n\t" \
    "v_add_f32 v237, v237, v155\n\t" \
    "v_add_f32 v237, v237, v156\n\t" \
    "v_add_f32 v237, v237, v157\n\t" \
    "v_add_f32 v237, v237, v158\n\t" \
    "v_add_f32 v237, v237, v159\n\t" \
    "v_cvt_pk_bf16_f32 v160, v144, v145\n\t" \
    "v_cvt_pk_bf16_f32 v161, v146, v147\n\t" \
    "v_cvt_pk_bf16_f32 v162, v148, v149\n\t" \
    "v_cvt_pk_bf16_f32 v163, v150, v151\n\t" \
    "v_cvt_pk_bf16_f32 v164, v152, v153\n\t" \
    "v_cvt_pk_bf16_f32 v165, v154, v155\n\t" \
    "v_cvt_pk_bf16_f32 v166, v156, v157\n\t" \
    "v_cvt_pk_bf16_f32 v167, v158, v159\n\t" \
    "s_nop 1\n\t" \
    "s_branch BACKA_%=\n\t" \
    "RAREB_%=:\n\t" \
    "s_nop 15\n\t" \
    "v_max3_f32 v239, v112, v113, v114\n\t" \
    "v_max3_f32 v239, v239, v115, v116\n\t" \
    "v_max3_f32 v239, v239, v117, v118\n\t" \
    "v_max3_f32 v239, v239, v119, v120\n\t" \
    "v_max3_f32 v239, v239, v121, v122\n\t" \
    "v_max3_f32 v239, v239, v123, v124\n\t" \
    "v_max3_f32 v239, v239, v125, v126\n\t" \
    "v_max_f32 v239, v239, v127\n\t" \
    "v_max_f32 v239, 0, v239\n\t" \
    "v_mov_b32 v240, v239\n\t" \
    "s_nop 1\n\t" \
    "v_permlane32_swap_b32 v239, v240\n\t" \
    "s_nop 1\n\t" \
    "v_max_f32 v239, v239, v240\n\t" \
    "v_add_f32 v238, v238, v239\n\t" \
    "v_exp_f32 v240, -v239\n\t" \
    "v_sub_f32 v241, 0, v238\n\t" \
    "v_sub_f32 v112, v112, v239\n\t" \
    "v_sub_f32 v128, v128, v239\n\t" \
    "v_mov_b32 v96, v241\n\t" \
    "v_sub_f32 v113, v113, v239\n\t" \
    "v_sub_f32 v129, v129, v239\n\t" \
    "v_mov_b32 v97, v241\n\t" \
    "v_sub_f32 v114, v114, v239\n\t" \
    "v_sub_f32 v130, v130, v239\n\t" \
    "v_mov_b32 v98, v241\n\t" \
    "v_sub_f32 v115, v115, v239\n\t" \
    "v_sub_f32 v131, v131, v239\n\t" \
    "v_mov_b32 v99, v241\n\t" \
    "v_sub_f32 v116, v116, v239\n\t" \
    "v_sub_f32 v132, v132, v239\n\t" \
    "v_mov_b32 v100, v241\n\t" \
    "v_sub_f32 v117, v117, v239\n\t" \
    "v_sub_f32 v133, v133, v239\n\t" \
    "v_mov_b32 v101, v241\n\t" \
    "v_sub_f32 v118, v118, v239\n\t" \
    "v_sub_f32 v134, v134, v239\n\t" \
    "v_mov_b32 v102, v241\n\t" \
    "v_sub_f32 v119, v119, v239\n\t" \
    "v_sub_f32 v135, v135, v239\n\t" \
    "v_mov_b32 v103, v241\n\t" \
    "v_sub_f32 v120, v120, v239\n\t" \
    "v_sub_f32 v136, v136, v239\n\t" \
    "v_mov_b32 v104, v241\n\t" \
    "v_sub_f32 v121, v121, v239\n\t" \
    "v_sub_f32 v137, v137, v239\n\t" \
    "v_mov_b32 v105, v241\n\t" \
    "v_sub_f32 v122, v122, v239\n\t" \
    "v_sub_f32 v138, v138, v239\n\t" \
    "v_mov_b32 v106, v241\n\t" \
    "v_sub_f32 v123, v123, v239\n\t" \
    "v_sub_f32 v139, v139, v239\n\t" \
    "v_mov_b32 v107, v241\n\t" \
    "v_sub_f32 v124, v124, v239\n\t" \
    "v_sub_f32 v140, v140, v239\n\t" \
    "v_mov_b32 v108, v241\n\t" \
    "v_sub_f32 v125, v125, v239\n\t" \
    "v_sub_f32 v141, v141, v239\n\t" \
    "v_mov_b32 v109, v241\n\t" \
    "v_sub_f32 v126, v126, v239\n\t" \
    "v_sub_f32 v142, v142, v239\n\t" \
    "v_mov_b32 v110, v241\n\t" \
    "v_sub_f32 v127, v127, v239\n\t" \
    "v_sub_f32 v143, v143, v239\n\t" \
    "v_mov_b32 v111, v241\n\t" \
    "v_exp_f32 v144, v112\n\t" \
    "v_exp_f32 v145, v113\n\t" \
    "v_exp_f32 v146, v114\n\t" \
    "v_exp_f32 v147, v115\n\t" \
    "v_exp_f32 v148, v116\n\t" \
    "v_exp_f32 v149, v117\n\t" \
    "v_exp_f32 v150, v118\n\t" \
    "v_exp_f32 v151, v119\n\t" \
    "v_exp_f32 v152, v120\n\t" \
    "v_exp_f32 v153, v121\n\t" \
    "v_exp_f32 v154, v122\n\t" \
    "v_exp_f32 v155, v123\n\t" \
    "v_exp_f32 v156, v124\n\t" \
    "v_exp_f32 v157, v125\n\t" \
    "v_exp_f32 v158, v126\n\t" \
    "v_exp_f32 v159, v127\n\t" \
    "v_mul_f32 v236, v236, v240\n\t" \
    "v_mul_f32 v0, v0, v240\n\t" \
    "v_mul_f32 v1, v1, v240\n\t" \
    "v_mul_f32 v2, v2, v240\n\t" \
    "v_mul_f32 v3, v3, v240\n\t" \
    "v_mul_f32 v4, v4, v240\n\t" \
    "v_mul_f32 v5, v5, v240\n\t" \
    "v_mul_f32 v6, v6, v240\n\t" \
    "v_mul_f32 v7, v7, v240\n\t" \
    "v_mul_f32 v8, v8, v240\n\t" \
    "v_mul_f32 v9, v9, v240\n\t" \
    "v_mul_f32 v10, v10, v240\n\t" \
    "v_mul_f32 v11, v11, v240\n\t" \
    "v_mul_f32 v12, v12, v240\n\t" \
    "v_mul_f32 v13, v13, v240\n\t" \
    "v_mul_f32 v14, v14, v240\n\t" \
    "v_mul_f32 v15, v15, v240\n\t" \
    "v_mul_f32 v16, v16, v240\n\t" \
    "v_mul_f32 v17, v17, v240\n\t" \
    "v_mul_f32 v18, v18, v240\n\t" \
    "v_mul_f32 v19, v19, v240\n\t" \
    "v_mul_f32 v20, v20, v240\n\t" \
    "v_mul_f32 v21, v21, v240\n\t" \
    "v_mul_f32 v22, v22, v240\n\t" \
    "v_mul_f32 v23, v23, v240\n\t" \
    "v_mul_f32 v24, v24, v240\n\t" \
    "v_mul_f32 v25, v25, v240\n\t" \
    "v_mul_f32 v26, v26, v240\n\t" \
    "v_mul_f32 v27, v27, v240\n\t" \
    "v_mul_f32 v28, v28, v240\n\t" \
    "v_mul_f32 v29, v29, v240\n\t" \
    "v_mul_f32 v30, v30, v240\n\t" \
    "v_mul_f32 v31, v31, v240\n\t" \
    "v_mul_f32 v32, v32, v240\n\t" \
    "v_mul_f32 v33, v33, v240\n\t" \
    "v_mul_f32 v34, v34, v240\n\t" \
    "v_mul_f32 v35, v35, v240\n\t" \
    "v_mul_f32 v36, v36, v240\n\t" \
    "v_mul_f32 v37, v37, v240\n\t" \
    "v_mul_f32 v38, v38, v240\n\t" \
    "v_mul_f32 v39, v39, v240\n\t" \
    "v_mul_f32 v40, v40, v240\n\t" \
    "v_mul_f32 v41, v41, v240\n\t" \
    "v_mul_f32 v42, v42, v240\n\t" \
    "v_mul_f32 v43, v43, v240\n\t" \
    "v_mul_f32 v44, v44, v240\n\t" \
    "v_mul_f32 v45, v45, v240\n\t" \
    "v_mul_f32 v46, v46, v240\n\t" \
    "v_mul_f32 v47, v47, v240\n\t" \
    "v_mul_f32 v48, v48, v240\n\t" \
    "v_mul_f32 v49, v49, v240\n\t" \
    "v_mul_f32 v50, v50, v240\n\t" \
    "v_mul_f32 v51, v51, v240\n\t" \
    "v_mul_f32 v52, v52, v240\n\t" \
    "v_mul_f32 v53, v53, v240\n\t" \
    "v_mul_f32 v54, v54, v240\n\t" \
    "v_mul_f32 v55, v55, v240\n\t" \
    "v_mul_f32 v56, v56, v240\n\t" \
    "v_mul_f32 v57, v57, v240\n\t" \
    "v_mul_f32 v58, v58, v240\n\t" \
    "v_mul_f32 v59, v59, v240\n\t" \
    "v_mul_f32 v60, v60, v240\n\t" \
    "v_mul_f32 v61, v61, v240\n\t" \
    "v_mul_f32 v62, v62, v240\n\t" \
    "v_mul_f32 v63, v63, v240\n\t" \
    "v_add_f32 v237, v144, v145\n\t" \
    "v_add_f32 v237, v237, v146\n\t" \
    "v_add_f32 v237, v237, v147\n\t" \
    "v_add_f32 v237, v237, v148\n\t" \
    "v_add_f32 v237, v237, v149\n\t" \
    "v_add_f32 v237, v237, v150\n\t" \
    "v_add_f32 v237, v237, v151\n\t" \
    "v_add_f32 v237, v237, v152\n\t" \
    "v_add_f32 v237, v237, v153\n\t" \
    "v_add_f32 v237, v237, v154\n\t" \
    "v_add_f32 v237, v237, v155\n\t" \
    "v_add_f32 v237, v237, v156\n\t" \
    "v_add_f32 v237, v237, v157\n\t" \
    "v_add_f32 v237, v237, v158\n\t" \
    "v_add_f32 v237, v237, v159\n\t" \
    "v_cvt_pk_bf16_f32 v160, v144, v145\n\t" \
    "v_cvt_pk_bf16_f32 v161, v146, v147\n\t" \
    "v_cvt_pk_bf16_f32 v162, v148, v149\n\t" \
    "v_cvt_pk_bf16_f32 v163, v150, v151\n\t" \
    "v_cvt_pk_bf16_f32 v164, v152, v153\n\t" \
    "v_cvt_pk_bf16_f32 v165, v154, v155\n\t" \
    "v_cvt_pk_bf16_f32 v166, v156, v157\n\t" \
    "v_cvt_pk_bf16_f32 v167, v158, v159\n\t" \
    "s_nop 1\n\t" \
    "s_branch BACKB_%=\n\t" \
    "RAREC_%=:\n\t" \
    "s_nop 15\n\t" \
    "v_max3_f32 v239, v128, v129, v130\n\t" \
    "v_max3_f32 v239, v239, v131, v132\n\t" \
    "v_max3_f32 v239, v239, v133, v134\n\t" \
    "v_max3_f32 v239, v239, v135, v136\n\t" \
    "v_max3_f32 v239, v239, v137, v138\n\t" \
    "v_max3_f32 v239, v239, v139, v140\n\t" \
    "v_max3_f32 v239, v239, v141, v142\n\t" \
    "v_max_f32 v239, v239, v143\n\t" \
    "v_max_f32 v239, 0, v239\n\t" \
    "v_mov_b32 v240, v239\n\t" \
    "s_nop 1\n\t" \
    "v_permlane32_swap_b32 v239, v240\n\t" \
    "s_nop 1\n\t" \
    "v_max_f32 v239, v239, v240\n\t" \
    "v_add_f32 v238, v238, v239\n\t" \
    "v_exp_f32 v240, -v239\n\t" \
    "v_sub_f32 v241, 0, v238\n\t" \
    "v_sub_f32 v128, v128, v239\n\t" \
    "v_sub_f32 v112, v112, v239\n\t" \
    "v_mov_b32 v96, v241\n\t" \
    "v_sub_f32 v129, v129, v239\n\t" \
    "v_sub_f32 v113, v113, v239\n\t" \
    "v_mov_b32 v97, v241\n\t" \
    "v_sub_f32 v130, v130, v239\n\t" \
    "v_sub_f32 v114, v114, v239\n\t" \
    "v_mov_b32 v98, v241\n\t" \
    "v_sub_f32 v131, v131, v239\n\t" \
    "v_sub_f32 v115, v115, v239\n\t" \
    "v_mov_b32 v99, v241\n\t" \
    "v_sub_f32 v132, v132, v239\n\t" \
    "v_sub_f32 v116, v116, v239\n\t" \
    "v_mov_b32 v100, v241\n\t" \
    "v_sub_f32 v133, v133, v239\n\t" \
    "v_sub_f32 v117, v117, v239\n\t" \
    "v_mov_b32 v101, v241\n\t" \
    "v_sub_f32 v134, v134, v239\n\t" \
    "v_sub_f32 v118, v118, v239\n\t" \
    "v_mov_b32 v102, v241\n\t" \
    "v_sub_f32 v135, v135, v239\n\t" \
    "v_sub_f32 v119, v119, v239\n\t" \
    "v_mov_b32 v103, v241\n\t" \
    "v_sub_f32 v136, v136, v239\n\t" \
    "v_sub_f32 v120, v120, v239\n\t" \
    "v_mov_b32 v104, v241\n\t" \
    "v_sub_f32 v137, v137, v239\n\t" \
    "v_sub_f32 v121, v121, v239\n\t" \
    "v_mov_b32 v105, v241\n\t" \
    "v_sub_f32 v138, v138, v239\n\t" \
    "v_sub_f32 v122, v122, v239\n\t" \
    "v_mov_b32 v106, v241\n\t" \
    "v_sub_f32 v139, v139, v239\n\t" \
    "v_sub_f32 v123, v123, v239\n\t" \
    "v_mov_b32 v107, v241\n\t" \
    "v_sub_f32 v140, v140, v239\n\t" \
    "v_sub_f32 v124, v124, v239\n\t" \
    "v_mov_b32 v108, v241\n\t" \
    "v_sub_f32 v141, v141, v239\n\t" \
    "v_sub_f32 v125, v125, v239\n\t" \
    "v_mov_b32 v109, v241\n\t" \
    "v_sub_f32 v142, v142, v239\n\t" \
    "v_sub_f32 v126, v126, v239\n\t" \
    "v_mov_b32 v110, v241\n\t" \
    "v_sub_f32 v143, v143, v239\n\t" \
    "v_sub_f32 v127, v127, v239\n\t" \
    "v_mov_b32 v111, v241\n\t" \
    "v_exp_f32 v144, v128\n\t" \
    "v_exp_f32 v145, v129\n\t" \
    "v_exp_f32 v146, v130\n\t" \
    "v_exp_f32 v147, v131\n\t" \
    "v_exp_f32 v148, v132\n\t" \
    "v_exp_f32 v149, v133\n\t" \
    "v_exp_f32 v150, v134\n\t" \
    "v_exp_f32 v151, v135\n\t" \
    "v_exp_f32 v152, v136\n\t" \
    "v_exp_f32 v153, v137\n\t" \
    "v_exp_f32 v154, v138\n\t" \
    "v_exp_f32 v155, v139\n\t" \
    "v_exp_f32 v156, v140\n\t" \
    "v_exp_f32 v157, v141\n\t" \
    "v_exp_f32 v158, v142\n\t" \
    "v_exp_f32 v159, v143\n\t" \
    "v_mul_f32 v236, v236, v240\n\t" \
    "v_mul_f32 v0, v0, v240\n\t" \
    "v_mul_f32 v1, v1, v240\n\t" \
    "v_mul_f32 v2, v2, v240\n\t" \
    "v_mul_f32 v3, v3, v240\n\t" \
    "v_mul_f32 v4, v4, v240\n\t" \
    "v_mul_f32 v5, v5, v240\n\t" \
    "v_mul_f32 v6, v6, v240\n\t" \
    "v_mul_f32 v7, v7, v240\n\t" \
    "v_mul_f32 v8, v8, v240\n\t" \
    "v_mul_f32 v9, v9, v240\n\t" \
    "v_mul_f32 v10, v10, v240\n\t" \
    "v_mul_f32 v11, v11, v240\n\t" \
    "v_mul_f32 v12, v12, v240\n\t" \
    "v_mul_f32 v13, v13, v240\n\t" \
    "v_mul_f32 v14, v14, v240\n\t" \
    "v_mul_f32 v15, v15, v240\n\t" \
    "v_mul_f32 v16, v16, v240\n\t" \
    "v_mul_f32 v17, v17, v240\n\t" \
    "v_mul_f32 v18, v18, v240\n\t" \
    "v_mul_f32 v19, v19, v240\n\t" \
    "v_mul_f32 v20, v20, v240\n\t" \
    "v_mul_f32 v21, v21, v240\n\t" \
    "v_mul_f32 v22, v22, v240\n\t" \
    "v_mul_f32 v23, v23, v240\n\t" \
    "v_mul_f32 v24, v24, v240\n\t" \
    "v_mul_f32 v25, v25, v240\n\t" \
    "v_mul_f32 v26, v26, v240\n\t" \
    "v_mul_f32 v27, v27, v240\n\t" \
    "v_mul_f32 v28, v28, v240\n\t" \
    "v_mul_f32 v29, v29, v240\n\t" \
    "v_mul_f32 v30, v30, v240\n\t" \
    "v_mul_f32 v31, v31, v240\n\t" \
    "v_mul_f32 v32, v32, v240\n\t" \
    "v_mul_f32 v33, v33, v240\n\t" \
    "v_mul_f32 v34, v34, v240\n\t" \
    "v_mul_f32 v35, v35, v240\n\t" \
    "v_mul_f32 v36, v36, v240\n\t" \
    "v_mul_f32 v37, v37, v240\n\t" \
    "v_mul_f32 v38, v38, v240\n\t" \
    "v_mul_f32 v39, v39, v240\n\t" \
    "v_mul_f32 v40, v40, v240\n\t" \
    "v_mul_f32 v41, v41, v240\n\t" \
    "v_mul_f32 v42, v42, v240\n\t" \
    "v_mul_f32 v43, v43, v240\n\t" \
    "v_mul_f32 v44, v44, v240\n\t" \
    "v_mul_f32 v45, v45, v240\n\t" \
    "v_mul_f32 v46, v46, v240\n\t" \
    "v_mul_f32 v47, v47, v240\n\t" \
    "v_mul_f32 v48, v48, v240\n\t" \
    "v_mul_f32 v49, v49, v240\n\t" \
    "v_mul_f32 v50, v50, v240\n\t" \
    "v_mul_f32 v51, v51, v240\n\t" \
    "v_mul_f32 v52, v52, v240\n\t" \
    "v_mul_f32 v53, v53, v240\n\t" \
    "v_mul_f32 v54, v54, v240\n\t" \
    "v_mul_f32 v55, v55, v240\n\t" \
    "v_mul_f32 v56, v56, v240\n\t" \
    "v_mul_f32 v57, v57, v240\n\t" \
    "v_mul_f32 v58, v58, v240\n\t" \
    "v_mul_f32 v59, v59, v240\n\t" \
    "v_mul_f32 v60, v60, v240\n\t" \
    "v_mul_f32 v61, v61, v240\n\t" \
    "v_mul_f32 v62, v62, v240\n\t" \
    "v_mul_f32 v63, v63, v240\n\t" \
    "v_add_f32 v237, v144, v145\n\t" \
    "v_add_f32 v237, v237, v146\n\t" \
    "v_add_f32 v237, v237, v147\n\t" \
    "v_add_f32 v237, v237, v148\n\t" \
    "v_add_f32 v237, v237, v149\n\t" \
    "v_add_f32 v237, v237, v150\n\t" \
    "v_add_f32 v237, v237, v151\n\t" \
    "v_add_f32 v237, v237, v152\n\t" \
    "v_add_f32 v237, v237, v153\n\t" \
    "v_add_f32 v237, v237, v154\n\t" \
    "v_add_f32 v237, v237, v155\n\t" \
    "v_add_f32 v237, v237, v156\n\t" \
    "v_add_f32 v237, v237, v157\n\t" \
    "v_add_f32 v237, v237, v158\n\t" \
    "v_add_f32 v237, v237, v159\n\t" \
    "v_cvt_pk_bf16_f32 v160, v144, v145\n\t" \
    "v_cvt_pk_bf16_f32 v161, v146, v147\n\t" \
    "v_cvt_pk_bf16_f32 v162, v148, v149\n\t" \
    "v_cvt_pk_bf16_f32 v163, v150, v151\n\t" \
    "v_cvt_pk_bf16_f32 v164, v152, v153\n\t" \
    "v_cvt_pk_bf16_f32 v165, v154, v155\n\t" \
    "v_cvt_pk_bf16_f32 v166, v156, v157\n\t" \
    "v_cvt_pk_bf16_f32 v167, v158, v159\n\t" \
    "s_nop 1\n\t" \
    "s_branch BACKC_%=\n\t" \
    "RARED_%=:\n\t" \
    "s_nop 15\n\t" \
    "v_max3_f32 v239, v112, v113, v114\n\t" \
    "v_max3_f32 v239, v239, v115, v116\n\t" \
    "v_max3_f32 v239, v239, v117, v118\n\t" \
    "v_max3_f32 v239, v239, v119, v120\n\t" \
    "v_max3_f32 v239, v239, v121, v122\n\t" \
    "v_max3_f32 v239, v239, v123, v124\n\t" \
    "v_max3_f32 v239, v239, v125, v126\n\t" \
    "v_max_f32 v239, v239, v127\n\t" \
    "v_max_f32 v239, 0, v239\n\t" \
    "v_mov_b32 v240, v239\n\t" \
    "s_nop 1\n\t" \
    "v_permlane32_swap_b32 v239, v240\n\t" \
    "s_nop 1\n\t" \
    "v_max_f32 v239, v239, v240\n\t" \
    "v_add_f32 v238, v238, v239\n\t" \
    "v_exp_f32 v240, -v239\n\t" \
    "v_sub_f32 v241, 0, v238\n\t" \
    "v_sub_f32 v112, v112, v239\n\t" \
    "v_sub_f32 v128, v128, v239\n\t" \
    "v_mov_b32 v96, v241\n\t" \
    "v_sub_f32 v113, v113, v239\n\t" \
    "v_sub_f32 v129, v129, v239\n\t" \
    "v_mov_b32 v97, v241\n\t" \
    "v_sub_f32 v114, v114, v239\n\t" \
    "v_sub_f32 v130, v130, v239\n\t" \
    "v_mov_b32 v98, v241\n\t" \
    "v_sub_f32 v115, v115, v239\n\t" \
    "v_sub_f32 v131, v131, v239\n\t" \
    "v_mov_b32 v99, v241\n\t" \
    "v_sub_f32 v116, v116, v239\n\t" \
    "v_sub_f32 v132, v132, v239\n\t" \
    "v_mov_b32 v100, v241\n\t" \
    "v_sub_f32 v117, v117, v239\n\t" \
    "v_sub_f32 v133, v133, v239\n\t" \
    "v_mov_b32 v101, v241\n\t" \
    "v_sub_f32 v118, v118, v239\n\t" \
    "v_sub_f32 v134, v134, v239\n\t" \
    "v_mov_b32 v102, v241\n\t" \
    "v_sub_f32 v119, v119, v239\n\t" \
    "v_sub_f32 v135, v135, v239\n\t" \
    "v_mov_b32 v103, v241\n\t" \
    "v_sub_f32 v120, v120, v239\n\t" \
    "v_sub_f32 v136, v136, v239\n\t" \
    "v_mov_b32 v104, v241\n\t" \
    "v_sub_f32 v121, v121, v239\n\t" \
    "v_sub_f32 v137, v137, v239\n\t" \
    "v_mov_b32 v105, v241\n\t" \
    "v_sub_f32 v122, v122, v239\n\t" \
    "v_sub_f32 v138, v138, v239\n\t" \
    "v_mov_b32 v106, v241\n\t" \
    "v_sub_f32 v123, v123, v239\n\t" \
    "v_sub_f32 v139, v139, v239\n\t" \
    "v_mov_b32 v107, v241\n\t" \
    "v_sub_f32 v124, v124, v239\n\t" \
    "v_sub_f32 v140, v140, v239\n\t" \
    "v_mov_b32 v108, v241\n\t" \
    "v_sub_f32 v125, v125, v239\n\t" \
    "v_sub_f32 v141, v141, v239\n\t" \
    "v_mov_b32 v109, v241\n\t" \
    "v_sub_f32 v126, v126, v239\n\t" \
    "v_sub_f32 v142, v142, v239\n\t" \
    "v_mov_b32 v110, v241\n\t" \
    "v_sub_f32 v127, v127, v239\n\t" \
    "v_sub_f32 v143, v143, v239\n\t" \
    "v_mov_b32 v111, v241\n\t" \
    "v_exp_f32 v144, v112\n\t" \
    "v_exp_f32 v145, v113\n\t" \
    "v_exp_f32 v146, v114\n\t" \
    "v_exp_f32 v147, v115\n\t" \
    "v_exp_f32 v148, v116\n\t" \
    "v_exp_f32 v149, v117\n\t" \
    "v_exp_f32 v150, v118\n\t" \
    "v_exp_f32 v151, v119\n\t" \
    "v_exp_f32 v152, v120\n\t" \
    "v_exp_f32 v153, v121\n\t" \
    "v_exp_f32 v154, v122\n\t" \
    "v_exp_f32 v155, v123\n\t" \
    "v_exp_f32 v156, v124\n\t" \
    "v_exp_f32 v157, v125\n\t" \
    "v_exp_f32 v158, v126\n\t" \
    "v_exp_f32 v159, v127\n\t" \
    "v_mul_f32 v236, v236, v240\n\t" \
    "v_mul_f32 v0, v0, v240\n\t" \
    "v_mul_f32 v1, v1, v240\n\t" \
    "v_mul_f32 v2, v2, v240\n\t" \
    "v_mul_f32 v3, v3, v240\n\t" \
    "v_mul_f32 v4, v4, v240\n\t" \
    "v_mul_f32 v5, v5, v240\n\t" \
    "v_mul_f32 v6, v6, v240\n\t" \
    "v_mul_f32 v7, v7, v240\n\t" \
    "v_mul_f32 v8, v8, v240\n\t" \
    "v_mul_f32 v9, v9, v240\n\t" \
    "v_mul_f32 v10, v10, v240\n\t" \
    "v_mul_f32 v11, v11, v240\n\t" \
    "v_mul_f32 v12, v12, v240\n\t" \
    "v_mul_f32 v13, v13, v240\n\t" \
    "v_mul_f32 v14, v14, v240\n\t" \
    "v_mul_f32 v15, v15, v240\n\t" \
    "v_mul_f32 v16, v16, v240\n\t" \
    "v_mul_f32 v17, v17, v240\n\t" \
    "v_mul_f32 v18, v18, v240\n\t" \
    "v_mul_f32 v19, v19, v240\n\t" \
    "v_mul_f32 v20, v20, v240\n\t" \
    "v_mul_f32 v21, v21, v240\n\t" \
    "v_mul_f32 v22, v22, v240\n\t" \
    "v_mul_f32 v23, v23, v240\n\t" \
    "v_mul_f32 v24, v24, v240\n\t" \
    "v_mul_f32 v25, v25, v240\n\t" \
    "v_mul_f32 v26, v26, v240\n\t" \
    "v_mul_f32 v27, v27, v240\n\t" \
    "v_mul_f32 v28, v28, v240\n\t" \
    "v_mul_f32 v29, v29, v240\n\t" \
    "v_mul_f32 v30, v30, v240\n\t" \
    "v_mul_f32 v31, v31, v240\n\t" \
    "v_mul_f32 v32, v32, v240\n\t" \
    "v_mul_f32 v33, v33, v240\n\t" \
    "v_mul_f32 v34, v34, v240\n\t" \
    "v_mul_f32 v35, v35, v240\n\t" \
    "v_mul_f32 v36, v36, v240\n\t" \
    "v_mul_f32 v37, v37, v240\n\t" \
    "v_mul_f32 v38, v38, v240\n\t" \
    "v_mul_f32 v39, v39, v240\n\t" \
    "v_mul_f32 v40, v40, v240\n\t" \
    "v_mul_f32 v41, v41, v240\n\t" \
    "v_mul_f32 v42, v42, v240\n\t" \
    "v_mul_f32 v43, v43, v240\n\t" \
    "v_mul_f32 v44, v44, v240\n\t" \
    "v_mul_f32 v45, v45, v240\n\t" \
    "v_mul_f32 v46, v46, v240\n\t" \
    "v_mul_f32 v47, v47, v240\n\t" \
    "v_mul_f32 v48, v48, v240\n\t" \
    "v_mul_f32 v49, v49, v240\n\t" \
    "v_mul_f32 v50, v50, v240\n\t" \
    "v_mul_f32 v51, v51, v240\n\t" \
    "v_mul_f32 v52, v52, v240\n\t" \
    "v_mul_f32 v53, v53, v240\n\t" \
    "v_mul_f32 v54, v54, v240\n\t" \
    "v_mul_f32 v55, v55, v240\n\t" \
    "v_mul_f32 v56, v56, v240\n\t" \
    "v_mul_f32 v57, v57, v240\n\t" \
    "v_mul_f32 v58, v58, v240\n\t" \
    "v_mul_f32 v59, v59, v240\n\t" \
    "v_mul_f32 v60, v60, v240\n\t" \
    "v_mul_f32 v61, v61, v240\n\t" \
    "v_mul_f32 v62, v62, v240\n\t" \
    "v_mul_f32 v63, v63, v240\n\t" \
    "v_add_f32 v237, v144, v145\n\t" \
    "v_add_f32 v237, v237, v146\n\t" \
    "v_add_f32 v237, v237, v147\n\t" \
    "v_add_f32 v237, v237, v148\n\t" \
    "v_add_f32 v237, v237, v149\n\t" \
    "v_add_f32 v237, v237, v150\n\t" \
    "v_add_f32 v237, v237, v151\n\t" \
    "v_add_f32 v237, v237, v152\n\t" \
    "v_add_f32 v237, v237, v153\n\t" \
    "v_add_f32 v237, v237, v154\n\t" \
    "v_add_f32 v237, v237, v155\n\t" \
    "v_add_f32 v237, v237, v156\n\t" \
    "v_add_f32 v237, v237, v157\n\t" \
    "v_add_f32 v237, v237, v158\n\t" \
    "v_add_f32 v237, v237, v159\n\t" \
    "v_cvt_pk_bf16_f32 v160, v144, v145\n\t" \
    "v_cvt_pk_bf16_f32 v161, v146, v147\n\t" \
    "v_cvt_pk_bf16_f32 v162, v148, v149\n\t" \
    "v_cvt_pk_bf16_f32 v163, v150, v151\n\t" \
    "v_cvt_pk_bf16_f32 v164, v152, v153\n\t" \
    "v_cvt_pk_bf16_f32 v165, v154, v155\n\t" \
    "v_cvt_pk_bf16_f32 v166, v156, v157\n\t" \
    "v_cvt_pk_bf16_f32 v167, v158, v159\n\t" \
    "s_nop 1\n\t" \
    "s_branch BACKD_%=\n\t" \
    "RAREE_%=:\n\t" \
    "s_nop 15\n\t" \
    "v_max3_f32 v239, v128, v129, v130\n\t" \
    "v_max3_f32 v239, v239, v131, v132\n\t" \
    "v_max3_f32 v239, v239, v133, v134\n\t" \
    "v_max3_f32 v239, v239, v135, v136\n\t" \
    "v_max3_f32 v239, v239, v137, v138\n\t" \
    "v_max3_f32 v239, v239, v139, v140\n\t" \
    "v_max3_f32 v239, v239, v141, v142\n\t" \
    "v_max_f32 v239, v239, v143\n\t" \
    "v_max_f32 v239, 0, v239\n\t" \
    "v_mov_b32 v240, v239\n\t" \
    "s_nop 1\n\t" \
    "v_permlane32_swap_b32 v239, v240\n\t" \
    "s_nop 1\n\t" \
    "v_max_f32 v239, v239, v240\n\t" \
    "v_add_f32 v238, v238, v239\n\t" \
    "v_exp_f32 v240, -v239\n\t" \
    "v_sub_f32 v241, 0, v238\n\t" \
    "v_sub_f32 v128, v128, v239\n\t" \
    "v_mov_b32 v96, v241\n\t" \
    "v_sub_f32 v129, v129, v239\n\t" \
    "v_mov_b32 v97, v241\n\t" \
    "v_sub_f32 v130, v130, v239\n\t" \
    "v_mov_b32 v98, v241\n\t" \
    "v_sub_f32 v131, v131, v239\n\t" \
    "v_mov_b32 v99, v241\n\t" \
    "v_sub_f32 v132, v132, v239\n\t" \
    "v_mov_b32 v100, v241\n\t" \
    "v_sub_f32 v133, v133, v239\n\t" \
    "v_mov_b32 v101, v241\n\t" \
    "v_sub_f32 v134, v134, v239\n\t" \
    "v_mov_b32 v102, v241\n\t" \
    "v_sub_f32 v135, v135, v239\n\t" \
    "v_mov_b32 v103, v241\n\t" \
    "v_sub_f32 v136, v136, v239\n\t" \
    "v_mov_b32 v104, v241\n\t" \
    "v_sub_f32 v137, v137, v239\n\t" \
    "v_mov_b32 v105, v241\n\t" \
    "v_sub_f32 v138, v138, v239\n\t" \
    "v_mov_b32 v106, v241\n\t" \
    "v_sub_f32 v139, v139, v239\n\t" \
    "v_mov_b32 v107, v241\n\t" \
    "v_sub_f32 v140, v140, v239\n\t" \
    "v_mov_b32 v108, v241\n\t" \
    "v_sub_f32 v141, v141, v239\n\t" \
    "v_mov_b32 v109, v241\n\t" \
    "v_sub_f32 v142, v142, v239\n\t" \
    "v_mov_b32 v110, v241\n\t" \
    "v_sub_f32 v143, v143, v239\n\t" \
    "v_mov_b32 v111, v241\n\t" \
    "v_exp_f32 v144, v128\n\t" \
    "v_exp_f32 v145, v129\n\t" \
    "v_exp_f32 v146, v130\n\t" \
    "v_exp_f32 v147, v131\n\t" \
    "v_exp_f32 v148, v132\n\t" \
    "v_exp_f32 v149, v133\n\t" \
    "v_exp_f32 v150, v134\n\t" \
    "v_exp_f32 v151, v135\n\t" \
    "v_exp_f32 v152, v136\n\t" \
    "v_exp_f32 v153, v137\n\t" \
    "v_exp_f32 v154, v138\n\t" \
    "v_exp_f32 v155, v139\n\t" \
    "v_exp_f32 v156, v140\n\t" \
    "v_exp_f32 v157, v141\n\t" \
    "v_exp_f32 v158, v142\n\t" \
    "v_exp_f32 v159, v143\n\t" \
    "v_mul_f32 v236, v236, v240\n\t" \
    "v_mul_f32 v0, v0, v240\n\t" \
    "v_mul_f32 v1, v1, v240\n\t" \
    "v_mul_f32 v2, v2, v240\n\t" \
    "v_mul_f32 v3, v3, v240\n\t" \
    "v_mul_f32 v4, v4, v240\n\t" \
    "v_mul_f32 v5, v5, v240\n\t" \
    "v_mul_f32 v6, v6, v240\n\t" \
    "v_mul_f32 v7, v7, v240\n\t" \
    "v_mul_f32 v8, v8, v240\n\t" \
    "v_mul_f32 v9, v9, v240\n\t" \
    "v_mul_f32 v10, v10, v240\n\t" \
    "v_mul_f32 v11, v11, v240\n\t" \
    "v_mul_f32 v12, v12, v240\n\t" \
    "v_mul_f32 v13, v13, v240\n\t" \
    "v_mul_f32 v14, v14, v240\n\t" \
    "v_mul_f32 v15, v15, v240\n\t" \
    "v_mul_f32 v16, v16, v240\n\t" \
    "v_mul_f32 v17, v17, v240\n\t" \
    "v_mul_f32 v18, v18, v240\n\t" \
    "v_mul_f32 v19, v19, v240\n\t" \
    "v_mul_f32 v20, v20, v240\n\t" \
    "v_mul_f32 v21, v21, v240\n\t" \
    "v_mul_f32 v22, v22, v240\n\t" \
    "v_mul_f32 v23, v23, v240\n\t" \
    "v_mul_f32 v24, v24, v240\n\t" \
    "v_mul_f32 v25, v25, v240\n\t" \
    "v_mul_f32 v26, v26, v240\n\t" \
    "v_mul_f32 v27, v27, v240\n\t" \
    "v_mul_f32 v28, v28, v240\n\t" \
    "v_mul_f32 v29, v29, v240\n\t" \
    "v_mul_f32 v30, v30, v240\n\t" \
    "v_mul_f32 v31, v31, v240\n\t" \
    "v_mul_f32 v32, v32, v240\n\t" \
    "v_mul_f32 v33, v33, v240\n\t" \
    "v_mul_f32 v34, v34, v240\n\t" \
    "v_mul_f32 v35, v35, v240\n\t" \
    "v_mul_f32 v36, v36, v240\n\t" \
    "v_mul_f32 v37, v37, v240\n\t" \
    "v_mul_f32 v38, v38, v240\n\t" \
    "v_mul_f32 v39, v39, v240\n\t" \
    "v_mul_f32 v40, v40, v240\n\t" \
    "v_mul_f32 v41, v41, v240\n\t" \
    "v_mul_f32 v42, v42, v240\n\t" \
    "v_mul_f32 v43, v43, v240\n\t" \
    "v_mul_f32 v44, v44, v240\n\t" \
    "v_mul_f32 v45, v45, v240\n\t" \
    "v_mul_f32 v46, v46, v240\n\t" \
    "v_mul_f32 v47, v47, v240\n\t" \
    "v_mul_f32 v48, v48, v240\n\t" \
    "v_mul_f32 v49, v49, v240\n\t" \
    "v_mul_f32 v50, v50, v240\n\t" \
    "v_mul_f32 v51, v51, v240\n\t" \
    "v_mul_f32 v52, v52, v240\n\t" \
    "v_mul_f32 v53, v53, v240\n\t" \
    "v_mul_f32 v54, v54, v240\n\t" \
    "v_mul_f32 v55, v55, v240\n\t" \
    "v_mul_f32 v56, v56, v240\n\t" \
    "v_mul_f32 v57, v57, v240\n\t" \
    "v_mul_f32 v58, v58, v240\n\t" \
    "v_mul_f32 v59, v59, v240\n\t" \
    "v_mul_f32 v60, v60, v240\n\t" \
    "v_mul_f32 v61, v61, v240\n\t" \
    "v_mul_f32 v62, v62, v240\n\t" \
    "v_mul_f32 v63, v63, v240\n\t" \
    "v_add_f32 v237, v144, v145\n\t" \
    "v_add_f32 v237, v237, v146\n\t" \
    "v_add_f32 v237, v237, v147\n\t" \
    "v_add_f32 v237, v237, v148\n\t" \
    "v_add_f32 v237, v237, v149\n\t" \
    "v_add_f32 v237, v237, v150\n\t" \
    "v_add_f32 v237, v237, v151\n\t" \
    "v_add_f32 v237, v237, v152\n\t" \
    "v_add_f32 v237, v237, v153\n\t" \
    "v_add_f32 v237, v237, v154\n\t" \
    "v_add_f32 v237, v237, v155\n\t" \
    "v_add_f32 v237, v237, v156\n\t" \
    "v_add_f32 v237, v237, v157\n\t" \
    "v_add_f32 v237, v237, v158\n\t" \
    "v_add_f32 v237, v237, v159\n\t" \
    "v_cvt_pk_bf16_f32 v160, v144, v145\n\t" \
    "v_cvt_pk_bf16_f32 v161, v146, v147\n\t" \
    "v_cvt_pk_bf16_f32 v162, v148, v149\n\t" \
    "v_cvt_pk_bf16_f32 v163, v150, v151\n\t" \
    "v_cvt_pk_bf16_f32 v164, v152, v153\n\t" \
    "v_cvt_pk_bf16_f32 v165, v154, v155\n\t" \
    "v_cvt_pk_bf16_f32 v166, v156, v157\n\t" \
    "v_cvt_pk_bf16_f32 v167, v158, v159\n\t" \
    "s_nop 1\n\t" \
    "s_branch BACKE_%=\n\t" \
    "RAREF_%=:\n\t" \
    "s_nop 15\n\t" \
    "v_max3_f32 v239, v128, v129, v130\n\t" \
    "v_max3_f32 v239, v239, v131, v132\n\t" \
    "v_max3_f32 v239, v239, v133, v134\n\t" \
    "v_max3_f32 v239, v239, v135, v136\n\t" \
    "v_max3_f32 v239, v239, v137, v138\n\t" \
    "v_max3_f32 v239, v239, v139, v140\n\t" \
    "v_max3_f32 v239, v239, v141, v142\n\t" \
    "v_max_f32 v239, v239, v143\n\t" \
    "v_max_f32 v239, 0, v239\n\t" \
    "v_mov_b32 v240, v239\n\t" \
    "s_nop 1\n\t" \
    "v_permlane32_swap_b32 v239, v240\n\t" \
    "s_nop 1\n\t" \
    "v_max_f32 v239, v239, v240\n\t" \
    "v_add_f32 v238, v238, v239\n\t" \
    "v_exp_f32 v240, -v239\n\t" \
    "v_sub_f32 v241, 0, v238\n\t" \
    "v_sub_f32 v128, v128, v239\n\t" \
    "v_sub_f32 v112, v112, v239\n\t" \
    "v_mov_b32 v96, v241\n\t" \
    "v_sub_f32 v129, v129, v239\n\t" \
    "v_sub_f32 v113, v113, v239\n\t" \
    "v_mov_b32 v97, v241\n\t" \
    "v_sub_f32 v130, v130, v239\n\t" \
    "v_sub_f32 v114, v114, v239\n\t" \
    "v_mov_b32 v98, v241\n\t" \
    "v_sub_f32 v131, v131, v239\n\t" \
    "v_sub_f32 v115, v115, v239\n\t" \
    "v_mov_b32 v99, v241\n\t" \
    "v_sub_f32 v132, v132, v239\n\t" \
    "v_sub_f32 v116, v116, v239\n\t" \
    "v_mov_b32 v100, v241\n\t" \
    "v_sub_f32 v133, v133, v239\n\t" \
    "v_sub_f32 v117, v117, v239\n\t" \
    "v_mov_b32 v101, v241\n\t" \
    "v_sub_f32 v134, v134, v239\n\t" \
    "v_sub_f32 v118, v118, v239\n\t" \
    "v_mov_b32 v102, v241\n\t" \
    "v_sub_f32 v135, v135, v239\n\t" \
    "v_sub_f32 v119, v119, v239\n\t" \
    "v_mov_b32 v103, v241\n\t" \
    "v_sub_f32 v136, v136, v239\n\t" \
    "v_sub_f32 v120, v120, v239\n\t" \
    "v_mov_b32 v104, v241\n\t" \
    "v_sub_f32 v137, v137, v239\n\t" \
    "v_sub_f32 v121, v121, v239\n\t" \
    "v_mov_b32 v105, v241\n\t" \
    "v_sub_f32 v138, v138, v239\n\t" \
    "v_sub_f32 v122, v122, v239\n\t" \
    "v_mov_b32 v106, v241\n\t" \
    "v_sub_f32 v139, v139, v239\n\t" \
    "v_sub_f32 v123, v123, v239\n\t" \
    "v_mov_b32 v107, v241\n\t" \
    "v_sub_f32 v140, v140, v239\n\t" \
    "v_sub_f32 v124, v124, v239\n\t" \
    "v_mov_b32 v108, v241\n\t" \
    "v_sub_f32 v141, v141, v239\n\t" \
    "v_sub_f32 v125, v125, v239\n\t" \
    "v_mov_b32 v109, v241\n\t" \
    "v_sub_f32 v142, v142, v239\n\t" \
    "v_sub_f32 v126, v126, v239\n\t" \
    "v_mov_b32 v110, v241\n\t" \
    "v_sub_f32 v143, v143, v239\n\t" \
    "v_sub_f32 v127, v127, v239\n\t" \
    "v_mov_b32 v111, v241\n\t" \
    "v_exp_f32 v144, v128\n\t" \
    "v_exp_f32 v145, v129\n\t" \
    "v_exp_f32 v146, v130\n\t" \
    "v_exp_f32 v147, v131\n\t" \
    "v_exp_f32 v148, v132\n\t" \
    "v_exp_f32 v149, v133\n\t" \
    "v_exp_f32 v150, v134\n\t" \
    "v_exp_f32 v151, v135\n\t" \
    "v_exp_f32 v152, v136\n\t" \
    "v_exp_f32 v153, v137\n\t" \
    "v_exp_f32 v154, v138\n\t" \
    "v_exp_f32 v155, v139\n\t" \
    "v_exp_f32 v156, v140\n\t" \
    "v_exp_f32 v157, v141\n\t" \
    "v_exp_f32 v158, v142\n\t" \
    "v_exp_f32 v159, v143\n\t" \
    "v_mul_f32 v236, v236, v240\n\t" \
    "v_mul_f32 v0, v0, v240\n\t" \
    "v_mul_f32 v1, v1, v240\n\t" \
    "v_mul_f32 v2, v2, v240\n\t" \
    "v_mul_f32 v3, v3, v240\n\t" \
    "v_mul_f32 v4, v4, v240\n\t" \
    "v_mul_f32 v5, v5, v240\n\t" \
    "v_mul_f32 v6, v6, v240\n\t" \
    "v_mul_f32 v7, v7, v240\n\t" \
    "v_mul_f32 v8, v8, v240\n\t" \
    "v_mul_f32 v9, v9, v240\n\t" \
    "v_mul_f32 v10, v10, v240\n\t" \
    "v_mul_f32 v11, v11, v240\n\t" \
    "v_mul_f32 v12, v12, v240\n\t" \
    "v_mul_f32 v13, v13, v240\n\t" \
    "v_mul_f32 v14, v14, v240\n\t" \
    "v_mul_f32 v15, v15, v240\n\t" \
    "v_mul_f32 v16, v16, v240\n\t" \
    "v_mul_f32 v17, v17, v240\n\t" \
    "v_mul_f32 v18, v18, v240\n\t" \
    "v_mul_f32 v19, v19, v240\n\t" \
    "v_mul_f32 v20, v20, v240\n\t" \
    "v_mul_f32 v21, v21, v240\n\t" \
    "v_mul_f32 v22, v22, v240\n\t" \
    "v_mul_f32 v23, v23, v240\n\t" \
    "v_mul_f32 v24, v24, v240\n\t" \
    "v_mul_f32 v25, v25, v240\n\t" \
    "v_mul_f32 v26, v26, v240\n\t" \
    "v_mul_f32 v27, v27, v240\n\t" \
    "v_mul_f32 v28, v28, v240\n\t" \
    "v_mul_f32 v29, v29, v240\n\t" \
    "v_mul_f32 v30, v30, v240\n\t" \
    "v_mul_f32 v31, v31, v240\n\t" \
    "v_mul_f32 v32, v32, v240\n\t" \
    "v_mul_f32 v33, v33, v240\n\t" \
    "v_mul_f32 v34, v34, v240\n\t" \
    "v_mul_f32 v35, v35, v240\n\t" \
    "v_mul_f32 v36, v36, v240\n\t" \
    "v_mul_f32 v37, v37, v240\n\t" \
    "v_mul_f32 v38, v38, v240\n\t" \
    "v_mul_f32 v39, v39, v240\n\t" \
    "v_mul_f32 v40, v40, v240\n\t" \
    "v_mul_f32 v41, v41, v240\n\t" \
    "v_mul_f32 v42, v42, v240\n\t" \
    "v_mul_f32 v43, v43, v240\n\t" \
    "v_mul_f32 v44, v44, v240\n\t" \
    "v_mul_f32 v45, v45, v240\n\t" \
    "v_mul_f32 v46, v46, v240\n\t" \
    "v_mul_f32 v47, v47, v240\n\t" \
    "v_mul_f32 v48, v48, v240\n\t" \
    "v_mul_f32 v49, v49, v240\n\t" \
    "v_mul_f32 v50, v50, v240\n\t" \
    "v_mul_f32 v51, v51, v240\n\t" \
    "v_mul_f32 v52, v52, v240\n\t" \
    "v_mul_f32 v53, v53, v240\n\t" \
    "v_mul_f32 v54, v54, v240\n\t" \
    "v_mul_f32 v55, v55, v240\n\t" \
    "v_mul_f32 v56, v56, v240\n\t" \
    "v_mul_f32 v57, v57, v240\n\t" \
    "v_mul_f32 v58, v58, v240\n\t" \
    "v_mul_f32 v59, v59, v240\n\t" \
    "v_mul_f32 v60, v60, v240\n\t" \
    "v_mul_f32 v61, v61, v240\n\t" \
    "v_mul_f32 v62, v62, v240\n\t" \
    "v_mul_f32 v63, v63, v240\n\t" \
    "v_add_f32 v237, v144, v145\n\t" \
    "v_add_f32 v237, v237, v146\n\t" \
    "v_add_f32 v237, v237, v147\n\t" \
    "v_add_f32 v237, v237, v148\n\t" \
    "v_add_f32 v237, v237, v149\n\t" \
    "v_add_f32 v237, v237, v150\n\t" \
    "v_add_f32 v237, v237, v151\n\t" \
    "v_add_f32 v237, v237, v152\n\t" \
    "v_add_f32 v237, v237, v153\n\t" \
    "v_add_f32 v237, v237, v154\n\t" \
    "v_add_f32 v237, v237, v155\n\t" \
    "v_add_f32 v237, v237, v156\n\t" \
    "v_add_f32 v237, v237, v157\n\t" \
    "v_add_f32 v237, v237, v158\n\t" \
    "v_add_f32 v237, v237, v159\n\t" \
    "v_cvt_pk_bf16_f32 v160, v144, v145\n\t" \
    "v_cvt_pk_bf16_f32 v161, v146, v147\n\t" \
    "v_cvt_pk_bf16_f32 v162, v148, v149\n\t" \
    "v_cvt_pk_bf16_f32 v163, v150, v151\n\t" \
    "v_cvt_pk_bf16_f32 v164, v152, v153\n\t" \
    "v_cvt_pk_bf16_f32 v165, v154, v155\n\t" \
    "v_cvt_pk_bf16_f32 v166, v156, v157\n\t" \
    "v_cvt_pk_bf16_f32 v167, v158, v159\n\t" \
    "s_nop 1\n\t" \
    "s_branch BACKF_%=\n\t" \
    "RAREG_%=:\n\t" \
    "s_nop 15\n\t" \
    "v_max3_f32 v239, v112, v113, v114\n\t" \
    "v_max3_f32 v239, v239, v115, v116\n\t" \
    "v_max3_f32 v239, v239, v117, v118\n\t" \
    "v_max3_f32 v239, v239, v119, v120\n\t" \
    "v_max3_f32 v239, v239, v121, v122\n\t" \
    "v_max3_f32 v239, v239, v123, v124\n\t" \
    "v_max3_f32 v239, v239, v125, v126\n\t" \
    "v_max_f32 v239, v239, v127\n\t" \
    "v_max_f32 v239, 0, v239\n\t" \
    "v_mov_b32 v240, v239\n\t" \
    "s_nop 1\n\t" \
    "v_permlane32_swap_b32 v239, v240\n\t" \
    "s_nop 1\n\t" \
    "v_max_f32 v239, v239, v240\n\t" \
    "v_add_f32 v238, v238, v239\n\t" \
    "v_exp_f32 v240, -v239\n\t" \
    "v_sub_f32 v241, 0, v238\n\t" \
    "v_sub_f32 v112, v112, v239\n\t" \
    "v_mov_b32 v96, v241\n\t" \
    "v_sub_f32 v113, v113, v239\n\t" \
    "v_mov_b32 v97, v241\n\t" \
    "v_sub_f32 v114, v114, v239\n\t" \
    "v_mov_b32 v98, v241\n\t" \
    "v_sub_f32 v115, v115, v239\n\t" \
    "v_mov_b32 v99, v241\n\t" \
    "v_sub_f32 v116, v116, v239\n\t" \
    "v_mov_b32 v100, v241\n\t" \
    "v_sub_f32 v117, v117, v239\n\t" \
    "v_mov_b32 v101, v241\n\t" \
    "v_sub_f32 v118, v118, v239\n\t" \
    "v_mov_b32 v102, v241\n\t" \
    "v_sub_f32 v119, v119, v239\n\t" \
    "v_mov_b32 v103, v241\n\t" \
    "v_sub_f32 v120, v120, v239\n\t" \
    "v_mov_b32 v104, v241\n\t" \
    "v_sub_f32 v121, v121, v239\n\t" \
    "v_mov_b32 v105, v241\n\t" \
    "v_sub_f32 v122, v122, v239\n\t" \
    "v_mov_b32 v106, v241\n\t" \
    "v_sub_f32 v123, v123, v239\n\t" \
    "v_mov_b32 v107, v241\n\t" \
    "v_sub_f32 v124, v124, v239\n\t" \
    "v_mov_b32 v108, v241\n\t" \
    "v_sub_f32 v125, v125, v239\n\t" \
    "v_mov_b32 v109, v241\n\t" \
    "v_sub_f32 v126, v126, v239\n\t" \
    "v_mov_b32 v110, v241\n\t" \
    "v_sub_f32 v127, v127, v239\n\t" \
    "v_mov_b32 v111, v241\n\t" \
    "v_exp_f32 v144, v112\n\t" \
    "v_exp_f32 v145, v113\n\t" \
    "v_exp_f32 v146, v114\n\t" \
    "v_exp_f32 v147, v115\n\t" \
    "v_exp_f32 v148, v116\n\t" \
    "v_exp_f32 v149, v117\n\t" \
    "v_exp_f32 v150, v118\n\t" \
    "v_exp_f32 v151, v119\n\t" \
    "v_exp_f32 v152, v120\n\t" \
    "v_exp_f32 v153, v121\n\t" \
    "v_exp_f32 v154, v122\n\t" \
    "v_exp_f32 v155, v123\n\t" \
    "v_exp_f32 v156, v124\n\t" \
    "v_exp_f32 v157, v125\n\t" \
    "v_exp_f32 v158, v126\n\t" \
    "v_exp_f32 v159, v127\n\t" \
    "v_mul_f32 v236, v236, v240\n\t" \
    "v_mul_f32 v0, v0, v240\n\t" \
    "v_mul_f32 v1, v1, v240\n\t" \
    "v_mul_f32 v2, v2, v240\n\t" \
    "v_mul_f32 v3, v3, v240\n\t" \
    "v_mul_f32 v4, v4, v240\n\t" \
    "v_mul_f32 v5, v5, v240\n\t" \
    "v_mul_f32 v6, v6, v240\n\t" \
    "v_mul_f32 v7, v7, v240\n\t" \
    "v_mul_f32 v8, v8, v240\n\t" \
    "v_mul_f32 v9, v9, v240\n\t" \
    "v_mul_f32 v10, v10, v240\n\t" \
    "v_mul_f32 v11, v11, v240\n\t" \
    "v_mul_f32 v12, v12, v240\n\t" \
    "v_mul_f32 v13, v13, v240\n\t" \
    "v_mul_f32 v14, v14, v240\n\t" \
    "v_mul_f32 v15, v15, v240\n\t" \
    "v_mul_f32 v16, v16, v240\n\t" \
    "v_mul_f32 v17, v17, v240\n\t" \
    "v_mul_f32 v18, v18, v240\n\t" \
    "v_mul_f32 v19, v19, v240\n\t" \
    "v_mul_f32 v20, v20, v240\n\t" \
    "v_mul_f32 v21, v21, v240\n\t" \
    "v_mul_f32 v22, v22, v240\n\t" \
    "v_mul_f32 v23, v23, v240\n\t" \
    "v_mul_f32 v24, v24, v240\n\t" \
    "v_mul_f32 v25, v25, v240\n\t" \
    "v_mul_f32 v26, v26, v240\n\t" \
    "v_mul_f32 v27, v27, v240\n\t" \
    "v_mul_f32 v28, v28, v240\n\t" \
    "v_mul_f32 v29, v29, v240\n\t" \
    "v_mul_f32 v30, v30, v240\n\t" \
    "v_mul_f32 v31, v31, v240\n\t" \
    "v_mul_f32 v32, v32, v240\n\t" \
    "v_mul_f32 v33, v33, v240\n\t" \
    "v_mul_f32 v34, v34, v240\n\t" \
    "v_mul_f32 v35, v35, v240\n\t" \
    "v_mul_f32 v36, v36, v240\n\t" \
    "v_mul_f32 v37, v37, v240\n\t" \
    "v_mul_f32 v38, v38, v240\n\t" \
    "v_mul_f32 v39, v39, v240\n\t" \
    "v_mul_f32 v40, v40, v240\n\t" \
    "v_mul_f32 v41, v41, v240\n\t" \
    "v_mul_f32 v42, v42, v240\n\t" \
    "v_mul_f32 v43, v43, v240\n\t" \
    "v_mul_f32 v44, v44, v240\n\t" \
    "v_mul_f32 v45, v45, v240\n\t" \
    "v_mul_f32 v46, v46, v240\n\t" \
    "v_mul_f32 v47, v47, v240\n\t" \
    "v_mul_f32 v48, v48, v240\n\t" \
    "v_mul_f32 v49, v49, v240\n\t" \
    "v_mul_f32 v50, v50, v240\n\t" \
    "v_mul_f32 v51, v51, v240\n\t" \
    "v_mul_f32 v52, v52, v240\n\t" \
    "v_mul_f32 v53, v53, v240\n\t" \
    "v_mul_f32 v54, v54, v240\n\t" \
    "v_mul_f32 v55, v55, v240\n\t" \
    "v_mul_f32 v56, v56, v240\n\t" \
    "v_mul_f32 v57, v57, v240\n\t" \
    "v_mul_f32 v58, v58, v240\n\t" \
    "v_mul_f32 v59, v59, v240\n\t" \
    "v_mul_f32 v60, v60, v240\n\t" \
    "v_mul_f32 v61, v61, v240\n\t" \
    "v_mul_f32 v62, v62, v240\n\t" \
    "v_mul_f32 v63, v63, v240\n\t" \
    "v_add_f32 v237, v144, v145\n\t" \
    "v_add_f32 v237, v237, v146\n\t" \
    "v_add_f32 v237, v237, v147\n\t" \
    "v_add_f32 v237, v237, v148\n\t" \
    "v_add_f32 v237, v237, v149\n\t" \
    "v_add_f32 v237, v237, v150\n\t" \
    "v_add_f32 v237, v237, v151\n\t" \
    "v_add_f32 v237, v237, v152\n\t" \
    "v_add_f32 v237, v237, v153\n\t" \
    "v_add_f32 v237, v237, v154\n\t" \
    "v_add_f32 v237, v237, v155\n\t" \
    "v_add_f32 v237, v237, v156\n\t" \
    "v_add_f32 v237, v237, v157\n\t" \
    "v_add_f32 v237, v237, v158\n\t" \
    "v_add_f32 v237, v237, v159\n\t" \
    "v_cvt_pk_bf16_f32 v160, v144, v145\n\t" \
    "v_cvt_pk_bf16_f32 v161, v146, v147\n\t" \
    "v_cvt_pk_bf16_f32 v162, v148, v149\n\t" \
    "v_cvt_pk_bf16_f32 v163, v150, v151\n\t" \
    "v_cvt_pk_bf16_f32 v164, v152, v153\n\t" \
    "v_cvt_pk_bf16_f32 v165, v154, v155\n\t" \
    "v_cvt_pk_bf16_f32 v166, v156, v157\n\t" \
    "v_cvt_pk_bf16_f32 v167, v158, v159\n\t" \
    "s_nop 1\n\t" \
    "s_branch BACKG_%=\n\t" \
    "END_%=:\n\t" \
    "s_waitcnt lgkmcnt(0)\n\t" \
    "s_nop 15\n\t"
#define MX3(a, b, c) __builtin_fmaxf(__builtin_fmaxf((a), (b)), (c))
DI float max16(const f32x16& p) {
    float a = MX3(p[0], p[1], p[2]), b = MX3(p[3], p[4], p[5]);
    a = MX3(a, p[6], p[7]); b = MX3(b, p[8], p[9]); a = MX3(a, p[10], p[11]); b = MX3(b, p[12], p[13]); a = MX3(a, p[14], p[15]);
    return __builtin_fmaxf(a, b);
}
DI float halfmax(float m) { auto rr = __builtin_amdgcn_permlane32_swap(__float_as_uint(m), __float_as_uint(m), false, false); return __builtin_fmaxf(__uint_as_float(rr[0]), __uint_as_float(rr[1])); }
DI void cmask(f32x16& p, int r32, int hi) {
#pragma unroll
    for (int r = 0; r < 16; ++r) { const int kv = 4 * hi + (r & 3) + 8 * (r >> 2); if (kv > r32) p[r] = -INFINITY; }
}
#define WAITBAR(N) asm volatile("s_waitcnt vmcnt(" #N ")\n\ts_barrier" ::: "memory")
typedef unsigned u32x16 __attribute__((ext_vector_type(16)));

__global__ __launch_bounds__(512, 2) void k2_attn(const char* __restrict__ Q, const char* __restrict__ K, const char* __restrict__ VT, char* __restrict__ CTX, const char* __restrict__ wo, const char* __restrict__ w2) {
    extern __shared__ __attribute__((aligned(16))) unsigned char smem[];
    const lds_cptr lds = (lds_cptr)smem;
    const int tid = threadIdx.x, lane = tid & 63, r32 = lane & 31, hi = lane >> 5, wid = __builtin_amdgcn_readfirstlane(tid >> 6);
    const int xcd = blockIdx.x & 7, idx = blockIdx.x >> 3, h = xcd * 4 + (idx >> 3), ib = idx & 7;
    const int G = wid < 4 ? 4 * ib + wid : 63 - 4 * ib - (wid - 4);
    const int NT = 32 - 2 * ib;
    const char* Kh = K + (size_t)h * 32 * SLOT; const char* Vh = VT + (size_t)h * 32 * SLOT;
    const unsigned lds0 = (unsigned)(uintptr_t)smem;
    const unsigned dmaw = (unsigned)(wid - 4) >> 31;
    const unsigned dvo = (wid & 3) * 4096 + lane * 16, kdst = lds0 + LDS_K + (wid & 3) * 4096, vdst = lds0 + LDS_V + (wid & 3) * 4096;
#define DMA_T(base, dst, t, sl) do { const char* g_ = (base) + (size_t)(t) * SLOT; const unsigned l_ = (unsigned)__builtin_amdgcn_readfirstlane((dst) + ((sl) & 3) * SLOT); glds2(g_, dvo, l_); glds2(g_ + 2048, dvo, l_ + 2048); } while (0)
#define DMA_K(t, sl) DMA_T(Kh, kdst, t, sl)
#define DMA_V(t, sl) DMA_T(Vh, vdst, t, sl)
#define DMA_STEP(s) do { if (dmaw) { DMA_K(min((s) + 3, NT - 1), (s) + 3); DMA_V(min((s) + 2, NT - 1), (s) + 2); } } while (0)
    STAMP(2, 0);
    u32x16 qa, qb;
    { const char* qp = Q + ((size_t)h * 32 + (G >> 1)) * SLOT; const unsigned qo = hi * 1024 + ((G & 1) * 32 + r32) * 16;
      u32x4 q[8];
#define QLD(d) asm volatile("global_load_dwordx4 %0, %1, %2 offset:%c3" : "=&v"(q[d]) : "v"(qo + ((d) >> 1) * 4096), "s"(qp), "i"(((d) & 1) * 2048) : "memory")
      QLD(0); QLD(1); QLD(2); QLD(3); QLD(4); QLD(5); QLD(6); QLD(7);
#undef QLD
      if (dmaw) { DMA_K(0, 0); DMA_V(0, 0); DMA_K(1, 1); DMA_K(2, 2); DMA_V(1, 1); } else WAIT_V(0);
      asm volatile("s_waitcnt vmcnt(8)\n\ts_barrier" : "+v"(q[0]), "+v"(q[1]), "+v"(q[2]), "+v"(q[3]), "+v"(q[4]), "+v"(q[5]), "+v"(q[6]), "+v"(q[7]) :: "memory");
#pragma unroll
      for (int i = 0; i < 4; ++i)
#pragma unroll
          for (int e = 0; e < 4; ++e) { qa[4 * i + e] = q[i][e]; qb[4 * i + e] = q[4 + i][e]; }
    }
    STAMP(2, 1);
    unsigned pf0 = 0, pf1 = 0;
    const unsigned kp0 = lds0 + LDS_K + hi * 1024 + r32 * 16, vp0 = lds0 + LDS_V + hi * 2048 + r32 * 16;
    f32x16 o0 = f32x16{}, o1 = f32x16{}, o2 = f32x16{}, o3 = f32x16{}, negm, cA, cB, P = f32x16{};
    u32x16 kfa, kfb;
    float l_reg = 0.f, mhat;
    {
        f32x16 s = f32x16{};
#pragma unroll
        for (int j = 0; j < 8; ++j) {
            const bf16x8 kf = lds_rd(lds + LDS_K + hi * 1024 + r32 * 16 + j * 2048); const u32x4 kw = __builtin_bit_cast(u32x4, kf);
#pragma unroll
            for (int e = 0; e < 4; ++e) { if (j < 4) kfa[4 * j + e] = kw[e]; else kfb[4 * (j - 4) + e] = kw[e]; }
            const u32x4 qw = j < 4 ? (u32x4){qa[4 * j], qa[4 * j + 1], qa[4 * j + 2], qa[4 * j + 3]} : (u32x4){qb[4 * j - 16], qb[4 * j - 15], qb[4 * j - 14], qb[4 * j - 13]};
            s = MFMA32(kf, __builtin_bit_cast(bf16x8, qw), s);
        }
        if (G == 0) cmask(s, r32, hi);
        mhat = halfmax(max16(s));
#pragma unroll
        for (int r = 0; r < 16; ++r) { negm[r] = -mhat; cB[r] = -INFINITY; cA[r] = -INFINITY; }
    }
    const float thr = 65536.0f;
    int t = 0; unsigned soff = 0;
    unsigned vk = kp0, vkn = kp0 + SLOT, vv = vp0, vvp = vp0;
#define K2_STATE [o0] "+&{v[0:15]}"(o0), [o1] "+&{v[16:31]}"(o1), [o2] "+&{v[32:47]}"(o2), [o3] "+&{v[48:63]}"(o3), [negm] "+&{v[96:111]}"(negm), [ca] "+&{v[112:127]}"(cA), [cb] "+&{v[128:143]}"(cB), \
        [p] "+&{v[144:159]}"(P), [kfa] "+&{v[168:183]}"(kfa), [kfb] "+&{v[184:199]}"(kfb), [vk] "+&{v232}"(vk), [vkn] "+&{v233}"(vkn), [vv] "+&{v234}"(vv), [vvp] "+&{v235}"(vvp), [l] "+&{v236}"(l_reg), [mh] "+&{v238}"(mhat)
#define K2_IN [qa] "{v[64:79]}"(qa), [qb] "{v[80:95]}"(qb), [thr] "s"(thr)
#define K2_CLOB "v160", "v161", "v162", "v163", "v164", "v165", "v166", "v167", "v200", "v201", "v202", "v203", "v204", "v205", "v206", "v207", "v208", "v209", "v210", "v211", "v212", "v213", "v214", "v215", \
        "v216", "v217", "v218", "v219", "v220", "v221", "v222", "v223", "v224", "v225", "v226", "v227", "v228", "v229", "v230", "v231", "v237", "v239", "v240", "v241", "vcc", "scc", "memory"
    const int nloop = G >> 1;
    asm volatile(K2_ASM : K2_STATE, [t] "+&s"(t), [soff] "+&s"(soff)
                 : K2_IN, [n] "s"(nloop), [odd] "s"(G & 1), [dmaw] "s"(dmaw), [ntm1] "s"(NT - 1), [khl] "s"((unsigned)(uintptr_t)Kh), [khh] "s"((unsigned)((uintptr_t)Kh >> 32)),
                   [vhl] "s"((unsigned)(uintptr_t)Vh), [vhh] "s"((unsigned)((uintptr_t)Vh >> 32)), [kdst] "s"(kdst), [vdst] "s"(vdst), [dvo] "v"(dvo), [kp0] "v"(kp0), [vp0] "v"(vp0), [dq] "v"(r32 - 4 * hi)
                 : K2_CLOB, "s60", "s61", "s62", "s63");
    STAMP(2, 2);
    {
        auto rr = __builtin_amdgcn_permlane32_swap(__float_as_uint(l_reg), __float_as_uint(l_reg), false, false);
        const float inv = 1.0f / (__uint_as_float(rr[0]) + __uint_as_float(rr[1]));
        char* op = CTX + ((size_t)((G >> 2) * 64 + 2 * h) * 8 + hi) * 2048 + ((G & 3) * 32 + r32) * 16;
#define K2_ST(o, db) do { _Pragma("unroll") for (int g = 0; g < 4; g += 2) { \
            const unsigned x0 = cvtpk(o[4 * g] * inv, o[4 * g + 1] * inv), x1 = cvtpk(o[4 * g + 2] * inv, o[4 * g + 3] * inv); \
            const unsigned y0 = cvtpk(o[4 * g + 4] * inv, o[4 * g + 5] * inv), y1 = cvtpk(o[4 * g + 6] * inv, o[4 * g + 7] * inv); \
            const auto r0 = __builtin_amdgcn_permlane32_swap(x0, y0, false, false), r1 = __builtin_amdgcn_permlane32_swap(x1, y1, false, false); \
            ST16(op + (size_t)(((db) >> 1) * 8 + 4 * ((db) & 1) + g) * 2048, ((u32x4){r0[0], r1[0], r0[1], r1[1]})); } } while (0)
        K2_ST(o0, 0); K2_ST(o1, 1); K2_ST(o2, 2); K2_ST(o3, 3);
    }
    WAITBAR(16); ++t;
    if (t < NT) { DMA_STEP(t); WAITBAR(16); ++t;
        for (; t < NT; ++t) { DMA_STEP(t); WAITBAR(8); } }
    STAMP(2, 3);
    WAIT_V(0);
    asm volatile("" :: "v"(pf0), "v"(pf1));
    STAMP(2, 4);
}
}

namespace k3 {
constexpr int STG = 49152, LDS_BYTES = 3 * STG;
typedef __attribute__((address_space(1))) unsigned gu32;
#define K3_ASM \
    "s_mov_b64 s[60:61], %[a1]\n\t" \
    "s_mov_b64 s[62:63], %[b1]\n\t" \
    "s_mov_b32 s67, %[nl]\n\t" \
    "s_mov_b32 s68, 0\n\t" \
    "s_add_u32 m0, %[mA], s68\n\t" \
    "s_add_u32 s66, %[mB], s68\n\t" \
    "global_load_lds_dwordx4 %[va], s[60:61] offset:0\n\t" \
    "global_load_lds_dwordx4 %[va], s[60:61] offset:1024\n\t" \
    "global_load_lds_dwordx4 %[va], s[60:61] offset:2048\n\t" \
    "global_load_lds_dwordx4 %[va], s[60:61] offset:3072\n\t" \
    "s_mov_b32 m0, s66\n\t" \
    "s_add_u32 s64, s62, 0x10000\n\t" \
    "s_addc_u32 s65, s63, 0\n\t" \
    "global_load_lds_dwordx4 %[vb], s[62:63]\n\t" \
    "s_add_u32 m0, s66, 8192\n\t" \
    "s_add_u32 s60, s60, 0x4000\n\t" \
    "global_load_lds_dwordx4 %[vb], s[64:65]\n\t" \
    "s_addc_u32 s61, s61, 0\n\t" \
    "s_add_u32 s62, s62, 0x20000\n\t" \
    "s_addc_u32 s63, s63, 0\n\t" \
    "s_mov_b32 s68, 49152\n\t" \
    "s_add_u32 m0, %[mA], s68\n\t" \
    "s_add_u32 s66, %[mB], s68\n\t" \
    "global_load_lds_dwordx4 %[va], s[60:61] offset:0\n\t" \
    "global_load_lds_dwordx4 %[va], s[60:61] offset:1024\n\t" \
    "global_load_lds_dwordx4 %[va], s[60:61] offset:2048\n\t" \
    "global_load_lds_dwordx4 %[va], s[60:61] offset:3072\n\t" \
    "s_mov_b32 m0, s66\n\t" \
    "s_add_u32 s64, s62, 0x10000\n\t" \
    "s_addc_u32 s65, s63, 0\n\t" \
    "global_load_lds_dwordx4 %[vb], s[62:63]\n\t" \
    "s_add_u32 m0, s66, 8192\n\t" \
    "s_add_u32 s60, s60, 0x4000\n\t" \
    "global_load_lds_dwordx4 %[vb], s[64:65]\n\t" \
    "s_addc_u32 s61, s61, 0\n\t" \
    "s_add_u32 s62, s62, 0x20000\n\t" \
    "s_addc_u32 s63, s63, 0\n\t" \
    "s_mov_b32 s68, 98304\n\t" \
    "s_add_u32 m0, %[mA], s68\n\t" \
    "s_add_u32 s66, %[mB], s68\n\t" \
    "global_load_lds_dwordx4 %[va], s[60:61] offset:0\n\t" \
    "global_load_lds_dwordx4 %[va], s[60:61] offset:1024\n\t" \
    "global_load_lds_dwordx4 %[va], s[60:61] offset:2048\n\t" \
    "global_load_lds_dwordx4 %[va], s[60:61] offset:3072\n\t" \
    "s_mov_b32 m0, s66\n\t" \
    "s_add_u32 s64, s62, 0x10000\n\t" \
    "s_addc_u32 s65, s63, 0\n\t" \
    "global_load_lds_dwordx4 %[vb], s[62:63]\n\t" \
    "s_add_u32 m0, s66, 8192\n\t" \
    "s_add_u32 s60, s60, 0x4000\n\t" \
    "global_load_lds_dwordx4 %[vb], s[64:65]\n\t" \
    "s_addc_u32 s61, s61, 0\n\t" \
    "s_add_u32 s62, s62, 0x20000\n\t" \
    "s_addc_u32 s63, s63, 0\n\t" \
    "s_mov_b32 s68, 0\n\t" \
    "s_mov_b32 s69, 98304\n\t" \
    "v_mov_b32 v128, v134\n\t" \
    "v_mov_b32 v129, v135\n\t" \
    "v_mov_b32 v130, v136\n\t" \
    "v_add_u32 v131, 49152, v134\n\t" \
    "v_add_u32 v132, 49152, v135\n\t" \
    "v_add_u32 v133, 49152, v136\n\t" \
    "s_waitcnt vmcnt(12)\n\t" \
    "s_barrier\n\t" \
    "ds_read_b128 v[64:67], v128 offset:0\n\t" \
    "ds_read_b64_tr_b16 v[80:81], v129 offset:0\n\t" \
    "ds_read_b64_tr_b16 v[82:83], v129 offset:256\n\t" \
    "ds_read_b64_tr_b16 v[84:85], v130 offset:0\n\t" \
    "ds_read_b64_tr_b16 v[86:87], v130 offset:256\n\t" \
    "ds_read_b128 v[68:71], v128 offset:256\n\t" \
    "ds_read_b64_tr_b16 v[88:89], v129 offset:512\n\t" \
    "ds_read_b64_tr_b16 v[90:91], v129 offset:768\n\t" \
    "ds_read_b64_tr_b16 v[92:93], v130 offset:512\n\t" \
    "ds_read_b64_tr_b16 v[94:95], v130 offset:768\n\t" \
    "ds_read_b128 v[72:75], v128 offset:512\n\t" \
    "ds_read_b128 v[76:79], v128 offset:768\n\t" \
    "LOOP_%=:\n\t" \
    "s_add_u32 m0, %[mA], s68\n\t" \
    "s_add_u32 s66, %[mB], s68\n\t" \
    "global_load_lds_dwordx4 %[va], s[60:61] offset:0\n\t" \
    "global_load_lds_dwordx4 %[va], s[60:61] offset:1024\n\t" \
    "global_load_lds_dwordx4 %[va], s[60:61] offset:2048\n\t" \
    "global_load_lds_dwordx4 %[va], s[60:61] offset:3072\n\t" \
    "s_mov_b32 m0, s66\n\t" \
    "s_add_u32 s64, s62, 0x10000\n\t" \
    "s_addc_u32 s65, s63, 0\n\t" \
    "global_load_lds_dwordx4 %[vb], s[62:63]\n\t" \
    "s_add_u32 m0, s66, 8192\n\t" \
    "s_add_u32 s60, s60, 0x4000\n\t" \
    "global_load_lds_dwordx4 %[vb], s[64:65]\n\t" \
    "s_addc_u32 s61, s61, 0\n\t" \
    "s_add_u32 s62, s62, 0x20000\n\t" \
    "s_addc_u32 s63, s63, 0\n\t" \
    "v_mfma_f32_16x16x32_bf16 v[0:3], v[112:115], v[96:99], v[0:3]\n\t" \
    "ds_read_b128 v[64:67], v131 offset:0\n\t" \
    "ds_read_b64_tr_b16 v[80:81], v132 offset:0\n\t" \
    "v_mfma_f32_16x16x32_bf16 v[4:7], v[116:119], v[96:99], v[4:7]\n\t" \
    "ds_read_b64_tr_b16 v[82:83], v132 offset:256\n\t" \
    "ds_read_b64_tr_b16 v[84:85], v133 offset:0\n\t" \
    "v_mfma_f32_16x16x32_bf16 v[8:11], v[120:123], v[96:99], v[8:11]\n\t" \
    "ds_read_b64_tr_b16 v[86:87], v133 offset:256\n\t" \
    "ds_read_b128 v[68:71], v131 offset:256\n\t" \
    "v_mfma_f32_16x16x32_bf16 v[12:15], v[124:127], v[96:99], v[12:15]\n\t" \
    "ds_read_b64_tr_b16 v[88:89], v132 offset:512\n\t" \
    "ds_read_b64_tr_b16 v[90:91], v132 offset:768\n\t" \
    "v_mfma_f32_16x16x32_bf16 v[16:19], v[112:115], v[100:103], v[16:19]\n\t" \
    "ds_read_b64_tr_b16 v[92:93], v133 offset:512\n\t" \
    "ds_read_b64_tr_b16 v[94:95], v133 offset:768\n\t" \
    "v_mfma_f32_16x16x32_bf16 v[20:23], v[116:119], v[100:103], v[20:23]\n\t" \
    "ds_read_b128 v[72:75], v131 offset:512\n\t" \
    "ds_read_b128 v[76:79], v131 offset:768\n\t" \
    "v_mfma_f32_16x16x32_bf16 v[24:27], v[120:123], v[100:103], v[24:27]\n\t" \
    "s_add_u32 m0, %[mA], s68\n\t" \
    "s_add_u32 s66, %[mB], s68\n\t" \
    "global_load_lds_dwordx4 %[va], s[60:61] offset:0\n\t" \
    "v_mfma_f32_16x16x32_bf16 v[28:31], v[124:127], v[100:103], v[28:31]\n\t" \
    "global_load_lds_dwordx4 %[va], s[60:61] offset:1024\n\t" \
    "global_load_lds_dwordx4 %[va], s[60:61] offset:2048\n\t" \
    "global_load_lds_dwordx4 %[va], s[60:61] offset:3072\n\t" \
    "v_mfma_f32_16x16x32_bf16 v[32:35], v[112:115], v[104:107], v[32:35]\n\t" \
    "s_mov_b32 m0, s66\n\t" \
    "s_add_u32 s64, s62, 0x10000\n\t" \
    "s_addc_u32 s65, s63, 0\n\t" \
    "v_mfma_f32_16x16x32_bf16 v[36:39], v[116:119], v[104:107], v[36:39]\n\t" \
    "global_load_lds_dwordx4 %[vb], s[62:63]\n\t" \
    "s_add_u32 m0, s66, 8192\n\t" \
    "s_add_u32 s60, s60, 0x4000\n\t" \
    "v_mfma_f32_16x16x32_bf16 v[40:43], v[120:123], v[104:107], v[40:43]\n\t" \
    "global_load_lds_dwordx4 %[vb], s[64:65]\n\t" \
    "s_addc_u32 s61, s61, 0\n\t" \
    "s_add_u32 s62, s62, 0x20000\n\t" \
    "v_mfma_f32_16x16x32_bf16 v[44:47], v[124:127], v[104:107], v[44:47]\n\t" \
    "s_addc_u32 s63, s63, 0\n\t" \
    "v_mov_b32 v128, v131\n\t" \
    "v_mov_b32 v129, v132\n\t" \
    "v_mfma_f32_16x16x32_bf16 v[48:51], v[112:115], v[108:111], v[48:51]\n\t" \
    "v_mov_b32 v130, v133\n\t" \
    "v_add_u32 v131, s69, v134\n\t" \
    "v_add_u32 v132, s69, v135\n\t" \
    "v_add_u32 v133, s69, v136\n\t" \
    "v_mfma_f32_16x16x32_bf16 v[52:55], v[116:119], v[108:111], v[52:55]\n\t" \
    "s_add_u32 s68, s68, 49152\n\t" \
    "s_cmp_eq_u32 s68, 147456\n\t" \
    "s_cselect_b32 s68, 0, s68\n\t" \
    "s_add_u32 s69, s69, 49152\n\t" \
    "v_mfma_f32_16x16x32_bf16 v[56:59], v[120:123], v[108:111], v[56:59]\n\t" \
    "s_cmp_eq_u32 s69, 147456\n\t" \
    "s_cselect_b32 s69, 0, s69\n\t" \
    "v_mfma_f32_16x16x32_bf16 v[60:63], v[124:127], v[108:111], v[60:63]\n\t" \
    "s_sub_u32 s67, s67, 1\n\t" \
    "s_cmp_lg_u32 s67, 0\n\t" \
    "s_cbranch_scc1 LOOP_%=\n\t" \
    "s_waitcnt lgkmcnt(0)\n\t" \
    "v_mfma_f32_16x16x32_bf16 v[0:3], v[80:83], v[64:67], v[0:3]\n\t" \
    "ds_read_b128 v[96:99], v128 offset:8192\n\t" \
    "ds_read_b64_tr_b16 v[112:113], v129 offset:8192\n\t" \
    "v_mfma_f32_16x16x32_bf16 v[4:7], v[84:87], v[64:67], v[4:7]\n\t" \
    "ds_read_b64_tr_b16 v[114:115], v129 offset:8448\n\t" \
    "ds_read_b64_tr_b16 v[116:117], v130 offset:8192\n\t" \
    "v_mfma_f32_16x16x32_bf16 v[8:11], v[88:91], v[64:67], v[8:11]\n\t" \
    "ds_read_b64_tr_b16 v[118:119], v130 offset:8448\n\t" \
    "ds_read_b128 v[100:103], v128 offset:8448\n\t" \
    "v_mfma_f32_16x16x32_bf16 v[12:15], v[92:95], v[64:67], v[12:15]\n\t" \
    "ds_read_b64_tr_b16 v[120:121], v129 offset:8704\n\t" \
    "ds_read_b64_tr_b16 v[122:123], v129 offset:8960\n\t" \
    "v_mfma_f32_16x16x32_bf16 v[16:19], v[80:83], v[68:71], v[16:19]\n\t" \
    "ds_read_b64_tr_b16 v[124:125], v130 offset:8704\n\t" \
    "ds_read_b64_tr_b16 v[126:127], v130 offset:8960\n\t" \
    "v_mfma_f32_16x16x32_bf16 v[20:23], v[84:87], v[68:71], v[20:23]\n\t" \
    "ds_read_b128 v[104:107], v128 offset:8704\n\t" \
    "ds_read_b128 v[108:111], v128 offset:8960\n\t" \
    "v_mfma_f32_16x16x32_bf16 v[24:27], v[88:91], v[68:71], v[24:27]\n\t" \
    "v_mfma_f32_16x16x32_bf16 v[28:31], v[92:95], v[68:71], v[28:31]\n\t" \
    "v_mfma_f32_16x16x32_bf16 v[32:35], v[80:83], v[72:75], v[32:35]\n\t" \
    "v_mfma_f32_16x16x32_bf16 v[36:39], v[84:87], v[72:75], v[36:39]\n\t" \
    "v_mfma_f32_16x16x32_bf16 v[40:43], v[88:91], v[72:75], v[40:43]\n\t" \
    "v_mfma_f32_16x16x32_bf16 v[44:47], v[92:95], v[72:75], v[44:47]\n\t" \
    "v_mfma_f32_16x16x32_bf16 v[48:51], v[80:83], v[76:79], v[48:51]\n\t" \
    "v_mfma_f32_16x16x32_bf16 v[52:55], v[84:87], v[76:79], v[52:55]\n\t" \
    "v_mfma_f32_16x16x32_bf16 v[56:59], v[88:91], v[76:79], v[56:59]\n\t" \
    "v_mfma_f32_16x16x32_bf16 v[60:63], v[92:95], v[76:79], v[60:63]\n\t" \
    "s_waitcnt vmcnt(6) lgkmcnt(0)\n\t" \
    "s_barrier\n\t" \
    "v_mfma_f32_16x16x32_bf16 v[0:3], v[112:115], v[96:99], v[0:3]\n\t" \
    "ds_read_b128 v[64:67], v131 offset:0\n\t" \
    "ds_read_b64_tr_b16 v[80:81], v132 offset:0\n\t" \
    "v_mfma_f32_16x16x32_bf16 v[4:7], v[116:119], v[96:99], v[4:7]\n\t" \
    "ds_read_b64_tr_b16 v[82:83], v132 offset:256\n\t" \
    "ds_read_b64_tr_b16 v[84:85], v133 offset:0\n\t" \
    "v_mfma_f32_16x16x32_bf16 v[8:11], v[120:123], v[96:99], v[8:11]\n\t" \
    "ds_read_b64_tr_b16 v[86:87], v133 offset:256\n\t" \
    "ds_read_b128 v[68:71], v131 offset:256\n\t" \
    "v_mfma_f32_16x16x32_bf16 v[12:15], v[124:127], v[96:99], v[12:15]\n\t" \
    "ds_read_b64_tr_b16 v[88:89], v132 offset:512\n\t" \
    "ds_read_b64_tr_b16 v[90:91], v132 offset:768\n\t" \
    "v_mfma_f32_16x16x32_bf16 v[16:19], v[112:115], v[100:103], v[16:19]\n\t" \
    "ds_read_b64_tr_b16 v[92:93], v133 offset:512\n\t" \
    "ds_read_b64_tr_b16 v[94:95], v133 offset:768\n\t" \
    "v_mfma_f32_16x16x32_bf16 v[20:23], v[116:119], v[100:103], v[20:23]\n\t" \
    "ds_read_b128 v[72:75], v131 offset:512\n\t" \
    "ds_read_b128 v[76:79], v131 offset:768\n\t" \
    "v_mfma_f32_16x16x32_bf16 v[24:27], v[120:123], v[100:103], v[24:27]\n\t" \
    "v_mov_b32 v128, v131\n\t" \
    "v_mov_b32 v129, v132\n\t" \
    "v_mov_b32 v130, v133\n\t" \
    "v_mfma_f32_16x16x32_bf16 v[28:31], v[124:127], v[100:103], v[28:31]\n\t" \
    "v_add_u32 v131, s69, v134\n\t" \
    "v_add_u32 v132, s69, v135\n\t" \
    "v_add_u32 v133, s69, v136\n\t" \
    "v_mfma_f32_16x16x32_bf16 v[32:35], v[112:115], v[104:107], v[32:35]\n\t" \
    "s_add_u32 s68, s68, 49152\n\t" \
    "s_cmp_eq_u32 s68, 147456\n\t" \
    "s_cselect_b32 s68, 0, s68\n\t" \
    "v_mfma_f32_16x16x32_bf16 v[36:39], v[116:119], v[104:107], v[36:39]\n\t" \
    "s_add_u32 s69, s69, 49152\n\t" \
    "s_cmp_eq_u32 s69, 147456\n\t" \
    "s_cselect_b32 s69, 0, s69\n\t" \
    "v_mfma_f32_16x16x32_bf16 v[40:43], v[120:123], v[104:107], v[40:43]\n\t" \
    "v_mfma_f32_16x16x32_bf16 v[44:47], v[124:127], v[104:107], v[44:47]\n\t" \
    "v_mfma_f32_16x16x32_bf16 v[48:51], v[112:115], v[108:111], v[48:51]\n\t" \
    "v_mfma_f32_16x16x32_bf16 v[52:55], v[116:119], v[108:111], v[52:55]\n\t" \
    "v_mfma_f32_16x16x32_bf16 v[56:59], v[120:123], v[108:111], v[56:59]\n\t" \
    "v_mfma_f32_16x16x32_bf16 v[60:63], v[124:127], v[108:111], v[60:63]\n\t" \
    "s_waitcnt lgkmcnt(0)\n\t" \
    "v_mfma_f32_16x16x32_bf16 v[0:3], v[80:83], v[64:67], v[0:3]\n\t" \
    "ds_read_b128 v[96:99], v128 offset:8192\n\t" \
    "ds_read_b64_tr_b16 v[112:113], v129 offset:8192\n\t" \
    "v_mfma_f32_16x16x32_bf16 v[4:7], v[84:87], v[64:67], v[4:7]\n\t" \
    "ds_read_b64_tr_b16 v[114:115], v129 offset:8448\n\t" \
    "ds_read_b64_tr_b16 v[116:117], v130 offset:8192\n\t" \
    "v_mfma_f32_16x16x32_bf16 v[8:11], v[88:91], v[64:67], v[8:11]\n\t" \
    "ds_read_b64_tr_b16 v[118:119], v130 offset:8448\n\t" \
    "ds_read_b128 v[100:103], v128 offset:8448\n\t" \
    "v_mfma_f32_16x16x32_bf16 v[12:15], v[92:95], v[64:67], v[12:15]\n\t" \
    "ds_read_b64_tr_b16 v[120:121], v129 offset:8704\n\t" \
    "ds_read_b64_tr_b16 v[122:123], v129 offset:8960\n\t" \
    "v_mfma_f32_16x16x32_bf16 v[16:19], v[80:83], v[68:71], v[16:19]\n\t" \
    "ds_read_b64_tr_b16 v[124:125], v130 offset:8704\n\t" \
    "ds_read_b64_tr_b16 v[126:127], v130 offset:8960\n\t" \
    "v_mfma_f32_16x16x32_bf16 v[20:23], v[84:87], v[68:71], v[20:23]\n\t" \
    "ds_read_b128 v[104:107], v128 offset:8704\n\t" \
    "ds_read_b128 v[108:111], v128 offset:8960\n\t" \
    "v_mfma_f32_16x16x32_bf16 v[24:27], v[88:91], v[68:71], v[24:27]\n\t" \
    "v_mfma_f32_16x16x32_bf16 v[28:31], v[92:95], v[68:71], v[28:31]\n\t" \
    "v_mfma_f32_16x16x32_bf16 v[32:35], v[80:83], v[72:75], v[32:35]\n\t" \
    "v_mfma_f32_16x16x32_bf16 v[36:39], v[84:87], v[72:75], v[36:39]\n\t" \
    "v_mfma_f32_16x16x32_bf16 v[40:43], v[88:91], v[72:75], v[40:43]\n\t" \
    "v_mfma_f32_16x16x32_bf16 v[44:47], v[92:95], v[72:75], v[44:47]\n\t" \
    "v_mfma_f32_16x16x32_bf16 v[48:51], v[80:83], v[76:79], v[48:51]\n\t" \
    "v_mfma_f32_16x16x32_bf16 v[52:55], v[84:87], v[76:79], v[52:55]\n\t" \
    "v_mfma_f32_16x16x32_bf16 v[56:59], v[88:91], v[76:79], v[56:59]\n\t" \
    "v_mfma_f32_16x16x32_bf16 v[60:63], v[92:95], v[76:79], v[60:63]\n\t" \
    "s_waitcnt vmcnt(0) lgkmcnt(0)\n\t" \
    "s_barrier\n\t" \
    "v_mfma_f32_16x16x32_bf16 v[0:3], v[112:115], v[96:99], v[0:3]\n\t" \
    "ds_read_b128 v[64:67], v131 offset:0\n\t" \
    "ds_read_b64_tr_b16 v[80:81], v132 offset:0\n\t" \
    "v_mfma_f32_16x16x32_bf16 v[4:7], v[116:119], v[96:99], v[4:7]\n\t" \
    "ds_read_b64_tr_b16 v[82:83], v132 offset:256\n\t" \
    "ds_read_b64_tr_b16 v[84:85], v133 offset:0\n\t" \
    "v_mfma_f32_16x16x32_bf16 v[8:11], v[120:123], v[96:99], v[8:11]\n\t" \
    "ds_read_b64_tr_b16 v[86:87], v133 offset:256\n\t" \
    "ds_read_b128 v[68:71], v131 offset:256\n\t" \
    "v_mfma_f32_16x16x32_bf16 v[12:15], v[124:127], v[96:99], v[12:15]\n\t" \
    "ds_read_b64_tr_b16 v[88:89], v132 offset:512\n\t" \
    "ds_read_b64_tr_b16 v[90:91], v132 offset:768\n\t" \
    "v_mfma_f32_16x16x32_bf16 v[16:19], v[112:115], v[100:103], v[16:19]\n\t" \
    "ds_read_b64_tr_b16 v[92:93], v133 offset:512\n\t" \
    "ds_read_b64_tr_b16 v[94:95], v133 offset:768\n\t" \
    "v_mfma_f32_16x16x32_bf16 v[20:23], v[116:119], v[100:103], v[20:23]\n\t" \
    "ds_read_b128 v[72:75], v131 offset:512\n\t" \
    "ds_read_b128 v[76:79], v131 offset:768\n\t" \
    "v_mfma_f32_16x16x32_bf16 v[24:27], v[120:123], v[100:103], v[24:27]\n\t" \
    "v_mov_b32 v128, v131\n\t" \
    "v_mov_b32 v129, v132\n\t" \
    "v_mov_b32 v130, v133\n\t" \
    "v_mfma_f32_16x16x32_bf16 v[28:31], v[124:127], v[100:103], v[28:31]\n\t" \
    "v_add_u32 v131, s69, v134\n\t" \
    "v_add_u32 v132, s69, v135\n\t" \
    "v_add_u32 v133, s69, v136\n\t" \
    "v_mfma_f32_16x16x32_bf16 v[32:35], v[112:115], v[104:107], v[32:35]\n\t" \
    "s_add_u32 s68, s68, 49152\n\t" \
    "s_cmp_eq_u32 s68, 147456\n\t" \
    "s_cselect_b32 s68, 0, s68\n\t" \
    "v_mfma_f32_16x16x32_bf16 v[36:39], v[116:119], v[104:107], v[36:39]\n\t" \
    "s_add_u32 s69, s69, 49152\n\t" \
    "s_cmp_eq_u32 s69, 147456\n\t" \
    "s_cselect_b32 s69, 0, s69\n\t" \
    "v_mfma_f32_16x16x32_bf16 v[40:43], v[120:123], v[104:107], v[40:43]\n\t" \
    "v_mfma_f32_16x16x32_bf16 v[44:47], v[124:127], v[104:107], v[44:47]\n\t" \
    "v_mfma_f32_16x16x32_bf16 v[48:51], v[112:115], v[108:111], v[48:51]\n\t" \
    "v_mfma_f32_16x16x32_bf16 v[52:55], v[116:119], v[108:111], v[52:55]\n\t" \
    "v_mfma_f32_16x16x32_bf16 v[56:59], v[120:123], v[108:111], v[56:59]\n\t" \
    "v_mfma_f32_16x16x32_bf16 v[60:63], v[124:127], v[108:111], v[60:63]\n\t" \
    "s_waitcnt lgkmcnt(0)\n\t" \
    "v_mfma_f32_16x16x32_bf16 v[0:3], v[80:83], v[64:67], v[0:3]\n\t" \
    "ds_read_b128 v[96:99], v128 offset:8192\n\t" \
    "ds_read_b64_tr_b16 v[112:113], v129 offset:8192\n\t" \
    "v_mfma_f32_16x16x32_bf16 v[4:7], v[84:87], v[64:67], v[4:7]\n\t" \
    "ds_read_b64_tr_b16 v[114:115], v129 offset:8448\n\t" \
    "ds_read_b64_tr_b16 v[116:117], v130 offset:8192\n\t" \
    "v_mfma_f32_16x16x32_bf16 v[8:11], v[88:91], v[64:67], v[8:11]\n\t" \
    "ds_read_b64_tr_b16 v[118:119], v130 offset:8448\n\t" \
    "ds_read_b128 v[100:103], v128 offset:8448\n\t" \
    "v_mfma_f32_16x16x32_bf16 v[12:15], v[92:95], v[64:67], v[12:15]\n\t" \
    "ds_read_b64_tr_b16 v[120:121], v129 offset:8704\n\t" \
    "ds_read_b64_tr_b16 v[122:123], v129 offset:8960\n\t" \
    "v_mfma_f32_16x16x32_bf16 v[16:19], v[80:83], v[68:71], v[16:19]\n\t" \
    "ds_read_b64_tr_b16 v[124:125], v130 offset:8704\n\t" \
    "ds_read_b64_tr_b16 v[126:127], v130 offset:8960\n\t" \
    "v_mfma_f32_16x16x32_bf16 v[20:23], v[84:87], v[68:71], v[20:23]\n\t" \
    "ds_read_b128 v[104:107], v128 offset:8704\n\t" \
    "ds_read_b128 v[108:111], v128 offset:8960\n\t" \
    "v_mfma_f32_16x16x32_bf16 v[24:27], v[88:91], v[68:71], v[24:27]\n\t" \
    "v_mfma_f32_16x16x32_bf16 v[28:31], v[92:95], v[68:71], v[28:31]\n\t" \
    "v_mfma_f32_16x16x32_bf16 v[32:35], v[80:83], v[72:75], v[32:35]\n\t" \
    "v_mfma_f32_16x16x32_bf16 v[36:39], v[84:87], v[72:75], v[36:39]\n\t" \
    "v_mfma_f32_16x16x32_bf16 v[40:43], v[88:91], v[72:75], v[40:43]\n\t" \
    "v_mfma_f32_16x16x32_bf16 v[44:47], v[92:95], v[72:75], v[44:47]\n\t" \
    "v_mfma_f32_16x16x32_bf16 v[48:51], v[80:83], v[76:79], v[48:51]\n\t" \
    "v_mfma_f32_16x16x32_bf16 v[52:55], v[84:87], v[76:79], v[52:55]\n\t" \
    "v_mfma_f32_16x16x32_bf16 v[56:59], v[88:91], v[76:79], v[56:59]\n\t" \
    "v_mfma_f32_16x16x32_bf16 v[60:63], v[92:95], v[76:79], v[60:63]\n\t" \
    "s_waitcnt vmcnt(0) lgkmcnt(0)\n\t" \
    "s_barrier\n\t" \
    "v_mfma_f32_16x16x32_bf16 v[0:3], v[112:115], v[96:99], v[0:3]\n\t" \
    "v_mov_b32 v128, v131\n\t" \
    "v_mov_b32 v129, v132\n\t" \
    "v_mfma_f32_16x16x32_bf16 v[4:7], v[116:119], v[96:99], v[4:7]\n\t" \
    "v_mov_b32 v130, v133\n\t" \
    "v_add_u32 v131, s69, v134\n\t" \
    "v_mfma_f32_16x16x32_bf16 v[8:11], v[120:123], v[96:99], v[8:11]\n\t" \
    "v_add_u32 v132, s69, v135\n\t" \
    "v_add_u32 v133, s69, v136\n\t" \
    "v_mfma_f32_16x16x32_bf16 v[12:15], v[124:127], v[96:99], v[12:15]\n\t" \
    "s_add_u32 s68, s68, 49152\n\t" \
    "s_cmp_eq_u32 s68, 147456\n\t" \
    "v_mfma_f32_16x16x32_bf16 v[16:19], v[112:115], v[100:103], v[16:19]\n\t" \
    "s_cselect_b32 s68, 0, s68\n\t" \
    "s_add_u32 s69, s69, 49152\n\t" \
    "v_mfma_f32_16x16x32_bf16 v[20:23], v[116:119], v[100:103], v[20:23]\n\t" \
    "s_cmp_eq_u32 s69, 147456\n\t" \
    "s_cselect_b32 s69, 0, s69\n\t" \
    "v_mfma_f32_16x16x32_bf16 v[24:27], v[120:123], v[100:103], v[24:27]\n\t" \
    "v_mfma_f32_16x16x32_bf16 v[28:31], v[124:127], v[100:103], v[28:31]\n\t" \
    "v_mfma_f32_16x16x32_bf16 v[32:35], v[112:115], v[104:107], v[32:35]\n\t" \
    "v_mfma_f32_16x16x32_bf16 v[36:39], v[116:119], v[104:107], v[36:39]\n\t" \
    "v_mfma_f32_16x16x32_bf16 v[40:43], v[120:123], v[104:107], v[40:43]\n\t" \
    "v_mfma_f32_16x16x32_bf16 v[44:47], v[124:127], v[104:107], v[44:47]\n\t" \
    "v_mfma_f32_16x16x32_bf16 v[48:51], v[112:115], v[108:111], v[48:51]\n\t" \
    "v_mfma_f32_16x16x32_bf16 v[52:55], v[116:119], v[108:111], v[52:55]\n\t" \
    "v_mfma_f32_16x16x32_bf16 v[56:59], v[120:123], v[108:111], v[56:59]\n\t" \
    "v_mfma_f32_16x16x32_bf16 v[60:63], v[124:127], v[108:111], v[60:63]\n\t" \
    "s_nop 15\n\t"
DI u32x4 pack8(const f32x16& c, int n) { return (u32x4){cvtpk(c[4 * n], c[4 * n + 1]), cvtpk(c[4 * n + 2], c[4 * n + 3]), cvtpk(c[4 * n + 4], c[4 * n + 5]), cvtpk(c[4 * n + 6], c[4 * n + 7])}; }
DI void add8(f32x16& c, int n, u32x4 w) {
#pragma unroll
    for (int i = 0; i < 4; ++i) { c[4 * n + 2 * i] += bflo(w[i]); c[4 * n + 2 * i + 1] += bfhi(w[i]); }
}
DI void poll(unsigned* flags, int first, int count, int lane) {
    gu32* f = (gu32*)(flags + (first + (lane % count)) * 32);
    for (int spin = 0; spin < (1 << 20); ++spin) {
        if (__all(__hip_atomic_load(f, __ATOMIC_RELAXED, __HIP_MEMORY_SCOPE_AGENT) == 1u)) break;
        __builtin_amdgcn_s_sleep(1);
    }
}
__global__ __launch_bounds__(512, 2) void k3_out(const char* __restrict__ ctx, const char* __restrict__ H, const bf16_t* __restrict__ wo, const bf16_t* __restrict__ w2,
                                                 const bf16_t* __restrict__ b2, const bf16_t* __restrict__ x, float* part, unsigned* flags, bf16_t* __restrict__ out) {
    extern __shared__ __attribute__((aligned(16))) unsigned char smem[];
    const int tid = threadIdx.x, wid = __builtin_amdgcn_readfirstlane(tid >> 6), lane = tid & 63, wr = wid >> 1, wc = wid & 1, fr = lane & 15, fq = lane >> 4;
    const int xcd = blockIdx.x & 7, idx = blockIdx.x >> 3, sl = xcd >> 1, mt = 4 * (xcd & 1) + (idx >> 3), ntile = idx & 7, tile = mt * 8 + ntile;
    const unsigned lds0 = (unsigned)(uintptr_t)smem;
    const size_t aw = (size_t)(wid & 3) * 4096, bw = (size_t)ntile * 256 + (size_t)(8 * (wid >> 1)) * (DM * 2) + 128 * (wid & 1);
    const int kt0 = sl < 2 ? 32 * sl : 28 * (sl - 2);
    const char* a1 = (sl < 2 ? ctx + (size_t)(2 * mt + (wid >> 2)) * 64 * 16384 : H + (size_t)(2 * mt + (wid >> 2)) * 56 * 16384) + aw + (size_t)kt0 * 16384;
    const char* b1 = (const char*)(sl < 2 ? wo : w2) + bw + (size_t)kt0 * (64 * DM * 2);
    const int nl = (sl < 2 ? 32 : 28) - 3;
    const unsigned va = lane * 16, vb = ((lane & 31) >> 2) * (DM * 2) + (32 * (lane >> 5) + 8 * ((lane & 3) ^ (wid & 2))) * 2;
    const unsigned mA = lds0 + (wid >> 2) * 16384 + (wid & 3) * 4096, mB = lds0 + 32768 + wid * 1024;
    const unsigned ra = lds0 + (wr >> 1) * 16384 + fq * 2048 + ((wr & 1) * 64 + fr) * 16;
    const unsigned rb0 = lds0 + 32768 + fq * 2048 + wc * 1024 + (fr >> 2) * 64 + (fq & 1) * 32 + (fr & 3) * 8, rb1 = rb0 ^ 32;
    f32x16 c0 = f32x16{}, c1 = f32x16{}, c2 = f32x16{}, c3 = f32x16{};
    u32x2 xb[4][4], bb[4];
    if (sl == 2) {
#pragma unroll
        for (int n = 0; n < 4; ++n) { bb[n] = *(const u32x2*)(b2 + ntile * 128 + wc * 64 + fq * 4 + n * 16);
#pragma unroll
            for (int m = 0; m < 4; ++m) xb[m][n] = *(const u32x2*)(x + (size_t)(mt * 256 + wr * 64 + m * 16 + fr) * DM + ntile * 128 + wc * 64 + fq * 4 + n * 16); }
    } else {
#pragma unroll
        for (int n = 0; n < 4; ++n) { bb[n] = (u32x2){0u, 0u};
#pragma unroll
            for (int m = 0; m < 4; ++m) xb[m][n] = (u32x2){0u, 0u}; }
    }
    STAMP(3, 0);
    asm volatile(K3_ASM
                 : "+&{v[0:15]}"(c0), "+&{v[16:31]}"(c1), "+&{v[32:47]}"(c2), "+&{v[48:63]}"(c3)
                 : "{v134}"(ra), "{v135}"(rb0), "{v136}"(rb1),
                   [va] "v"(va), [vb] "v"(vb), [a1] "s"(a1), [b1] "s"(b1), [nl] "s"(nl), [mA] "s"(mA), [mB] "s"(mB)
                 : "v64", "v65", "v66", "v67", "v68", "v69", "v70", "v71", "v72", "v73", "v74", "v75", "v76", "v77", "v78", "v79", "v80", "v81", "v82", "v83", "v84", "v85", "v86", "v87", "v88", "v89", "v90", "v91", "v92", "v93", "v94", "v95", "v96", "v97", "v98", "v99", "v100", "v101", "v102", "v103", "v104", "v105", "v106", "v107", "v108", "v109", "v110", "v111", "v112", "v113", "v114", "v115", "v116", "v117", "v118", "v119", "v120", "v121", "v122", "v123", "v124", "v125", "v126", "v127", "v128", "v129", "v130", "v131", "v132", "v133", "s60", "s61", "s62", "s63", "s64", "s65", "s66", "s67", "s68", "s69", "scc", "memory");
    STAMP(3, 1);
    const auto rs = __builtin_amdgcn_make_buffer_rsrc((void*)((char*)part + (size_t)tile * 4 * 65536), 0, 4 * 65536, 0x00020000);
#define K3_SEND(c, m) do { __builtin_amdgcn_raw_buffer_store_b128(pack8(c, 0), rs, tid * 16 + (m) * 16384 + sl * 65536, 0, 16); __builtin_amdgcn_raw_buffer_store_b128(pack8(c, 2), rs, tid * 16 + (m) * 16384 + 8192 + sl * 65536, 0, 16); } while (0)
#define K3_GET(src, m, p) __builtin_amdgcn_raw_buffer_load_b128(rs, tid * 16 + (m) * 16384 + (p) * 8192, (src) * 65536, 16)
    gu32* myflag = (gu32*)(flags + (tile * 4 + sl) * 32);
    if (sl >= 2) {
#define K3_RES(c, m) do { _Pragma("unroll") for (int n = 0; n < 4; ++n) { \
            c[4 * n] += bflo(xb[m][n][0]) + bflo(bb[n][0]); c[4 * n + 1] += bfhi(xb[m][n][0]) + bfhi(bb[n][0]); c[4 * n + 2] += bflo(xb[m][n][1]) + bflo(bb[n][1]); c[4 * n + 3] += bfhi(xb[m][n][1]) + bfhi(bb[n][1]); } } while (0)
        K3_RES(c0, 0); K3_RES(c1, 1); K3_RES(c2, 2); K3_RES(c3, 3);
        K3_SEND(c0, 0); K3_SEND(c1, 1); K3_SEND(c2, 2); K3_SEND(c3, 3);
        WAIT_V(0);
        __syncthreads();
        if (tid == 0) __hip_atomic_store(myflag, 1u, __ATOMIC_RELAXED, __HIP_MEMORY_SCOPE_AGENT);
        STAMP(3, 2);
        return;
    }
    f32x16 f0, f1;
    if (sl == 0) { K3_SEND(c2, 2); K3_SEND(c3, 3); f0 = c0; f1 = c1; } else { K3_SEND(c0, 0); K3_SEND(c1, 1); f0 = c2; f1 = c3; }
    const int m0 = 2 * sl;
    WAIT_V(0);
    __syncthreads();
    if (tid == 0) __hip_atomic_store(myflag, 1u, __ATOMIC_RELAXED, __HIP_MEMORY_SCOPE_AGENT);
    STAMP(3, 2);
    if (wid == 0) poll(flags, tile * 4, 4, lane);
    asm volatile("" ::: "memory"); BAR(); asm volatile("" ::: "memory");
    STAMP(3, 3);
    u32x4 pa[3][2][2];
#pragma unroll
    for (int k = 0; k < 3; ++k)
#pragma unroll
        for (int m = 0; m < 2; ++m)
#pragma unroll
            for (int p = 0; p < 2; ++p) pa[k][m][p] = K3_GET(k == 0 ? 1 - sl : 1 + k, m0 + m, p);
#pragma unroll
    for (int k = 0; k < 3; ++k) { add8(f0, 0, pa[k][0][0]); add8(f0, 2, pa[k][0][1]); add8(f1, 0, pa[k][1][0]); add8(f1, 2, pa[k][1][1]); }
    const int row = mt * 256 + wr * 64 + m0 * 16 + fr;
    const int cg = ((fq & 1) << 1) | (fq >> 1);
    bf16_t* orow = out + (size_t)row * DM + ntile * 128 + wc * 64 + cg * 8;
#define K3_F4(c, n) (f32x4){c[4 * (n)], c[4 * (n) + 1], c[4 * (n) + 2], c[4 * (n) + 3]}
    *(u32x4*)(orow) = k1::pack_swap16(K3_F4(f0, 0), K3_F4(f0, 1)); *(u32x4*)(orow + 32) = k1::pack_swap16(K3_F4(f0, 2), K3_F4(f0, 3));
    *(u32x4*)(orow + 16 * DM) = k1::pack_swap16(K3_F4(f1, 0), K3_F4(f1, 1)); *(u32x4*)(orow + 16 * DM + 32) = k1::pack_swap16(K3_F4(f1, 2), K3_F4(f1, 3));
    STAMP(3, 4);
}
}

extern "C" void kernel_launch(void* const* d_in, const int* in_sizes, int n_in, void* d_out, int out_size, void* d_ws, size_t ws_size, hipStream_t stream) {
    const bf16_t* x = (const bf16_t*)d_in[0]; const bf16_t* ckv = (const bf16_t*)d_in[1];
    const bf16_t* wq = (const bf16_t*)d_in[2]; const bf16_t* wuk = (const bf16_t*)d_in[3]; const bf16_t* wuv = (const bf16_t*)d_in[4]; const bf16_t* wo = (const bf16_t*)d_in[5];
    const bf16_t* w1 = (const bf16_t*)d_in[7]; const bf16_t* b1 = (const bf16_t*)d_in[8]; const bf16_t* w2 = (const bf16_t*)d_in[9]; const bf16_t* b2 = (const bf16_t*)d_in[10];
    char* ws = (char*)d_ws;
    (void)hipFuncSetAttribute((const void*)k1::k1_proj, hipFuncAttributeMaxDynamicSharedMemorySize, k1::LDS_BYTES);
    (void)hipFuncSetAttribute((const void*)k2::k2_attn, hipFuncAttributeMaxDynamicSharedMemorySize, k2::LDS_BYTES);
    (void)hipFuncSetAttribute((const void*)k3::k3_out, hipFuncAttributeMaxDynamicSharedMemorySize, k3::LDS_BYTES);
    k0_ln<<<SEQ / 4, 256, 0, stream>>>(x, (bf16_t*)(ws + WS_XN), (unsigned*)(ws + WS_FLAG), (const char*)ckv, (const char*)wuk, (const char*)wuv, (const char*)wq, (const char*)w1);
    k1::k1_proj<<<256, 512, k1::LDS_BYTES, stream>>>(ckv, ws + WS_XN, wuk, wuv, wq, w1, b1, ws + WS_Q, ws + WS_K, ws + WS_VT, ws + WS_H);
    k2::k2_attn<<<256, 512, k2::LDS_BYTES, stream>>>(ws + WS_Q, ws + WS_K, ws + WS_VT, ws + WS_CTX, (const char*)wo, (const char*)w2);
    k3::k3_out<<<256, 512, k3::LDS_BYTES, stream>>>(ws + WS_CTX, ws + WS_H, wo, w2, b2, x, (float*)(ws + WS_PART), (unsigned*)(ws + WS_FLAG), (bf16_t*)d_out);
}
```

```cpp
#include <hip/hip_runtime.h>
#include <cstdint>

#define STAMP(k, i) do {} while (0)
#define LAS __attribute__((address_space(3)))
#define DI __device__ __forceinline__
typedef unsigned short bf16_t;
typedef short bf16x8 __attribute__((ext_vector_type(8)));
typedef short s16x4 __attribute__((ext_vector_type(4)));
typedef short v4i16_t __attribute__((ext_vector_type(4)));
typedef float f32x2 __attribute__((ext_vector_type(2)));
typedef float f32x4 __attribute__((ext_vector_type(4)));
typedef float f32x16 __attribute__((ext_vector_type(16)));
typedef unsigned u32x2 __attribute__((ext_vector_type(2)));
typedef unsigned u32x4 __attribute__((ext_vector_type(4)));
typedef __bf16 bf16x2_t __attribute__((ext_vector_type(2)));
typedef LAS unsigned char* lds_ptr;
typedef LAS const unsigned char* lds_cptr;

constexpr int SEQ = 2048, DM = 1024, NH = 32, HD = 128, DC = 512, FF = 3584, NQ = NH * HD;
constexpr size_t MiB = 1u << 20;
constexpr size_t WS_FLAG = 0;
constexpr size_t WS_XN = 1 * MiB;
constexpr size_t WS_Q = 8 * MiB;
constexpr size_t WS_K = 24 * MiB;
constexpr size_t WS_VT = 40 * MiB;
constexpr size_t WS_CTX = 56 * MiB;
constexpr size_t WS_H = 72 * MiB;
constexpr size_t WS_PART = 88 * MiB;

DI unsigned cvtpk(float lo, float hi) { f32x2 v = {lo, hi}; bf16x2_t b = __builtin_convertvector(v, bf16x2_t); return __builtin_bit_cast(unsigned, b); }
DI float bflo(unsigned w) { return __uint_as_float(w << 16); }
DI float bfhi(unsigned w) { return __uint_as_float(w & 0xffff0000u); }
DI s16x4 lds_tr(lds_cptr p) { return __builtin_bit_cast(s16x4, __builtin_amdgcn_ds_read_tr16_b64_v4i16((LAS v4i16_t*)p)); }
DI bf16x8 lds_rd(lds_cptr p) { return *(const LAS bf16x8*)p; }
DI bf16x8 lds_rdv(lds_cptr p) { return *(const volatile LAS bf16x8*)p; }
DI bf16x8 cat8(s16x4 a, s16x4 b) { return __builtin_shufflevector(a, b, 0, 1, 2, 3, 4, 5, 6, 7); }
DI void st16_sc1(void* p, u32x4 v) { asm volatile("global_store_dwordx4 %0, %1, off sc1\n\ts_nop 1" :: "v"(p), "v"(v) : "memory"); }
#define ST16(p, v) st16_sc1((void*)(p), v)
DI unsigned code_pf(unsigned line, unsigned nlines) {
    unsigned long long pc; unsigned d;
    asm volatile("s_getpc_b64 %0" : "=s"(pc));
    asm volatile("global_load_dword %0, %1, %2" : "=v"(d) : "v"((line < nlines ? line : 0u) * 128u), "s"(pc) : "memory");
    return d;
}
#define CODE_PF_DONE(d) asm volatile("" :: "v"(d))
#define SBAR() __builtin_amdgcn_sched_barrier(0)
#define PIN(x) asm volatile("" : "+v"(x))
#define WAIT_V(n) asm volatile("s_waitcnt vmcnt(" #n ")" ::: "memory")
#define WAIT_L(n) asm volatile("s_waitcnt lgkmcnt(" #n ")" ::: "memory")
#define BAR() __builtin_amdgcn_s_barrier()
#define MFMA16(a, b, c) __builtin_amdgcn_mfma_f32_16x16x32_bf16(a, b, c, 0, 0, 0)
#define MFMA32(a, b, c) __builtin_amdgcn_mfma_f32_32x32x16_bf16(a, b, c, 0, 0, 0)
DI void glds(const char* sbase, unsigned voff, unsigned ldsaddr) {
    asm volatile("s_mov_b32 m0, %2\n\ts_nop 0\n\tglobal_load_lds_dwordx4 %0, %1" :: "v"(voff), "s"(sbase), "s"(ldsaddr) : "memory");
}
DI void glds2(const char* sbase, unsigned voff, unsigned ldsaddr) {
    asm volatile("s_mov_b32 m0, %2\n\ts_nop 0\n\tglobal_load_lds_dwordx4 %0, %1\n\tglobal_load_lds_dwordx4 %0, %1 offset:1024" :: "v"(voff), "s"(sbase), "s"(ldsaddr) : "memory");
}

__global__ __launch_bounds__(256) void k0_ln(const bf16_t* __restrict__ x, bf16_t* __restrict__ xn, unsigned* __restrict__ flags) {
    const int lane = threadIdx.x & 63, row = blockIdx.x * 4 + (threadIdx.x >> 6);
    STAMP(0, 0);
    unsigned cpf = 0; if (blockIdx.x < 16) cpf = code_pf(threadIdx.x + (blockIdx.x >> 3) * 256, 300);
    const u32x4* src = (const u32x4*)(x + (size_t)row * DM);
    const u32x4 v0 = src[lane], v1 = src[lane + 64];
    float f[16];
#pragma unroll
    for (int i = 0; i < 4; ++i) { f[2 * i] = bflo(v0[i]); f[2 * i + 1] = bfhi(v0[i]); f[8 + 2 * i] = bflo(v1[i]); f[8 + 2 * i + 1] = bfhi(v1[i]); }
    float s = 0.f;
#pragma unroll
    for (int i = 0; i < 16; ++i) s += f[i];
#pragma unroll
    for (int o = 32; o >= 1; o >>= 1) s += __shfl_xor(s, o);
    const float mean = s * (1.0f / DM);
    float q = 0.f;
#pragma unroll
    for (int i = 0; i < 16; ++i) { f[i] -= mean; q += f[i] * f[i]; }
#pragma unroll
    for (int o = 32; o >= 1; o >>= 1) q += __shfl_xor(q, o);
    const float rstd = 1.0f / sqrtf(q * (1.0f / DM) + 1e-5f);
    u32x4 w0, w1;
#pragma unroll
    for (int i = 0; i < 4; ++i) { w0[i] = cvtpk(f[2 * i] * rstd, f[2 * i + 1] * rstd); w1[i] = cvtpk(f[8 + 2 * i] * rstd, f[8 + 2 * i + 1] * rstd); }
    const int mh = row >> 7, r = row & 127, c = lane & 7, kt = lane >> 3, sw = (c ^ ((r >> 1) & 7)) * 16;
    char* base = (char*)xn + (size_t)mh * 16 * 16384 + r * 128 + sw;
    *(u32x4*)(base + (size_t)kt * 16384) = w0;
    *(u32x4*)(base + (size_t)(kt + 8) * 16384) = w1;
    STAMP(0, 1);
    WAIT_V(0); CODE_PF_DONE(cpf);
    if (blockIdx.x == 0) flags[threadIdx.x * 32] = 0u;
}

namespace k1 {
constexpr int HTB = 16384, LDS_BYTES = 8 * HTB;
enum { T_K = 0, T_V = 1, T_Q = 2, T_H = 3 };
struct Src {
    const char* a; const char* b;
    unsigned aK, aH, aD, bK, bD;
    unsigned va, vb;
};
struct Frag { bf16x8 At[4][2], B0[2][2], B1[2][2]; };

DI f32x2 gelu_tanh2(f32x2 v) {
    const f32x2 z = v * (v * v * -0.10294324f + -2.3022082f);
    f32x2 e; e.x = __builtin_amdgcn_exp2f(z.x); e.y = __builtin_amdgcn_exp2f(z.y);
    e = e + 1.0f;
    f32x2 r; r.x = __builtin_amdgcn_rcpf(e.x); r.y = __builtin_amdgcn_rcpf(e.y);
    return v * r;
}

#define K1_SA(b, h) (((b) * 2 + (h)) * HTB)
#define K1_SB(b, h) ((4 + (b) * 2 + (h)) * HTB)
#define K1_STAGE(bufoff, gbase, voff, d2) do { glds((const char*)(gbase), voff, ldsw + (bufoff)); glds((const char*)(gbase) + (d2), voff, ldsw + (bufoff) + 8192); } while (0)
#define K1_LDA(dst, b, h) do { _Pragma("unroll") for (int m = 0; m < 4; ++m) { dst[m][0] = lds_rd(lds + K1_SA(b, h) + aoff0 + m * 2048); dst[m][1] = lds_rd(lds + K1_SA(b, h) + aoff1 + m * 2048); } } while (0)
#define K1_LDB(dst, b, h) do { _Pragma("unroll") for (int k = 0; k < 2; ++k) { \
        dst[0][k] = cat8(lds_tr(lds + K1_SB(b, h) + boff0 + k * 8192), lds_tr(lds + K1_SB(b, h) + boff0 + k * 8192 + 256)); \
        dst[1][k] = cat8(lds_tr(lds + K1_SB(b, h) + boff1 + k * 8192), lds_tr(lds + K1_SB(b, h) + boff1 + k * 8192 + 256)); } } while (0)
#define K1_MMA(ai, bj, At, Bt) do { __builtin_amdgcn_s_setprio(1); _Pragma("unroll") for (int m = 0; m < 4; ++m) _Pragma("unroll") for (int n = 0; n < 2; ++n) _Pragma("unroll") for (int k = 0; k < 2; ++k) \
        acc[ai][bj][m][n] = SWAP ? MFMA16(At[m][k], Bt[n][k], acc[ai][bj][m][n]) : MFMA16(Bt[n][k], At[m][k], acc[ai][bj][m][n]); __builtin_amdgcn_s_setprio(0); } while (0)

template <bool SWAP>
DI void kloop(lds_ptr lds, f32x4 (&acc)[2][2][4][2], const Src& cur, const Src& nxt, int nt, unsigned ldsw, int aoff0, int aoff1, int boff0, int boff1) {
    bf16x8 At[4][2], B0[2][2], B1[2][2];
    for (int t = 0; t < nt; t += 2) {
        const bool last = (t == nt - 2);
        const char* a1h = cur.a + (size_t)(t + 1) * cur.aK + cur.aH;
        const char* a2 = last ? nxt.a : cur.a + (size_t)(t + 2) * cur.aK; const char* b2 = last ? nxt.b : cur.b + (size_t)(t + 2) * cur.bK;
        const unsigned aK = last ? nxt.aK : cur.aK, aH = last ? nxt.aH : cur.aH, aD = last ? nxt.aD : cur.aD, bK = last ? nxt.bK : cur.bK, bD = last ? nxt.bD : cur.bD;
        const unsigned va = last ? nxt.va : cur.va, vb = last ? nxt.vb : cur.vb;
        const char* a3 = a2 + aK; const char* b3 = b2 + bK;
        K1_LDB(B0, 0, 0); K1_LDB(B1, 0, 1); SBAR(); K1_LDA(At, 0, 0); K1_STAGE(K1_SA(1, 1), a1h, cur.va, cur.aD);
        WAIT_V(8); WAIT_L(0); BAR(); K1_MMA(0, 0, At, B0); K1_MMA(0, 1, At, B1); BAR(); SBAR();
        K1_LDA(At, 0, 1); K1_STAGE(K1_SB(0, 0), b2, vb, bD); K1_STAGE(K1_SB(0, 1), b2 + 256, vb, bD); K1_STAGE(K1_SA(0, 0), a2, va, aD);
        WAIT_V(8); WAIT_L(0); BAR(); K1_MMA(1, 0, At, B0); K1_MMA(1, 1, At, B1); BAR(); SBAR();
        K1_LDB(B0, 1, 0); K1_LDB(B1, 1, 1); SBAR(); K1_LDA(At, 1, 0); K1_STAGE(K1_SA(0, 1), a2 + aH, va, aD);
        WAIT_V(8); WAIT_L(0); BAR(); K1_MMA(0, 0, At, B0); K1_MMA(0, 1, At, B1); BAR(); SBAR();
        K1_LDA(At, 1, 1); K1_STAGE(K1_SB(1, 0), b3, vb, bD); K1_STAGE(K1_SB(1, 1), b3 + 256, vb, bD); K1_STAGE(K1_SA(1, 0), a3, va, aD);
        WAIT_V(8); WAIT_L(0); BAR(); K1_MMA(1, 0, At, B0); K1_MMA(1, 1, At, B1); BAR(); SBAR();
    }
}

DI void zero_acc(f32x4 (&acc)[2][2][4][2]) {
#pragma unroll
    for (int a = 0; a < 2; ++a)
#pragma unroll
        for (int b = 0; b < 2; ++b)
#pragma unroll
            for (int m = 0; m < 4; ++m)
#pragma unroll
                for (int n = 0; n < 2; ++n) acc[a][b][m][n] = (f32x4){0.f, 0.f, 0.f, 0.f};
}
DI u32x4 pack_swap16(f32x4 v0, f32x4 v1) {
    const unsigned x0 = cvtpk(v0[0], v0[1]), x1 = cvtpk(v0[2], v0[3]), y0 = cvtpk(v1[0], v1[1]), y1 = cvtpk(v1[2], v1[3]);
    const auto r0 = __builtin_amdgcn_permlane16_swap(x0, y0, false, false), r1 = __builtin_amdgcn_permlane16_swap(x1, y1, false, false);
    return (u32x4){r0[0], r1[0], r0[1], r1[1]};
}
DI u32x4 pack_swap32(f32x4 v0, f32x4 v1) {
    const unsigned x0 = cvtpk(v0[0], v0[1]), x1 = cvtpk(v0[2], v0[3]), y0 = cvtpk(v1[0], v1[1]), y1 = cvtpk(v1[2], v1[3]);
    const auto r0 = __builtin_amdgcn_permlane32_swap(x0, y0, false, false), r1 = __builtin_amdgcn_permlane32_swap(x1, y1, false, false);
    return (u32x4){r0[0], r1[0], r0[1], r1[1]};
}
DI void epi_qk(const f32x4 (&acc)[2][2][4][2], char* dst, int pm, int pn, float sc, int wr, int wc, int fr, int fq) {
    const int cg = ((fq & 1) << 1) | (fq >> 1);
    char* p = dst + (size_t)(2 * pn * 32 + 4 * pm + wr) * 16384 + (4 * wc + cg) * 1024 + fr * 16;
#pragma unroll
    for (int ai = 0; ai < 2; ++ai)
#pragma unroll
        for (int bj = 0; bj < 2; ++bj)
#pragma unroll
            for (int m = 0; m < 4; ++m)
                ST16(p + (size_t)(bj * 32 + ai * 2) * 16384 + m * 256, pack_swap16(acc[ai][bj][m][0] * sc, acc[ai][bj][m][1] * sc));
}
DI void epi_v(const f32x4 (&acc)[2][2][4][2], char* dst, int pm, int pn, int wr, int wc, int fr, int fq) {
    char* p = dst + (size_t)(2 * pn * 32 + 4 * pm + wr) * 16384 + fq * 2048 + (32 * wc + fr) * 16;
#pragma unroll
    for (int ai = 0; ai < 2; ++ai)
#pragma unroll
        for (int bj = 0; bj < 2; ++bj)
#pragma unroll
            for (int m = 0; m < 4; m += 2)
#pragma unroll
                for (int n = 0; n < 2; ++n)
                    ST16(p + (size_t)(bj * 32 + ai * 2) * 16384 + m * 4096 + n * 256, pack_swap32(acc[ai][bj][m][n], acc[ai][bj][m + 1][n]));
}
DI void epi_h(const f32x4 (&acc)[2][2][4][2], char* dst, const bf16_t* b1, int pm, int pn, int wr, int wc, int fr, int fq) {
    const int cg = ((fq & 1) << 1) | (fq >> 1);
    f32x4 bv[2][2];
#pragma unroll
    for (int bj = 0; bj < 2; ++bj)
#pragma unroll
        for (int n = 0; n < 2; ++n) { const u32x2 w = *(const u32x2*)(b1 + pn * 256 + bj * 128 + wc * 32 + n * 16 + fq * 4); bv[bj][n] = (f32x4){bflo(w[0]), bfhi(w[0]), bflo(w[1]), bfhi(w[1])}; }
    char* p = dst + ((size_t)(2 * pm * 56 + 4 * pn + (wc >> 1)) * 8 + 4 * (wc & 1) + cg) * 2048 + (64 * wr + fr) * 16;
#pragma unroll
    for (int ai = 0; ai < 2; ++ai)
#pragma unroll
        for (int bj = 0; bj < 2; ++bj)
#pragma unroll
            for (int m = 0; m < 4; ++m) {
                f32x4 v0 = acc[ai][bj][m][0] + bv[bj][0], v1 = acc[ai][bj][m][1] + bv[bj][1];
                { const f32x2 a = gelu_tanh2((f32x2){v0[0], v0[1]}), b = gelu_tanh2((f32x2){v0[2], v0[3]}), c = gelu_tanh2((f32x2){v1[0], v1[1]}), d = gelu_tanh2((f32x2){v1[2], v1[3]});
                  v0 = (f32x4){a.x, a.y, b.x, b.y}; v1 = (f32x4){c.x, c.y, d.x, d.y}; }
                ST16(p + (size_t)(ai * 56 + bj * 2) * 16384 + m * 256, pack_swap16(v0, v1));
            }
}

__global__ __launch_bounds__(512, 2) void k1_proj(const bf16_t* __restrict__ ckv, const char* __restrict__ xn, const bf16_t* __restrict__ wuk, const bf16_t* __restrict__ wuv,
                                                  const bf16_t* __restrict__ wq, const bf16_t* __restrict__ w1, const bf16_t* __restrict__ b1,
                                                  char* __restrict__ Qo, char* __restrict__ Ko, char* __restrict__ VTo, char* __restrict__ Ho) {
    extern __shared__ __attribute__((aligned(16))) unsigned char smem[];
    lds_ptr lds = (lds_ptr)smem;
    const int tid = threadIdx.x, wid = __builtin_amdgcn_readfirstlane(tid >> 6), lane = tid & 63, wr = wid >> 2, wc = wid & 3, fr = lane & 15, fq = lane >> 4;
    const int xcd = blockIdx.x & 7, idx = blockIdx.x >> 3, pm = idx & 7, pj = xcd + 8 * (idx >> 3);
    const bool isV = pj >= 16, has2 = pj < 30, isH = pj >= 16;
    const unsigned ldsw = (unsigned)(uintptr_t)smem + (unsigned)wid * 1024u;
    const int aoff0 = (wr * 64 + fr) * 128 + ((fq ^ (fr >> 1)) * 16), aoff1 = aoff0 ^ 64;
    const int boff0 = fq * 2048 + wc * 512 + (fr >> 2) * 64 + (fq & 1) * 32 + (fr & 3) * 8, boff1 = boff0 ^ 32;
    const unsigned brow = 8 * (wid >> 1) + ((lane & 31) >> 2), bcol = (64 * (wid & 1) + 32 * (lane >> 5) + 8 * ((lane & 3) ^ (wid & 2))) * 2;
    Src u0, u1;
    u0.a = (const char*)ckv + (size_t)pm * 256 * DC * 2; u0.aK = 128; u0.aH = 128 * DC * 2; u0.aD = 64 * DC * 2;
    u0.va = (tid >> 3) * (DC * 2) + (((tid & 7) ^ ((tid >> 4) & 7)) * 16);
    u0.b = (const char*)(isV ? wuv : wuk) + (pj & 15) * 512; u0.bK = 64 * NQ * 2; u0.bD = 32 * NQ * 2; u0.vb = brow * (NQ * 2) + bcol;
    if (has2) {
        const unsigned ldb2 = isH ? FF * 2 : NQ * 2;
        u1.a = xn + (size_t)pm * 2 * 16 * 16384; u1.aK = 16384; u1.aH = 16 * 16384; u1.aD = 8192; u1.va = tid * 16;
        u1.b = (const char*)(isH ? w1 : wq) + (pj & 15) * 512; u1.bK = 64 * ldb2; u1.bD = 32 * ldb2; u1.vb = brow * ldb2 + bcol;
    } else u1 = u0;
    f32x4 acc[2][2][4][2];
    zero_acc(acc);
    STAMP(1, 0);
    const unsigned cpf = code_pf(tid, 136);
    K1_STAGE(K1_SB(0, 0), u0.b, u0.vb, u0.bD); K1_STAGE(K1_SB(0, 1), u0.b + 256, u0.vb, u0.bD); K1_STAGE(K1_SA(0, 0), u0.a, u0.va, u0.aD); K1_STAGE(K1_SA(0, 1), u0.a + u0.aH, u0.va, u0.aD);
    if (wr == 1) BAR();
    WAIT_V(2); BAR();
    CODE_PF_DONE(cpf);
    K1_STAGE(K1_SB(1, 0), u0.b + u0.bK, u0.vb, u0.bD); K1_STAGE(K1_SA(1, 0), u0.a + u0.aK, u0.va, u0.aD); K1_STAGE(K1_SB(1, 1), u0.b + u0.bK + 256, u0.vb, u0.bD);
    WAIT_V(6); BAR();
    STAMP(1, 1);
    if (isV) kloop<true>(lds, acc, u0, u1, DC / 64, ldsw, aoff0, aoff1, boff0, boff1);
    else     kloop<false>(lds, acc, u0, u1, DC / 64, ldsw, aoff0, aoff1, boff0, boff1);
    STAMP(1, 2);
    if (wr == 0) BAR();
    if (isV) epi_v(acc, VTo, pm, pj & 15, wr, wc, fr, fq); else epi_qk(acc, Ko, pm, pj & 15, 1.0f, wr, wc, fr, fq);
    if (has2) {
        zero_acc(acc);
        STAMP(1, 3);
        if (wr == 1) BAR();
        kloop<false>(lds, acc, u1, u1, DM / 64, ldsw, aoff0, aoff1, boff0, boff1);
        STAMP(1, 4);
        if (wr == 0) BAR();
        if (isH) epi_h(acc, Ho, b1, pm, pj & 15, wr, wc, fr, fq); else epi_qk(acc, Qo, pm, pj & 15, 0.12751743082459868f, wr, wc, fr, fq);
    }
    STAMP(1, 5);
    WAIT_V(0);
    STAMP(1, 6);
}
#undef K1_SA
#undef K1_SB
#undef K1_STAGE
#undef K1_LDA
#undef K1_LDB
#undef K1_MMA
}

namespace k2 {
constexpr int SLOT = 16384, LDS_K = 0, LDS_V = 4 * SLOT, LDS_BYTES = 8 * SLOT;
#define K2_ASM \
    "s_nop 1\n\t" \
    "s_cmp_eq_u32 %[n], 0\n\t" \
    "s_cbranch_scc1 TAIL_%=\n\t" \
    "LOOP_%=:\n\t" \
    "s_cmp_lg_u32 %[dmaw], 0\n\t" \
    "s_cbranch_scc1 DMAL_%=\n\t" \
    "DMABL_%=:\n\t" \
    "s_waitcnt lgkmcnt(0)\n\t" \
    "v_mfma_f32_32x32x16_bf16 v[112:127], v[168:171], v[64:67], v[96:111]\n\t" \
    "ds_read_b128 v[200:203], v235 offset:8192\n\t" \
    "ds_read_b128 v[204:207], v235 offset:8704\n\t" \
    "v_add_f32 v237, v144, v145\n\t" \
    "v_cvt_pk_bf16_f32 v160, v144, v145\n\t" \
    "v_mfma_f32_32x32x16_bf16 v[112:127], v[172:175], v[68:71], v[112:127]\n\t" \
    "ds_read_b128 v[208:211], v235 offset:9216\n\t" \
    "ds_read_b128 v[212:215], v235 offset:9728\n\t" \
    "v_add_f32 v237, v237, v146\n\t" \
    "v_add_f32 v237, v237, v147\n\t" \
    "v_cvt_pk_bf16_f32 v161, v146, v147\n\t" \
    "v_mfma_f32_32x32x16_bf16 v[112:127], v[176:179], v[72:75], v[112:127]\n\t" \
    "ds_read_b128 v[216:219], v235 offset:12288\n\t" \
    "ds_read_b128 v[220:223], v235 offset:12800\n\t" \
    "v_add_f32 v237, v237, v148\n\t" \
    "v_add_f32 v237, v237, v149\n\t" \
    "v_cvt_pk_bf16_f32 v162, v148, v149\n\t" \
    "v_mfma_f32_32x32x16_bf16 v[112:127], v[180:183], v[76:79], v[112:127]\n\t" \
    "ds_read_b128 v[224:227], v235 offset:13312\n\t" \
    "ds_read_b128 v[228:231], v235 offset:13824\n\t" \
    "v_add_f32 v237, v237, v150\n\t" \
    "v_add_f32 v237, v237, v151\n\t" \
    "v_cvt_pk_bf16_f32 v163, v150, v151\n\t" \
    "v_mfma_f32_32x32x16_bf16 v[112:127], v[184:187], v[80:83], v[112:127]\n\t" \
    "v_add_f32 v237, v237, v152\n\t" \
    "v_add_f32 v237, v237, v153\n\t" \
    "v_cvt_pk_bf16_f32 v164, v152, v153\n\t" \
    "v_mfma_f32_32x32x16_bf16 v[112:127], v[188:191], v[84:87], v[112:127]\n\t" \
    "v_add_f32 v237, v237, v154\n\t" \
    "v_add_f32 v237, v237, v155\n\t" \
    "v_cvt_pk_bf16_f32 v165, v154, v155\n\t" \
    "v_mfma_f32_32x32x16_bf16 v[112:127], v[192:195], v[88:91], v[112:127]\n\t" \
    "v_add_f32 v237, v237, v156\n\t" \
    "v_add_f32 v237, v237, v157\n\t" \
    "v_cvt_pk_bf16_f32 v166, v156, v157\n\t" \
    "v_mfma_f32_32x32x16_bf16 v[112:127], v[196:199], v[92:95], v[112:127]\n\t" \
    "v_add_f32 v237, v237, v158\n\t" \
    "v_add_f32 v237, v237, v159\n\t" \
    "v_cvt_pk_bf16_f32 v167, v158, v159\n\t" \
    "v_cmp_lt_f32 vcc, %[thr], v237\n\t" \
    "s_cbranch_vccnz RAREA_%=\n\t" \
    "BACKA_%=:\n\t" \
    "s_waitcnt lgkmcnt(0)\n\t" \
    "v_mfma_f32_32x32x16_bf16 v[0:15], v[200:203], v[160:163], v[0:15]\n\t" \
    "ds_read_b128 v[168:171], v232 offset:512\n\t" \
    "ds_read_b128 v[172:175], v232 offset:2560\n\t" \
    "v_add_f32 v236, v236, v237\n\t" \
    "v_mfma_f32_32x32x16_bf16 v[16:31], v[204:207], v[160:163], v[16:31]\n\t" \
    "ds_read_b128 v[176:179], v232 offset:4608\n\t" \
    "ds_read_b128 v[180:183], v232 offset:6656\n\t" \
    "v_exp_f32 v144, v112\n\t" \
    "v_exp_f32 v145, v113\n\t" \
    "v_mfma_f32_32x32x16_bf16 v[32:47], v[208:211], v[160:163], v[32:47]\n\t" \
    "ds_read_b128 v[184:187], v232 offset:8704\n\t" \
    "ds_read_b128 v[188:191], v232 offset:10752\n\t" \
    "v_exp_f32 v146, v114\n\t" \
    "v_exp_f32 v147, v115\n\t" \
    "v_mfma_f32_32x32x16_bf16 v[48:63], v[212:215], v[160:163], v[48:63]\n\t" \
    "ds_read_b128 v[192:195], v232 offset:12800\n\t" \
    "ds_read_b128 v[196:199], v232 offset:14848\n\t" \
    "v_exp_f32 v148, v116\n\t" \
    "v_exp_f32 v149, v117\n\t" \
    "v_mfma_f32_32x32x16_bf16 v[0:15], v[216:219], v[164:167], v[0:15]\n\t" \
    "v_exp_f32 v150, v118\n\t" \
    "v_exp_f32 v151, v119\n\t" \
    "v_exp_f32 v152, v120\n\t" \
    "v_mfma_f32_32x32x16_bf16 v[16:31], v[220:223], v[164:167], v[16:31]\n\t" \
    "v_exp_f32 v153, v121\n\t" \
    "v_exp_f32 v154, v122\n\t" \
    "v_exp_f32 v155, v123\n\t" \
    "v_mfma_f32_32x32x16_bf16 v[32:47], v[224:227], v[164:167], v[32:47]\n\t" \
    "v_exp_f32 v156, v124\n\t" \
    "v_exp_f32 v157, v125\n\t" \
    "v_mfma_f32_32x32x16_bf16 v[48:63], v[228:231], v[164:167], v[48:63]\n\t" \
    "v_exp_f32 v158, v126\n\t" \
    "v_exp_f32 v159, v127\n\t" \
    "s_waitcnt lgkmcnt(0)\n\t" \
    "v_mfma_f32_32x32x16_bf16 v[128:143], v[168:171], v[64:67], v[96:111]\n\t" \
    "ds_read_b128 v[200:203], v234 offset:0\n\t" \
    "ds_read_b128 v[204:207], v234 offset:512\n\t" \
    "v_add_f32 v237, v144, v145\n\t" \
    "v_cvt_pk_bf16_f32 v160, v144, v145\n\t" \
    "v_mfma_f32_32x32x16_bf16 v[128:143], v[172:175], v[68:71], v[128:143]\n\t" \
    "ds_read_b128 v[208:211], v234 offset:1024\n\t" \
    "ds_read_b128 v[212:215], v234 offset:1536\n\t" \
    "v_add_f32 v237, v237, v146\n\t" \
    "v_add_f32 v237, v237, v147\n\t" \
    "v_cvt_pk_bf16_f32 v161, v146, v147\n\t" \
    "v_mfma_f32_32x32x16_bf16 v[128:143], v[176:179], v[72:75], v[128:143]\n\t" \
    "ds_read_b128 v[216:219], v234 offset:4096\n\t" \
    "ds_read_b128 v[220:223], v234 offset:4608\n\t" \
    "v_add_f32 v237, v237, v148\n\t" \
    "v_add_f32 v237, v237, v149\n\t" \
    "v_cvt_pk_bf16_f32 v162, v148, v149\n\t" \
    "v_mfma_f32_32x32x16_bf16 v[128:143], v[180:183], v[76:79], v[128:143]\n\t" \
    "ds_read_b128 v[224:227], v234 offset:5120\n\t" \
    "ds_read_b128 v[228:231], v234 offset:5632\n\t" \
    "v_add_f32 v237, v237, v150\n\t" \
    "v_add_f32 v237, v237, v151\n\t" \
    "v_cvt_pk_bf16_f32 v163, v150, v151\n\t" \
    "v_mfma_f32_32x32x16_bf16 v[128:143], v[184:187], v[80:83], v[128:143]\n\t" \
    "v_add_f32 v237, v237, v152\n\t" \
    "v_add_f32 v237, v237, v153\n\t" \
    "v_cvt_pk_bf16_f32 v164, v152, v153\n\t" \
    "v_mfma_f32_32x32x16_bf16 v[128:143], v[188:191], v[84:87], v[128:143]\n\t" \
    "v_add_f32 v237, v237, v154\n\t" \
    "v_add_f32 v237, v237, v155\n\t" \
    "v_cvt_pk_bf16_f32 v165, v154, v155\n\t" \
    "v_mfma_f32_32x32x16_bf16 v[128:143], v[192:195], v[88:91], v[128:143]\n\t" \
    "v_add_f32 v237, v237, v156\n\t" \
    "v_add_f32 v237, v237, v157\n\t" \
    "v_cvt_pk_bf16_f32 v166, v156, v157\n\t" \
    "v_mfma_f32_32x32x16_bf16 v[128:143], v[196:199], v[92:95], v[128:143]\n\t" \
    "v_add_f32 v237, v237, v158\n\t" \
    "v_add_f32 v237, v237, v159\n\t" \
    "v_cvt_pk_bf16_f32 v167, v158, v159\n\t" \
    "v_cmp_lt_f32 vcc, %[thr], v237\n\t" \
    "s_cbranch_vccnz RAREB_%=\n\t" \
    "BACKB_%=:\n\t" \
    "s_waitcnt lgkmcnt(0)\n\t" \
    "v_mfma_f32_32x32x16_bf16 v[0:15], v[200:203], v[160:163], v[0:15]\n\t" \
    "ds_read_b128 v[168:171], v233 offset:0\n\t" \
    "ds_read_b128 v[172:175], v233 offset:2048\n\t" \
    "v_add_f32 v236, v236, v237\n\t" \
    "v_mfma_f32_32x32x16_bf16 v[16:31], v[204:207], v[160:163], v[16:31]\n\t" \
    "ds_read_b128 v[176:179], v233 offset:4096\n\t" \
    "ds_read_b128 v[180:183], v233 offset:6144\n\t" \
    "v_exp_f32 v144, v128\n\t" \
    "v_exp_f32 v145, v129\n\t" \
    "v_mfma_f32_32x32x16_bf16 v[32:47], v[208:211], v[160:163], v[32:47]\n\t" \
    "ds_read_b128 v[184:187], v233 offset:8192\n\t" \
    "ds_read_b128 v[188:191], v233 offset:10240\n\t" \
    "v_exp_f32 v146, v130\n\t" \
    "v_exp_f32 v147, v131\n\t" \
    "v_mfma_f32_32x32x16_bf16 v[48:63], v[212:215], v[160:163], v[48:63]\n\t" \
    "ds_read_b128 v[192:195], v233 offset:12288\n\t" \
    "ds_read_b128 v[196:199], v233 offset:14336\n\t" \
    "v_exp_f32 v148, v132\n\t" \
    "v_exp_f32 v149, v133\n\t" \
    "v_mfma_f32_32x32x16_bf16 v[0:15], v[216:219], v[164:167], v[0:15]\n\t" \
    "v_exp_f32 v150, v134\n\t" \
    "v_exp_f32 v151, v135\n\t" \
    "v_exp_f32 v152, v136\n\t" \
    "v_mfma_f32_32x32x16_bf16 v[16:31], v[220:223], v[164:167], v[16:31]\n\t" \
    "v_exp_f32 v153, v137\n\t" \
    "v_exp_f32 v154, v138\n\t" \
    "v_exp_f32 v155, v139\n\t" \
    "s_add_u32 %[soff], %[soff], 0x4000\n\t" \
    "s_and_b32 %[soff], %[soff], 0xffff\n\t" \
    "v_mfma_f32_32x32x16_bf16 v[32:47], v[224:227], v[164:167], v[32:47]\n\t" \
    "v_exp_f32 v156, v140\n\t" \
    "v_exp_f32 v157, v141\n\t" \
    "v_mov_b32 v235, v234\n\t" \
    "v_mov_b32 v232, v233\n\t" \
    "v_mfma_f32_32x32x16_bf16 v[48:63], v[228:231], v[164:167], v[48:63]\n\t" \
    "v_exp_f32 v158, v142\n\t" \
    "v_exp_f32 v159, v143\n\t" \
    "v_add_u32 v234, %[soff], %[vp0]\n\t" \
    "s_add_u32 s60, %[soff], 0x4000\n\t" \
    "s_and_b32 s60, s60, 0xffff\n\t" \
    "v_add_u32 v233, s60, %[kp0]\n\t" \
    "s_waitcnt vmcnt(8)\n\t" \
    "s_barrier\n\t" \
    "s_add_u32 %[t], %[t], 1\n\t" \
    "s_cmp_lt_u32 %[t], %[n]\n\t" \
    "s_cbranch_scc1 LOOP_%=\n\t" \
    "TAIL_%=:\n\t" \
    "s_cmp_lg_u32 %[dmaw], 0\n\t" \
    "s_cbranch_scc1 DMAT_%=\n\t" \
    "DMABT_%=:\n\t" \
    "s_cmp_eq_u32 %[odd], 0\n\t" \
    "s_cbranch_scc1 EVEN_%=\n\t" \
    "s_waitcnt lgkmcnt(0)\n\t" \
    "v_mfma_f32_32x32x16_bf16 v[112:127], v[168:171], v[64:67], v[96:111]\n\t" \
    "ds_read_b128 v[200:203], v235 offset:8192\n\t" \
    "ds_read_b128 v[204:207], v235 offset:8704\n\t" \
    "v_add_f32 v237, v144, v145\n\t" \
    "v_cvt_pk_bf16_f32 v160, v144, v145\n\t" \
    "v_mfma_f32_32x32x16_bf16 v[112:127], v[172:175], v[68:71], v[112:127]\n\t" \
    "ds_read_b128 v[208:211], v235 offset:9216\n\t" \
    "ds_read_b128 v[212:215], v235 offset:9728\n\t" \
    "v_add_f32 v237, v237, v146\n\t" \
    "v_add_f32 v237, v237, v147\n\t" \
    "v_cvt_pk_bf16_f32 v161, v146, v147\n\t" \
    "v_mfma_f32_32x32x16_bf16 v[112:127], v[176:179], v[72:75], v[112:127]\n\t" \
    "ds_read_b128 v[216:219], v235 offset:12288\n\t" \
    "ds_read_b128 v[220:223], v235 offset:12800\n\t" \
    "v_add_f32 v237, v237, v148\n\t" \
    "v_add_f32 v237, v237, v149\n\t" \
    "v_cvt_pk_bf16_f32 v162, v148, v149\n\t" \
    "v_mfma_f32_32x32x16_bf16 v[112:127], v[180:183], v[76:79], v[112:127]\n\t" \
    "ds_read_b128 v[224:227], v235 offset:13312\n\t" \
    "ds_read_b128 v[228:231], v235 offset:13824\n\t" \
    "v_add_f32 v237, v237, v150\n\t" \
    "v_add_f32 v237, v237, v151\n\t" \
    "v_cvt_pk_bf16_f32 v163, v150, v151\n\t" \
    "v_mfma_f32_32x32x16_bf16 v[112:127], v[184:187], v[80:83], v[112:127]\n\t" \
    "v_add_f32 v237, v237, v152\n\t" \
    "v_add_f32 v237, v237, v153\n\t" \
    "v_cvt_pk_bf16_f32 v164, v152, v153\n\t" \
    "v_mfma_f32_32x32x16_bf16 v[112:127], v[188:191], v[84:87], v[112:127]\n\t" \
    "v_add_f32 v237, v237, v154\n\t" \
    "v_add_f32 v237, v237, v155\n\t" \
    "v_cvt_pk_bf16_f32 v165, v154, v155\n\t" \
    "v_mfma_f32_32x32x16_bf16 v[112:127], v[192:195], v[88:91], v[112:127]\n\t" \
    "v_add_f32 v237, v237, v156\n\t" \
    "v_add_f32 v237, v237, v157\n\t" \
    "v_cvt_pk_bf16_f32 v166, v156, v157\n\t" \
    "v_mfma_f32_32x32x16_bf16 v[112:127], v[196:199], v[92:95], v[112:127]\n\t" \
    "v_add_f32 v237, v237, v158\n\t" \
    "v_add_f32 v237, v237, v159\n\t" \
    "v_cvt_pk_bf16_f32 v167, v158, v159\n\t" \
    "v_cmp_lt_f32 vcc, %[thr], v237\n\t" \
    "s_cbranch_vccnz RAREC_%=\n\t" \
    "BACKC_%=:\n\t" \
    "s_waitcnt lgkmcnt(0)\n\t" \
    "v_mfma_f32_32x32x16_bf16 v[0:15], v[200:203], v[160:163], v[0:15]\n\t" \
    "ds_read_b128 v[168:171], v232 offset:512\n\t" \
    "ds_read_b128 v[172:175], v232 offset:2560\n\t" \
    "v_add_f32 v236, v236, v237\n\t" \
    "v_mfma_f32_32x32x16_bf16 v[16:31], v[204:207], v[160:163], v[16:31]\n\t" \
    "ds_read_b128 v[176:179], v232 offset:4608\n\t" \
    "ds_read_b128 v[180:183], v232 offset:6656\n\t" \
    "v_exp_f32 v144, v112\n\t" \
    "v_exp_f32 v145, v113\n\t" \
    "v_mfma_f32_32x32x16_bf16 v[32:47], v[208:211], v[160:163], v[32:47]\n\t" \
    "ds_read_b128 v[184:187], v232 offset:8704\n\t" \
    "ds_read_b128 v[188:191], v232 offset:10752\n\t" \
    "v_exp_f32 v146, v114\n\t" \
    "v_exp_f32 v147, v115\n\t" \
    "v_mfma_f32_32x32x16_bf16 v[48:63], v[212:215], v[160:163], v[48:63]\n\t" \
    "ds_read_b128 v[192:195], v232 offset:12800\n\t" \
    "ds_read_b128 v[196:199], v232 offset:14848\n\t" \
    "v_exp_f32 v148, v116\n\t" \
    "v_exp_f32 v149, v117\n\t" \
    "v_mfma_f32_32x32x16_bf16 v[0:15], v[216:219], v[164:167], v[0:15]\n\t" \
    "v_exp_f32 v150, v118\n\t" \
    "v_exp_f32 v151, v119\n\t" \
    "v_exp_f32 v152, v120\n\t" \
    "v_mfma_f32_32x32x16_bf16 v[16:31], v[220:223], v[164:167], v[16:31]\n\t" \
    "v_exp_f32 v153, v121\n\t" \
    "v_exp_f32 v154, v122\n\t" \
    "v_exp_f32 v155, v123\n\t" \
    "v_mfma_f32_32x32x16_bf16 v[32:47], v[224:227], v[164:167], v[32:47]\n\t" \
    "v_exp_f32 v156, v124\n\t" \
    "v_exp_f32 v157, v125\n\t" \
    "v_mfma_f32_32x32x16_bf16 v[48:63], v[228:231], v[164:167], v[48:63]\n\t" \
    "v_exp_f32 v158, v126\n\t" \
    "v_exp_f32 v159, v127\n\t" \
    "v_mov_b32 v239, 0xff800000\n\t" \
    "v_cmp_gt_i32 vcc, 0, %[dq]\n\t" \
    "s_nop 1\n\t" \
    "v_cndmask_b32 v96, v96, v239, vcc\n\t" \
    "v_cmp_gt_i32 vcc, 1, %[dq]\n\t" \
    "s_nop 1\n\t" \
    "v_cndmask_b32 v97, v97, v239, vcc\n\t" \
    "v_cmp_gt_i32 vcc, 2, %[dq]\n\t" \
    "s_nop 1\n\t" \
    "v_cndmask_b32 v98, v98, v239, vcc\n\t" \
    "v_cmp_gt_i32 vcc, 3, %[dq]\n\t" \
    "s_nop 1\n\t" \
    "v_cndmask_b32 v99, v99, v239, vcc\n\t" \
    "v_cmp_gt_i32 vcc, 8, %[dq]\n\t" \
    "s_nop 1\n\t" \
    "v_cndmask_b32 v100, v100, v239, vcc\n\t" \
    "v_cmp_gt_i32 vcc, 9, %[dq]\n\t" \
    "s_nop 1\n\t" \
    "v_cndmask_b32 v101, v101, v239, vcc\n\t" \
    "v_cmp_gt_i32 vcc, 10, %[dq]\n\t" \
    "s_nop 1\n\t" \
    "v_cndmask_b32 v102, v102, v239, vcc\n\t" \
    "v_cmp_gt_i32 vcc, 11, %[dq]\n\t" \
    "s_nop 1\n\t" \
    "v_cndmask_b32 v103, v103, v239, vcc\n\t" \
    "v_cmp_gt_i32 vcc, 16, %[dq]\n\t" \
    "s_nop 1\n\t" \
    "v_cndmask_b32 v104, v104, v239, vcc\n\t" \
    "v_cmp_gt_i32 vcc, 17, %[dq]\n\t" \
    "s_nop 1\n\t" \
    "v_cndmask_b32 v105, v105, v239, vcc\n\t" \
    "v_cmp_gt_i32 vcc, 18, %[dq]\n\t" \
    "s_nop 1\n\t" \
    "v_cndmask_b32 v106, v106, v239, vcc\n\t" \
    "v_cmp_gt_i32 vcc, 19, %[dq]\n\t" \
    "s_nop 1\n\t" \
    "v_cndmask_b32 v107, v107, v239, vcc\n\t" \
    "v_cmp_gt_i32 vcc, 24, %[dq]\n\t" \
    "s_nop 1\n\t" \
    "v_cndmask_b32 v108, v108, v239, vcc\n\t" \
    "v_cmp_gt_i32 vcc, 25, %[dq]\n\t" \
    "s_nop 1\n\t" \
    "v_cndmask_b32 v109, v109, v239, vcc\n\t" \
    "v_cmp_gt_i32 vcc, 26, %[dq]\n\t" \
    "s_nop 1\n\t" \
    "v_cndmask_b32 v110, v110, v239, vcc\n\t" \
    "v_cmp_gt_i32 vcc, 27, %[dq]\n\t" \
    "s_nop 1\n\t" \
    "v_cndmask_b32 v111, v111, v239, vcc\n\t" \
    "s_nop 1\n\t" \
    "s_waitcnt lgkmcnt(0)\n\t" \
    "v_mfma_f32_32x32x16_bf16 v[128:143], v[168:171], v[64:67], v[96:111]\n\t" \
    "ds_read_b128 v[200:203], v234 offset:0\n\t" \
    "ds_read_b128 v[204:207], v234 offset:512\n\t" \
    "v_add_f32 v237, v144, v145\n\t" \
    "v_cvt_pk_bf16_f32 v160, v144, v145\n\t" \
    "v_mfma_f32_32x32x16_bf16 v[128:143], v[172:175], v[68:71], v[128:143]\n\t" \
    "ds_read_b128 v[208:211], v234 offset:1024\n\t" \
    "ds_read_b128 v[212:215], v234 offset:1536\n\t" \
    "v_add_f32 v237, v237, v146\n\t" \
    "v_add_f32 v237, v237, v147\n\t" \
    "v_cvt_pk_bf16_f32 v161, v146, v147\n\t" \
    "v_mfma_f32_32x32x16_bf16 v[128:143], v[176:179], v[72:75], v[128:143]\n\t" \
    "ds_read_b128 v[216:219], v234 offset:4096\n\t" \
    "ds_read_b128 v[220:223], v234 offset:4608\n\t" \
    "v_add_f32 v237, v237, v148\n\t" \
    "v_add_f32 v237, v237, v149\n\t" \
    "v_cvt_pk_bf16_f32 v162, v148, v149\n\t" \
    "v_mfma_f32_32x32x16_bf16 v[128:143], v[180:183], v[76:79], v[128:143]\n\t" \
    "ds_read_b128 v[224:227], v234 offset:5120\n\t" \
    "ds_read_b128 v[228:231], v234 offset:5632\n\t" \
    "v_add_f32 v237, v237, v150\n\t" \
    "v_add_f32 v237, v237, v151\n\t" \
    "v_cvt_pk_bf16_f32 v163, v150, v151\n\t" \
    "v_mfma_f32_32x32x16_bf16 v[128:143], v[184:187], v[80:83], v[128:143]\n\t" \
    "v_add_f32 v237, v237, v152\n\t" \
    "v_add_f32 v237, v237, v153\n\t" \
    "v_cvt_pk_bf16_f32 v164, v152, v153\n\t" \
    "v_mfma_f32_32x32x16_bf16 v[128:143], v[188:191], v[84:87], v[128:143]\n\t" \
    "v_add_f32 v237, v237, v154\n\t" \
    "v_add_f32 v237, v237, v155\n\t" \
    "v_cvt_pk_bf16_f32 v165, v154, v155\n\t" \
    "v_mfma_f32_32x32x16_bf16 v[128:143], v[192:195], v[88:91], v[128:143]\n\t" \
    "v_add_f32 v237, v237, v156\n\t" \
    "v_add_f32 v237, v237, v157\n\t" \
    "v_cvt_pk_bf16_f32 v166, v156, v157\n\t" \
    "v_mfma_f32_32x32x16_bf16 v[128:143], v[196:199], v[92:95], v[128:143]\n\t" \
    "v_add_f32 v237, v237, v158\n\t" \
    "v_add_f32 v237, v237, v159\n\t" \
    "v_cvt_pk_bf16_f32 v167, v158, v159\n\t" \
    "v_cmp_lt_f32 vcc, %[thr], v237\n\t" \
    "s_cbranch_vccnz RARED_%=\n\t" \
    "BACKD_%=:\n\t" \
    "s_waitcnt lgkmcnt(0)\n\t" \
    "v_mfma_f32_32x32x16_bf16 v[0:15], v[200:203], v[160:163], v[0:15]\n\t" \
    "ds_read_b128 v[168:171], v233 offset:0\n\t" \
    "ds_read_b128 v[172:175], v233 offset:2048\n\t" \
    "v_add_f32 v236, v236, v237\n\t" \
    "v_mfma_f32_32x32x16_bf16 v[16:31], v[204:207], v[160:163], v[16:31]\n\t" \
    "ds_read_b128 v[176:179], v233 offset:4096\n\t" \
    "ds_read_b128 v[180:183], v233 offset:6144\n\t" \
    "v_exp_f32 v144, v128\n\t" \
    "v_exp_f32 v145, v129\n\t" \
    "v_mfma_f32_32x32x16_bf16 v[32:47], v[208:211], v[160:163], v[32:47]\n\t" \
    "ds_read_b128 v[184:187], v233 offset:8192\n\t" \
    "ds_read_b128 v[188:191], v233 offset:10240\n\t" \
    "v_exp_f32 v146, v130\n\t" \
    "v_exp_f32 v147, v131\n\t" \
    "v_mfma_f32_32x32x16_bf16 v[48:63], v[212:215], v[160:163], v[48:63]\n\t" \
    "ds_read_b128 v[192:195], v233 offset:12288\n\t" \
    "ds_read_b128 v[196:199], v233 offset:14336\n\t" \
    "v_exp_f32 v148, v132\n\t" \
    "v_exp_f32 v149, v133\n\t" \
    "v_mfma_f32_32x32x16_bf16 v[0:15], v[216:219], v[164:167], v[0:15]\n\t" \
    "v_exp_f32 v150, v134\n\t" \
    "v_exp_f32 v151, v135\n\t" \
    "v_exp_f32 v152, v136\n\t" \
    "v_mfma_f32_32x32x16_bf16 v[16:31], v[220:223], v[164:167], v[16:31]\n\t" \
    "v_exp_f32 v153, v137\n\t" \
    "v_exp_f32 v154, v138\n\t" \
    "v_exp_f32 v155, v139\n\t" \
    "v_mfma_f32_32x32x16_bf16 v[32:47], v[224:227], v[164:167], v[32:47]\n\t" \
    "v_exp_f32 v156, v140\n\t" \
    "v_exp_f32 v157, v141\n\t" \
    "v_mfma_f32_32x32x16_bf16 v[48:63], v[228:231], v[164:167], v[48:63]\n\t" \
    "v_exp_f32 v158, v142\n\t" \
    "v_exp_f32 v159, v143\n\t" \
    "s_waitcnt lgkmcnt(0)\n\t" \
    "ds_read_b128 v[200:203], v234 offset:8192\n\t" \
    "ds_read_b128 v[204:207], v234 offset:8704\n\t" \
    "ds_read_b128 v[208:211], v234 offset:9216\n\t" \
    "ds_read_b128 v[212:215], v234 offset:9728\n\t" \
    "ds_read_b128 v[216:219], v234 offset:12288\n\t" \
    "ds_read_b128 v[220:223], v234 offset:12800\n\t" \
    "ds_read_b128 v[224:227], v234 offset:13312\n\t" \
    "ds_read_b128 v[228:231], v234 offset:13824\n\t" \
    "v_add_f32 v237, v144, v145\n\t" \
    "v_cvt_pk_bf16_f32 v160, v144, v145\n\t" \
    "v_add_f32 v237, v237, v146\n\t" \
    "v_add_f32 v237, v237, v147\n\t" \
    "v_cvt_pk_bf16_f32 v161, v146, v147\n\t" \
    "v_add_f32 v237, v237, v148\n\t" \
    "v_add_f32 v237, v237, v149\n\t" \
    "v_cvt_pk_bf16_f32 v162, v148, v149\n\t" \
    "v_add_f32 v237, v237, v150\n\t" \
    "v_add_f32 v237, v237, v151\n\t" \
    "v_cvt_pk_bf16_f32 v163, v150, v151\n\t" \
    "v_add_f32 v237, v237, v152\n\t" \
    "v_add_f32 v237, v237, v153\n\t" \
    "v_cvt_pk_bf16_f32 v164, v152, v153\n\t" \
    "v_add_f32 v237, v237, v154\n\t" \
    "v_add_f32 v237, v237, v155\n\t" \
    "v_cvt_pk_bf16_f32 v165, v154, v155\n\t" \
    "v_add_f32 v237, v237, v156\n\t" \
    "v_add_f32 v237, v237, v157\n\t" \
    "v_cvt_pk_bf16_f32 v166, v156, v157\n\t" \
    "v_add_f32 v237, v237, v158\n\t" \
    "v_add_f32 v237, v237, v159\n\t" \
    "v_cvt_pk_bf16_f32 v167, v158, v159\n\t" \
    "v_cmp_lt_f32 vcc, %[thr], v237\n\t" \
    "s_cbranch_vccnz RAREE_%=\n\t" \
    "BACKE_%=:\n\t" \
    "s_waitcnt lgkmcnt(0)\n\t" \
    "v_add_f32 v236, v236, v237\n\t" \
    "v_mfma_f32_32x32x16_bf16 v[0:15], v[200:203], v[160:163], v[0:15]\n\t" \
    "v_mfma_f32_32x32x16_bf16 v[16:31], v[204:207], v[160:163], v[16:31]\n\t" \
    "v_mfma_f32_32x32x16_bf16 v[32:47], v[208:211], v[160:163], v[32:47]\n\t" \
    "v_mfma_f32_32x32x16_bf16 v[48:63], v[212:215], v[160:163], v[48:63]\n\t" \
    "v_mfma_f32_32x32x16_bf16 v[0:15], v[216:219], v[164:167], v[0:15]\n\t" \
    "v_mfma_f32_32x32x16_bf16 v[16:31], v[220:223], v[164:167], v[16:31]\n\t" \
    "v_mfma_f32_32x32x16_bf16 v[32:47], v[224:227], v[164:167], v[32:47]\n\t" \
    "v_mfma_f32_32x32x16_bf16 v[48:63], v[228:231], v[164:167], v[48:63]\n\t" \
    "s_branch END_%=\n\t" \
    "EVEN_%=:\n\t" \
    "v_mov_b32 v239, 0xff800000\n\t" \
    "v_cmp_gt_i32 vcc, 0, %[dq]\n\t" \
    "s_nop 1\n\t" \
    "v_cndmask_b32 v96, v96, v239, vcc\n\t" \
    "v_cmp_gt_i32 vcc, 1, %[dq]\n\t" \
    "s_nop 1\n\t" \
    "v_cndmask_b32 v97, v97, v239, vcc\n\t" \
    "v_cmp_gt_i32 vcc, 2, %[dq]\n\t" \
    "s_nop 1\n\t" \
    "v_cndmask_b32 v98, v98, v239, vcc\n\t" \
    "v_cmp_gt_i32 vcc, 3, %[dq]\n\t" \
    "s_nop 1\n\t" \
    "v_cndmask_b32 v99, v99, v239, vcc\n\t" \
    "v_cmp_gt_i32 vcc, 8, %[dq]\n\t" \
    "s_nop 1\n\t" \
    "v_cndmask_b32 v100, v100, v239, vcc\n\t" \
    "v_cmp_gt_i32 vcc, 9, %[dq]\n\t" \
    "s_nop 1\n\t" \
    "v_cndmask_b32 v101, v101, v239, vcc\n\t" \
    "v_cmp_gt_i32 vcc, 10, %[dq]\n\t" \
    "s_nop 1\n\t" \
    "v_cndmask_b32 v102, v102, v239, vcc\n\t" \
    "v_cmp_gt_i32 vcc, 11, %[dq]\n\t" \
    "s_nop 1\n\t" \
    "v_cndmask_b32 v103, v103, v239, vcc\n\t" \
    "v_cmp_gt_i32 vcc, 16, %[dq]\n\t" \
    "s_nop 1\n\t" \
    "v_cndmask_b32 v104, v104, v239, vcc\n\t" \
    "v_cmp_gt_i32 vcc, 17, %[dq]\n\t" \
    "s_nop 1\n\t" \
    "v_cndmask_b32 v105, v105, v239, vcc\n\t" \
    "v_cmp_gt_i32 vcc, 18, %[dq]\n\t" \
    "s_nop 1\n\t" \
    "v_cndmask_b32 v106, v106, v239, vcc\n\t" \
    "v_cmp_gt_i32 vcc, 19, %[dq]\n\t" \
    "s_nop 1\n\t" \
    "v_cndmask_b32 v107, v107, v239, vcc\n\t" \
    "v_cmp_gt_i32 vcc, 24, %[dq]\n\t" \
    "s_nop 1\n\t" \
    "v_cndmask_b32 v108, v108, v239, vcc\n\t" \
    "v_cmp_gt_i32 vcc, 25, %[dq]\n\t" \
    "s_nop 1\n\t" \
    "v_cndmask_b32 v109, v109, v239, vcc\n\t" \
    "v_cmp_gt_i32 vcc, 26, %[dq]\n\t" \
    "s_nop 1\n\t" \
    "v_cndmask_b32 v110, v110, v239, vcc\n\t" \
    "v_cmp_gt_i32 vcc, 27, %[dq]\n\t" \
    "s_nop 1\n\t" \
    "v_cndmask_b32 v111, v111, v239, vcc\n\t" \
    "s_nop 1\n\t" \
    "s_waitcnt lgkmcnt(0)\n\t" \
    "v_mfma_f32_32x32x16_bf16 v[112:127], v[168:171], v[64:67], v[96:111]\n\t" \
    "ds_read_b128 v[200:203], v235 offset:8192\n\t" \
    "ds_read_b128 v[204:207], v235 offset:8704\n\t" \
    "v_add_f32 v237, v144, v145\n\t" \
    "v_cvt_pk_bf16_f32 v160, v144, v145\n\t" \
    "v_mfma_f32_32x32x16_bf16 v[112:127], v[172:175], v[68:71], v[112:127]\n\t" \
    "ds_read_b128 v[208:211], v235 offset:9216\n\t" \
    "ds_read_b128 v[212:215], v235 offset:9728\n\t" \
    "v_add_f32 v237, v237, v146\n\t" \
    "v_add_f32 v237, v237, v147\n\t" \
    "v_cvt_pk_bf16_f32 v161, v146, v147\n\t" \
    "v_mfma_f32_32x32x16_bf16 v[112:127], v[176:179], v[72:75], v[112:127]\n\t" \
    "ds_read_b128 v[216:219], v235 offset:12288\n\t" \
    "ds_read_b128 v[220:223], v235 offset:12800\n\t" \
    "v_add_f32 v237, v237, v148\n\t" \
    "v_add_f32 v237, v237, v149\n\t" \
    "v_cvt_pk_bf16_f32 v162, v148, v149\n\t" \
    "v_mfma_f32_32x32x16_bf16 v[112:127], v[180:183], v[76:79], v[112:127]\n\t" \
    "ds_read_b128 v[224:227], v235 offset:13312\n\t" \
    "ds_read_b128 v[228:231], v235 offset:13824\n\t" \
    "v_add_f32 v237, v237, v150\n\t" \
    "v_add_f32 v237, v237, v151\n\t" \
    "v_cvt_pk_bf16_f32 v163, v150, v151\n\t" \
    "v_mfma_f32_32x32x16_bf16 v[112:127], v[184:187], v[80:83], v[112:127]\n\t" \
    "v_add_f32 v237, v237, v152\n\t" \
    "v_add_f32 v237, v237, v153\n\t" \
    "v_cvt_pk_bf16_f32 v164, v152, v153\n\t" \
    "v_mfma_f32_32x32x16_bf16 v[112:127], v[188:191], v[84:87], v[112:127]\n\t" \
    "v_add_f32 v237, v237, v154\n\t" \
    "v_add_f32 v237, v237, v155\n\t" \
    "v_cvt_pk_bf16_f32 v165, v154, v155\n\t" \
    "v_mfma_f32_32x32x16_bf16 v[112:127], v[192:195], v[88:91], v[112:127]\n\t" \
    "v_add_f32 v237, v237, v156\n\t" \
    "v_add_f32 v237, v237, v157\n\t" \
    "v_cvt_pk_bf16_f32 v166, v156, v157\n\t" \
    "v_mfma_f32_32x32x16_bf16 v[112:127], v[196:199], v[92:95], v[112:127]\n\t" \
    "v_add_f32 v237, v237, v158\n\t" \
    "v_add_f32 v237, v237, v159\n\t" \
    "v_cvt_pk_bf16_f32 v167, v158, v159\n\t" \
    "v_cmp_lt_f32 vcc, %[thr], v237\n\t" \
    "s_cbranch_vccnz RAREF_%=\n\t" \
    "BACKF_%=:\n\t" \
    "s_waitcnt lgkmcnt(0)\n\t" \
    "v_mfma_f32_32x32x16_bf16 v[0:15], v[200:203], v[160:163], v[0:15]\n\t" \
    "ds_read_b128 v[168:171], v232 offset:512\n\t" \
    "ds_read_b128 v[172:175], v232 offset:2560\n\t" \
    "v_add_f32 v236, v236, v237\n\t" \
    "v_mfma_f32_32x32x16_bf16 v[16:31], v[204:207], v[160:163], v[16:31]\n\t" \
    "ds_read_b128 v[176:179], v232 offset:4608\n\t" \
    "ds_read_b128 v[180:183], v232 offset:6656\n\t" \
    "v_exp_f32 v144, v112\n\t" \
    "v_exp_f32 v145, v113\n\t" \
    "v_mfma_f32_32x32x16_bf16 v[32:47], v[208:211], v[160:163], v[32:47]\n\t" \
    "ds_read_b128 v[184:187], v232 offset:8704\n\t" \
    "ds_read_b128 v[188:191], v232 offset:10752\n\t" \
    "v_exp_f32 v146, v114\n\t" \
    "v_exp_f32 v147, v115\n\t" \
    "v_mfma_f32_32x32x16_bf16 v[48:63], v[212:215], v[160:163], v[48:63]\n\t" \
    "ds_read_b128 v[192:195], v232 offset:12800\n\t" \
    "ds_read_b128 v[196:199], v232 offset:14848\n\t" \
    "v_exp_f32 v148, v116\n\t" \
    "v_exp_f32 v149, v117\n\t" \
    "v_mfma_f32_32x32x16_bf16 v[0:15], v[216:219], v[164:167], v[0:15]\n\t" \
    "v_exp_f32 v150, v118\n\t" \
    "v_exp_f32 v151, v119\n\t" \
    "v_exp_f32 v152, v120\n\t" \
    "v_mfma_f32_32x32x16_bf16 v[16:31], v[220:223], v[164:167], v[16:31]\n\t" \
    "v_exp_f32 v153, v121\n\t" \
    "v_exp_f32 v154, v122\n\t" \
    "v_exp_f32 v155, v123\n\t" \
    "v_mfma_f32_32x32x16_bf16 v[32:47], v[224:227], v[164:167], v[32:47]\n\t" \
    "v_exp_f32 v156, v124\n\t" \
    "v_exp_f32 v157, v125\n\t" \
    "v_mfma_f32_32x32x16_bf16 v[48:63], v[228:231], v[164:167], v[48:63]\n\t" \
    "v_exp_f32 v158, v126\n\t" \
    "v_exp_f32 v159, v127\n\t" \
    "s_waitcnt lgkmcnt(0)\n\t" \
    "ds_read_b128 v[200:203], v234 offset:0\n\t" \
    "ds_read_b128 v[204:207], v234 offset:512\n\t" \
    "ds_read_b128 v[208:211], v234 offset:1024\n\t" \
    "ds_read_b128 v[212:215], v234 offset:1536\n\t" \
    "ds_read_b128 v[216:219], v234 offset:4096\n\t" \
    "ds_read_b128 v[220:223], v234 offset:4608\n\t" \
    "ds_read_b128 v[224:227], v234 offset:5120\n\t" \
    "ds_read_b128 v[228:231], v234 offset:5632\n\t" \
    "v_add_f32 v237, v144, v145\n\t" \
    "v_cvt_pk_bf16_f32 v160, v144, v145\n\t" \
    "v_add_f32 v237, v237, v146\n\t" \
    "v_add_f32 v237, v237, v147\n\t" \
    "v_cvt_pk_bf16_f32 v161, v146, v147\n\t" \
    "v_add_f32 v237, v237, v148\n\t" \
    "v_add_f32 v237, v237, v149\n\t" \
    "v_cvt_pk_bf16_f32 v162, v148, v149\n\t" \
    "v_add_f32 v237, v237, v150\n\t" \
    "v_add_f32 v237, v237, v151\n\t" \
    "v_cvt_pk_bf16_f32 v163, v150, v151\n\t" \
    "v_add_f32 v237, v237, v152\n\t" \
    "v_add_f32 v237, v237, v153\n\t" \
    "v_cvt_pk_bf16_f32 v164, v152, v153\n\t" \
    "v_add_f32 v237, v237, v154\n\t" \
    "v_add_f32 v237, v237, v155\n\t" \
    "v_cvt_pk_bf16_f32 v165, v154, v155\n\t" \
    "v_add_f32 v237, v237, v156\n\t" \
    "v_add_f32 v237, v237, v157\n\t" \
    "v_cvt_pk_bf16_f32 v166, v156, v157\n\t" \
    "v_add_f32 v237, v237, v158\n\t" \
    "v_add_f32 v237, v237, v159\n\t" \
    "v_cvt_pk_bf16_f32 v167, v158, v159\n\t" \
    "v_cmp_lt_f32 vcc, %[thr], v237\n\t" \
    "s_cbranch_vccnz RAREG_%=\n\t" \
    "BACKG_%=:\n\t" \
    "s_waitcnt lgkmcnt(0)\n\t" \
    "v_add_f32 v236, v236, v237\n\t" \
    "v_mfma_f32_32x32x16_bf16 v[0:15], v[200:203], v[160:163], v[0:15]\n\t" \
    "v_mfma_f32_32x32x16_bf16 v[16:31], v[204:207], v[160:163], v[16:31]\n\t" \
    "v_mfma_f32_32x32x16_bf16 v[32:47], v[208:211], v[160:163], v[32:47]\n\t" \
    "v_mfma_f32_32x32x16_bf16 v[48:63], v[212:215], v[160:163], v[48:63]\n\t" \
    "v_mfma_f32_32x32x16_bf16 v[0:15], v[216:219], v[164:167], v[0:15]\n\t" \
    "v_mfma_f32_32x32x16_bf16 v[16:31], v[220:223], v[164:167], v[16:31]\n\t" \
    "v_mfma_f32_32x32x16_bf16 v[32:47], v[224:227], v[164:167], v[32:47]\n\t" \
    "v_mfma_f32_32x32x16_bf16 v[48:63], v[228:231], v[164:167], v[48:63]\n\t" \
    "s_branch END_%=\n\t" \
    "DMAL_%=:\n\t" \
    "s_add_u32 s60, %[t], 3\n\t" \
    "s_min_u32 s60, s60, %[ntm1]\n\t" \
    "s_lshl_b32 s60, s60, 14\n\t" \
    "s_add_u32 s62, %[khl], s60\n\t" \
    "s_addc_u32 s63, %[khh], 0\n\t" \
    "s_add_u32 s61, %[soff], 0xc000\n\t" \
    "s_and_b32 s61, s61, 0xffff\n\t" \
    "s_add_u32 m0, s61, %[kdst]\n\t" \
    "s_nop 0\n\t" \
    "global_load_lds_dwordx4 %[dvo], s[62:63] offset:0\n\t" \
    "global_load_lds_dwordx4 %[dvo], s[62:63] offset:1024\n\t" \
    "global_load_lds_dwordx4 %[dvo], s[62:63] offset:2048\n\t" \
    "global_load_lds_dwordx4 %[dvo], s[62:63] offset:3072\n\t" \
    "s_add_u32 s60, %[t], 2\n\t" \
    "s_min_u32 s60, s60, %[ntm1]\n\t" \
    "s_lshl_b32 s60, s60, 14\n\t" \
    "s_add_u32 s62, %[vhl], s60\n\t" \
    "s_addc_u32 s63, %[vhh], 0\n\t" \
    "s_add_u32 s61, %[soff], 0x8000\n\t" \
    "s_and_b32 s61, s61, 0xffff\n\t" \
    "s_add_u32 m0, s61, %[vdst]\n\t" \
    "s_nop 0\n\t" \
    "global_load_lds_dwordx4 %[dvo], s[62:63] offset:0\n\t" \
    "global_load_lds_dwordx4 %[dvo], s[62:63] offset:1024\n\t" \
    "global_load_lds_dwordx4 %[dvo], s[62:63] offset:2048\n\t" \
    "global_load_lds_dwordx4 %[dvo], s[62:63] offset:3072\n\t" \
    "s_branch DMABL_%=\n\t" \
    "DMAT_%=:\n\t" \
    "s_add_u32 s60, %[t], 3\n\t" \
    "s_min_u32 s60, s60, %[ntm1]\n\t" \
    "s_lshl_b32 s60, s60, 14\n\t" \
    "s_add_u32 s62, %[khl], s60\n\t" \
    "s_addc_u32 s63, %[khh], 0\n\t" \
    "s_add_u32 s61, %[soff], 0xc000\n\t" \
    "s_and_b32 s61, s61, 0xffff\n\t" \
    "s_add_u32 m0, s61, %[kdst]\n\t" \
    "s_nop 0\n\t" \
    "global_load_lds_dwordx4 %[dvo], s[62:63] offset:0\n\t" \
    "global_load_lds_dwordx4 %[dvo], s[62:63] offset:1024\n\t" \
    "global_load_lds_dwordx4 %[dvo], s[62:63] offset:2048\n\t" \
    "global_load_lds_dwordx4 %[dvo], s[62:63] offset:3072\n\t" \
    "s_add_u32 s60, %[t], 2\n\t" \
    "s_min_u32 s60, s60, %[ntm1]\n\t" \
    "s_lshl_b32 s60, s60, 14\n\t" \
    "s_add_u32 s62, %[vhl], s60\n\t" \
    "s_addc_u32 s63, %[vhh], 0\n\t" \
    "s_add_u32 s61, %[soff], 0x8000\n\t" \
    "s_and_b32 s61, s61, 0xffff\n\t" \
    "s_add_u32 m0, s61, %[vdst]\n\t" \
    "s_nop 0\n\t" \
    "global_load_lds_dwordx4 %[dvo], s[62:63] offset:0\n\t" \
    "global_load_lds_dwordx4 %[dvo], s[62:63] offset:1024\n\t" \
    "global_load_lds_dwordx4 %[dvo], s[62:63] offset:2048\n\t" \
    "global_load_lds_dwordx4 %[dvo], s[62:63] offset:3072\n\t" \
    "s_branch DMABT_%=\n\t" \
    "RAREA_%=:\n\t" \
    "s_nop 15\n\t" \
    "v_max3_f32 v239, v128, v129, v130\n\t" \
    "v_max3_f32 v239, v239, v131, v132\n\t" \
    "v_max3_f32 v239, v239, v133, v134\n\t" \
    "v_max3_f32 v239, v239, v135, v136\n\t" \
    "v_max3_f32 v239, v239, v137, v138\n\t" \
    "v_max3_f32 v239, v239, v139, v140\n\t" \
    "v_max3_f32 v239, v239, v141, v142\n\t" \
    "v_max_f32 v239, v239, v143\n\t" \
    "v_max_f32 v239, 0, v239\n\t" \
    "v_mov_b32 v240, v239\n\t" \
    "s_nop 1\n\t" \
    "v_permlane32_swap_b32 v239, v240\n\t" \
    "s_nop 1\n\t" \
    "v_max_f32 v239, v239, v240\n\t" \
    "v_add_f32 v238, v238, v239\n\t" \
    "v_exp_f32 v240, -v239\n\t" \
    "v_sub_f32 v241, 0, v238\n\t" \
    "v_sub_f32 v128, v128, v239\n\t" \
    "v_sub_f32 v112, v112, v239\n\t" \
    "v_mov_b32 v96, v241\n\t" \
    "v_sub_f32 v129, v129, v239\n\t" \
    "v_sub_f32 v113, v113, v239\n\t" \
    "v_mov_b32 v97, v241\n\t" \
    "v_sub_f32 v130, v130, v239\n\t" \
    "v_sub_f32 v114, v114, v239\n\t" \
    "v_mov_b32 v98, v241\n\t" \
    "v_sub_f32 v131, v131, v239\n\t" \
    "v_sub_f32 v115, v115, v239\n\t" \
    "v_mov_b32 v99, v241\n\t" \
    "v_sub_f32 v132, v132, v239\n\t" \
    "v_sub_f32 v116, v116, v239\n\t" \
    "v_mov_b32 v100, v241\n\t" \
    "v_sub_f32 v133, v133, v239\n\t" \
    "v_sub_f32 v117, v117, v239\n\t" \
    "v_mov_b32 v101, v241\n\t" \
    "v_sub_f32 v134, v134, v239\n\t" \
    "v_sub_f32 v118, v118, v239\n\t" \
    "v_mov_b32 v102, v241\n\t" \
    "v_sub_f32 v135, v135, v239\n\t" \
    "v_sub_f32 v119, v119, v239\n\t" \
    "v_mov_b32 v103, v241\n\t" \
    "v_sub_f32 v136, v136, v239\n\t" \
    "v_sub_f32 v120, v120, v239\n\t" \
    "v_mov_b32 v104, v241\n\t" \
    "v_sub_f32 v137, v137, v239\n\t" \
    "v_sub_f32 v121, v121, v239\n\t" \
    "v_mov_b32 v105, v241\n\t" \
    "v_sub_f32 v138, v138, v239\n\t" \
    "v_sub_f32 v122, v122, v239\n\t" \
    "v_mov_b32 v106, v241\n\t" \
    "v_sub_f32 v139, v139, v239\n\t" \
    "v_sub_f32 v123, v123, v239\n\t" \
    "v_mov_b32 v107, v241\n\t" \
    "v_sub_f32 v140, v140, v239\n\t" \
    "v_sub_f32 v124, v124, v239\n\t" \
    "v_mov_b32 v108, v241\n\t" \
    "v_sub_f32 v141, v141, v239\n\t" \
    "v_sub_f32 v125, v125, v239\n\t" \
    "v_mov_b32 v109, v241\n\t" \
    "v_sub_f32 v142, v142, v239\n\t" \
    "v_sub_f32 v126, v126, v239\n\t" \
    "v_mov_b32 v110, v241\n\t" \
    "v_sub_f32 v143, v143, v239\n\t" \
    "v_sub_f32 v127, v127, v239\n\t" \
    "v_mov_b32 v111, v241\n\t" \
    "v_exp_f32 v144, v128\n\t" \
    "v_exp_f32 v145, v129\n\t" \
    "v_exp_f32 v146, v130\n\t" \
    "v_exp_f32 v147, v131\n\t" \
    "v_exp_f32 v148, v132\n\t" \
    "v_exp_f32 v149, v133\n\t" \
    "v_exp_f32 v150, v134\n\t" \
    "v_exp_f32 v151, v135\n\t" \
    "v_exp_f32 v152, v136\n\t" \
    "v_exp_f32 v153, v137\n\t" \
    "v_exp_f32 v154, v138\n\t" \
    "v_exp_f32 v155, v139\n\t" \
    "v_exp_f32 v156, v140\n\t" \
    "v_exp_f32 v157, v141\n\t" \
    "v_exp_f32 v158, v142\n\t" \
    "v_exp_f32 v159, v143\n\t" \
    "v_mul_f32 v236, v236, v240\n\t" \
    "v_mul_f32 v0, v0, v240\n\t" \
    "v_mul_f32 v1, v1, v240\n\t" \
    "v_mul_f32 v2, v2, v240\n\t" \
    "v_mul_f32 v3, v3, v240\n\t" \
    "v_mul_f32 v4, v4, v240\n\t" \
    "v_mul_f32 v5, v5, v240\n\t" \
    "v_mul_f32 v6, v6, v240\n\t" \
    "v_mul_f32 v7, v7, v240\n\t" \
    "v_mul_f32 v8, v8, v240\n\t" \
    "v_mul_f32 v9, v9, v240\n\t" \
    "v_mul_f32 v10, v10, v240\n\t" \
    "v_mul_f32 v11, v11, v240\n\t" \
    "v_mul_f32 v12, v12, v240\n\t" \
    "v_mul_f32 v13, v13, v240\n\t" \
    "v_mul_f32 v14, v14, v240\n\t" \
    "v_mul_f32 v15, v15, v240\n\t" \
    "v_mul_f32 v16, v16, v240\n\t" \
    "v_mul_f32 v17, v17, v240\n\t" \
    "v_mul_f32 v18, v18, v240\n\t" \
    "v_mul_f32 v19, v19, v240\n\t" \
    "v_mul_f32 v20, v20, v240\n\t" \
    "v_mul_f32 v21, v21, v240\n\t" \
    "v_mul_f32 v22, v22, v240\n\t" \
    "v_mul_f32 v23, v23, v240\n\t" \
    "v_mul_f32 v24, v24, v240\n\t" \
    "v_mul_f32 v25, v25, v240\n\t" \
    "v_mul_f32 v26, v26, v240\n\t" \
    "v_mul_f32 v27, v27, v240\n\t" \
    "v_mul_f32 v28, v28, v240\n\t" \
    "v_mul_f32 v29, v29, v240\n\t" \
    "v_mul_f32 v30, v30, v240\n\t" \
    "v_mul_f32 v31, v31, v240\n\t" \
    "v_mul_f32 v32, v32, v240\n\t" \
    "v_mul_f32 v33, v33, v240\n\t" \
    "v_mul_f32 v34, v34, v240\n\t" \
    "v_mul_f32 v35, v35, v240\n\t" \
    "v_mul_f32 v36, v36, v240\n\t" \
    "v_mul_f32 v37, v37, v240\n\t" \
    "v_mul_f32 v38, v38, v240\n\t" \
    "v_mul_f32 v39, v39, v240\n\t" \
    "v_mul_f32 v40, v40, v240\n\t" \
    "v_mul_f32 v41, v41, v240\n\t" \
    "v_mul_f32 v42, v42, v240\n\t" \
    "v_mul_f32 v43, v43, v240\n\t" \
    "v_mul_f32 v44, v44, v240\n\t" \
    "v_mul_f32 v45, v45, v240\n\t" \
    "v_mul_f32 v46, v46, v240\n\t" \
    "v_mul_f32 v47, v47, v240\n\t" \
    "v_mul_f32 v48, v48, v240\n\t" \
    "v_mul_f32 v49, v49, v240\n\t" \
    "v_mul_f32 v50, v50, v240\n\t" \
    "v_mul_f32 v51, v51, v240\n\t" \
    "v_mul_f32 v52, v52, v240\n\t" \
    "v_mul_f32 v53, v53, v240\n\t" \
    "v_mul_f32 v54, v54, v240\n\t" \
    "v_mul_f32 v55, v55, v240\n\t" \
    "v_mul_f32 v56, v56, v240\n\t" \
    "v_mul_f32 v57, v57, v240\n\t" \
    "v_mul_f32 v58, v58, v240\n\t" \
    "v_mul_f32 v59, v59, v240\n\t" \
    "v_mul_f32 v60, v60, v240\n\t" \
    "v_mul_f32 v61, v61, v240\n\t" \
    "v_mul_f32 v62, v62, v240\n\t" \
    "v_mul_f32 v63, v63, v240\n\t" \
    "v_add_f32 v237, v144, v145\n\t" \
    "v_add_f32 v237, v237, v146\n\t" \
    "v_add_f32 v237, v237, v147\n\t" \
    "v_add_f32 v237, v237, v148\n\t" \
    "v_add_f32 v237, v237, v149\n\t" \
    "v_add_f32 v237, v237, v150\n\t" \
    "v_add_f32 v237, v237, v151\n\t" \
    "v_add_f32 v237, v237, v152\n\t" \
    "v_add_f32 v237, v237, v153\n\t" \
    "v_add_f32 v237, v237, v154\n\t" \
    "v_add_f32 v237, v237, v155\n\t" \
    "v_add_f32 v237, v237, v156\n\t" \
    "v_add_f32 v237, v237, v157\n\t" \
    "v_add_f32 v237, v237, v158\n\t" \
    "v_add_f32 v237, v237, v159\n\t" \
    "v_cvt_pk_bf16_f32 v160, v144, v145\n\t" \
    "v_cvt_pk_bf16_f32 v161, v146, v147\n\t" \
    "v_cvt_pk_bf16_f32 v162, v148, v149\n\t" \
    "v_cvt_pk_bf16_f32 v163, v150, v151\n\t" \
    "v_cvt_pk_bf16_f32 v164, v152, v153\n\t" \
    "v_cvt_pk_bf16_f32 v165, v154, v155\n\t" \
    "v_cvt_pk_bf16_f32 v166, v156, v157\n\t" \
    "v_cvt_pk_bf16_f32 v167, v158, v159\n\t" \
    "s_nop 1\n\t" \
    "s_branch BACKA_%=\n\t" \
    "RAREB_%=:\n\t" \
    "s_nop 15\n\t" \
    "v_max3_f32 v239, v112, v113, v114\n\t" \
    "v_max3_f32 v239, v239, v115, v116\n\t" \
    "v_max3_f32 v239, v239, v117, v118\n\t" \
    "v_max3_f32 v239, v239, v119, v120\n\t" \
    "v_max3_f32 v239, v239, v121, v122\n\t" \
    "v_max3_f32 v239, v239, v123, v124\n\t" \
    "v_max3_f32 v239, v239, v125, v126\n\t" \
    "v_max_f32 v239, v239, v127\n\t" \
    "v_max_f32 v239, 0, v239\n\t" \
    "v_mov_b32 v240, v239\n\t" \
    "s_nop 1\n\t" \
    "v_permlane32_swap_b32 v239, v240\n\t" \
    "s_nop 1\n\t" \
    "v_max_f32 v239, v239, v240\n\t" \
    "v_add_f32 v238, v238, v239\n\t" \
    "v_exp_f32 v240, -v239\n\t" \
    "v_sub_f32 v241, 0, v238\n\t" \
    "v_sub_f32 v112, v112, v239\n\t" \
    "v_sub_f32 v128, v128, v239\n\t" \
    "v_mov_b32 v96, v241\n\t" \
    "v_sub_f32 v113, v113, v239\n\t" \
    "v_sub_f32 v129, v129, v239\n\t" \
    "v_mov_b32 v97, v241\n\t" \
    "v_sub_f32 v114, v114, v239\n\t" \
    "v_sub_f32 v130, v130, v239\n\t" \
    "v_mov_b32 v98, v241\n\t" \
    "v_sub_f32 v115, v115, v239\n\t" \
    "v_sub_f32 v131, v131, v239\n\t" \
    "v_mov_b32 v99, v241\n\t" \
    "v_sub_f32 v116, v116, v239\n\t" \
    "v_sub_f32 v132, v132, v239\n\t" \
    "v_mov_b32 v100, v241\n\t" \
    "v_sub_f32 v117, v117, v239\n\t" \
    "v_sub_f32 v133, v133, v239\n\t" \
    "v_mov_b32 v101, v241\n\t" \
    "v_sub_f32 v118, v118, v239\n\t" \
    "v_sub_f32 v134, v134, v239\n\t" \
    "v_mov_b32 v102, v241\n\t" \
    "v_sub_f32 v119, v119, v239\n\t" \
    "v_sub_f32 v135, v135, v239\n\t" \
    "v_mov_b32 v103, v241\n\t" \
    "v_sub_f32 v120, v120, v239\n\t" \
    "v_sub_f32 v136, v136, v239\n\t" \
    "v_mov_b32 v104, v241\n\t" \
    "v_sub_f32 v121, v121, v239\n\t" \
    "v_sub_f32 v137, v137, v239\n\t" \
    "v_mov_b32 v105, v241\n\t" \
    "v_sub_f32 v122, v122, v239\n\t" \
    "v_sub_f32 v138, v138, v239\n\t" \
    "v_mov_b32 v106, v241\n\t" \
    "v_sub_f32 v123, v123, v239\n\t" \
    "v_sub_f32 v139, v139, v239\n\t" \
    "v_mov_b32 v107, v241\n\t" \
    "v_sub_f32 v124, v124, v239\n\t" \
    "v_sub_f32 v140, v140, v239\n\t" \
    "v_mov_b32 v108, v241\n\t" \
    "v_sub_f32 v125, v125, v239\n\t" \
    "v_sub_f32 v141, v141, v239\n\t" \
    "v_mov_b32 v109, v241\n\t" \
    "v_sub_f32 v126, v126, v239\n\t" \
    "v_sub_f32 v142, v142, v239\n\t" \
    "v_mov_b32 v110, v241\n\t" \
    "v_sub_f32 v127, v127, v239\n\t" \
    "v_sub_f32 v143, v143, v239\n\t" \
    "v_mov_b32 v111, v241\n\t" \
    "v_exp_f32 v144, v112\n\t" \
    "v_exp_f32 v145, v113\n\t" \
    "v_exp_f32 v146, v114\n\t" \
    "v_exp_f32 v147, v115\n\t" \
    "v_exp_f32 v148, v116\n\t" \
    "v_exp_f32 v149, v117\n\t" \
    "v_exp_f32 v150, v118\n\t" \
    "v_exp_f32 v151, v119\n\t" \
    "v_exp_f32 v152, v120\n\t" \
    "v_exp_f32 v153, v121\n\t" \
    "v_exp_f32 v154, v122\n\t" \
    "v_exp_f32 v155, v123\n\t" \
    "v_exp_f32 v156, v124\n\t" \
    "v_exp_f32 v157, v125\n\t" \
    "v_exp_f32 v158, v126\n\t" \
    "v_exp_f32 v159, v127\n\t" \
    "v_mul_f32 v236, v236, v240\n\t" \
    "v_mul_f32 v0, v0, v240\n\t" \
    "v_mul_f32 v1, v1, v240\n\t" \
    "v_mul_f32 v2, v2, v240\n\t" \
    "v_mul_f32 v3, v3, v240\n\t" \
    "v_mul_f32 v4, v4, v240\n\t" \
    "v_mul_f32 v5, v5, v240\n\t" \
    "v_mul_f32 v6, v6, v240\n\t" \
    "v_mul_f32 v7, v7, v240\n\t" \
    "v_mul_f32 v8, v8, v240\n\t" \
    "v_mul_f32 v9, v9, v240\n\t" \
    "v_mul_f32 v10, v10, v240\n\t" \
    "v_mul_f32 v11, v11, v240\n\t" \
    "v_mul_f32 v12, v12, v240\n\t" \
    "v_mul_f32 v13, v13, v240\n\t" \
    "v_mul_f32 v14, v14, v240\n\t" \
    "v_mul_f32 v15, v15, v240\n\t" \
    "v_mul_f32 v16, v16, v240\n\t" \
    "v_mul_f32 v17, v17, v240\n\t" \
    "v_mul_f32 v18, v18, v240\n\t" \
    "v_mul_f32 v19, v19, v240\n\t" \
    "v_mul_f32 v20, v20, v240\n\t" \
    "v_mul_f32 v21, v21, v240\n\t" \
    "v_mul_f32 v22, v22, v240\n\t" \
    "v_mul_f32 v23, v23, v240\n\t" \
    "v_mul_f32 v24, v24, v240\n\t" \
    "v_mul_f32 v25, v25, v240\n\t" \
    "v_mul_f32 v26, v26, v240\n\t" \
    "v_mul_f32 v27, v27, v240\n\t" \
    "v_mul_f32 v28, v28, v240\n\t" \
    "v_mul_f32 v29, v29, v240\n\t" \
    "v_mul_f32 v30, v30, v240\n\t" \
    "v_mul_f32 v31, v31, v240\n\t" \
    "v_mul_f32 v32, v32, v240\n\t" \
    "v_mul_f32 v33, v33, v240\n\t" \
    "v_mul_f32 v34, v34, v240\n\t" \
    "v_mul_f32 v35, v35, v240\n\t" \
    "v_mul_f32 v36, v36, v240\n\t" \
    "v_mul_f32 v37, v37, v240\n\t" \
    "v_mul_f32 v38, v38, v240\n\t" \
    "v_mul_f32 v39, v39, v240\n\t" \
    "v_mul_f32 v40, v40, v240\n\t" \
    "v_mul_f32 v41, v41, v240\n\t" \
    "v_mul_f32 v42, v42, v240\n\t" \
    "v_mul_f32 v43, v43, v240\n\t" \
    "v_mul_f32 v44, v44, v240\n\t" \
    "v_mul_f32 v45, v45, v240\n\t" \
    "v_mul_f32 v46, v46, v240\n\t" \
    "v_mul_f32 v47, v47, v240\n\t" \
    "v_mul_f32 v48, v48, v240\n\t" \
    "v_mul_f32 v49, v49, v240\n\t" \
    "v_mul_f32 v50, v50, v240\n\t" \
    "v_mul_f32 v51, v51, v240\n\t" \
    "v_mul_f32 v52, v52, v240\n\t" \
    "v_mul_f32 v53, v53, v240\n\t" \
    "v_mul_f32 v54, v54, v240\n\t" \
    "v_mul_f32 v55, v55, v240\n\t" \
    "v_mul_f32 v56, v56, v240\n\t" \
    "v_mul_f32 v57, v57, v240\n\t" \
    "v_mul_f32 v58, v58, v240\n\t" \
    "v_mul_f32 v59, v59, v240\n\t" \
    "v_mul_f32 v60, v60, v240\n\t" \
    "v_mul_f32 v61, v61, v240\n\t" \
    "v_mul_f32 v62, v62, v240\n\t" \
    "v_mul_f32 v63, v63, v240\n\t" \
    "v_add_f32 v237, v144, v145\n\t" \
    "v_add_f32 v237, v237, v146\n\t" \
    "v_add_f32 v237, v237, v147\n\t" \
    "v_add_f32 v237, v237, v148\n\t" \
    "v_add_f32 v237, v237, v149\n\t" \
    "v_add_f32 v237, v237, v150\n\t" \
    "v_add_f32 v237, v237, v151\n\t" \
    "v_add_f32 v237, v237, v152\n\t" \
    "v_add_f32 v237, v237, v153\n\t" \
    "v_add_f32 v237, v237, v154\n\t" \
    "v_add_f32 v237, v237, v155\n\t" \
    "v_add_f32 v237, v237, v156\n\t" \
    "v_add_f32 v237, v237, v157\n\t" \
    "v_add_f32 v237, v237, v158\n\t" \
    "v_add_f32 v237, v237, v159\n\t" \
    "v_cvt_pk_bf16_f32 v160, v144, v145\n\t" \
    "v_cvt_pk_bf16_f32 v161, v146, v147\n\t" \
    "v_cvt_pk_bf16_f32 v162, v148, v149\n\t" \
    "v_cvt_pk_bf16_f32 v163, v150, v151\n\t" \
    "v_cvt_pk_bf16_f32 v164, v152, v153\n\t" \
    "v_cvt_pk_bf16_f32 v165, v154, v155\n\t" \
    "v_cvt_pk_bf16_f32 v166, v156, v157\n\t" \
    "v_cvt_pk_bf16_f32 v167, v158, v159\n\t" \
    "s_nop 1\n\t" \
    "s_branch BACKB_%=\n\t" \
    "RAREC_%=:\n\t" \
    "s_nop 15\n\t" \
    "v_max3_f32 v239, v128, v129, v130\n\t" \
    "v_max3_f32 v239, v239, v131, v132\n\t" \
    "v_max3_f32 v239, v239, v133, v134\n\t" \
    "v_max3_f32 v239, v239, v135, v136\n\t" \
    "v_max3_f32 v239, v239, v137, v138\n\t" \
    "v_max3_f32 v239, v239, v139, v140\n\t" \
    "v_max3_f32 v239, v239, v141, v142\n\t" \
    "v_max_f32 v239, v239, v143\n\t" \
    "v_max_f32 v239, 0, v239\n\t" \
    "v_mov_b32 v240, v239\n\t" \
    "s_nop 1\n\t" \
    "v_permlane32_swap_b32 v239, v240\n\t" \
    "s_nop 1\n\t" \
    "v_max_f32 v239, v239, v240\n\t" \
    "v_add_f32 v238, v238, v239\n\t" \
    "v_exp_f32 v240, -v239\n\t" \
    "v_sub_f32 v241, 0, v238\n\t" \
    "v_sub_f32 v128, v128, v239\n\t" \
    "v_sub_f32 v112, v112, v239\n\t" \
    "v_mov_b32 v96, v241\n\t" \
    "v_sub_f32 v129, v129, v239\n\t" \
    "v_sub_f32 v113, v113, v239\n\t" \
    "v_mov_b32 v97, v241\n\t" \
    "v_sub_f32 v130, v130, v239\n\t" \
    "v_sub_f32 v114, v114, v239\n\t" \
    "v_mov_b32 v98, v241\n\t" \
    "v_sub_f32 v131, v131, v239\n\t" \
    "v_sub_f32 v115, v115, v239\n\t" \
    "v_mov_b32 v99, v241\n\t" \
    "v_sub_f32 v132, v132, v239\n\t" \
    "v_sub_f32 v116, v116, v239\n\t" \
    "v_mov_b32 v100, v241\n\t" \
    "v_sub_f32 v133, v133, v239\n\t" \
    "v_sub_f32 v117, v117, v239\n\t" \
    "v_mov_b32 v101, v241\n\t" \
    "v_sub_f32 v134, v134, v239\n\t" \
    "v_sub_f32 v118, v118, v239\n\t" \
    "v_mov_b32 v102, v241\n\t" \
    "v_sub_f32 v135, v135, v239\n\t" \
    "v_sub_f32 v119, v119, v239\n\t" \
    "v_mov_b32 v103, v241\n\t" \
    "v_sub_f32 v136, v136, v239\n\t" \
    "v_sub_f32 v120, v120, v239\n\t" \
    "v_mov_b32 v104, v241\n\t" \
    "v_sub_f32 v137, v137, v239\n\t" \
    "v_sub_f32 v121, v121, v239\n\t" \
    "v_mov_b32 v105, v241\n\t" \
    "v_sub_f32 v138, v138, v239\n\t" \
    "v_sub_f32 v122, v122, v239\n\t" \
    "v_mov_b32 v106, v241\n\t" \
    "v_sub_f32 v139, v139, v239\n\t" \
    "v_sub_f32 v123, v123, v239\n\t" \
    "v_mov_b32 v107, v241\n\t" \
    "v_sub_f32 v140, v140, v239\n\t" \
    "v_sub_f32 v124, v124, v239\n\t" \
    "v_mov_b32 v108, v241\n\t" \
    "v_sub_f32 v141, v141, v239\n\t" \
    "v_sub_f32 v125, v125, v239\n\t" \
    "v_mov_b32 v109, v241\n\t" \
    "v_sub_f32 v142, v142, v239\n\t" \
    "v_sub_f32 v126, v126, v239\n\t" \
    "v_mov_b32 v110, v241\n\t" \
    "v_sub_f32 v143, v143, v239\n\t" \
    "v_sub_f32 v127, v127, v239\n\t" \
    "v_mov_b32 v111, v241\n\t" \
    "v_exp_f32 v144, v128\n\t" \
    "v_exp_f32 v145, v129\n\t" \
    "v_exp_f32 v146, v130\n\t" \
    "v_exp_f32 v147, v131\n\t" \
    "v_exp_f32 v148, v132\n\t" \
    "v_exp_f32 v149, v133\n\t" \
    "v_exp_f32 v150, v134\n\t" \
    "v_exp_f32 v151, v135\n\t" \
    "v_exp_f32 v152, v136\n\t" \
    "v_exp_f32 v153, v137\n\t" \
    "v_exp_f32 v154, v138\n\t" \
    "v_exp_f32 v155, v139\n\t" \
    "v_exp_f32 v156, v140\n\t" \
    "v_exp_f32 v157, v141\n\t" \
    "v_exp_f32 v158, v142\n\t" \
    "v_exp_f32 v159, v143\n\t" \
    "v_mul_f32 v236, v236, v240\n\t" \
    "v_mul_f32 v0, v0, v240\n\t" \
    "v_mul_f32 v1, v1, v240\n\t" \
    "v_mul_f32 v2, v2, v240\n\t" \
    "v_mul_f32 v3, v3, v240\n\t" \
    "v_mul_f32 v4, v4, v240\n\t" \
    "v_mul_f32 v5, v5, v240\n\t" \
    "v_mul_f32 v6, v6, v240\n\t" \
    "v_mul_f32 v7, v7, v240\n\t" \
    "v_mul_f32 v8, v8, v240\n\t" \
    "v_mul_f32 v9, v9, v240\n\t" \
    "v_mul_f32 v10, v10, v240\n\t" \
    "v_mul_f32 v11, v11, v240\n\t" \
    "v_mul_f32 v12, v12, v240\n\t" \
    "v_mul_f32 v13, v13, v240\n\t" \
    "v_mul_f32 v14, v14, v240\n\t" \
    "v_mul_f32 v15, v15, v240\n\t" \
    "v_mul_f32 v16, v16, v240\n\t" \
    "v_mul_f32 v17, v17, v240\n\t" \
    "v_mul_f32 v18, v18, v240\n\t" \
    "v_mul_f32 v19, v19, v240\n\t" \
    "v_mul_f32 v20, v20, v240\n\t" \
    "v_mul_f32 v21, v21, v240\n\t" \
    "v_mul_f32 v22, v22, v240\n\t" \
    "v_mul_f32 v23, v23, v240\n\t" \
    "v_mul_f32 v24, v24, v240\n\t" \
    "v_mul_f32 v25, v25, v240\n\t" \
    "v_mul_f32 v26, v26, v240\n\t" \
    "v_mul_f32 v27, v27, v240\n\t" \
    "v_mul_f32 v28, v28, v240\n\t" \
    "v_mul_f32 v29, v29, v240\n\t" \
    "v_mul_f32 v30, v30, v240\n\t" \
    "v_mul_f32 v31, v31, v240\n\t" \
    "v_mul_f32 v32, v32, v240\n\t" \
    "v_mul_f32 v33, v33, v240\n\t" \
    "v_mul_f32 v34, v34, v240\n\t" \
    "v_mul_f32 v35, v35, v240\n\t" \
    "v_mul_f32 v36, v36, v240\n\t" \
    "v_mul_f32 v37, v37, v240\n\t" \
    "v_mul_f32 v38, v38, v240\n\t" \
    "v_mul_f32 v39, v39, v240\n\t" \
    "v_mul_f32 v40, v40, v240\n\t" \
    "v_mul_f32 v41, v41, v240\n\t" \
    "v_mul_f32 v42, v42, v240\n\t" \
    "v_mul_f32 v43, v43, v240\n\t" \
    "v_mul_f32 v44, v44, v240\n\t" \
    "v_mul_f32 v45, v45, v240\n\t" \
    "v_mul_f32 v46, v46, v240\n\t" \
    "v_mul_f32 v47, v47, v240\n\t" \
    "v_mul_f32 v48, v48, v240\n\t" \
    "v_mul_f32 v49, v49, v240\n\t" \
    "v_mul_f32 v50, v50, v240\n\t" \
    "v_mul_f32 v51, v51, v240\n\t" \
    "v_mul_f32 v52, v52, v240\n\t" \
    "v_mul_f32 v53, v53, v240\n\t" \
    "v_mul_f32 v54, v54, v240\n\t" \
    "v_mul_f32 v55, v55, v240\n\t" \
    "v_mul_f32 v56, v56, v240\n\t" \
    "v_mul_f32 v57, v57, v240\n\t" \
    "v_mul_f32 v58, v58, v240\n\t" \
    "v_mul_f32 v59, v59, v240\n\t" \
    "v_mul_f32 v60, v60, v240\n\t" \
    "v_mul_f32 v61, v61, v240\n\t" \
    "v_mul_f32 v62, v62, v240\n\t" \
    "v_mul_f32 v63, v63, v240\n\t" \
    "v_add_f32 v237, v144, v145\n\t" \
    "v_add_f32 v237, v237, v146\n\t" \
    "v_add_f32 v237, v237, v147\n\t" \
    "v_add_f32 v237, v237, v148\n\t" \
    "v_add_f32 v237, v237, v149\n\t" \
    "v_add_f32 v237, v237, v150\n\t" \
    "v_add_f32 v237, v237, v151\n\t" \
    "v_add_f32 v237, v237, v152\n\t" \
    "v_add_f32 v237, v237, v153\n\t" \
    "v_add_f32 v237, v237, v154\n\t" \
    "v_add_f32 v237, v237, v155\n\t" \
    "v_add_f32 v237, v237, v156\n\t" \
    "v_add_f32 v237, v237, v157\n\t" \
    "v_add_f32 v237, v237, v158\n\t" \
    "v_add_f32 v237, v237, v159\n\t" \
    "v_cvt_pk_bf16_f32 v160, v144, v145\n\t" \
    "v_cvt_pk_bf16_f32 v161, v146, v147\n\t" \
    "v_cvt_pk_bf16_f32 v162, v148, v149\n\t" \
    "v_cvt_pk_bf16_f32 v163, v150, v151\n\t" \
    "v_cvt_pk_bf16_f32 v164, v152, v153\n\t" \
    "v_cvt_pk_bf16_f32 v165, v154, v155\n\t" \
    "v_cvt_pk_bf16_f32 v166, v156, v157\n\t" \
    "v_cvt_pk_bf16_f32 v167, v158, v159\n\t" \
    "s_nop 1\n\t" \
    "s_branch BACKC_%=\n\t" \
    "RARED_%=:\n\t" \
    "s_nop 15\n\t" \
    "v_max3_f32 v239, v112, v113, v114\n\t" \
    "v_max3_f32 v239, v239, v115, v116\n\t" \
    "v_max3_f32 v239, v239, v117, v118\n\t" \
    "v_max3_f32 v239, v239, v119, v120\n\t" \
    "v_max3_f32 v239, v239, v121, v122\n\t" \
    "v_max3_f32 v239, v239, v123, v124\n\t" \
    "v_max3_f32 v239, v239, v125, v126\n\t" \
    "v_max_f32 v239, v239, v127\n\t" \
    "v_max_f32 v239, 0, v239\n\t" \
    "v_mov_b32 v240, v239\n\t" \
    "s_nop 1\n\t" \
    "v_permlane32_swap_b32 v239, v240\n\t" \
    "s_nop 1\n\t" \
    "v_max_f32 v239, v239, v240\n\t" \
    "v_add_f32 v238, v238, v239\n\t" \
    "v_exp_f32 v240, -v239\n\t" \
    "v_sub_f32 v241, 0, v238\n\t" \
    "v_sub_f32 v112, v112, v239\n\t" \
    "v_sub_f32 v128, v128, v239\n\t" \
    "v_mov_b32 v96, v241\n\t" \
    "v_sub_f32 v113, v113, v239\n\t" \
    "v_sub_f32 v129, v129, v239\n\t" \
    "v_mov_b32 v97, v241\n\t" \
    "v_sub_f32 v114, v114, v239\n\t" \
    "v_sub_f32 v130, v130, v239\n\t" \
    "v_mov_b32 v98, v241\n\t" \
    "v_sub_f32 v115, v115, v239\n\t" \
    "v_sub_f32 v131, v131, v239\n\t" \
    "v_mov_b32 v99, v241\n\t" \
    "v_sub_f32 v116, v116, v239\n\t" \
    "v_sub_f32 v132, v132, v239\n\t" \
    "v_mov_b32 v100, v241\n\t" \
    "v_sub_f32 v117, v117, v239\n\t" \
    "v_sub_f32 v133, v133, v239\n\t" \
    "v_mov_b32 v101, v241\n\t" \
    "v_sub_f32 v118, v118, v239\n\t" \
    "v_sub_f32 v134, v134, v239\n\t" \
    "v_mov_b32 v102, v241\n\t" \
    "v_sub_f32 v119, v119, v239\n\t" \
    "v_sub_f32 v135, v135, v239\n\t" \
    "v_mov_b32 v103, v241\n\t" \
    "v_sub_f32 v120, v120, v239\n\t" \
    "v_sub_f32 v136, v136, v239\n\t" \
    "v_mov_b32 v104, v241\n\t" \
    "v_sub_f32 v121, v121, v239\n\t" \
    "v_sub_f32 v137, v137, v239\n\t" \
    "v_mov_b32 v105, v241\n\t" \
    "v_sub_f32 v122, v122, v239\n\t" \
    "v_sub_f32 v138, v138, v239\n\t" \
    "v_mov_b32 v106, v241\n\t" \
    "v_sub_f32 v123, v123, v239\n\t" \
    "v_sub_f32 v139, v139, v239\n\t" \
    "v_mov_b32 v107, v241\n\t" \
    "v_sub_f32 v124, v124, v239\n\t" \
    "v_sub_f32 v140, v140, v239\n\t" \
    "v_mov_b32 v108, v241\n\t" \
    "v_sub_f32 v125, v125, v239\n\t" \
    "v_sub_f32 v141, v141, v239\n\t" \
    "v_mov_b32 v109, v241\n\t" \
    "v_sub_f32 v126, v126, v239\n\t" \
    "v_sub_f32 v142, v142, v239\n\t" \
    "v_mov_b32 v110, v241\n\t" \
    "v_sub_f32 v127, v127, v239\n\t" \
    "v_sub_f32 v143, v143, v239\n\t" \
    "v_mov_b32 v111, v241\n\t" \
    "v_exp_f32 v144, v112\n\t" \
    "v_exp_f32 v145, v113\n\t" \
    "v_exp_f32 v146, v114\n\t" \
    "v_exp_f32 v147, v115\n\t" \
    "v_exp_f32 v148, v116\n\t" \
    "v_exp_f32 v149, v117\n\t" \
    "v_exp_f32 v150, v118\n\t" \
    "v_exp_f32 v151, v119\n\t" \
    "v_exp_f32 v152, v120\n\t" \
    "v_exp_f32 v153, v121\n\t" \
    "v_exp_f32 v154, v122\n\t" \
    "v_exp_f32 v155, v123\n\t" \
    "v_exp_f32 v156, v124\n\t" \
    "v_exp_f32 v157, v125\n\t" \
    "v_exp_f32 v158, v126\n\t" \
    "v_exp_f32 v159, v127\n\t" \
    "v_mul_f32 v236, v236, v240\n\t" \
    "v_mul_f32 v0, v0, v240\n\t" \
    "v_mul_f32 v1, v1, v240\n\t" \
    "v_mul_f32 v2, v2, v240\n\t" \
    "v_mul_f32 v3, v3, v240\n\t" \
    "v_mul_f32 v4, v4, v240\n\t" \
    "v_mul_f32 v5, v5, v240\n\t" \
    "v_mul_f32 v6, v6, v240\n\t" \
    "v_mul_f32 v7, v7, v240\n\t" \
    "v_mul_f32 v8, v8, v240\n\t" \
    "v_mul_f32 v9, v9, v240\n\t" \
    "v_mul_f32 v10, v10, v240\n\t" \
    "v_mul_f32 v11, v11, v240\n\t" \
    "v_mul_f32 v12, v12, v240\n\t" \
    "v_mul_f32 v13, v13, v240\n\t" \
    "v_mul_f32 v14, v14, v240\n\t" \
    "v_mul_f32 v15, v15, v240\n\t" \
    "v_mul_f32 v16, v16, v240\n\t" \
    "v_mul_f32 v17, v17, v240\n\t" \
    "v_mul_f32 v18, v18, v240\n\t" \
    "v_mul_f32 v19, v19, v240\n\t" \
    "v_mul_f32 v20, v20, v240\n\t" \
    "v_mul_f32 v21, v21, v240\n\t" \
    "v_mul_f32 v22, v22, v240\n\t" \
    "v_mul_f32 v23, v23, v240\n\t" \
    "v_mul_f32 v24, v24, v240\n\t" \
    "v_mul_f32 v25, v25, v240\n\t" \
    "v_mul_f32 v26, v26, v240\n\t" \
    "v_mul_f32 v27, v27, v240\n\t" \
    "v_mul_f32 v28, v28, v240\n\t" \
    "v_mul_f32 v29, v29, v240\n\t" \
    "v_mul_f32 v30, v30, v240\n\t" \
    "v_mul_f32 v31, v31, v240\n\t" \
    "v_mul_f32 v32, v32, v240\n\t" \
    "v_mul_f32 v33, v33, v240\n\t" \
    "v_mul_f32 v34, v34, v240\n\t" \
    "v_mul_f32 v35, v35, v240\n\t" \
    "v_mul_f32 v36, v36, v240\n\t" \
    "v_mul_f32 v37, v37, v240\n\t" \
    "v_mul_f32 v38, v38, v240\n\t" \
    "v_mul_f32 v39, v39, v240\n\t" \
    "v_mul_f32 v40, v40, v240\n\t" \
    "v_mul_f32 v41, v41, v240\n\t" \
    "v_mul_f32 v42, v42, v240\n\t" \
    "v_mul_f32 v43, v43, v240\n\t" \
    "v_mul_f32 v44, v44, v240\n\t" \
    "v_mul_f32 v45, v45, v240\n\t" \
    "v_mul_f32 v46, v46, v240\n\t" \
    "v_mul_f32 v47, v47, v240\n\t" \
    "v_mul_f32 v48, v48, v240\n\t" \
    "v_mul_f32 v49, v49, v240\n\t" \
    "v_mul_f32 v50, v50, v240\n\t" \
    "v_mul_f32 v51, v51, v240\n\t" \
    "v_mul_f32 v52, v52, v240\n\t" \
    "v_mul_f32 v53, v53, v240\n\t" \
    "v_mul_f32 v54, v54, v240\n\t" \
    "v_mul_f32 v55, v55, v240\n\t" \
    "v_mul_f32 v56, v56, v240\n\t" \
    "v_mul_f32 v57, v57, v240\n\t" \
    "v_mul_f32 v58, v58, v240\n\t" \
    "v_mul_f32 v59, v59, v240\n\t" \
    "v_mul_f32 v60, v60, v240\n\t" \
    "v_mul_f32 v61, v61, v240\n\t" \
    "v_mul_f32 v62, v62, v240\n\t" \
    "v_mul_f32 v63, v63, v240\n\t" \
    "v_add_f32 v237, v144, v145\n\t" \
    "v_add_f32 v237, v237, v146\n\t" \
    "v_add_f32 v237, v237, v147\n\t" \
    "v_add_f32 v237, v237, v148\n\t" \
    "v_add_f32 v237, v237, v149\n\t" \
    "v_add_f32 v237, v237, v150\n\t" \
    "v_add_f32 v237, v237, v151\n\t" \
    "v_add_f32 v237, v237, v152\n\t" \
    "v_add_f32 v237, v237, v153\n\t" \
    "v_add_f32 v237, v237, v154\n\t" \
    "v_add_f32 v237, v237, v155\n\t" \
    "v_add_f32 v237, v237, v156\n\t" \
    "v_add_f32 v237, v237, v157\n\t" \
    "v_add_f32 v237, v237, v158\n\t" \
    "v_add_f32 v237, v237, v159\n\t" \
    "v_cvt_pk_bf16_f32 v160, v144, v145\n\t" \
    "v_cvt_pk_bf16_f32 v161, v146, v147\n\t" \
    "v_cvt_pk_bf16_f32 v162, v148, v149\n\t" \
    "v_cvt_pk_bf16_f32 v163, v150, v151\n\t" \
    "v_cvt_pk_bf16_f32 v164, v152, v153\n\t" \
    "v_cvt_pk_bf16_f32 v165, v154, v155\n\t" \
    "v_cvt_pk_bf16_f32 v166, v156, v157\n\t" \
    "v_cvt_pk_bf16_f32 v167, v158, v159\n\t" \
    "s_nop 1\n\t" \
    "s_branch BACKD_%=\n\t" \
    "RAREE_%=:\n\t" \
    "s_nop 15\n\t" \
    "v_max3_f32 v239, v128, v129, v130\n\t" \
    "v_max3_f32 v239, v239, v131, v132\n\t" \
    "v_max3_f32 v239, v239, v133, v134\n\t" \
    "v_max3_f32 v239, v239, v135, v136\n\t" \
    "v_max3_f32 v239, v239, v137, v138\n\t" \
    "v_max3_f32 v239, v239, v139, v140\n\t" \
    "v_max3_f32 v239, v239, v141, v142\n\t" \
    "v_max_f32 v239, v239, v143\n\t" \
    "v_max_f32 v239, 0, v239\n\t" \
    "v_mov_b32 v240, v239\n\t" \
    "s_nop 1\n\t" \
    "v_permlane32_swap_b32 v239, v240\n\t" \
    "s_nop 1\n\t" \
    "v_max_f32 v239, v239, v240\n\t" \
    "v_add_f32 v238, v238, v239\n\t" \
    "v_exp_f32 v240, -v239\n\t" \
    "v_sub_f32 v241, 0, v238\n\t" \
    "v_sub_f32 v128, v128, v239\n\t" \
    "v_mov_b32 v96, v241\n\t" \
    "v_sub_f32 v129, v129, v239\n\t" \
    "v_mov_b32 v97, v241\n\t" \
    "v_sub_f32 v130, v130, v239\n\t" \
    "v_mov_b32 v98, v241\n\t" \
    "v_sub_f32 v131, v131, v239\n\t" \
    "v_mov_b32 v99, v241\n\t" \
    "v_sub_f32 v132, v132, v239\n\t" \
    "v_mov_b32 v100, v241\n\t" \
    "v_sub_f32 v133, v133, v239\n\t" \
    "v_mov_b32 v101, v241\n\t" \
    "v_sub_f32 v134, v134, v239\n\t" \
    "v_mov_b32 v102, v241\n\t" \
    "v_sub_f32 v135, v135, v239\n\t" \
    "v_mov_b32 v103, v241\n\t" \
    "v_sub_f32 v136, v136, v239\n\t" \
    "v_mov_b32 v104, v241\n\t" \
    "v_sub_f32 v137, v137, v239\n\t" \
    "v_mov_b32 v105, v241\n\t" \
    "v_sub_f32 v138, v138, v239\n\t" \
    "v_mov_b32 v106, v241\n\t" \
    "v_sub_f32 v139, v139, v239\n\t" \
    "v_mov_b32 v107, v241\n\t" \
    "v_sub_f32 v140, v140, v239\n\t" \
    "v_mov_b32 v108, v241\n\t" \
    "v_sub_f32 v141, v141, v239\n\t" \
    "v_mov_b32 v109, v241\n\t" \
    "v_sub_f32 v142, v142, v239\n\t" \
    "v_mov_b32 v110, v241\n\t" \
    "v_sub_f32 v143, v143, v239\n\t" \
    "v_mov_b32 v111, v241\n\t" \
    "v_exp_f32 v144, v128\n\t" \
    "v_exp_f32 v145, v129\n\t" \
    "v_exp_f32 v146, v130\n\t" \
    "v_exp_f32 v147, v131\n\t" \
    "v_exp_f32 v148, v132\n\t" \
    "v_exp_f32 v149, v133\n\t" \
    "v_exp_f32 v150, v134\n\t" \
    "v_exp_f32 v151, v135\n\t" \
    "v_exp_f32 v152, v136\n\t" \
    "v_exp_f32 v153, v137\n\t" \
    "v_exp_f32 v154, v138\n\t" \
    "v_exp_f32 v155, v139\n\t" \
    "v_exp_f32 v156, v140\n\t" \
    "v_exp_f32 v157, v141\n\t" \
    "v_exp_f32 v158, v142\n\t" \
    "v_exp_f32 v159, v143\n\t" \
    "v_mul_f32 v236, v236, v240\n\t" \
    "v_mul_f32 v0, v0, v240\n\t" \
    "v_mul_f32 v1, v1, v240\n\t" \
    "v_mul_f32 v2, v2, v240\n\t" \
    "v_mul_f32 v3, v3, v240\n\t" \
    "v_mul_f32 v4, v4, v240\n\t" \
    "v_mul_f32 v5, v5, v240\n\t" \
    "v_mul_f32 v6, v6, v240\n\t" \
    "v_mul_f32 v7, v7, v240\n\t" \
    "v_mul_f32 v8, v8, v240\n\t" \
    "v_mul_f32 v9, v9, v240\n\t" \
    "v_mul_f32 v10, v10, v240\n\t" \
    "v_mul_f32 v11, v11, v240\n\t" \
    "v_mul_f32 v12, v12, v240\n\t" \
    "v_mul_f32 v13, v13, v240\n\t" \
    "v_mul_f32 v14, v14, v240\n\t" \
    "v_mul_f32 v15, v15, v240\n\t" \
    "v_mul_f32 v16, v16, v240\n\t" \
    "v_mul_f32 v17, v17, v240\n\t" \
    "v_mul_f32 v18, v18, v240\n\t" \
    "v_mul_f32 v19, v19, v240\n\t" \
    "v_mul_f32 v20, v20, v240\n\t" \
    "v_mul_f32 v21, v21, v240\n\t" \
    "v_mul_f32 v22, v22, v240\n\t" \
    "v_mul_f32 v23, v23, v240\n\t" \
    "v_mul_f32 v24, v24, v240\n\t" \
    "v_mul_f32 v25, v25, v240\n\t" \
    "v_mul_f32 v26, v26, v240\n\t" \
    "v_mul_f32 v27, v27, v240\n\t" \
    "v_mul_f32 v28, v28, v240\n\t" \
    "v_mul_f32 v29, v29, v240\n\t" \
    "v_mul_f32 v30, v30, v240\n\t" \
    "v_mul_f32 v31, v31, v240\n\t" \
    "v_mul_f32 v32, v32, v240\n\t" \
    "v_mul_f32 v33, v33, v240\n\t" \
    "v_mul_f32 v34, v34, v240\n\t" \
    "v_mul_f32 v35, v35, v240\n\t" \
    "v_mul_f32 v36, v36, v240\n\t" \
    "v_mul_f32 v37, v37, v240\n\t" \
    "v_mul_f32 v38, v38, v240\n\t" \
    "v_mul_f32 v39, v39, v240\n\t" \
    "v_mul_f32 v40, v40, v240\n\t" \
    "v_mul_f32 v41, v41, v240\n\t" \
    "v_mul_f32 v42, v42, v240\n\t" \
    "v_mul_f32 v43, v43, v240\n\t" \
    "v_mul_f32 v44, v44, v240\n\t" \
    "v_mul_f32 v45, v45, v240\n\t" \
    "v_mul_f32 v46, v46, v240\n\t" \
    "v_mul_f32 v47, v47, v240\n\t" \
    "v_mul_f32 v48, v48, v240\n\t" \
    "v_mul_f32 v49, v49, v240\n\t" \
    "v_mul_f32 v50, v50, v240\n\t" \
    "v_mul_f32 v51, v51, v240\n\t" \
    "v_mul_f32 v52, v52, v240\n\t" \
    "v_mul_f32 v53, v53, v240\n\t" \
    "v_mul_f32 v54, v54, v240\n\t" \
    "v_mul_f32 v55, v55, v240\n\t" \
    "v_mul_f32 v56, v56, v240\n\t" \
    "v_mul_f32 v57, v57, v240\n\t" \
    "v_mul_f32 v58, v58, v240\n\t" \
    "v_mul_f32 v59, v59, v240\n\t" \
    "v_mul_f32 v60, v60, v240\n\t" \
    "v_mul_f32 v61, v61, v240\n\t" \
    "v_mul_f32 v62, v62, v240\n\t" \
    "v_mul_f32 v63, v63, v240\n\t" \
    "v_add_f32 v237, v144, v145\n\t" \
    "v_add_f32 v237, v237, v146\n\t" \
    "v_add_f32 v237, v237, v147\n\t" \
    "v_add_f32 v237, v237, v148\n\t" \
    "v_add_f32 v237, v237, v149\n\t" \
    "v_add_f32 v237, v237, v150\n\t" \
    "v_add_f32 v237, v237, v151\n\t" \
    "v_add_f32 v237, v237, v152\n\t" \
    "v_add_f32 v237, v237, v153\n\t" \
    "v_add_f32 v237, v237, v154\n\t" \
    "v_add_f32 v237, v237, v155\n\t" \
    "v_add_f32 v237, v237, v156\n\t" \
    "v_add_f32 v237, v237, v157\n\t" \
    "v_add_f32 v237, v237, v158\n\t" \
    "v_add_f32 v237, v237, v159\n\t" \
    "v_cvt_pk_bf16_f32 v160, v144, v145\n\t" \
    "v_cvt_pk_bf16_f32 v161, v146, v147\n\t" \
    "v_cvt_pk_bf16_f32 v162, v148, v149\n\t" \
    "v_cvt_pk_bf16_f32 v163, v150, v151\n\t" \
    "v_cvt_pk_bf16_f32 v164, v152, v153\n\t" \
    "v_cvt_pk_bf16_f32 v165, v154, v155\n\t" \
    "v_cvt_pk_bf16_f32 v166, v156, v157\n\t" \
    "v_cvt_pk_bf16_f32 v167, v158, v159\n\t" \
    "s_nop 1\n\t" \
    "s_branch BACKE_%=\n\t" \
    "RAREF_%=:\n\t" \
    "s_nop 15\n\t" \
    "v_max3_f32 v239, v128, v129, v130\n\t" \
    "v_max3_f32 v239, v239, v131, v132\n\t" \
    "v_max3_f32 v239, v239, v133, v134\n\t" \
    "v_max3_f32 v239, v239, v135, v136\n\t" \
    "v_max3_f32 v239, v239, v137, v138\n\t" \
    "v_max3_f32 v239, v239, v139, v140\n\t" \
    "v_max3_f32 v239, v239, v141, v142\n\t" \
    "v_max_f32 v239, v239, v143\n\t" \
    "v_max_f32 v239, 0, v239\n\t" \
    "v_mov_b32 v240, v239\n\t" \
    "s_nop 1\n\t" \
    "v_permlane32_swap_b32 v239, v240\n\t" \
    "s_nop 1\n\t" \
    "v_max_f32 v239, v239, v240\n\t" \
    "v_add_f32 v238, v238, v239\n\t" \
    "v_exp_f32 v240, -v239\n\t" \
    "v_sub_f32 v241, 0, v238\n\t" \
    "v_sub_f32 v128, v128, v239\n\t" \
    "v_sub_f32 v112, v112, v239\n\t" \
    "v_mov_b32 v96, v241\n\t" \
    "v_sub_f32 v129, v129, v239\n\t" \
    "v_sub_f32 v113, v113, v239\n\t" \
    "v_mov_b32 v97, v241\n\t" \
    "v_sub_f32 v130, v130, v239\n\t" \
    "v_sub_f32 v114, v114, v239\n\t" \
    "v_mov_b32 v98, v241\n\t" \
    "v_sub_f32 v131, v131, v239\n\t" \
    "v_sub_f32 v115, v115, v239\n\t" \
    "v_mov_b32 v99, v241\n\t" \
    "v_sub_f32 v132, v132, v239\n\t" \
    "v_sub_f32 v116, v116, v239\n\t" \
    "v_mov_b32 v100, v241\n\t" \
    "v_sub_f32 v133, v133, v239\n\t" \
    "v_sub_f32 v117, v117, v239\n\t" \
    "v_mov_b32 v101, v241\n\t" \
    "v_sub_f32 v134, v134, v239\n\t" \
    "v_sub_f32 v118, v118, v239\n\t" \
    "v_mov_b32 v102, v241\n\t" \
    "v_sub_f32 v135, v135, v239\n\t" \
    "v_sub_f32 v119, v119, v239\n\t" \
    "v_mov_b32 v103, v241\n\t" \
    "v_sub_f32 v136, v136, v239\n\t" \
    "v_sub_f32 v120, v120, v239\n\t" \
    "v_mov_b32 v104, v241\n\t" \
    "v_sub_f32 v137, v137, v239\n\t" \
    "v_sub_f32 v121, v121, v239\n\t" \
    "v_mov_b32 v105, v241\n\t" \
    "v_sub_f32 v138, v138, v239\n\t" \
    "v_sub_f32 v122, v122, v239\n\t" \
    "v_mov_b32 v106, v241\n\t" \
    "v_sub_f32 v139, v139, v239\n\t" \
    "v_sub_f32 v123, v123, v239\n\t" \
    "v_mov_b32 v107, v241\n\t" \
    "v_sub_f32 v140, v140, v239\n\t" \
    "v_sub_f32 v124, v124, v239\n\t" \
    "v_mov_b32 v108, v241\n\t" \
    "v_sub_f32 v141, v141, v239\n\t" \
    "v_sub_f32 v125, v125, v239\n\t" \
    "v_mov_b32 v109, v241\n\t" \
    "v_sub_f32 v142, v142, v239\n\t" \
    "v_sub_f32 v126, v126, v239\n\t" \
    "v_mov_b32 v110, v241\n\t" \
    "v_sub_f32 v143, v143, v239\n\t" \
    "v_sub_f32 v127, v127, v239\n\t" \
    "v_mov_b32 v111, v241\n\t" \
    "v_exp_f32 v144, v128\n\t" \
    "v_exp_f32 v145, v129\n\t" \
    "v_exp_f32 v146, v130\n\t" \
    "v_exp_f32 v147, v131\n\t" \
    "v_exp_f32 v148, v132\n\t" \
    "v_exp_f32 v149, v133\n\t" \
    "v_exp_f32 v150, v134\n\t" \
    "v_exp_f32 v151, v135\n\t" \
    "v_exp_f32 v152, v136\n\t" \
    "v_exp_f32 v153, v137\n\t" \
    "v_exp_f32 v154, v138\n\t" \
    "v_exp_f32 v155, v139\n\t" \
    "v_exp_f32 v156, v140\n\t" \
    "v_exp_f32 v157, v141\n\t" \
    "v_exp_f32 v158, v142\n\t" \
    "v_exp_f32 v159, v143\n\t" \
    "v_mul_f32 v236, v236, v240\n\t" \
    "v_mul_f32 v0, v0, v240\n\t" \
    "v_mul_f32 v1, v1, v240\n\t" \
    "v_mul_f32 v2, v2, v240\n\t" \
    "v_mul_f32 v3, v3, v240\n\t" \
    "v_mul_f32 v4, v4, v240\n\t" \
    "v_mul_f32 v5, v5, v240\n\t" \
    "v_mul_f32 v6, v6, v240\n\t" \
    "v_mul_f32 v7, v7, v240\n\t" \
    "v_mul_f32 v8, v8, v240\n\t" \
    "v_mul_f32 v9, v9, v240\n\t" \
    "v_mul_f32 v10, v10, v240\n\t" \
    "v_mul_f32 v11, v11, v240\n\t" \
    "v_mul_f32 v12, v12, v240\n\t" \
    "v_mul_f32 v13, v13, v240\n\t" \
    "v_mul_f32 v14, v14, v240\n\t" \
    "v_mul_f32 v15, v15, v240\n\t" \
    "v_mul_f32 v16, v16, v240\n\t" \
    "v_mul_f32 v17, v17, v240\n\t" \
    "v_mul_f32 v18, v18, v240\n\t" \
    "v_mul_f32 v19, v19, v240\n\t" \
    "v_mul_f32 v20, v20, v240\n\t" \
    "v_mul_f32 v21, v21, v240\n\t" \
    "v_mul_f32 v22, v22, v240\n\t" \
    "v_mul_f32 v23, v23, v240\n\t" \
    "v_mul_f32 v24, v24, v240\n\t" \
    "v_mul_f32 v25, v25, v240\n\t" \
    "v_mul_f32 v26, v26, v240\n\t" \
    "v_mul_f32 v27, v27, v240\n\t" \
    "v_mul_f32 v28, v28, v240\n\t" \
    "v_mul_f32 v29, v29, v240\n\t" \
    "v_mul_f32 v30, v30, v240\n\t" \
    "v_mul_f32 v31, v31, v240\n\t" \
    "v_mul_f32 v32, v32, v240\n\t" \
    "v_mul_f32 v33, v33, v240\n\t" \
    "v_mul_f32 v34, v34, v240\n\t" \
    "v_mul_f32 v35, v35, v240\n\t" \
    "v_mul_f32 v36, v36, v240\n\t" \
    "v_mul_f32 v37, v37, v240\n\t" \
    "v_mul_f32 v38, v38, v240\n\t" \
    "v_mul_f32 v39, v39, v240\n\t" \
    "v_mul_f32 v40, v40, v240\n\t" \
    "v_mul_f32 v41, v41, v240\n\t" \
    "v_mul_f32 v42, v42, v240\n\t" \
    "v_mul_f32 v43, v43, v240\n\t" \
    "v_mul_f32 v44, v44, v240\n\t" \
    "v_mul_f32 v45, v45, v240\n\t" \
    "v_mul_f32 v46, v46, v240\n\t" \
    "v_mul_f32 v47, v47, v240\n\t" \
    "v_mul_f32 v48, v48, v240\n\t" \
    "v_mul_f32 v49, v49, v240\n\t" \
    "v_mul_f32 v50, v50, v240\n\t" \
    "v_mul_f32 v51, v51, v240\n\t" \
    "v_mul_f32 v52, v52, v240\n\t" \
    "v_mul_f32 v53, v53, v240\n\t" \
    "v_mul_f32 v54, v54, v240\n\t" \
    "v_mul_f32 v55, v55, v240\n\t" \
    "v_mul_f32 v56, v56, v240\n\t" \
    "v_mul_f32 v57, v57, v240\n\t" \
    "v_mul_f32 v58, v58, v240\n\t" \
    "v_mul_f32 v59, v59, v240\n\t" \
    "v_mul_f32 v60, v60, v240\n\t" \
    "v_mul_f32 v61, v61, v240\n\t" \
    "v_mul_f32 v62, v62, v240\n\t" \
    "v_mul_f32 v63, v63, v240\n\t" \
    "v_add_f32 v237, v144, v145\n\t" \
    "v_add_f32 v237, v237, v146\n\t" \
    "v_add_f32 v237, v237, v147\n\t" \
    "v_add_f32 v237, v237, v148\n\t" \
    "v_add_f32 v237, v237, v149\n\t" \
    "v_add_f32 v237, v237, v150\n\t" \
    "v_add_f32 v237, v237, v151\n\t" \
    "v_add_f32 v237, v237, v152\n\t" \
    "v_add_f32 v237, v237, v153\n\t" \
    "v_add_f32 v237, v237, v154\n\t" \
    "v_add_f32 v237, v237, v155\n\t" \
    "v_add_f32 v237, v237, v156\n\t" \
    "v_add_f32 v237, v237, v157\n\t" \
    "v_add_f32 v237, v237, v158\n\t" \
    "v_add_f32 v237, v237, v159\n\t" \
    "v_cvt_pk_bf16_f32 v160, v144, v145\n\t" \
    "v_cvt_pk_bf16_f32 v161, v146, v147\n\t" \
    "v_cvt_pk_bf16_f32 v162, v148, v149\n\t" \
    "v_cvt_pk_bf16_f32 v163, v150, v151\n\t" \
    "v_cvt_pk_bf16_f32 v164, v152, v153\n\t" \
    "v_cvt_pk_bf16_f32 v165, v154, v155\n\t" \
    "v_cvt_pk_bf16_f32 v166, v156, v157\n\t" \
    "v_cvt_pk_bf16_f32 v167, v158, v159\n\t" \
    "s_nop 1\n\t" \
    "s_branch BACKF_%=\n\t" \
    "RAREG_%=:\n\t" \
    "s_nop 15\n\t" \
    "v_max3_f32 v239, v112, v113, v114\n\t" \
    "v_max3_f32 v239, v239, v115, v116\n\t" \
    "v_max3_f32 v239, v239, v117, v118\n\t" \
    "v_max3_f32 v239, v239, v119, v120\n\t" \
    "v_max3_f32 v239, v239, v121, v122\n\t" \
    "v_max3_f32 v239, v239, v123, v124\n\t" \
    "v_max3_f32 v239, v239, v125, v126\n\t" \
    "v_max_f32 v239, v239, v127\n\t" \
    "v_max_f32 v239, 0, v239\n\t" \
    "v_mov_b32 v240, v239\n\t" \
    "s_nop 1\n\t" \
    "v_permlane32_swap_b32 v239, v240\n\t" \
    "s_nop 1\n\t" \
    "v_max_f32 v239, v239, v240\n\t" \
    "v_add_f32 v238, v238, v239\n\t" \
    "v_exp_f32 v240, -v239\n\t" \
    "v_sub_f32 v241, 0, v238\n\t" \
    "v_sub_f32 v112, v112, v239\n\t" \
    "v_mov_b32 v96, v241\n\t" \
    "v_sub_f32 v113, v113, v239\n\t" \
    "v_mov_b32 v97, v241\n\t" \
    "v_sub_f32 v114, v114, v239\n\t" \
    "v_mov_b32 v98, v241\n\t" \
    "v_sub_f32 v115, v115, v239\n\t" \
    "v_mov_b32 v99, v241\n\t" \
    "v_sub_f32 v116, v116, v239\n\t" \
    "v_mov_b32 v100, v241\n\t" \
    "v_sub_f32 v117, v117, v239\n\t" \
    "v_mov_b32 v101, v241\n\t" \
    "v_sub_f32 v118, v118, v239\n\t" \
    "v_mov_b32 v102, v241\n\t" \
    "v_sub_f32 v119, v119, v239\n\t" \
    "v_mov_b32 v103, v241\n\t" \
    "v_sub_f32 v120, v120, v239\n\t" \
    "v_mov_b32 v104, v241\n\t" \
    "v_sub_f32 v121, v121, v239\n\t" \
    "v_mov_b32 v105, v241\n\t" \
    "v_sub_f32 v122, v122, v239\n\t" \
    "v_mov_b32 v106, v241\n\t" \
    "v_sub_f32 v123, v123, v239\n\t" \
    "v_mov_b32 v107, v241\n\t" \
    "v_sub_f32 v124, v124, v239\n\t" \
    "v_mov_b32 v108, v241\n\t" \
    "v_sub_f32 v125, v125, v239\n\t" \
    "v_mov_b32 v109, v241\n\t" \
    "v_sub_f32 v126, v126, v239\n\t" \
    "v_mov_b32 v110, v241\n\t" \
    "v_sub_f32 v127, v127, v239\n\t" \
    "v_mov_b32 v111, v241\n\t" \
    "v_exp_f32 v144, v112\n\t" \
    "v_exp_f32 v145, v113\n\t" \
    "v_exp_f32 v146, v114\n\t" \
    "v_exp_f32 v147, v115\n\t" \
    "v_exp_f32 v148, v116\n\t" \
    "v_exp_f32 v149, v117\n\t" \
    "v_exp_f32 v150, v118\n\t" \
    "v_exp_f32 v151, v119\n\t" \
    "v_exp_f32 v152, v120\n\t" \
    "v_exp_f32 v153, v121\n\t" \
    "v_exp_f32 v154, v122\n\t" \
    "v_exp_f32 v155, v123\n\t" \
    "v_exp_f32 v156, v124\n\t" \
    "v_exp_f32 v157, v125\n\t" \
    "v_exp_f32 v158, v126\n\t" \
    "v_exp_f32 v159, v127\n\t" \
    "v_mul_f32 v236, v236, v240\n\t" \
    "v_mul_f32 v0, v0, v240\n\t" \
    "v_mul_f32 v1, v1, v240\n\t" \
    "v_mul_f32 v2, v2, v240\n\t" \
    "v_mul_f32 v3, v3, v240\n\t" \
    "v_mul_f32 v4, v4, v240\n\t" \
    "v_mul_f32 v5, v5, v240\n\t" \
    "v_mul_f32 v6, v6, v240\n\t" \
    "v_mul_f32 v7, v7, v240\n\t" \
    "v_mul_f32 v8, v8, v240\n\t" \
    "v_mul_f32 v9, v9, v240\n\t" \
    "v_mul_f32 v10, v10, v240\n\t" \
    "v_mul_f32 v11, v11, v240\n\t" \
    "v_mul_f32 v12, v12, v240\n\t" \
    "v_mul_f32 v13, v13, v240\n\t" \
    "v_mul_f32 v14, v14, v240\n\t" \
    "v_mul_f32 v15, v15, v240\n\t" \
    "v_mul_f32 v16, v16, v240\n\t" \
    "v_mul_f32 v17, v17, v240\n\t" \
    "v_mul_f32 v18, v18, v240\n\t" \
    "v_mul_f32 v19, v19, v240\n\t" \
    "v_mul_f32 v20, v20, v240\n\t" \
    "v_mul_f32 v21, v21, v240\n\t" \
    "v_mul_f32 v22, v22, v240\n\t" \
    "v_mul_f32 v23, v23, v240\n\t" \
    "v_mul_f32 v24, v24, v240\n\t" \
    "v_mul_f32 v25, v25, v240\n\t" \
    "v_mul_f32 v26, v26, v240\n\t" \
    "v_mul_f32 v27, v27, v240\n\t" \
    "v_mul_f32 v28, v28, v240\n\t" \
    "v_mul_f32 v29, v29, v240\n\t" \
    "v_mul_f32 v30, v30, v240\n\t" \
    "v_mul_f32 v31, v31, v240\n\t" \
    "v_mul_f32 v32, v32, v240\n\t" \
    "v_mul_f32 v33, v33, v240\n\t" \
    "v_mul_f32 v34, v34, v240\n\t" \
    "v_mul_f32 v35, v35, v240\n\t" \
    "v_mul_f32 v36, v36, v240\n\t" \
    "v_mul_f32 v37, v37, v240\n\t" \
    "v_mul_f32 v38, v38, v240\n\t" \
    "v_mul_f32 v39, v39, v240\n\t" \
    "v_mul_f32 v40, v40, v240\n\t" \
    "v_mul_f32 v41, v41, v240\n\t" \
    "v_mul_f32 v42, v42, v240\n\t" \
    "v_mul_f32 v43, v43, v240\n\t" \
    "v_mul_f32 v44, v44, v240\n\t" \
    "v_mul_f32 v45, v45, v240\n\t" \
    "v_mul_f32 v46, v46, v240\n\t" \
    "v_mul_f32 v47, v47, v240\n\t" \
    "v_mul_f32 v48, v48, v240\n\t" \
    "v_mul_f32 v49, v49, v240\n\t" \
    "v_mul_f32 v50, v50, v240\n\t" \
    "v_mul_f32 v51, v51, v240\n\t" \
    "v_mul_f32 v52, v52, v240\n\t" \
    "v_mul_f32 v53, v53, v240\n\t" \
    "v_mul_f32 v54, v54, v240\n\t" \
    "v_mul_f32 v55, v55, v240\n\t" \
    "v_mul_f32 v56, v56, v240\n\t" \
    "v_mul_f32 v57, v57, v240\n\t" \
    "v_mul_f32 v58, v58, v240\n\t" \
    "v_mul_f32 v59, v59, v240\n\t" \
    "v_mul_f32 v60, v60, v240\n\t" \
    "v_mul_f32 v61, v61, v240\n\t" \
    "v_mul_f32 v62, v62, v240\n\t" \
    "v_mul_f32 v63, v63, v240\n\t" \
    "v_add_f32 v237, v144, v145\n\t" \
    "v_add_f32 v237, v237, v146\n\t" \
    "v_add_f32 v237, v237, v147\n\t" \
    "v_add_f32 v237, v237, v148\n\t" \
    "v_add_f32 v237, v237, v149\n\t" \
    "v_add_f32 v237, v237, v150\n\t" \
    "v_add_f32 v237, v237, v151\n\t" \
    "v_add_f32 v237, v237, v152\n\t" \
    "v_add_f32 v237, v237, v153\n\t" \
    "v_add_f32 v237, v237, v154\n\t" \
    "v_add_f32 v237, v237, v155\n\t" \
    "v_add_f32 v237, v237, v156\n\t" \
    "v_add_f32 v237, v237, v157\n\t" \
    "v_add_f32 v237, v237, v158\n\t" \
    "v_add_f32 v237, v237, v159\n\t" \
    "v_cvt_pk_bf16_f32 v160, v144, v145\n\t" \
    "v_cvt_pk_bf16_f32 v161, v146, v147\n\t" \
    "v_cvt_pk_bf16_f32 v162, v148, v149\n\t" \
    "v_cvt_pk_bf16_f32 v163, v150, v151\n\t" \
    "v_cvt_pk_bf16_f32 v164, v152, v153\n\t" \
    "v_cvt_pk_bf16_f32 v165, v154, v155\n\t" \
    "v_cvt_pk_bf16_f32 v166, v156, v157\n\t" \
    "v_cvt_pk_bf16_f32 v167, v158, v159\n\t" \
    "s_nop 1\n\t" \
    "s_branch BACKG_%=\n\t" \
    "END_%=:\n\t" \
    "s_waitcnt lgkmcnt(0)\n\t" \
    "s_nop 15\n\t"
#define MX3(a, b, c) __builtin_fmaxf(__builtin_fmaxf((a), (b)), (c))
DI float max16(const f32x16& p) {
    float a = MX3(p[0], p[1], p[2]), b = MX3(p[3], p[4], p[5]);
    a = MX3(a, p[6], p[7]); b = MX3(b, p[8], p[9]); a = MX3(a, p[10], p[11]); b = MX3(b, p[12], p[13]); a = MX3(a, p[14], p[15]);
    return __builtin_fmaxf(a, b);
}
DI float halfmax(float m) { auto rr = __builtin_amdgcn_permlane32_swap(__float_as_uint(m), __float_as_uint(m), false, false); return __builtin_fmaxf(__uint_as_float(rr[0]), __uint_as_float(rr[1])); }
DI void cmask(f32x16& p, int r32, int hi) {
#pragma unroll
    for (int r = 0; r < 16; ++r) { const int kv = 4 * hi + (r & 3) + 8 * (r >> 2); if (kv > r32) p[r] = -INFINITY; }
}
#define WAITBAR(N) asm volatile("s_waitcnt vmcnt(" #N ")\n\ts_barrier" ::: "memory")
typedef unsigned u32x16 __attribute__((ext_vector_type(16)));

__global__ __launch_bounds__(512, 2) void k2_attn(const char* __restrict__ Q, const char* __restrict__ K, const char* __restrict__ VT, char* __restrict__ CTX) {
    extern __shared__ __attribute__((aligned(16))) unsigned char smem[];
    const lds_cptr lds = (lds_cptr)smem;
    const int tid = threadIdx.x, lane = tid & 63, r32 = lane & 31, hi = lane >> 5, wid = __builtin_amdgcn_readfirstlane(tid >> 6);
    const int xcd = blockIdx.x & 7, idx = blockIdx.x >> 3, h = xcd * 4 + (idx >> 3), ib = idx & 7;
    const int G = wid < 4 ? 4 * ib + wid : 63 - 4 * ib - (wid - 4);
    const int NT = 32 - 2 * ib;
    const char* Kh = K + (size_t)h * 32 * SLOT; const char* Vh = VT + (size_t)h * 32 * SLOT;
    const unsigned lds0 = (unsigned)(uintptr_t)smem;
    const unsigned dmaw = (unsigned)(wid - 4) >> 31;
    const unsigned dvo = (wid & 3) * 4096 + lane * 16, kdst = lds0 + LDS_K + (wid & 3) * 4096, vdst = lds0 + LDS_V + (wid & 3) * 4096;
#define DMA_T(base, dst, t, sl) do { const char* g_ = (base) + (size_t)(t) * SLOT; const unsigned l_ = (unsigned)__builtin_amdgcn_readfirstlane((dst) + ((sl) & 3) * SLOT); glds2(g_, dvo, l_); glds2(g_ + 2048, dvo, l_ + 2048); } while (0)
#define DMA_K(t, sl) DMA_T(Kh, kdst, t, sl)
#define DMA_V(t, sl) DMA_T(Vh, vdst, t, sl)
#define DMA_STEP(s) do { if (dmaw) { DMA_K(min((s) + 3, NT - 1), (s) + 3); DMA_V(min((s) + 2, NT - 1), (s) + 2); } } while (0)
    STAMP(2, 0);
    const unsigned cpf = code_pf(tid, 84);
    u32x16 qa, qb;
    { const char* qp = Q + ((size_t)h * 32 + (G >> 1)) * SLOT; const unsigned qo = hi * 1024 + ((G & 1) * 32 + r32) * 16;
      u32x4 q[8];
#define QLD(d) asm volatile("global_load_dwordx4 %0, %1, %2 offset:%c3" : "=&v"(q[d]) : "v"(qo + ((d) >> 1) * 4096), "s"(qp), "i"(((d) & 1) * 2048) : "memory")
      QLD(0); QLD(1); QLD(2); QLD(3); QLD(4); QLD(5); QLD(6); QLD(7);
#undef QLD
      if (dmaw) { DMA_K(0, 0); DMA_V(0, 0); DMA_K(1, 1); DMA_K(2, 2); DMA_V(1, 1); } else WAIT_V(0);
      asm volatile("s_waitcnt vmcnt(8)\n\ts_barrier" : "+v"(q[0]), "+v"(q[1]), "+v"(q[2]), "+v"(q[3]), "+v"(q[4]), "+v"(q[5]), "+v"(q[6]), "+v"(q[7]) :: "memory");
#pragma unroll
      for (int i = 0; i < 4; ++i)
#pragma unroll
          for (int e = 0; e < 4; ++e) { qa[4 * i + e] = q[i][e]; qb[4 * i + e] = q[4 + i][e]; }
    }
    STAMP(2, 1);
    CODE_PF_DONE(cpf);
    const unsigned kp0 = lds0 + LDS_K + hi * 1024 + r32 * 16, vp0 = lds0 + LDS_V + hi * 2048 + r32 * 16;
    f32x16 o0 = f32x16{}, o1 = f32x16{}, o2 = f32x16{}, o3 = f32x16{}, negm, cA, cB, P = f32x16{};
    u32x16 kfa, kfb;
    float l_reg = 0.f, mhat;
    {
        f32x16 s = f32x16{};
#pragma unroll
        for (int j = 0; j < 8; ++j) {
            const bf16x8 kf = lds_rd(lds + LDS_K + hi * 1024 + r32 * 16 + j * 2048); const u32x4 kw = __builtin_bit_cast(u32x4, kf);
#pragma unroll
            for (int e = 0; e < 4; ++e) { if (j < 4) kfa[4 * j + e] = kw[e]; else kfb[4 * (j - 4) + e] = kw[e]; }
            const u32x4 qw = j < 4 ? (u32x4){qa[4 * j], qa[4 * j + 1], qa[4 * j + 2], qa[4 * j + 3]} : (u32x4){qb[4 * j - 16], qb[4 * j - 15], qb[4 * j - 14], qb[4 * j - 13]};
            s = MFMA32(kf, __builtin_bit_cast(bf16x8, qw), s);
        }
        if (G == 0) cmask(s, r32, hi);
        mhat = halfmax(max16(s));
#pragma unroll
        for (int r = 0; r < 16; ++r) { negm[r] = -mhat; cB[r] = -INFINITY; cA[r] = -INFINITY; }
    }
    const float thr = 65536.0f;
    int t = 0; unsigned soff = 0;
    unsigned vk = kp0, vkn = kp0 + SLOT, vv = vp0, vvp = vp0;
#define K2_STATE [o0] "+&{v[0:15]}"(o0), [o1] "+&{v[16:31]}"(o1), [o2] "+&{v[32:47]}"(o2), [o3] "+&{v[48:63]}"(o3), [negm] "+&{v[96:111]}"(negm), [ca] "+&{v[112:127]}"(cA), [cb] "+&{v[128:143]}"(cB), \
        [p] "+&{v[144:159]}"(P), [kfa] "+&{v[168:183]}"(kfa), [kfb] "+&{v[184:199]}"(kfb), [vk] "+&{v232}"(vk), [vkn] "+&{v233}"(vkn), [vv] "+&{v234}"(vv), [vvp] "+&{v235}"(vvp), [l] "+&{v236}"(l_reg), [mh] "+&{v238}"(mhat)
#define K2_IN [qa] "{v[64:79]}"(qa), [qb] "{v[80:95]}"(qb), [thr] "s"(thr)
#define K2_CLOB "v160", "v161", "v162", "v163", "v164", "v165", "v166", "v167", "v200", "v201", "v202", "v203", "v204", "v205", "v206", "v207", "v208", "v209", "v210", "v211", "v212", "v213", "v214", "v215", \
        "v216", "v217", "v218", "v219", "v220", "v221", "v222", "v223", "v224", "v225", "v226", "v227", "v228", "v229", "v230", "v231", "v237", "v239", "v240", "v241", "vcc", "scc", "memory"
    const int nloop = G >> 1;
    asm volatile(K2_ASM : K2_STATE, [t] "+&s"(t), [soff] "+&s"(soff)
                 : K2_IN, [n] "s"(nloop), [odd] "s"(G & 1), [dmaw] "s"(dmaw), [ntm1] "s"(NT - 1), [khl] "s"((unsigned)(uintptr_t)Kh), [khh] "s"((unsigned)((uintptr_t)Kh >> 32)),
                   [vhl] "s"((unsigned)(uintptr_t)Vh), [vhh] "s"((unsigned)((uintptr_t)Vh >> 32)), [kdst] "s"(kdst), [vdst] "s"(vdst), [dvo] "v"(dvo), [kp0] "v"(kp0), [vp0] "v"(vp0), [dq] "v"(r32 - 4 * hi)
                 : K2_CLOB, "s60", "s61", "s62", "s63");
    STAMP(2, 2);
    {
        auto rr = __builtin_amdgcn_permlane32_swap(__float_as_uint(l_reg), __float_as_uint(l_reg), false, false);
        const float inv = 1.0f / (__uint_as_float(rr[0]) + __uint_as_float(rr[1]));
        char* op = CTX + ((size_t)((G >> 2) * 64 + 2 * h) * 8 + hi) * 2048 + ((G & 3) * 32 + r32) * 16;
#define K2_ST(o, db) do { _Pragma("unroll") for (int g = 0; g < 4; g += 2) { \
            const unsigned x0 = cvtpk(o[4 * g] * inv, o[4 * g + 1] * inv), x1 = cvtpk(o[4 * g + 2] * inv, o[4 * g + 3] * inv); \
            const unsigned y0 = cvtpk(o[4 * g + 4] * inv, o[4 * g + 5] * inv), y1 = cvtpk(o[4 * g + 6] * inv, o[4 * g + 7] * inv); \
            const auto r0 = __builtin_amdgcn_permlane32_swap(x0, y0, false, false), r1 = __builtin_amdgcn_permlane32_swap(x1, y1, false, false); \
            ST16(op + (size_t)(((db) >> 1) * 8 + 4 * ((db) & 1) + g) * 2048, ((u32x4){r0[0], r1[0], r0[1], r1[1]})); } } while (0)
        K2_ST(o0, 0); K2_ST(o1, 1); K2_ST(o2, 2); K2_ST(o3, 3);
    }
    WAITBAR(16); ++t;
    if (t < NT) { DMA_STEP(t); WAITBAR(16); ++t;
        for (; t < NT; ++t) { DMA_STEP(t); WAITBAR(8); } }
    STAMP(2, 3);
    WAIT_V(0);
    STAMP(2, 4);
}
}

namespace k3 {
constexpr int STG = 49152, LDS_BYTES = 3 * STG;
typedef __attribute__((address_space(1))) unsigned gu32;
#define K3_ASM \
    "s_mov_b64 s[60:61], %[a1]\n\t" \
    "s_mov_b64 s[62:63], %[b1]\n\t" \
    "s_mov_b32 s67, %[nl]\n\t" \
    "s_mov_b32 s68, 0\n\t" \
    "s_add_u32 m0, %[mA], s68\n\t" \
    "s_add_u32 s66, %[mB], s68\n\t" \
    "global_load_lds_dwordx4 %[va], s[60:61] offset:0\n\t" \
    "global_load_lds_dwordx4 %[va], s[60:61] offset:1024\n\t" \
    "global_load_lds_dwordx4 %[va], s[60:61] offset:2048\n\t" \
    "global_load_lds_dwordx4 %[va], s[60:61] offset:3072\n\t" \
    "s_mov_b32 m0, s66\n\t" \
    "s_add_u32 s64, s62, 0x10000\n\t" \
    "s_addc_u32 s65, s63, 0\n\t" \
    "global_load_lds_dwordx4 %[vb], s[62:63]\n\t" \
    "s_add_u32 m0, s66, 8192\n\t" \
    "s_add_u32 s60, s60, 0x4000\n\t" \
    "global_load_lds_dwordx4 %[vb], s[64:65]\n\t" \
    "s_addc_u32 s61, s61, 0\n\t" \
    "s_add_u32 s62, s62, 0x20000\n\t" \
    "s_addc_u32 s63, s63, 0\n\t" \
    "s_mov_b32 s68, 49152\n\t" \
    "s_add_u32 m0, %[mA], s68\n\t" \
    "s_add_u32 s66, %[mB], s68\n\t" \
    "global_load_lds_dwordx4 %[va], s[60:61] offset:0\n\t" \
    "global_load_lds_dwordx4 %[va], s[60:61] offset:1024\n\t" \
    "global_load_lds_dwordx4 %[va], s[60:61] offset:2048\n\t" \
    "global_load_lds_dwordx4 %[va], s[60:61] offset:3072\n\t" \
    "s_mov_b32 m0, s66\n\t" \
    "s_add_u32 s64, s62, 0x10000\n\t" \
    "s_addc_u32 s65, s63, 0\n\t" \
    "global_load_lds_dwordx4 %[vb], s[62:63]\n\t" \
    "s_add_u32 m0, s66, 8192\n\t" \
    "s_add_u32 s60, s60, 0x4000\n\t" \
    "global_load_lds_dwordx4 %[vb], s[64:65]\n\t" \
    "s_addc_u32 s61, s61, 0\n\t" \
    "s_add_u32 s62, s62, 0x20000\n\t" \
    "s_addc_u32 s63, s63, 0\n\t" \
    "s_mov_b32 s68, 98304\n\t" \
    "s_add_u32 m0, %[mA], s68\n\t" \
    "s_add_u32 s66, %[mB], s68\n\t" \
    "global_load_lds_dwordx4 %[va], s[60:61] offset:0\n\t" \
    "global_load_lds_dwordx4 %[va], s[60:61] offset:1024\n\t" \
    "global_load_lds_dwordx4 %[va], s[60:61] offset:2048\n\t" \
    "global_load_lds_dwordx4 %[va], s[60:61] offset:3072\n\t" \
    "s_mov_b32 m0, s66\n\t" \
    "s_add_u32 s64, s62, 0x10000\n\t" \
    "s_addc_u32 s65, s63, 0\n\t" \
    "global_load_lds_dwordx4 %[vb], s[62:63]\n\t" \
    "s_add_u32 m0, s66, 8192\n\t" \
    "s_add_u32 s60, s60, 0x4000\n\t" \
    "global_load_lds_dwordx4 %[vb], s[64:65]\n\t" \
    "s_addc_u32 s61, s61, 0\n\t" \
    "s_add_u32 s62, s62, 0x20000\n\t" \
    "s_addc_u32 s63, s63, 0\n\t" \
    "s_mov_b32 s68, 0\n\t" \
    "s_mov_b32 s69, 98304\n\t" \
    "v_mov_b32 v128, v134\n\t" \
    "v_mov_b32 v129, v135\n\t" \
    "v_mov_b32 v130, v136\n\t" \
    "v_add_u32 v131, 49152, v134\n\t" \
    "v_add_u32 v132, 49152, v135\n\t" \
    "v_add_u32 v133, 49152, v136\n\t" \
    "s_waitcnt vmcnt(12)\n\t" \
    "s_barrier\n\t" \
    "ds_read_b128 v[64:67], v128 offset:0\n\t" \
    "ds_read_b64_tr_b16 v[80:81], v129 offset:0\n\t" \
    "ds_read_b64_tr_b16 v[82:83], v129 offset:256\n\t" \
    "ds_read_b64_tr_b16 v[84:85], v130 offset:0\n\t" \
    "ds_read_b64_tr_b16 v[86:87], v130 offset:256\n\t" \
    "ds_read_b128 v[68:71], v128 offset:256\n\t" \
    "ds_read_b64_tr_b16 v[88:89], v129 offset:512\n\t" \
    "ds_read_b64_tr_b16 v[90:91], v129 offset:768\n\t" \
    "ds_read_b64_tr_b16 v[92:93], v130 offset:512\n\t" \
    "ds_read_b64_tr_b16 v[94:95], v130 offset:768\n\t" \
    "ds_read_b128 v[72:75], v128 offset:512\n\t" \
    "ds_read_b128 v[76:79], v128 offset:768\n\t" \
    "LOOP_%=:\n\t" \
    "s_add_u32 m0, %[mA], s68\n\t" \
    "s_add_u32 s66, %[mB], s68\n\t" \
    "global_load_lds_dwordx4 %[va], s[60:61] offset:0\n\t" \
    "global_load_lds_dwordx4 %[va], s[60:61] offset:1024\n\t" \
    "global_load_lds_dwordx4 %[va], s[60:61] offset:2048\n\t" \
    "global_load_lds_dwordx4 %[va], s[60:61] offset:3072\n\t" \
    "s_mov_b32 m0, s66\n\t" \
    "s_add_u32 s64, s62, 0x10000\n\t" \
    "s_addc_u32 s65, s63, 0\n\t" \
    "global_load_lds_dwordx4 %[vb], s[62:63]\n\t" \
    "s_add_u32 m0, s66, 8192\n\t" \
    "s_add_u32 s60, s60, 0x4000\n\t" \
    "global_load_lds_dwordx4 %[vb], s[64:65]\n\t" \
    "s_addc_u32 s61, s61, 0\n\t" \
    "s_add_u32 s62, s62, 0x20000\n\t" \
    "s_addc_u32 s63, s63, 0\n\t" \
    "v_mfma_f32_16x16x32_bf16 v[0:3], v[112:115], v[96:99], v[0:3]\n\t" \
    "ds_read_b128 v[64:67], v131 offset:0\n\t" \
    "ds_read_b64_tr_b16 v[80:81], v132 offset:0\n\t" \
    "v_mfma_f32_16x16x32_bf16 v[4:7], v[116:119], v[96:99], v[4:7]\n\t" \
    "ds_read_b64_tr_b16 v[82:83], v132 offset:256\n\t" \
    "ds_read_b64_tr_b16 v[84:85], v133 offset:0\n\t" \
    "v_mfma_f32_16x16x32_bf16 v[8:11], v[120:123], v[96:99], v[8:11]\n\t" \
    "ds_read_b64_tr_b16 v[86:87], v133 offset:256\n\t" \
    "ds_read_b128 v[68:71], v131 offset:256\n\t" \
    "v_mfma_f32_16x16x32_bf16 v[12:15], v[124:127], v[96:99], v[12:15]\n\t" \
    "ds_read_b64_tr_b16 v[88:89], v132 offset:512\n\t" \
    "ds_read_b64_tr_b16 v[90:91], v132 offset:768\n\t" \
    "v_mfma_f32_16x16x32_bf16 v[16:19], v[112:115], v[100:103], v[16:19]\n\t" \
    "ds_read_b64_tr_b16 v[92:93], v133 offset:512\n\t" \
    "ds_read_b64_tr_b16 v[94:95], v133 offset:768\n\t" \
    "v_mfma_f32_16x16x32_bf16 v[20:23], v[116:119], v[100:103], v[20:23]\n\t" \
    "ds_read_b128 v[72:75], v131 offset:512\n\t" \
    "ds_read_b128 v[76:79], v131 offset:768\n\t" \
    "v_mfma_f32_16x16x32_bf16 v[24:27], v[120:123], v[100:103], v[24:27]\n\t" \
    "s_add_u32 m0, %[mA], s68\n\t" \
    "s_add_u32 s66, %[mB], s68\n\t" \
    "global_load_lds_dwordx4 %[va], s[60:61] offset:0\n\t" \
    "v_mfma_f32_16x16x32_bf16 v[28:31], v[124:127], v[100:103], v[28:31]\n\t" \
    "global_load_lds_dwordx4 %[va], s[60:61] offset:1024\n\t" \
    "global_load_lds_dwordx4 %[va], s[60:61] offset:2048\n\t" \
    "global_load_lds_dwordx4 %[va], s[60:61] offset:3072\n\t" \
    "v_mfma_f32_16x16x32_bf16 v[32:35], v[112:115], v[104:107], v[32:35]\n\t" \
    "s_mov_b32 m0, s66\n\t" \
    "s_add_u32 s64, s62, 0x10000\n\t" \
    "s_addc_u32 s65, s63, 0\n\t" \
    "v_mfma_f32_16x16x32_bf16 v[36:39], v[116:119], v[104:107], v[36:39]\n\t" \
    "global_load_lds_dwordx4 %[vb], s[62:63]\n\t" \
    "s_add_u32 m0, s66, 8192\n\t" \
    "s_add_u32 s60, s60, 0x4000\n\t" \
    "v_mfma_f32_16x16x32_bf16 v[40:43], v[120:123], v[104:107], v[40:43]\n\t" \
    "global_load_lds_dwordx4 %[vb], s[64:65]\n\t" \
    "s_addc_u32 s61, s61, 0\n\t" \
    "s_add_u32 s62, s62, 0x20000\n\t" \
    "v_mfma_f32_16x16x32_bf16 v[44:47], v[124:127], v[104:107], v[44:47]\n\t" \
    "s_addc_u32 s63, s63, 0\n\t" \
    "v_mov_b32 v128, v131\n\t" \
    "v_mov_b32 v129, v132\n\t" \
    "v_mfma_f32_16x16x32_bf16 v[48:51], v[112:115], v[108:111], v[48:51]\n\t" \
    "v_mov_b32 v130, v133\n\t" \
    "v_add_u32 v131, s69, v134\n\t" \
    "v_add_u32 v132, s69, v135\n\t" \
    "v_add_u32 v133, s69, v136\n\t" \
    "v_mfma_f32_16x16x32_bf16 v[52:55], v[116:119], v[108:111], v[52:55]\n\t" \
    "s_add_u32 s68, s68, 49152\n\t" \
    "s_cmp_eq_u32 s68, 147456\n\t" \
    "s_cselect_b32 s68, 0, s68\n\t" \
    "s_add_u32 s69, s69, 49152\n\t" \
    "v_mfma_f32_16x16x32_bf16 v[56:59], v[120:123], v[108:111], v[56:59]\n\t" \
    "s_cmp_eq_u32 s69, 147456\n\t" \
    "s_cselect_b32 s69, 0, s69\n\t" \
    "v_mfma_f32_16x16x32_bf16 v[60:63], v[124:127], v[108:111], v[60:63]\n\t" \
    "s_sub_u32 s67, s67, 1\n\t" \
    "s_cmp_lg_u32 s67, 0\n\t" \
    "s_cbranch_scc1 LOOP_%=\n\t" \
    "s_waitcnt lgkmcnt(0)\n\t" \
    "v_mfma_f32_16x16x32_bf16 v[0:3], v[80:83], v[64:67], v[0:3]\n\t" \
    "ds_read_b128 v[96:99], v128 offset:8192\n\t" \
    "ds_read_b64_tr_b16 v[112:113], v129 offset:8192\n\t" \
    "v_mfma_f32_16x16x32_bf16 v[4:7], v[84:87], v[64:67], v[4:7]\n\t" \
    "ds_read_b64_tr_b16 v[114:115], v129 offset:8448\n\t" \
    "ds_read_b64_tr_b16 v[116:117], v130 offset:8192\n\t" \
    "v_mfma_f32_16x16x32_bf16 v[8:11], v[88:91], v[64:67], v[8:11]\n\t" \
    "ds_read_b64_tr_b16 v[118:119], v130 offset:8448\n\t" \
    "ds_read_b128 v[100:103], v128 offset:8448\n\t" \
    "v_mfma_f32_16x16x32_bf16 v[12:15], v[92:95], v[64:67], v[12:15]\n\t" \
    "ds_read_b64_tr_b16 v[120:121], v129 offset:8704\n\t" \
    "ds_read_b64_tr_b16 v[122:123], v129 offset:8960\n\t" \
    "v_mfma_f32_16x16x32_bf16 v[16:19], v[80:83], v[68:71], v[16:19]\n\t" \
    "ds_read_b64_tr_b16 v[124:125], v130 offset:8704\n\t" \
    "ds_read_b64_tr_b16 v[126:127], v130 offset:8960\n\t" \
    "v_mfma_f32_16x16x32_bf16 v[20:23], v[84:87], v[68:71], v[20:23]\n\t" \
    "ds_read_b128 v[104:107], v128 offset:8704\n\t" \
    "ds_read_b128 v[108:111], v128 offset:8960\n\t" \
    "v_mfma_f32_16x16x32_bf16 v[24:27], v[88:91], v[68:71], v[24:27]\n\t" \
    "v_mfma_f32_16x16x32_bf16 v[28:31], v[92:95], v[68:71], v[28:31]\n\t" \
    "v_mfma_f32_16x16x32_bf16 v[32:35], v[80:83], v[72:75], v[32:35]\n\t" \
    "v_mfma_f32_16x16x32_bf16 v[36:39], v[84:87], v[72:75], v[36:39]\n\t" \
    "v_mfma_f32_16x16x32_bf16 v[40:43], v[88:91], v[72:75], v[40:43]\n\t" \
    "v_mfma_f32_16x16x32_bf16 v[44:47], v[92:95], v[72:75], v[44:47]\n\t" \
    "v_mfma_f32_16x16x32_bf16 v[48:51], v[80:83], v[76:79], v[48:51]\n\t" \
    "v_mfma_f32_16x16x32_bf16 v[52:55], v[84:87], v[76:79], v[52:55]\n\t" \
    "v_mfma_f32_16x16x32_bf16 v[56:59], v[88:91], v[76:79], v[56:59]\n\t" \
    "v_mfma_f32_16x16x32_bf16 v[60:63], v[92:95], v[76:79], v[60:63]\n\t" \
    "s_waitcnt vmcnt(6) lgkmcnt(0)\n\t" \
    "s_barrier\n\t" \
    "v_mfma_f32_16x16x32_bf16 v[0:3], v[112:115], v[96:99], v[0:3]\n\t" \
    "ds_read_b128 v[64:67], v131 offset:0\n\t" \
    "ds_read_b64_tr_b16 v[80:81], v132 offset:0\n\t" \
    "v_mfma_f32_16x16x32_bf16 v[4:7], v[116:119], v[96:99], v[4:7]\n\t" \
    "ds_read_b64_tr_b16 v[82:83], v132 offset:256\n\t" \
    "ds_read_b64_tr_b16 v[84:85], v133 offset:0\n\t" \
    "v_mfma_f32_16x16x32_bf16 v[8:11], v[120:123], v[96:99], v[8:11]\n\t" \
    "ds_read_b64_tr_b16 v[86:87], v133 offset:256\n\t" \
    "ds_read_b128 v[68:71], v131 offset:256\n\t" \
    "v_mfma_f32_16x16x32_bf16 v[12:15], v[124:127], v[96:99], v[12:15]\n\t" \
    "ds_read_b64_tr_b16 v[88:89], v132 offset:512\n\t" \
    "ds_read_b64_tr_b16 v[90:91], v132 offset:768\n\t" \
    "v_mfma_f32_16x16x32_bf16 v[16:19], v[112:115], v[100:103], v[16:19]\n\t" \
    "ds_read_b64_tr_b16 v[92:93], v133 offset:512\n\t" \
    "ds_read_b64_tr_b16 v[94:95], v133 offset:768\n\t" \
    "v_mfma_f32_16x16x32_bf16 v[20:23], v[116:119], v[100:103], v[20:23]\n\t" \
    "ds_read_b128 v[72:75], v131 offset:512\n\t" \
    "ds_read_b128 v[76:79], v131 offset:768\n\t" \
    "v_mfma_f32_16x16x32_bf16 v[24:27], v[120:123], v[100:103], v[24:27]\n\t" \
    "v_mov_b32 v128, v131\n\t" \
    "v_mov_b32 v129, v132\n\t" \
    "v_mov_b32 v130, v133\n\t" \
    "v_mfma_f32_16x16x32_bf16 v[28:31], v[124:127], v[100:103], v[28:31]\n\t" \
    "v_add_u32 v131, s69, v134\n\t" \
    "v_add_u32 v132, s69, v135\n\t" \
    "v_add_u32 v133, s69, v136\n\t" \
    "v_mfma_f32_16x16x32_bf16 v[32:35], v[112:115], v[104:107], v[32:35]\n\t" \
    "s_add_u32 s68, s68, 49152\n\t" \
    "s_cmp_eq_u32 s68, 147456\n\t" \
    "s_cselect_b32 s68, 0, s68\n\t" \
    "v_mfma_f32_16x16x32_bf16 v[36:39], v[116:119], v[104:107], v[36:39]\n\t" \
    "s_add_u32 s69, s69, 49152\n\t" \
    "s_cmp_eq_u32 s69, 147456\n\t" \
    "s_cselect_b32 s69, 0, s69\n\t" \
    "v_mfma_f32_16x16x32_bf16 v[40:43], v[120:123], v[104:107], v[40:43]\n\t" \
    "v_mfma_f32_16x16x32_bf16 v[44:47], v[124:127], v[104:107], v[44:47]\n\t" \
    "v_mfma_f32_16x16x32_bf16 v[48:51], v[112:115], v[108:111], v[48:51]\n\t" \
    "v_mfma_f32_16x16x32_bf16 v[52:55], v[116:119], v[108:111], v[52:55]\n\t" \
    "v_mfma_f32_16x16x32_bf16 v[56:59], v[120:123], v[108:111], v[56:59]\n\t" \
    "v_mfma_f32_16x16x32_bf16 v[60:63], v[124:127], v[108:111], v[60:63]\n\t" \
    "s_waitcnt lgkmcnt(0)\n\t" \
    "v_mfma_f32_16x16x32_bf16 v[0:3], v[80:83], v[64:67], v[0:3]\n\t" \
    "ds_read_b128 v[96:99], v128 offset:8192\n\t" \
    "ds_read_b64_tr_b16 v[112:113], v129 offset:8192\n\t" \
    "v_mfma_f32_16x16x32_bf16 v[4:7], v[84:87], v[64:67], v[4:7]\n\t" \
    "ds_read_b64_tr_b16 v[114:115], v129 offset:8448\n\t" \
    "ds_read_b64_tr_b16 v[116:117], v130 offset:8192\n\t" \
    "v_mfma_f32_16x16x32_bf16 v[8:11], v[88:91], v[64:67], v[8:11]\n\t" \
    "ds_read_b64_tr_b16 v[118:119], v130 offset:8448\n\t" \
    "ds_read_b128 v[100:103], v128 offset:8448\n\t" \
    "v_mfma_f32_16x16x32_bf16 v[12:15], v[92:95], v[64:67], v[12:15]\n\t" \
    "ds_read_b64_tr_b16 v[120:121], v129 offset:8704\n\t" \
    "ds_read_b64_tr_b16 v[122:123], v129 offset:8960\n\t" \
    "v_mfma_f32_16x16x32_bf16 v[16:19], v[80:83], v[68:71], v[16:19]\n\t" \
    "ds_read_b64_tr_b16 v[124:125], v130 offset:8704\n\t" \
    "ds_read_b64_tr_b16 v[126:127], v130 offset:8960\n\t" \
    "v_mfma_f32_16x16x32_bf16 v[20:23], v[84:87], v[68:71], v[20:23]\n\t" \
    "ds_read_b128 v[104:107], v128 offset:8704\n\t" \
    "ds_read_b128 v[108:111], v128 offset:8960\n\t" \
    "v_mfma_f32_16x16x32_bf16 v[24:27], v[88:91], v[68:71], v[24:27]\n\t" \
    "v_mfma_f32_16x16x32_bf16 v[28:31], v[92:95], v[68:71], v[28:31]\n\t" \
    "v_mfma_f32_16x16x32_bf16 v[32:35], v[80:83], v[72:75], v[32:35]\n\t" \
    "v_mfma_f32_16x16x32_bf16 v[36:39], v[84:87], v[72:75], v[36:39]\n\t" \
    "v_mfma_f32_16x16x32_bf16 v[40:43], v[88:91], v[72:75], v[40:43]\n\t" \
    "v_mfma_f32_16x16x32_bf16 v[44:47], v[92:95], v[72:75], v[44:47]\n\t" \
    "v_mfma_f32_16x16x32_bf16 v[48:51], v[80:83], v[76:79], v[48:51]\n\t" \
    "v_mfma_f32_16x16x32_bf16 v[52:55], v[84:87], v[76:79], v[52:55]\n\t" \
    "v_mfma_f32_16x16x32_bf16 v[56:59], v[88:91], v[76:79], v[56:59]\n\t" \
    "v_mfma_f32_16x16x32_bf16 v[60:63], v[92:95], v[76:79], v[60:63]\n\t" \
    "s_waitcnt vmcnt(0) lgkmcnt(0)\n\t" \
    "s_barrier\n\t" \
    "v_mfma_f32_16x16x32_bf16 v[0:3], v[112:115], v[96:99], v[0:3]\n\t" \
    "ds_read_b128 v[64:67], v131 offset:0\n\t" \
    "ds_read_b64_tr_b16 v[80:81], v132 offset:0\n\t" \
    "v_mfma_f32_16x16x32_bf16 v[4:7], v[116:119], v[96:99], v[4:7]\n\t" \
    "ds_read_b64_tr_b16 v[82:83], v132 offset:256\n\t" \
    "ds_read_b64_tr_b16 v[84:85], v133 offset:0\n\t" \
    "v_mfma_f32_16x16x32_bf16 v[8:11], v[120:123], v[96:99], v[8:11]\n\t" \
    "ds_read_b64_tr_b16 v[86:87], v133 offset:256\n\t" \
    "ds_read_b128 v[68:71], v131 offset:256\n\t" \
    "v_mfma_f32_16x16x32_bf16 v[12:15], v[124:127], v[96:99], v[12:15]\n\t" \
    "ds_read_b64_tr_b16 v[88:89], v132 offset:512\n\t" \
    "ds_read_b64_tr_b16 v[90:91], v132 offset:768\n\t" \
    "v_mfma_f32_16x16x32_bf16 v[16:19], v[112:115], v[100:103], v[16:19]\n\t" \
    "ds_read_b64_tr_b16 v[92:93], v133 offset:512\n\t" \
    "ds_read_b64_tr_b16 v[94:95], v133 offset:768\n\t" \
    "v_mfma_f32_16x16x32_bf16 v[20:23], v[116:119], v[100:103], v[20:23]\n\t" \
    "ds_read_b128 v[72:75], v131 offset:512\n\t" \
    "ds_read_b128 v[76:79], v131 offset:768\n\t" \
    "v_mfma_f32_16x16x32_bf16 v[24:27], v[120:123], v[100:103], v[24:27]\n\t" \
    "v_mov_b32 v128, v131\n\t" \
    "v_mov_b32 v129, v132\n\t" \
    "v_mov_b32 v130, v133\n\t" \
    "v_mfma_f32_16x16x32_bf16 v[28:31], v[124:127], v[100:103], v[28:31]\n\t" \
    "v_add_u32 v131, s69, v134\n\t" \
    "v_add_u32 v132, s69, v135\n\t" \
    "v_add_u32 v133, s69, v136\n\t" \
    "v_mfma_f32_16x16x32_bf16 v[32:35], v[112:115], v[104:107], v[32:35]\n\t" \
    "s_add_u32 s68, s68, 49152\n\t" \
    "s_cmp_eq_u32 s68, 147456\n\t" \
    "s_cselect_b32 s68, 0, s68\n\t" \
    "v_mfma_f32_16x16x32_bf16 v[36:39], v[116:119], v[104:107], v[36:39]\n\t" \
    "s_add_u32 s69, s69, 49152\n\t" \
    "s_cmp_eq_u32 s69, 147456\n\t" \
    "s_cselect_b32 s69, 0, s69\n\t" \
    "v_mfma_f32_16x16x32_bf16 v[40:43], v[120:123], v[104:107], v[40:43]\n\t" \
    "v_mfma_f32_16x16x32_bf16 v[44:47], v[124:127], v[104:107], v[44:47]\n\t" \
    "v_mfma_f32_16x16x32_bf16 v[48:51], v[112:115], v[108:111], v[48:51]\n\t" \
    "v_mfma_f32_16x16x32_bf16 v[52:55], v[116:119], v[108:111], v[52:55]\n\t" \
    "v_mfma_f32_16x16x32_bf16 v[56:59], v[120:123], v[108:111], v[56:59]\n\t" \
    "v_mfma_f32_16x16x32_bf16 v[60:63], v[124:127], v[108:111], v[60:63]\n\t" \
    "s_waitcnt lgkmcnt(0)\n\t" \
    "v_mfma_f32_16x16x32_bf16 v[0:3], v[80:83], v[64:67], v[0:3]\n\t" \
    "ds_read_b128 v[96:99], v128 offset:8192\n\t" \
    "ds_read_b64_tr_b16 v[112:113], v129 offset:8192\n\t" \
    "v_mfma_f32_16x16x32_bf16 v[4:7], v[84:87], v[64:67], v[4:7]\n\t" \
    "ds_read_b64_tr_b16 v[114:115], v129 offset:8448\n\t" \
    "ds_read_b64_tr_b16 v[116:117], v130 offset:8192\n\t" \
    "v_mfma_f32_16x16x32_bf16 v[8:11], v[88:91], v[64:67], v[8:11]\n\t" \
    "ds_read_b64_tr_b16 v[118:119], v130 offset:8448\n\t" \
    "ds_read_b128 v[100:103], v128 offset:8448\n\t" \
    "v_mfma_f32_16x16x32_bf16 v[12:15], v[92:95], v[64:67], v[12:15]\n\t" \
    "ds_read_b64_tr_b16 v[120:121], v129 offset:8704\n\t" \
    "ds_read_b64_tr_b16 v[122:123], v129 offset:8960\n\t" \
    "v_mfma_f32_16x16x32_bf16 v[16:19], v[80:83], v[68:71], v[16:19]\n\t" \
    "ds_read_b64_tr_b16 v[124:125], v130 offset:8704\n\t" \
    "ds_read_b64_tr_b16 v[126:127], v130 offset:8960\n\t" \
    "v_mfma_f32_16x16x32_bf16 v[20:23], v[84:87], v[68:71], v[20:23]\n\t" \
    "ds_read_b128 v[104:107], v128 offset:8704\n\t" \
    "ds_read_b128 v[108:111], v128 offset:8960\n\t" \
    "v_mfma_f32_16x16x32_bf16 v[24:27], v[88:91], v[68:71], v[24:27]\n\t" \
    "v_mfma_f32_16x16x32_bf16 v[28:31], v[92:95], v[68:71], v[28:31]\n\t" \
    "v_mfma_f32_16x16x32_bf16 v[32:35], v[80:83], v[72:75], v[32:35]\n\t" \
    "v_mfma_f32_16x16x32_bf16 v[36:39], v[84:87], v[72:75], v[36:39]\n\t" \
    "v_mfma_f32_16x16x32_bf16 v[40:43], v[88:91], v[72:75], v[40:43]\n\t" \
    "v_mfma_f32_16x16x32_bf16 v[44:47], v[92:95], v[72:75], v[44:47]\n\t" \
    "v_mfma_f32_16x16x32_bf16 v[48:51], v[80:83], v[76:79], v[48:51]\n\t" \
    "v_mfma_f32_16x16x32_bf16 v[52:55], v[84:87], v[76:79], v[52:55]\n\t" \
    "v_mfma_f32_16x16x32_bf16 v[56:59], v[88:91], v[76:79], v[56:59]\n\t" \
    "v_mfma_f32_16x16x32_bf16 v[60:63], v[92:95], v[76:79], v[60:63]\n\t" \
    "s_waitcnt vmcnt(0) lgkmcnt(0)\n\t" \
    "s_barrier\n\t" \
    "v_mfma_f32_16x16x32_bf16 v[0:3], v[112:115], v[96:99], v[0:3]\n\t" \
    "v_mov_b32 v128, v131\n\t" \
    "v_mov_b32 v129, v132\n\t" \
    "v_mfma_f32_16x16x32_bf16 v[4:7], v[116:119], v[96:99], v[4:7]\n\t" \
    "v_mov_b32 v130, v133\n\t" \
    "v_add_u32 v131, s69, v134\n\t" \
    "v_mfma_f32_16x16x32_bf16 v[8:11], v[120:123], v[96:99], v[8:11]\n\t" \
    "v_add_u32 v132, s69, v135\n\t" \
    "v_add_u32 v133, s69, v136\n\t" \
    "v_mfma_f32_16x16x32_bf16 v[12:15], v[124:127], v[96:99], v[12:15]\n\t" \
    "s_add_u32 s68, s68, 49152\n\t" \
    "s_cmp_eq_u32 s68, 147456\n\t" \
    "v_mfma_f32_16x16x32_bf16 v[16:19], v[112:115], v[100:103], v[16:19]\n\t" \
    "s_cselect_b32 s68, 0, s68\n\t" \
    "s_add_u32 s69, s69, 49152\n\t" \
    "v_mfma_f32_16x16x32_bf16 v[20:23], v[116:119], v[100:103], v[20:23]\n\t" \
    "s_cmp_eq_u32 s69, 147456\n\t" \
    "s_cselect_b32 s69, 0, s69\n\t" \
    "v_mfma_f32_16x16x32_bf16 v[24:27], v[120:123], v[100:103], v[24:27]\n\t" \
    "v_mfma_f32_16x16x32_bf16 v[28:31], v[124:127], v[100:103], v[28:31]\n\t" \
    "v_mfma_f32_16x16x32_bf16 v[32:35], v[112:115], v[104:107], v[32:35]\n\t" \
    "v_mfma_f32_16x16x32_bf16 v[36:39], v[116:119], v[104:107], v[36:39]\n\t" \
    "v_mfma_f32_16x16x32_bf16 v[40:43], v[120:123], v[104:107], v[40:43]\n\t" \
    "v_mfma_f32_16x16x32_bf16 v[44:47], v[124:127], v[104:107], v[44:47]\n\t" \
    "v_mfma_f32_16x16x32_bf16 v[48:51], v[112:115], v[108:111], v[48:51]\n\t" \
    "v_mfma_f32_16x16x32_bf16 v[52:55], v[116:119], v[108:111], v[52:55]\n\t" \
    "v_mfma_f32_16x16x32_bf16 v[56:59], v[120:123], v[108:111], v[56:59]\n\t" \
    "v_mfma_f32_16x16x32_bf16 v[60:63], v[124:127], v[108:111], v[60:63]\n\t" \
    "s_nop 15\n\t"
DI u32x4 pack8(const f32x16& c, int n) { return (u32x4){cvtpk(c[4 * n], c[4 * n + 1]), cvtpk(c[4 * n + 2], c[4 * n + 3]), cvtpk(c[4 * n + 4], c[4 * n + 5]), cvtpk(c[4 * n + 6], c[4 * n + 7])}; }
DI void add8(f32x16& c, int n, u32x4 w) {
#pragma unroll
    for (int i = 0; i < 4; ++i) { c[4 * n + 2 * i] += bflo(w[i]); c[4 * n + 2 * i + 1] += bfhi(w[i]); }
}
DI void poll(unsigned* flags, int first, int count, int lane) {
    gu32* f = (gu32*)(flags + (first + (lane % count)) * 32);
    for (int spin = 0; spin < (1 << 20); ++spin) {
        if (__all(__hip_atomic_load(f, __ATOMIC_RELAXED, __HIP_MEMORY_SCOPE_AGENT) == 1u)) break;
        __builtin_amdgcn_s_sleep(1);
    }
}
__global__ __launch_bounds__(512, 2) void k3_out(const char* __restrict__ ctx, const char* __restrict__ H, const bf16_t* __restrict__ wo, const bf16_t* __restrict__ w2,
                                                 const bf16_t* __restrict__ b2, const bf16_t* __restrict__ x, float* part, unsigned* flags, bf16_t* __restrict__ out) {
    extern __shared__ __attribute__((aligned(16))) unsigned char smem[];
    const int tid = threadIdx.x, wid = __builtin_amdgcn_readfirstlane(tid >> 6), lane = tid & 63, wr = wid >> 1, wc = wid & 1, fr = lane & 15, fq = lane >> 4;
    const int xcd = blockIdx.x & 7, idx = blockIdx.x >> 3, sl = xcd >> 1, mt = 4 * (xcd & 1) + (idx >> 3), ntile = idx & 7, tile = mt * 8 + ntile;
    const unsigned lds0 = (unsigned)(uintptr_t)smem;
    const size_t aw = (size_t)(wid & 3) * 4096, bw = (size_t)ntile * 256 + (size_t)(8 * (wid >> 1)) * (DM * 2) + 128 * (wid & 1);
    const int kt0 = sl < 2 ? 32 * sl : 28 * (sl - 2);
    const char* a1 = (sl < 2 ? ctx + (size_t)(2 * mt + (wid >> 2)) * 64 * 16384 : H + (size_t)(2 * mt + (wid >> 2)) * 56 * 16384) + aw + (size_t)kt0 * 16384;
    const char* b1 = (const char*)(sl < 2 ? wo : w2) + bw + (size_t)kt0 * (64 * DM * 2);
    const int nl = (sl < 2 ? 32 : 28) - 3;
    const unsigned va = lane * 16, vb = ((lane & 31) >> 2) * (DM * 2) + (32 * (lane >> 5) + 8 * ((lane & 3) ^ (wid & 2))) * 2;
    const unsigned mA = lds0 + (wid >> 2) * 16384 + (wid & 3) * 4096, mB = lds0 + 32768 + wid * 1024;
    const unsigned ra = lds0 + (wr >> 1) * 16384 + fq * 2048 + ((wr & 1) * 64 + fr) * 16;
    const unsigned rb0 = lds0 + 32768 + fq * 2048 + wc * 1024 + (fr >> 2) * 64 + (fq & 1) * 32 + (fr & 3) * 8, rb1 = rb0 ^ 32;
    f32x16 c0 = f32x16{}, c1 = f32x16{}, c2 = f32x16{}, c3 = f32x16{};
    u32x2 xb[4][4], bb[4];
    if (sl == 2) {
#pragma unroll
        for (int n = 0; n < 4; ++n) { bb[n] = *(const u32x2*)(b2 + ntile * 128 + wc * 64 + fq * 4 + n * 16);
#pragma unroll
            for (int m = 0; m < 4; ++m) xb[m][n] = *(const u32x2*)(x + (size_t)(mt * 256 + wr * 64 + m * 16 + fr) * DM + ntile * 128 + wc * 64 + fq * 4 + n * 16); }
    } else {
#pragma unroll
        for (int n = 0; n < 4; ++n) { bb[n] = (u32x2){0u, 0u};
#pragma unroll
            for (int m = 0; m < 4; ++m) xb[m][n] = (u32x2){0u, 0u}; }
    }
    STAMP(3, 0);
    const unsigned cpf = code_pf(tid, 52);
    asm volatile(K3_ASM
                 : "+&{v[0:15]}"(c0), "+&{v[16:31]}"(c1), "+&{v[32:47]}"(c2), "+&{v[48:63]}"(c3)
                 : "{v134}"(ra), "{v135}"(rb0), "{v136}"(rb1),
                   [va] "v"(va), [vb] "v"(vb), [a1] "s"(a1), [b1] "s"(b1), [nl] "s"(nl), [mA] "s"(mA), [mB] "s"(mB)
                 : "v64", "v65", "v66", "v67", "v68", "v69", "v70", "v71", "v72", "v73", "v74", "v75", "v76", "v77", "v78", "v79", "v80", "v81", "v82", "v83", "v84", "v85", "v86", "v87", "v88", "v89", "v90", "v91", "v92", "v93", "v94", "v95", "v96", "v97", "v98", "v99", "v100", "v101", "v102", "v103", "v104", "v105", "v106", "v107", "v108", "v109", "v110", "v111", "v112", "v113", "v114", "v115", "v116", "v117", "v118", "v119", "v120", "v121", "v122", "v123", "v124", "v125", "v126", "v127", "v128", "v129", "v130", "v131", "v132", "v133", "s60", "s61", "s62", "s63", "s64", "s65", "s66", "s67", "s68", "s69", "scc", "memory");
    STAMP(3, 1);
    CODE_PF_DONE(cpf);
    const auto rs = __builtin_amdgcn_make_buffer_rsrc((void*)((char*)part + (size_t)tile * 4 * 65536), 0, 4 * 65536, 0x00020000);
#define K3_SEND(c, m) do { __builtin_amdgcn_raw_buffer_store_b128(pack8(c, 0), rs, tid * 16 + (m) * 16384 + sl * 65536, 0, 16); __builtin_amdgcn_raw_buffer_store_b128(pack8(c, 2), rs, tid * 16 + (m) * 16384 + 8192 + sl * 65536, 0, 16); } while (0)
#define K3_GET(src, m, p) __builtin_amdgcn_raw_buffer_load_b128(rs, tid * 16 + (m) * 16384 + (p) * 8192, (src) * 65536, 16)
    gu32* myflag = (gu32*)(flags + (tile * 4 + sl) * 32);
    if (sl >= 2) {
#define K3_RES(c, m) do { _Pragma("unroll") for (int n = 0; n < 4; ++n) { \
            c[4 * n] += bflo(xb[m][n][0]) + bflo(bb[n][0]); c[4 * n + 1] += bfhi(xb[m][n][0]) + bfhi(bb[n][0]); c[4 * n + 2] += bflo(xb[m][n][1]) + bflo(bb[n][1]); c[4 * n + 3] += bfhi(xb[m][n][1]) + bfhi(bb[n][1]); } } while (0)
        K3_RES(c0, 0); K3_RES(c1, 1); K3_RES(c2, 2); K3_RES(c3, 3);
        K3_SEND(c0, 0); K3_SEND(c1, 1); K3_SEND(c2, 2); K3_SEND(c3, 3);
        WAIT_V(0);
        __syncthreads();
        if (tid == 0) __hip_atomic_store(myflag, 1u, __ATOMIC_RELAXED, __HIP_MEMORY_SCOPE_AGENT);
        STAMP(3, 2);
        return;
    }
    f32x16 f0, f1;
    if (sl == 0) { K3_SEND(c2, 2); K3_SEND(c3, 3); f0 = c0; f1 = c1; } else { K3_SEND(c0, 0); K3_SEND(c1, 1); f0 = c2; f1 = c3; }
    const int m0 = 2 * sl;
    WAIT_V(0);
    __syncthreads();
    if (tid == 0) __hip_atomic_store(myflag, 1u, __ATOMIC_RELAXED, __HIP_MEMORY_SCOPE_AGENT);
    STAMP(3, 2);
    if (wid == 0) poll(flags, tile * 4, 4, lane);
    asm volatile("" ::: "memory"); BAR(); asm volatile("" ::: "memory");
    STAMP(3, 3);
    u32x4 pa[3][2][2];
#pragma unroll
    for (int k = 0; k < 3; ++k)
#pragma unroll
        for (int m = 0; m < 2; ++m)
#pragma unroll
            for (int p = 0; p < 2; ++p) pa[k][m][p] = K3_GET(k == 0 ? 1 - sl : 1 + k, m0 + m, p);
#pragma unroll
    for (int k = 0; k < 3; ++k) { add8(f0, 0, pa[k][0][0]); add8(f0, 2, pa[k][0][1]); add8(f1, 0, pa[k][1][0]); add8(f1, 2, pa[k][1][1]); }
    const int row = mt * 256 + wr * 64 + m0 * 16 + fr;
    const int cg = ((fq & 1) << 1) | (fq >> 1);
    bf16_t* orow = out + (size_t)row * DM + ntile * 128 + wc * 64 + cg * 8;
#define K3_F4(c, n) (f32x4){c[4 * (n)], c[4 * (n) + 1], c[4 * (n) + 2], c[4 * (n) + 3]}
    *(u32x4*)(orow) = k1::pack_swap16(K3_F4(f0, 0), K3_F4(f0, 1)); *(u32x4*)(orow + 32) = k1::pack_swap16(K3_F4(f0, 2), K3_F4(f0, 3));
    *(u32x4*)(orow + 16 * DM) = k1::pack_swap16(K3_F4(f1, 0), K3_F4(f1, 1)); *(u32x4*)(orow + 16 * DM + 32) = k1::pack_swap16(K3_F4(f1, 2), K3_F4(f1, 3));
    STAMP(3, 4);
}
}

extern "C" void kernel_launch(void* const* d_in, const int* in_sizes, int n_in, void* d_out, int out_size, void* d_ws, size_t ws_size, hipStream_t stream) {
    const bf16_t* x = (const bf16_t*)d_in[0]; const bf16_t* ckv = (const bf16_t*)d_in[1];
    const bf16_t* wq = (const bf16_t*)d_in[2]; const bf16_t* wuk = (const bf16_t*)d_in[3]; const bf16_t* wuv = (const bf16_t*)d_in[4]; const bf16_t* wo = (const bf16_t*)d_in[5];
    const bf16_t* w1 = (const bf16_t*)d_in[7]; const bf16_t* b1 = (const bf16_t*)d_in[8]; const bf16_t* w2 = (const bf16_t*)d_in[9]; const bf16_t* b2 = (const bf16_t*)d_in[10];
    char* ws = (char*)d_ws;
    (void)hipFuncSetAttribute((const void*)k1::k1_proj, hipFuncAttributeMaxDynamicSharedMemorySize, k1::LDS_BYTES);
    (void)hipFuncSetAttribute((const void*)k2::k2_attn, hipFuncAttributeMaxDynamicSharedMemorySize, k2::LDS_BYTES);
    (void)hipFuncSetAttribute((const void*)k3::k3_out, hipFuncAttributeMaxDynamicSharedMemorySize, k3::LDS_BYTES);
    k0_ln<<<SEQ / 4, 256, 0, stream>>>(x, (bf16_t*)(ws + WS_XN), (unsigned*)(ws + WS_FLAG));
    k1::k1_proj<<<256, 512, k1::LDS_BYTES, stream>>>(ckv, ws + WS_XN, wuk, wuv, wq, w1, b1, ws + WS_Q, ws + WS_K, ws + WS_VT, ws + WS_H);
    k2::k2_attn<<<256, 512, k2::LDS_BYTES, stream>>>(ws + WS_Q, ws + WS_K, ws + WS_VT, ws + WS_CTX);
    k3::k3_out<<<256, 512, k3::LDS_BYTES, stream>>>(ws + WS_CTX, ws + WS_H, wo, w2, b2, x, (float*)(ws + WS_PART), (unsigned*)(ws + WS_FLAG), (bf16_t*)d_out);
}
```

```cpp
#include <hip/hip_runtime.h>
#include <cstdint>

#define STAMP(k, i) do {} while (0)
#define LAS __attribute__((address_space(3)))
#define DI __device__ __forceinline__
typedef unsigned short bf16_t;
typedef short bf16x8 __attribute__((ext_vector_type(8)));
typedef short s16x4 __attribute__((ext_vector_type(4)));
typedef short v4i16_t __attribute__((ext_vector_type(4)));
typedef float f32x2 __attribute__((ext_vector_type(2)));
typedef float f32x4 __attribute__((ext_vector_type(4)));
typedef float f32x16 __attribute__((ext_vector_type(16)));
typedef unsigned u32x2 __attribute__((ext_vector_type(2)));
typedef unsigned u32x4 __attribute__((ext_vector_type(4)));
typedef __bf16 bf16x2_t __attribute__((ext_vector_type(2)));
typedef LAS unsigned char* lds_ptr;
typedef LAS const unsigned char* lds_cptr;

constexpr int SEQ = 2048, DM = 1024, NH = 32, HD = 128, DC = 512, FF = 3584, NQ = NH * HD;
constexpr size_t MiB = 1u << 20;
constexpr size_t WS_FLAG = 0;
constexpr size_t WS_XN = 1 * MiB;
constexpr size_t WS_Q = 8 * MiB;
constexpr size_t WS_K = 24 * MiB;
constexpr size_t WS_VT = 40 * MiB;
constexpr size_t WS_CTX = 56 * MiB;
constexpr size_t WS_H = 72 * MiB;
constexpr size_t WS_PART = 88 * MiB;

DI unsigned cvtpk(float lo, float hi) { f32x2 v = {lo, hi}; bf16x2_t b = __builtin_convertvector(v, bf16x2_t); return __builtin_bit_cast(unsigned, b); }
DI float bflo(unsigned w) { return __uint_as_float(w << 16); }
DI float bfhi(unsigned w) { return __uint_as_float(w & 0xffff0000u); }
DI s16x4 lds_tr(lds_cptr p) { return __builtin_bit_cast(s16x4, __builtin_amdgcn_ds_read_tr16_b64_v4i16((LAS v4i16_t*)p)); }
DI bf16x8 lds_rd(lds_cptr p) { return *(const LAS bf16x8*)p; }
DI bf16x8 lds_rdv(lds_cptr p) { return *(const volatile LAS bf16x8*)p; }
DI bf16x8 cat8(s16x4 a, s16x4 b) { return __builtin_shufflevector(a, b, 0, 1, 2, 3, 4, 5, 6, 7); }
DI void st16_sc1(void* p, u32x4 v) { asm volatile("global_store_dwordx4 %0, %1, off sc1\n\ts_nop 1" :: "v"(p), "v"(v) : "memory"); }
#define ST16(p, v) st16_sc1((void*)(p), v)
DI unsigned code_pf(unsigned line, unsigned nlines) {
    unsigned long long pc; unsigned d;
    asm volatile("s_getpc_b64 %0" : "=s"(pc));
    asm volatile("global_load_dword %0, %1, %2" : "=v"(d) : "v"((line < nlines ? line : 0u) * 128u), "s"(pc) : "memory");
    return d;
}
#define CODE_PF_DONE(d) asm volatile("" :: "v"(d))
#define SBAR() __builtin_amdgcn_sched_barrier(0)
#define PIN(x) asm volatile("" : "+v"(x))
#define WAIT_V(n) asm volatile("s_waitcnt vmcnt(" #n ")" ::: "memory")
#define WAIT_L(n) asm volatile("s_waitcnt lgkmcnt(" #n ")" ::: "memory")
#define BAR() __builtin_amdgcn_s_barrier()
#define MFMA16(a, b, c) __builtin_amdgcn_mfma_f32_16x16x32_bf16(a, b, c, 0, 0, 0)
#define MFMA32(a, b, c) __builtin_amdgcn_mfma_f32_32x32x16_bf16(a, b, c, 0, 0, 0)
DI void glds(const char* sbase, unsigned voff, unsigned ldsaddr) {
    asm volatile("s_mov_b32 m0, %2\n\ts_nop 0\n\tglobal_load_lds_dwordx4 %0, %1" :: "v"(voff), "s"(sbase), "s"(ldsaddr) : "memory");
}
DI void glds2(const char* sbase, unsigned voff, unsigned ldsaddr) {
    asm volatile("s_mov_b32 m0, %2\n\ts_nop 0\n\tglobal_load_lds_dwordx4 %0, %1\n\tglobal_load_lds_dwordx4 %0, %1 offset:1024" :: "v"(voff), "s"(sbase), "s"(ldsaddr) : "memory");
}

__global__ __launch_bounds__(256) void k0_ln(const bf16_t* __restrict__ x, bf16_t* __restrict__ xn, unsigned* __restrict__ flags) {
    const int lane = threadIdx.x & 63, row = blockIdx.x * 4 + (threadIdx.x >> 6);
    STAMP(0, 0);
    unsigned cpf = 0; if (blockIdx.x < 16) cpf = code_pf(threadIdx.x + (blockIdx.x >> 3) * 256, 300);
    const u32x4* src = (const u32x4*)(x + (size_t)row * DM);
    const u32x4 v0 = src[lane], v1 = src[lane + 64];
    float f[16];
#pragma unroll
    for (int i = 0; i < 4; ++i) { f[2 * i] = bflo(v0[i]); f[2 * i + 1] = bfhi(v0[i]); f[8 + 2 * i] = bflo(v1[i]); f[8 + 2 * i + 1] = bfhi(v1[i]); }
    float s = 0.f;
#pragma unroll
    for (int i = 0; i < 16; ++i) s += f[i];
#pragma unroll
    for (int o = 32; o >= 1; o >>= 1) s += __shfl_xor(s, o);
    const float mean = s * (1.0f / DM);
    float q = 0.f;
#pragma unroll
    for (int i = 0; i < 16; ++i) { f[i] -= mean; q += f[i] * f[i]; }
#pragma unroll
    for (int o = 32; o >= 1; o >>= 1) q += __shfl_xor(q, o);
    const float rstd = 1.0f / sqrtf(q * (1.0f / DM) + 1e-5f);
    u32x4 w0, w1;
#pragma unroll
    for (int i = 0; i < 4; ++i) { w0[i] = cvtpk(f[2 * i] * rstd, f[2 * i + 1] * rstd); w1[i] = cvtpk(f[8 + 2 * i] * rstd, f[8 + 2 * i + 1] * rstd); }
    const int mh = row >> 7, r = row & 127, c = lane & 7, kt = lane >> 3, sw = (c ^ ((r >> 1) & 7)) * 16;
    char* base = (char*)xn + (size_t)mh * 16 * 16384 + r * 128 + sw;
    *(u32x4*)(base + (size_t)kt * 16384) = w0;
    *(u32x4*)(base + (size_t)(kt + 8) * 16384) = w1;
    STAMP(0, 1);
    WAIT_V(0); CODE_PF_DONE(cpf);
    if (blockIdx.x == 0) flags[threadIdx.x * 32] = 0u;
}

namespace k1 {
constexpr int HTB = 16384, LDS_BYTES = 8 * HTB;
enum { T_K = 0, T_V = 1, T_Q = 2, T_H = 3 };
struct Src {
    const char* a; const char* b;
    unsigned aK, aH, aD, bK, bD;
    unsigned va, vb;
};
struct Frag { bf16x8 At[4][2], B0[2][2], B1[2][2]; };

DI f32x2 gelu_tanh2(f32x2 v) {
    const f32x2 z = v * (v * v * -0.10294324f + -2.3022082f);
    f32x2 e; e.x = __builtin_amdgcn_exp2f(z.x); e.y = __builtin_amdgcn_exp2f(z.y);
    e = e + 1.0f;
    f32x2 r; r.x = __builtin_amdgcn_rcpf(e.x); r.y = __builtin_amdgcn_rcpf(e.y);
    return v * r;
}

#define K1_SA(b, h) (((b) * 2 + (h)) * HTB)
#define K1_SB(b, h) ((4 + (b) * 2 + (h)) * HTB)
#define K1_STAGE(bufoff, gbase, voff, d2) do { glds((const char*)(gbase), voff, ldsw + (bufoff)); glds((const char*)(gbase) + (d2), voff, ldsw + (bufoff) + 8192); } while (0)
#define K1_LDA(dst, b, h) do { _Pragma("unroll") for (int m = 0; m < 4; ++m) { dst[m][0] = lds_rd(lds + K1_SA(b, h) + aoff0 + m * 2048); dst[m][1] = lds_rd(lds + K1_SA(b, h) + aoff1 + m * 2048); } } while (0)
#define K1_LDB(dst, b, h) do { _Pragma("unroll") for (int k = 0; k < 2; ++k) { \
        dst[0][k] = cat8(lds_tr(lds + K1_SB(b, h) + boff0 + k * 8192), lds_tr(lds + K1_SB(b, h) + boff0 + k * 8192 + 256)); \
        dst[1][k] = cat8(lds_tr(lds + K1_SB(b, h) + boff1 + k * 8192), lds_tr(lds + K1_SB(b, h) + boff1 + k * 8192 + 256)); } } while (0)
#define K1_MMA(ai, bj, At, Bt) do { __builtin_amdgcn_s_setprio(1); _Pragma("unroll") for (int m = 0; m < 4; ++m) _Pragma("unroll") for (int n = 0; n < 2; ++n) _Pragma("unroll") for (int k = 0; k < 2; ++k) \
        acc[ai][bj][m][n] = SWAP ? MFMA16(At[m][k], Bt[n][k], acc[ai][bj][m][n]) : MFMA16(Bt[n][k], At[m][k], acc[ai][bj][m][n]); __builtin_amdgcn_s_setprio(0); } while (0)

template <bool SWAP>
DI void kloop(lds_ptr lds, f32x4 (&acc)[2][2][4][2], const Src& cur, const Src& nxt, int nt, unsigned ldsw, int aoff0, int aoff1, int boff0, int boff1) {
    bf16x8 At[4][2], B0[2][2], B1[2][2];
    for (int t = 0; t < nt; t += 2) {
        const bool last = (t == nt - 2);
        const char* a1h = cur.a + (size_t)(t + 1) * cur.aK + cur.aH;
        const char* a2 = last ? nxt.a : cur.a + (size_t)(t + 2) * cur.aK; const char* b2 = last ? nxt.b : cur.b + (size_t)(t + 2) * cur.bK;
        const unsigned aK = last ? nxt.aK : cur.aK, aH = last ? nxt.aH : cur.aH, aD = last ? nxt.aD : cur.aD, bK = last ? nxt.bK : cur.bK, bD = last ? nxt.bD : cur.bD;
        const unsigned va = last ? nxt.va : cur.va, vb = last ? nxt.vb : cur.vb;
        const char* a3 = a2 + aK; const char* b3 = b2 + bK;
        K1_LDB(B0, 0, 0); K1_LDB(B1, 0, 1); SBAR(); K1_LDA(At, 0, 0); K1_STAGE(K1_SA(1, 1), a1h, cur.va, cur.aD);
        WAIT_V(8); WAIT_L(0); BAR(); K1_MMA(0, 0, At, B0); K1_MMA(0, 1, At, B1); BAR(); SBAR();
        K1_LDA(At, 0, 1); K1_STAGE(K1_SB(0, 0), b2, vb, bD); K1_STAGE(K1_SB(0, 1), b2 + 256, vb, bD); K1_STAGE(K1_SA(0, 0), a2, va, aD);
        WAIT_V(8); WAIT_L(0); BAR(); K1_MMA(1, 0, At, B0); K1_MMA(1, 1, At, B1); BAR(); SBAR();
        K1_LDB(B0, 1, 0); K1_LDB(B1, 1, 1); SBAR(); K1_LDA(At, 1, 0); K1_STAGE(K1_SA(0, 1), a2 + aH, va, aD);
        WAIT_V(8); WAIT_L(0); BAR(); K1_MMA(0, 0, At, B0); K1_MMA(0, 1, At, B1); BAR(); SBAR();
        K1_LDA(At, 1, 1); K1_STAGE(K1_SB(1, 0), b3, vb, bD); K1_STAGE(K1_SB(1, 1), b3 + 256, vb, bD); K1_STAGE(K1_SA(1, 0), a3, va, aD);
        WAIT_V(8); WAIT_L(0); BAR(); K1_MMA(1, 0, At, B0); K1_MMA(1, 1, At, B1); BAR(); SBAR();
    }
}

DI void zero_acc(f32x4 (&acc)[2][2][4][2]) {
#pragma unroll
    for (int a = 0; a < 2; ++a)
#pragma unroll
        for (int b = 0; b < 2; ++b)
#pragma unroll
            for (int m = 0; m < 4; ++m)
#pragma unroll
                for (int n = 0; n < 2; ++n) acc[a][b][m][n] = (f32x4){0.f, 0.f, 0.f, 0.f};
}
DI u32x4 pack_swap16(f32x4 v0, f32x4 v1) {
    const unsigned x0 = cvtpk(v0[0], v0[1]), x1 = cvtpk(v0[2], v0[3]), y0 = cvtpk(v1[0], v1[1]), y1 = cvtpk(v1[2], v1[3]);
    const auto r0 = __builtin_amdgcn_permlane16_swap(x0, y0, false, false), r1 = __builtin_amdgcn_permlane16_swap(x1, y1, false, false);
    return (u32x4){r0[0], r1[0], r0[1], r1[1]};
}
DI u32x4 pack_swap32(f32x4 v0, f32x4 v1) {
    const unsigned x0 = cvtpk(v0[0], v0[1]), x1 = cvtpk(v0[2], v0[3]), y0 = cvtpk(v1[0], v1[1]), y1 = cvtpk(v1[2], v1[3]);
    const auto r0 = __builtin_amdgcn_permlane32_swap(x0, y0, false, false), r1 = __builtin_amdgcn_permlane32_swap(x1, y1, false, false);
    return (u32x4){r0[0], r1[0], r0[1], r1[1]};
}
DI void epi_qk(const f32x4 (&acc)[2][2][4][2], char* dst, int pm, int pn, float sc, int wr, int wc, int fr, int fq) {
    const int cg = ((fq & 1) << 1) | (fq >> 1);
    char* p = dst + (size_t)(2 * pn * 32 + 4 * pm + wr) * 16384 + (4 * wc + cg) * 1024 + fr * 16;
#pragma unroll
    for (int ai = 0; ai < 2; ++ai)
#pragma unroll
        for (int bj = 0; bj < 2; ++bj)
#pragma unroll
            for (int m = 0; m < 4; ++m)
                ST16(p + (size_t)(bj * 32 + ai * 2) * 16384 + m * 256, pack_swap16(acc[ai][bj][m][0] * sc, acc[ai][bj][m][1] * sc));
}
DI void epi_v(const f32x4 (&acc)[2][2][4][2], char* dst, int pm, int pn, int wr, int wc, int fr, int fq) {
    char* p = dst + (size_t)(2 * pn * 32 + 4 * pm + wr) * 16384 + fq * 2048 + (32 * wc + fr) * 16;
#pragma unroll
    for (int ai = 0; ai < 2; ++ai)
#pragma unroll
        for (int bj = 0; bj < 2; ++bj)
#pragma unroll
            for (int m = 0; m < 4; m += 2)
#pragma unroll
                for (int n = 0; n < 2; ++n)
                    ST16(p + (size_t)(bj * 32 + ai * 2) * 16384 + m * 4096 + n * 256, pack_swap32(acc[ai][bj][m][n], acc[ai][bj][m + 1][n]));
}
DI void epi_h(const f32x4 (&acc)[2][2][4][2], char* dst, const bf16_t* b1, int pm, int pn, int wr, int wc, int fr, int fq) {
    const int cg = ((fq & 1) << 1) | (fq >> 1);
    f32x4 bv[2][2];
#pragma unroll
    for (int bj = 0; bj < 2; ++bj)
#pragma unroll
        for (int n = 0; n < 2; ++n) { const u32x2 w = *(const u32x2*)(b1 + pn * 256 + bj * 128 + wc * 32 + n * 16 + fq * 4); bv[bj][n] = (f32x4){bflo(w[0]), bfhi(w[0]), bflo(w[1]), bfhi(w[1])}; }
    char* p = dst + ((size_t)(2 * pm * 56 + 4 * pn + (wc >> 1)) * 8 + 4 * (wc & 1) + cg) * 2048 + (64 * wr + fr) * 16;
#pragma unroll
    for (int ai = 0; ai < 2; ++ai)
#pragma unroll
        for (int bj = 0; bj < 2; ++bj)
#pragma unroll
            for (int m = 0; m < 4; ++m) {
                f32x4 v0 = acc[ai][bj][m][0] + bv[bj][0], v1 = acc[ai][bj][m][1] + bv[bj][1];
                { const f32x2 a = gelu_tanh2((f32x2){v0[0], v0[1]}), b = gelu_tanh2((f32x2){v0[2], v0[3]}), c = gelu_tanh2((f32x2){v1[0], v1[1]}), d = gelu_tanh2((f32x2){v1[2], v1[3]});
                  v0 = (f32x4){a.x, a.y, b.x, b.y}; v1 = (f32x4){c.x, c.y, d.x, d.y}; }
                ST16(p + (size_t)(ai * 56 + bj * 2) * 16384 + m * 256, pack_swap16(v0, v1));
            }
}

__global__ __launch_bounds__(512, 2) void k1_proj(const bf16_t* __restrict__ ckv, const char* __restrict__ xn, const bf16_t* __restrict__ wuk, const bf16_t* __restrict__ wuv,
                                                  const bf16_t* __restrict__ wq, const bf16_t* __restrict__ w1, const bf16_t* __restrict__ b1,
                                                  char* __restrict__ Qo, char* __restrict__ Ko, char* __restrict__ VTo, char* __restrict__ Ho) {
    extern __shared__ __attribute__((aligned(16))) unsigned char smem[];
    lds_ptr lds = (lds_ptr)smem;
    const int tid = threadIdx.x, wid = __builtin_amdgcn_readfirstlane(tid >> 6), lane = tid & 63, wr = wid >> 2, wc = wid & 3, fr = lane & 15, fq = lane >> 4;
    const int xcd = blockIdx.x & 7, idx = blockIdx.x >> 3, pm = idx & 7, pj = xcd + 8 * (idx >> 3);
    const bool isV = pj >= 16, has2 = pj < 30, isH = pj >= 16;
    const unsigned ldsw = (unsigned)(uintptr_t)smem + (unsigned)wid * 1024u;
    const int aoff0 = (wr * 64 + fr) * 128 + ((fq ^ (fr >> 1)) * 16), aoff1 = aoff0 ^ 64;
    const int boff0 = fq * 2048 + wc * 512 + (fr >> 2) * 64 + (fq & 1) * 32 + (fr & 3) * 8, boff1 = boff0 ^ 32;
    const unsigned brow = 8 * (wid >> 1) + ((lane & 31) >> 2), bcol = (64 * (wid & 1) + 32 * (lane >> 5) + 8 * ((lane & 3) ^ (wid & 2))) * 2;
    Src u0, u1;
    u0.a = (const char*)ckv + (size_t)pm * 256 * DC * 2; u0.aK = 128; u0.aH = 128 * DC * 2; u0.aD = 64 * DC * 2;
    u0.va = (tid >> 3) * (DC * 2) + (((tid & 7) ^ ((tid >> 4) & 7)) * 16);
    u0.b = (const char*)(isV ? wuv : wuk) + (pj & 15) * 512; u0.bK = 64 * NQ * 2; u0.bD = 32 * NQ * 2; u0.vb = brow * (NQ * 2) + bcol;
    if (has2) {
        const unsigned ldb2 = isH ? FF * 2 : NQ * 2;
        u1.a = xn + (size_t)pm * 2 * 16 * 16384; u1.aK = 16384; u1.aH = 16 * 16384; u1.aD = 8192; u1.va = tid * 16;
        u1.b = (const char*)(isH ? w1 : wq) + (pj & 15) * 512; u1.bK = 64 * ldb2; u1.bD = 32 * ldb2; u1.vb = brow * ldb2 + bcol;
    } else u1 = u0;
    f32x4 acc[2][2][4][2];
    zero_acc(acc);
    STAMP(1, 0);
    const unsigned cpf = code_pf(tid, 136);
    K1_STAGE(K1_SB(0, 0), u0.b, u0.vb, u0.bD); K1_STAGE(K1_SB(0, 1), u0.b + 256, u0.vb, u0.bD); K1_STAGE(K1_SA(0, 0), u0.a, u0.va, u0.aD); K1_STAGE(K1_SA(0, 1), u0.a + u0.aH, u0.va, u0.aD);
    if (wr == 1) BAR();
    WAIT_V(2); BAR();
    CODE_PF_DONE(cpf);
    K1_STAGE(K1_SB(1, 0), u0.b + u0.bK, u0.vb, u0.bD); K1_STAGE(K1_SA(1, 0), u0.a + u0.aK, u0.va, u0.aD); K1_STAGE(K1_SB(1, 1), u0.b + u0.bK + 256, u0.vb, u0.bD);
    WAIT_V(6); BAR();
    STAMP(1, 1);
    if (isV) kloop<true>(lds, acc, u0, u1, DC / 64, ldsw, aoff0, aoff1, boff0, boff1);
    else     kloop<false>(lds, acc, u0, u1, DC / 64, ldsw, aoff0, aoff1, boff0, boff1);
    STAMP(1, 2);
    if (wr == 0) BAR();
    if (isV) epi_v(acc, VTo, pm, pj & 15, wr, wc, fr, fq); else epi_qk(acc, Ko, pm, pj & 15, 1.0f, wr, wc, fr, fq);
    if (has2) {
        zero_acc(acc);
        STAMP(1, 3);
        if (wr == 1) BAR();
        kloop<false>(lds, acc, u1, u1, DM / 64, ldsw, aoff0, aoff1, boff0, boff1);
        STAMP(1, 4);
        if (wr == 0) BAR();
        if (isH) epi_h(acc, Ho, b1, pm, pj & 15, wr, wc, fr, fq); else epi_qk(acc, Qo, pm, pj & 15, 0.12751743082459868f, wr, wc, fr, fq);
    }
    STAMP(1, 5);
    WAIT_V(0);
    STAMP(1, 6);
}
#undef K1_SA
#undef K1_SB
#undef K1_STAGE
#undef K1_LDA
#undef K1_LDB
#undef K1_MMA
}

namespace k2 {
constexpr int SLOT = 16384, LDS_K = 0, LDS_V = 4 * SLOT, LDS_BYTES = 8 * SLOT;
#define K2_ASM \
    "s_nop 1\n\t" \
    "s_cmp_eq_u32 %[n], 0\n\t" \
    "s_cbranch_scc1 TAIL_%=\n\t" \
    "LOOP_%=:\n\t" \
    "s_cmp_lg_u32 %[dmaw], 0\n\t" \
    "s_cbranch_scc1 DMAL_%=\n\t" \
    "DMABL_%=:\n\t" \
    "s_sub_u32 s60, %[t], 8\n\t" \
    "s_and_b32 s60, s60, 0xfffffffb\n\t" \
    "s_or_b32 s60, s60, %[dmaw]\n\t" \
    "s_cmp_eq_u32 s60, 0\n\t" \
    "s_cbranch_scc1 PF_%=\n\t" \
    "PFB_%=:\n\t" \
    "s_waitcnt lgkmcnt(0)\n\t" \
    "v_mfma_f32_32x32x16_bf16 v[112:127], v[168:171], v[64:67], v[96:111]\n\t" \
    "ds_read_b128 v[200:203], v235 offset:8192\n\t" \
    "ds_read_b128 v[204:207], v235 offset:8704\n\t" \
    "v_add_f32 v237, v144, v145\n\t" \
    "v_cvt_pk_bf16_f32 v160, v144, v145\n\t" \
    "v_mfma_f32_32x32x16_bf16 v[112:127], v[172:175], v[68:71], v[112:127]\n\t" \
    "ds_read_b128 v[208:211], v235 offset:9216\n\t" \
    "ds_read_b128 v[212:215], v235 offset:9728\n\t" \
    "v_add_f32 v237, v237, v146\n\t" \
    "v_add_f32 v237, v237, v147\n\t" \
    "v_cvt_pk_bf16_f32 v161, v146, v147\n\t" \
    "v_mfma_f32_32x32x16_bf16 v[112:127], v[176:179], v[72:75], v[112:127]\n\t" \
    "ds_read_b128 v[216:219], v235 offset:12288\n\t" \
    "ds_read_b128 v[220:223], v235 offset:12800\n\t" \
    "v_add_f32 v237, v237, v148\n\t" \
    "v_add_f32 v237, v237, v149\n\t" \
    "v_cvt_pk_bf16_f32 v162, v148, v149\n\t" \
    "v_mfma_f32_32x32x16_bf16 v[112:127], v[180:183], v[76:79], v[112:127]\n\t" \
    "ds_read_b128 v[224:227], v235 offset:13312\n\t" \
    "ds_read_b128 v[228:231], v235 offset:13824\n\t" \
    "v_add_f32 v237, v237, v150\n\t" \
    "v_add_f32 v237, v237, v151\n\t" \
    "v_cvt_pk_bf16_f32 v163, v150, v151\n\t" \
    "v_mfma_f32_32x32x16_bf16 v[112:127], v[184:187], v[80:83], v[112:127]\n\t" \
    "v_add_f32 v237, v237, v152\n\t" \
    "v_add_f32 v237, v237, v153\n\t" \
    "v_cvt_pk_bf16_f32 v164, v152, v153\n\t" \
    "v_mfma_f32_32x32x16_bf16 v[112:127], v[188:191], v[84:87], v[112:127]\n\t" \
    "v_add_f32 v237, v237, v154\n\t" \
    "v_add_f32 v237, v237, v155\n\t" \
    "v_cvt_pk_bf16_f32 v165, v154, v155\n\t" \
    "v_mfma_f32_32x32x16_bf16 v[112:127], v[192:195], v[88:91], v[112:127]\n\t" \
    "v_add_f32 v237, v237, v156\n\t" \
    "v_add_f32 v237, v237, v157\n\t" \
    "v_cvt_pk_bf16_f32 v166, v156, v157\n\t" \
    "v_mfma_f32_32x32x16_bf16 v[112:127], v[196:199], v[92:95], v[112:127]\n\t" \
    "v_add_f32 v237, v237, v158\n\t" \
    "v_add_f32 v237, v237, v159\n\t" \
    "v_cvt_pk_bf16_f32 v167, v158, v159\n\t" \
    "v_cmp_lt_f32 vcc, %[thr], v237\n\t" \
    "s_cbranch_vccnz RAREA_%=\n\t" \
    "BACKA_%=:\n\t" \
    "s_waitcnt lgkmcnt(0)\n\t" \
    "v_mfma_f32_32x32x16_bf16 v[0:15], v[200:203], v[160:163], v[0:15]\n\t" \
    "ds_read_b128 v[168:171], v232 offset:512\n\t" \
    "ds_read_b128 v[172:175], v232 offset:2560\n\t" \
    "v_add_f32 v236, v236, v237\n\t" \
    "v_mfma_f32_32x32x16_bf16 v[16:31], v[204:207], v[160:163], v[16:31]\n\t" \
    "ds_read_b128 v[176:179], v232 offset:4608\n\t" \
    "ds_read_b128 v[180:183], v232 offset:6656\n\t" \
    "v_exp_f32 v144, v112\n\t" \
    "v_exp_f32 v145, v113\n\t" \
    "v_mfma_f32_32x32x16_bf16 v[32:47], v[208:211], v[160:163], v[32:47]\n\t" \
    "ds_read_b128 v[184:187], v232 offset:8704\n\t" \
    "ds_read_b128 v[188:191], v232 offset:10752\n\t" \
    "v_exp_f32 v146, v114\n\t" \
    "v_exp_f32 v147, v115\n\t" \
    "v_mfma_f32_32x32x16_bf16 v[48:63], v[212:215], v[160:163], v[48:63]\n\t" \
    "ds_read_b128 v[192:195], v232 offset:12800\n\t" \
    "ds_read_b128 v[196:199], v232 offset:14848\n\t" \
    "v_exp_f32 v148, v116\n\t" \
    "v_exp_f32 v149, v117\n\t" \
    "v_mfma_f32_32x32x16_bf16 v[0:15], v[216:219], v[164:167], v[0:15]\n\t" \
    "v_exp_f32 v150, v118\n\t" \
    "v_exp_f32 v151, v119\n\t" \
    "v_exp_f32 v152, v120\n\t" \
    "v_mfma_f32_32x32x16_bf16 v[16:31], v[220:223], v[164:167], v[16:31]\n\t" \
    "v_exp_f32 v153, v121\n\t" \
    "v_exp_f32 v154, v122\n\t" \
    "v_exp_f32 v155, v123\n\t" \
    "v_mfma_f32_32x32x16_bf16 v[32:47], v[224:227], v[164:167], v[32:47]\n\t" \
    "v_exp_f32 v156, v124\n\t" \
    "v_exp_f32 v157, v125\n\t" \
    "v_mfma_f32_32x32x16_bf16 v[48:63], v[228:231], v[164:167], v[48:63]\n\t" \
    "v_exp_f32 v158, v126\n\t" \
    "v_exp_f32 v159, v127\n\t" \
    "s_waitcnt lgkmcnt(0)\n\t" \
    "v_mfma_f32_32x32x16_bf16 v[128:143], v[168:171], v[64:67], v[96:111]\n\t" \
    "ds_read_b128 v[200:203], v234 offset:0\n\t" \
    "ds_read_b128 v[204:207], v234 offset:512\n\t" \
    "v_add_f32 v237, v144, v145\n\t" \
    "v_cvt_pk_bf16_f32 v160, v144, v145\n\t" \
    "v_mfma_f32_32x32x16_bf16 v[128:143], v[172:175], v[68:71], v[128:143]\n\t" \
    "ds_read_b128 v[208:211], v234 offset:1024\n\t" \
    "ds_read_b128 v[212:215], v234 offset:1536\n\t" \
    "v_add_f32 v237, v237, v146\n\t" \
    "v_add_f32 v237, v237, v147\n\t" \
    "v_cvt_pk_bf16_f32 v161, v146, v147\n\t" \
    "v_mfma_f32_32x32x16_bf16 v[128:143], v[176:179], v[72:75], v[128:143]\n\t" \
    "ds_read_b128 v[216:219], v234 offset:4096\n\t" \
    "ds_read_b128 v[220:223], v234 offset:4608\n\t" \
    "v_add_f32 v237, v237, v148\n\t" \
    "v_add_f32 v237, v237, v149\n\t" \
    "v_cvt_pk_bf16_f32 v162, v148, v149\n\t" \
    "v_mfma_f32_32x32x16_bf16 v[128:143], v[180:183], v[76:79], v[128:143]\n\t" \
    "ds_read_b128 v[224:227], v234 offset:5120\n\t" \
    "ds_read_b128 v[228:231], v234 offset:5632\n\t" \
    "v_add_f32 v237, v237, v150\n\t" \
    "v_add_f32 v237, v237, v151\n\t" \
    "v_cvt_pk_bf16_f32 v163, v150, v151\n\t" \
    "v_mfma_f32_32x32x16_bf16 v[128:143], v[184:187], v[80:83], v[128:143]\n\t" \
    "v_add_f32 v237, v237, v152\n\t" \
    "v_add_f32 v237, v237, v153\n\t" \
    "v_cvt_pk_bf16_f32 v164, v152, v153\n\t" \
    "v_mfma_f32_32x32x16_bf16 v[128:143], v[188:191], v[84:87], v[128:143]\n\t" \
    "v_add_f32 v237, v237, v154\n\t" \
    "v_add_f32 v237, v237, v155\n\t" \
    "v_cvt_pk_bf16_f32 v165, v154, v155\n\t" \
    "v_mfma_f32_32x32x16_bf16 v[128:143], v[192:195], v[88:91], v[128:143]\n\t" \
    "v_add_f32 v237, v237, v156\n\t" \
    "v_add_f32 v237, v237, v157\n\t" \
    "v_cvt_pk_bf16_f32 v166, v156, v157\n\t" \
    "v_mfma_f32_32x32x16_bf16 v[128:143], v[196:199], v[92:95], v[128:143]\n\t" \
    "v_add_f32 v237, v237, v158\n\t" \
    "v_add_f32 v237, v237, v159\n\t" \
    "v_cvt_pk_bf16_f32 v167, v158, v159\n\t" \
    "v_cmp_lt_f32 vcc, %[thr], v237\n\t" \
    "s_cbranch_vccnz RAREB_%=\n\t" \
    "BACKB_%=:\n\t" \
    "s_waitcnt lgkmcnt(0)\n\t" \
    "v_mfma_f32_32x32x16_bf16 v[0:15], v[200:203], v[160:163], v[0:15]\n\t" \
    "ds_read_b128 v[168:171], v233 offset:0\n\t" \
    "ds_read_b128 v[172:175], v233 offset:2048\n\t" \
    "v_add_f32 v236, v236, v237\n\t" \
    "v_mfma_f32_32x32x16_bf16 v[16:31], v[204:207], v[160:163], v[16:31]\n\t" \
    "ds_read_b128 v[176:179], v233 offset:4096\n\t" \
    "ds_read_b128 v[180:183], v233 offset:6144\n\t" \
    "v_exp_f32 v144, v128\n\t" \
    "v_exp_f32 v145, v129\n\t" \
    "v_mfma_f32_32x32x16_bf16 v[32:47], v[208:211], v[160:163], v[32:47]\n\t" \
    "ds_read_b128 v[184:187], v233 offset:8192\n\t" \
    "ds_read_b128 v[188:191], v233 offset:10240\n\t" \
    "v_exp_f32 v146, v130\n\t" \
    "v_exp_f32 v147, v131\n\t" \
    "v_mfma_f32_32x32x16_bf16 v[48:63], v[212:215], v[160:163], v[48:63]\n\t" \
    "ds_read_b128 v[192:195], v233 offset:12288\n\t" \
    "ds_read_b128 v[196:199], v233 offset:14336\n\t" \
    "v_exp_f32 v148, v132\n\t" \
    "v_exp_f32 v149, v133\n\t" \
    "v_mfma_f32_32x32x16_bf16 v[0:15], v[216:219], v[164:167], v[0:15]\n\t" \
    "v_exp_f32 v150, v134\n\t" \
    "v_exp_f32 v151, v135\n\t" \
    "v_exp_f32 v152, v136\n\t" \
    "v_mfma_f32_32x32x16_bf16 v[16:31], v[220:223], v[164:167], v[16:31]\n\t" \
    "v_exp_f32 v153, v137\n\t" \
    "v_exp_f32 v154, v138\n\t" \
    "v_exp_f32 v155, v139\n\t" \
    "s_add_u32 %[soff], %[soff], 0x4000\n\t" \
    "s_and_b32 %[soff], %[soff], 0xffff\n\t" \
    "v_mfma_f32_32x32x16_bf16 v[32:47], v[224:227], v[164:167], v[32:47]\n\t" \
    "v_exp_f32 v156, v140\n\t" \
    "v_exp_f32 v157, v141\n\t" \
    "v_mov_b32 v235, v234\n\t" \
    "v_mov_b32 v232, v233\n\t" \
    "v_mfma_f32_32x32x16_bf16 v[48:63], v[228:231], v[164:167], v[48:63]\n\t" \
    "v_exp_f32 v158, v142\n\t" \
    "v_exp_f32 v159, v143\n\t" \
    "v_add_u32 v234, %[soff], %[vp0]\n\t" \
    "s_add_u32 s60, %[soff], 0x4000\n\t" \
    "s_and_b32 s60, s60, 0xffff\n\t" \
    "v_add_u32 v233, s60, %[kp0]\n\t" \
    "s_waitcnt vmcnt(8)\n\t" \
    "s_barrier\n\t" \
    "s_add_u32 %[t], %[t], 1\n\t" \
    "s_cmp_lt_u32 %[t], %[n]\n\t" \
    "s_cbranch_scc1 LOOP_%=\n\t" \
    "TAIL_%=:\n\t" \
    "s_cmp_lg_u32 %[dmaw], 0\n\t" \
    "s_cbranch_scc1 DMAT_%=\n\t" \
    "DMABT_%=:\n\t" \
    "s_cmp_eq_u32 %[odd], 0\n\t" \
    "s_cbranch_scc1 EVEN_%=\n\t" \
    "s_waitcnt lgkmcnt(0)\n\t" \
    "v_mfma_f32_32x32x16_bf16 v[112:127], v[168:171], v[64:67], v[96:111]\n\t" \
    "ds_read_b128 v[200:203], v235 offset:8192\n\t" \
    "ds_read_b128 v[204:207], v235 offset:8704\n\t" \
    "v_add_f32 v237, v144, v145\n\t" \
    "v_cvt_pk_bf16_f32 v160, v144, v145\n\t" \
    "v_mfma_f32_32x32x16_bf16 v[112:127], v[172:175], v[68:71], v[112:127]\n\t" \
    "ds_read_b128 v[208:211], v235 offset:9216\n\t" \
    "ds_read_b128 v[212:215], v235 offset:9728\n\t" \
    "v_add_f32 v237, v237, v146\n\t" \
    "v_add_f32 v237, v237, v147\n\t" \
    "v_cvt_pk_bf16_f32 v161, v146, v147\n\t" \
    "v_mfma_f32_32x32x16_bf16 v[112:127], v[176:179], v[72:75], v[112:127]\n\t" \
    "ds_read_b128 v[216:219], v235 offset:12288\n\t" \
    "ds_read_b128 v[220:223], v235 offset:12800\n\t" \
    "v_add_f32 v237, v237, v148\n\t" \
    "v_add_f32 v237, v237, v149\n\t" \
    "v_cvt_pk_bf16_f32 v162, v148, v149\n\t" \
    "v_mfma_f32_32x32x16_bf16 v[112:127], v[180:183], v[76:79], v[112:127]\n\t" \
    "ds_read_b128 v[224:227], v235 offset:13312\n\t" \
    "ds_read_b128 v[228:231], v235 offset:13824\n\t" \
    "v_add_f32 v237, v237, v150\n\t" \
    "v_add_f32 v237, v237, v151\n\t" \
    "v_cvt_pk_bf16_f32 v163, v150, v151\n\t" \
    "v_mfma_f32_32x32x16_bf16 v[112:127], v[184:187], v[80:83], v[112:127]\n\t" \
    "v_add_f32 v237, v237, v152\n\t" \
    "v_add_f32 v237, v237, v153\n\t" \
    "v_cvt_pk_bf16_f32 v164, v152, v153\n\t" \
    "v_mfma_f32_32x32x16_bf16 v[112:127], v[188:191], v[84:87], v[112:127]\n\t" \
    "v_add_f32 v237, v237, v154\n\t" \
    "v_add_f32 v237, v237, v155\n\t" \
    "v_cvt_pk_bf16_f32 v165, v154, v155\n\t" \
    "v_mfma_f32_32x32x16_bf16 v[112:127], v[192:195], v[88:91], v[112:127]\n\t" \
    "v_add_f32 v237, v237, v156\n\t" \
    "v_add_f32 v237, v237, v157\n\t" \
    "v_cvt_pk_bf16_f32 v166, v156, v157\n\t" \
    "v_mfma_f32_32x32x16_bf16 v[112:127], v[196:199], v[92:95], v[112:127]\n\t" \
    "v_add_f32 v237, v237, v158\n\t" \
    "v_add_f32 v237, v237, v159\n\t" \
    "v_cvt_pk_bf16_f32 v167, v158, v159\n\t" \
    "v_cmp_lt_f32 vcc, %[thr], v237\n\t" \
    "s_cbranch_vccnz RAREC_%=\n\t" \
    "BACKC_%=:\n\t" \
    "s_waitcnt lgkmcnt(0)\n\t" \
    "v_mfma_f32_32x32x16_bf16 v[0:15], v[200:203], v[160:163], v[0:15]\n\t" \
    "ds_read_b128 v[168:171], v232 offset:512\n\t" \
    "ds_read_b128 v[172:175], v232 offset:2560\n\t" \
    "v_add_f32 v236, v236, v237\n\t" \
    "v_mfma_f32_32x32x16_bf16 v[16:31], v[204:207], v[160:163], v[16:31]\n\t" \
    "ds_read_b128 v[176:179], v232 offset:4608\n\t" \
    "ds_read_b128 v[180:183], v232 offset:6656\n\t" \
    "v_exp_f32 v144, v112\n\t" \
    "v_exp_f32 v145, v113\n\t" \
    "v_mfma_f32_32x32x16_bf16 v[32:47], v[208:211], v[160:163], v[32:47]\n\t" \
    "ds_read_b128 v[184:187], v232 offset:8704\n\t" \
    "ds_read_b128 v[188:191], v232 offset:10752\n\t" \
    "v_exp_f32 v146, v114\n\t" \
    "v_exp_f32 v147, v115\n\t" \
    "v_mfma_f32_32x32x16_bf16 v[48:63], v[212:215], v[160:163], v[48:63]\n\t" \
    "ds_read_b128 v[192:195], v232 offset:12800\n\t" \
    "ds_read_b128 v[196:199], v232 offset:14848\n\t" \
    "v_exp_f32 v148, v116\n\t" \
    "v_exp_f32 v149, v117\n\t" \
    "v_mfma_f32_32x32x16_bf16 v[0:15], v[216:219], v[164:167], v[0:15]\n\t" \
    "v_exp_f32 v150, v118\n\t" \
    "v_exp_f32 v151, v119\n\t" \
    "v_exp_f32 v152, v120\n\t" \
    "v_mfma_f32_32x32x16_bf16 v[16:31], v[220:223], v[164:167], v[16:31]\n\t" \
    "v_exp_f32 v153, v121\n\t" \
    "v_exp_f32 v154, v122\n\t" \
    "v_exp_f32 v155, v123\n\t" \
    "v_mfma_f32_32x32x16_bf16 v[32:47], v[224:227], v[164:167], v[32:47]\n\t" \
    "v_exp_f32 v156, v124\n\t" \
    "v_exp_f32 v157, v125\n\t" \
    "v_mfma_f32_32x32x16_bf16 v[48:63], v[228:231], v[164:167], v[48:63]\n\t" \
    "v_exp_f32 v158, v126\n\t" \
    "v_exp_f32 v159, v127\n\t" \
    "v_mov_b32 v239, 0xff800000\n\t" \
    "v_cmp_gt_i32 vcc, 0, %[dq]\n\t" \
    "s_nop 1\n\t" \
    "v_cndmask_b32 v96, v96, v239, vcc\n\t" \
    "v_cmp_gt_i32 vcc, 1, %[dq]\n\t" \
    "s_nop 1\n\t" \
    "v_cndmask_b32 v97, v97, v239, vcc\n\t" \
    "v_cmp_gt_i32 vcc, 2, %[dq]\n\t" \
    "s_nop 1\n\t" \
    "v_cndmask_b32 v98, v98, v239, vcc\n\t" \
    "v_cmp_gt_i32 vcc, 3, %[dq]\n\t" \
    "s_nop 1\n\t" \
    "v_cndmask_b32 v99, v99, v239, vcc\n\t" \
    "v_cmp_gt_i32 vcc, 8, %[dq]\n\t" \
    "s_nop 1\n\t" \
    "v_cndmask_b32 v100, v100, v239, vcc\n\t" \
    "v_cmp_gt_i32 vcc, 9, %[dq]\n\t" \
    "s_nop 1\n\t" \
    "v_cndmask_b32 v101, v101, v239, vcc\n\t" \
    "v_cmp_gt_i32 vcc, 10, %[dq]\n\t" \
    "s_nop 1\n\t" \
    "v_cndmask_b32 v102, v102, v239, vcc\n\t" \
    "v_cmp_gt_i32 vcc, 11, %[dq]\n\t" \
    "s_nop 1\n\t" \
    "v_cndmask_b32 v103, v103, v239, vcc\n\t" \
    "v_cmp_gt_i32 vcc, 16, %[dq]\n\t" \
    "s_nop 1\n\t" \
    "v_cndmask_b32 v104, v104, v239, vcc\n\t" \
    "v_cmp_gt_i32 vcc, 17, %[dq]\n\t" \
    "s_nop 1\n\t" \
    "v_cndmask_b32 v105, v105, v239, vcc\n\t" \
    "v_cmp_gt_i32 vcc, 18, %[dq]\n\t" \
    "s_nop 1\n\t" \
    "v_cndmask_b32 v106, v106, v239, vcc\n\t" \
    "v_cmp_gt_i32 vcc, 19, %[dq]\n\t" \
    "s_nop 1\n\t" \
    "v_cndmask_b32 v107, v107, v239, vcc\n\t" \
    "v_cmp_gt_i32 vcc, 24, %[dq]\n\t" \
    "s_nop 1\n\t" \
    "v_cndmask_b32 v108, v108, v239, vcc\n\t" \
    "v_cmp_gt_i32 vcc, 25, %[dq]\n\t" \
    "s_nop 1\n\t" \
    "v_cndmask_b32 v109, v109, v239, vcc\n\t" \
    "v_cmp_gt_i32 vcc, 26, %[dq]\n\t" \
    "s_nop 1\n\t" \
    "v_cndmask_b32 v110, v110, v239, vcc\n\t" \
    "v_cmp_gt_i32 vcc, 27, %[dq]\n\t" \
    "s_nop 1\n\t" \
    "v_cndmask_b32 v111, v111, v239, vcc\n\t" \
    "s_nop 1\n\t" \
    "s_waitcnt lgkmcnt(0)\n\t" \
    "v_mfma_f32_32x32x16_bf16 v[128:143], v[168:171], v[64:67], v[96:111]\n\t" \
    "ds_read_b128 v[200:203], v234 offset:0\n\t" \
    "ds_read_b128 v[204:207], v234 offset:512\n\t" \
    "v_add_f32 v237, v144, v145\n\t" \
    "v_cvt_pk_bf16_f32 v160, v144, v145\n\t" \
    "v_mfma_f32_32x32x16_bf16 v[128:143], v[172:175], v[68:71], v[128:143]\n\t" \
    "ds_read_b128 v[208:211], v234 offset:1024\n\t" \
    "ds_read_b128 v[212:215], v234 offset:1536\n\t" \
    "v_add_f32 v237, v237, v146\n\t" \
    "v_add_f32 v237, v237, v147\n\t" \
    "v_cvt_pk_bf16_f32 v161, v146, v147\n\t" \
    "v_mfma_f32_32x32x16_bf16 v[128:143], v[176:179], v[72:75], v[128:143]\n\t" \
    "ds_read_b128 v[216:219], v234 offset:4096\n\t" \
    "ds_read_b128 v[220:223], v234 offset:4608\n\t" \
    "v_add_f32 v237, v237, v148\n\t" \
    "v_add_f32 v237, v237, v149\n\t" \
    "v_cvt_pk_bf16_f32 v162, v148, v149\n\t" \
    "v_mfma_f32_32x32x16_bf16 v[128:143], v[180:183], v[76:79], v[128:143]\n\t" \
    "ds_read_b128 v[224:227], v234 offset:5120\n\t" \
    "ds_read_b128 v[228:231], v234 offset:5632\n\t" \
    "v_add_f32 v237, v237, v150\n\t" \
    "v_add_f32 v237, v237, v151\n\t" \
    "v_cvt_pk_bf16_f32 v163, v150, v151\n\t" \
    "v_mfma_f32_32x32x16_bf16 v[128:143], v[184:187], v[80:83], v[128:143]\n\t" \
    "v_add_f32 v237, v237, v152\n\t" \
    "v_add_f32 v237, v237, v153\n\t" \
    "v_cvt_pk_bf16_f32 v164, v152, v153\n\t" \
    "v_mfma_f32_32x32x16_bf16 v[128:143], v[188:191], v[84:87], v[128:143]\n\t" \
    "v_add_f32 v237, v237, v154\n\t" \
    "v_add_f32 v237, v237, v155\n\t" \
    "v_cvt_pk_bf16_f32 v165, v154, v155\n\t" \
    "v_mfma_f32_32x32x16_bf16 v[128:143], v[192:195], v[88:91], v[128:143]\n\t" \
    "v_add_f32 v237, v237, v156\n\t" \
    "v_add_f32 v237, v237, v157\n\t" \
    "v_cvt_pk_bf16_f32 v166, v156, v157\n\t" \
    "v_mfma_f32_32x32x16_bf16 v[128:143], v[196:199], v[92:95], v[128:143]\n\t" \
    "v_add_f32 v237, v237, v158\n\t" \
    "v_add_f32 v237, v237, v159\n\t" \
    "v_cvt_pk_bf16_f32 v167, v158, v159\n\t" \
    "v_cmp_lt_f32 vcc, %[thr], v237\n\t" \
    "s_cbranch_vccnz RARED_%=\n\t" \
    "BACKD_%=:\n\t" \
    "s_waitcnt lgkmcnt(0)\n\t" \
    "v_mfma_f32_32x32x16_bf16 v[0:15], v[200:203], v[160:163], v[0:15]\n\t" \
    "ds_read_b128 v[168:171], v233 offset:0\n\t" \
    "ds_read_b128 v[172:175], v233 offset:2048\n\t" \
    "v_add_f32 v236, v236, v237\n\t" \
    "v_mfma_f32_32x32x16_bf16 v[16:31], v[204:207], v[160:163], v[16:31]\n\t" \
    "ds_read_b128 v[176:179], v233 offset:4096\n\t" \
    "ds_read_b128 v[180:183], v233 offset:6144\n\t" \
    "v_exp_f32 v144, v128\n\t" \
    "v_exp_f32 v145, v129\n\t" \
    "v_mfma_f32_32x32x16_bf16 v[32:47], v[208:211], v[160:163], v[32:47]\n\t" \
    "ds_read_b128 v[184:187], v233 offset:8192\n\t" \
    "ds_read_b128 v[188:191], v233 offset:10240\n\t" \
    "v_exp_f32 v146, v130\n\t" \
    "v_exp_f32 v147, v131\n\t" \
    "v_mfma_f32_32x32x16_bf16 v[48:63], v[212:215], v[160:163], v[48:63]\n\t" \
    "ds_read_b128 v[192:195], v233 offset:12288\n\t" \
    "ds_read_b128 v[196:199], v233 offset:14336\n\t" \
    "v_exp_f32 v148, v132\n\t" \
    "v_exp_f32 v149, v133\n\t" \
    "v_mfma_f32_32x32x16_bf16 v[0:15], v[216:219], v[164:167], v[0:15]\n\t" \
    "v_exp_f32 v150, v134\n\t" \
    "v_exp_f32 v151, v135\n\t" \
    "v_exp_f32 v152, v136\n\t" \
    "v_mfma_f32_32x32x16_bf16 v[16:31], v[220:223], v[164:167], v[16:31]\n\t" \
    "v_exp_f32 v153, v137\n\t" \
    "v_exp_f32 v154, v138\n\t" \
    "v_exp_f32 v155, v139\n\t" \
    "v_mfma_f32_32x32x16_bf16 v[32:47], v[224:227], v[164:167], v[32:47]\n\t" \
    "v_exp_f32 v156, v140\n\t" \
    "v_exp_f32 v157, v141\n\t" \
    "v_mfma_f32_32x32x16_bf16 v[48:63], v[228:231], v[164:167], v[48:63]\n\t" \
    "v_exp_f32 v158, v142\n\t" \
    "v_exp_f32 v159, v143\n\t" \
    "s_waitcnt lgkmcnt(0)\n\t" \
    "ds_read_b128 v[200:203], v234 offset:8192\n\t" \
    "ds_read_b128 v[204:207], v234 offset:8704\n\t" \
    "ds_read_b128 v[208:211], v234 offset:9216\n\t" \
    "ds_read_b128 v[212:215], v234 offset:9728\n\t" \
    "ds_read_b128 v[216:219], v234 offset:12288\n\t" \
    "ds_read_b128 v[220:223], v234 offset:12800\n\t" \
    "ds_read_b128 v[224:227], v234 offset:13312\n\t" \
    "ds_read_b128 v[228:231], v234 offset:13824\n\t" \
    "v_add_f32 v237, v144, v145\n\t" \
    "v_cvt_pk_bf16_f32 v160, v144, v145\n\t" \
    "v_add_f32 v237, v237, v146\n\t" \
    "v_add_f32 v237, v237, v147\n\t" \
    "v_cvt_pk_bf16_f32 v161, v146, v147\n\t" \
    "v_add_f32 v237, v237, v148\n\t" \
    "v_add_f32 v237, v237, v149\n\t" \
    "v_cvt_pk_bf16_f32 v162, v148, v149\n\t" \
    "v_add_f32 v237, v237, v150\n\t" \
    "v_add_f32 v237, v237, v151\n\t" \
    "v_cvt_pk_bf16_f32 v163, v150, v151\n\t" \
    "v_add_f32 v237, v237, v152\n\t" \
    "v_add_f32 v237, v237, v153\n\t" \
    "v_cvt_pk_bf16_f32 v164, v152, v153\n\t" \
    "v_add_f32 v237, v237, v154\n\t" \
    "v_add_f32 v237, v237, v155\n\t" \
    "v_cvt_pk_bf16_f32 v165, v154, v155\n\t" \
    "v_add_f32 v237, v237, v156\n\t" \
    "v_add_f32 v237, v237, v157\n\t" \
    "v_cvt_pk_bf16_f32 v166, v156, v157\n\t" \
    "v_add_f32 v237, v237, v158\n\t" \
    "v_add_f32 v237, v237, v159\n\t" \
    "v_cvt_pk_bf16_f32 v167, v158, v159\n\t" \
    "v_cmp_lt_f32 vcc, %[thr], v237\n\t" \
    "s_cbranch_vccnz RAREE_%=\n\t" \
    "BACKE_%=:\n\t" \
    "s_waitcnt lgkmcnt(0)\n\t" \
    "v_add_f32 v236, v236, v237\n\t" \
    "v_mfma_f32_32x32x16_bf16 v[0:15], v[200:203], v[160:163], v[0:15]\n\t" \
    "v_mfma_f32_32x32x16_bf16 v[16:31], v[204:207], v[160:163], v[16:31]\n\t" \
    "v_mfma_f32_32x32x16_bf16 v[32:47], v[208:211], v[160:163], v[32:47]\n\t" \
    "v_mfma_f32_32x32x16_bf16 v[48:63], v[212:215], v[160:163], v[48:63]\n\t" \
    "v_mfma_f32_32x32x16_bf16 v[0:15], v[216:219], v[164:167], v[0:15]\n\t" \
    "v_mfma_f32_32x32x16_bf16 v[16:31], v[220:223], v[164:167], v[16:31]\n\t" \
    "v_mfma_f32_32x32x16_bf16 v[32:47], v[224:227], v[164:167], v[32:47]\n\t" \
    "v_mfma_f32_32x32x16_bf16 v[48:63], v[228:231], v[164:167], v[48:63]\n\t" \
    "s_branch END_%=\n\t" \
    "EVEN_%=:\n\t" \
    "v_mov_b32 v239, 0xff800000\n\t" \
    "v_cmp_gt_i32 vcc, 0, %[dq]\n\t" \
    "s_nop 1\n\t" \
    "v_cndmask_b32 v96, v96, v239, vcc\n\t" \
    "v_cmp_gt_i32 vcc, 1, %[dq]\n\t" \
    "s_nop 1\n\t" \
    "v_cndmask_b32 v97, v97, v239, vcc\n\t" \
    "v_cmp_gt_i32 vcc, 2, %[dq]\n\t" \
    "s_nop 1\n\t" \
    "v_cndmask_b32 v98, v98, v239, vcc\n\t" \
    "v_cmp_gt_i32 vcc, 3, %[dq]\n\t" \
    "s_nop 1\n\t" \
    "v_cndmask_b32 v99, v99, v239, vcc\n\t" \
    "v_cmp_gt_i32 vcc, 8, %[dq]\n\t" \
    "s_nop 1\n\t" \
    "v_cndmask_b32 v100, v100, v239, vcc\n\t" \
    "v_cmp_gt_i32 vcc, 9, %[dq]\n\t" \
    "s_nop 1\n\t" \
    "v_cndmask_b32 v101, v101, v239, vcc\n\t" \
    "v_cmp_gt_i32 vcc, 10, %[dq]\n\t" \
    "s_nop 1\n\t" \
    "v_cndmask_b32 v102, v102, v239, vcc\n\t" \
    "v_cmp_gt_i32 vcc, 11, %[dq]\n\t" \
    "s_nop 1\n\t" \
    "v_cndmask_b32 v103, v103, v239, vcc\n\t" \
    "v_cmp_gt_i32 vcc, 16, %[dq]\n\t" \
    "s_nop 1\n\t" \
    "v_cndmask_b32 v104, v104, v239, vcc\n\t" \
    "v_cmp_gt_i32 vcc, 17, %[dq]\n\t" \
    "s_nop 1\n\t" \
    "v_cndmask_b32 v105, v105, v239, vcc\n\t" \
    "v_cmp_gt_i32 vcc, 18, %[dq]\n\t" \
    "s_nop 1\n\t" \
    "v_cndmask_b32 v106, v106, v239, vcc\n\t" \
    "v_cmp_gt_i32 vcc, 19, %[dq]\n\t" \
    "s_nop 1\n\t" \
    "v_cndmask_b32 v107, v107, v239, vcc\n\t" \
    "v_cmp_gt_i32 vcc, 24, %[dq]\n\t" \
    "s_nop 1\n\t" \
    "v_cndmask_b32 v108, v108, v239, vcc\n\t" \
    "v_cmp_gt_i32 vcc, 25, %[dq]\n\t" \
    "s_nop 1\n\t" \
    "v_cndmask_b32 v109, v109, v239, vcc\n\t" \
    "v_cmp_gt_i32 vcc, 26, %[dq]\n\t" \
    "s_nop 1\n\t" \
    "v_cndmask_b32 v110, v110, v239, vcc\n\t" \
    "v_cmp_gt_i32 vcc, 27, %[dq]\n\t" \
    "s_nop 1\n\t" \
    "v_cndmask_b32 v111, v111, v239, vcc\n\t" \
    "s_nop 1\n\t" \
    "s_waitcnt lgkmcnt(0)\n\t" \
    "v_mfma_f32_32x32x16_bf16 v[112:127], v[168:171], v[64:67], v[96:111]\n\t" \
    "ds_read_b128 v[200:203], v235 offset:8192\n\t" \
    "ds_read_b128 v[204:207], v235 offset:8704\n\t" \
    "v_add_f32 v237, v144, v145\n\t" \
    "v_cvt_pk_bf16_f32 v160, v144, v145\n\t" \
    "v_mfma_f32_32x32x16_bf16 v[112:127], v[172:175], v[68:71], v[112:127]\n\t" \
    "ds_read_b128 v[208:211], v235 offset:9216\n\t" \
    "ds_read_b128 v[212:215], v235 offset:9728\n\t" \
    "v_add_f32 v237, v237, v146\n\t" \
    "v_add_f32 v237, v237, v147\n\t" \
    "v_cvt_pk_bf16_f32 v161, v146, v147\n\t" \
    "v_mfma_f32_32x32x16_bf16 v[112:127], v[176:179], v[72:75], v[112:127]\n\t" \
    "ds_read_b128 v[216:219], v235 offset:12288\n\t" \
    "ds_read_b128 v[220:223], v235 offset:12800\n\t" \
    "v_add_f32 v237, v237, v148\n\t" \
    "v_add_f32 v237, v237, v149\n\t" \
    "v_cvt_pk_bf16_f32 v162, v148, v149\n\t" \
    "v_mfma_f32_32x32x16_bf16 v[112:127], v[180:183], v[76:79], v[112:127]\n\t" \
    "ds_read_b128 v[224:227], v235 offset:13312\n\t" \
    "ds_read_b128 v[228:231], v235 offset:13824\n\t" \
    "v_add_f32 v237, v237, v150\n\t" \
    "v_add_f32 v237, v237, v151\n\t" \
    "v_cvt_pk_bf16_f32 v163, v150, v151\n\t" \
    "v_mfma_f32_32x32x16_bf16 v[112:127], v[184:187], v[80:83], v[112:127]\n\t" \
    "v_add_f32 v237, v237, v152\n\t" \
    "v_add_f32 v237, v237, v153\n\t" \
    "v_cvt_pk_bf16_f32 v164, v152, v153\n\t" \
    "v_mfma_f32_32x32x16_bf16 v[112:127], v[188:191], v[84:87], v[112:127]\n\t" \
    "v_add_f32 v237, v237, v154\n\t" \
    "v_add_f32 v237, v237, v155\n\t" \
    "v_cvt_pk_bf16_f32 v165, v154, v155\n\t" \
    "v_mfma_f32_32x32x16_bf16 v[112:127], v[192:195], v[88:91], v[112:127]\n\t" \
    "v_add_f32 v237, v237, v156\n\t" \
    "v_add_f32 v237, v237, v157\n\t" \
    "v_cvt_pk_bf16_f32 v166, v156, v157\n\t" \
    "v_mfma_f32_32x32x16_bf16 v[112:127], v[196:199], v[92:95], v[112:127]\n\t" \
    "v_add_f32 v237, v237, v158\n\t" \
    "v_add_f32 v237, v237, v159\n\t" \
    "v_cvt_pk_bf16_f32 v167, v158, v159\n\t" \
    "v_cmp_lt_f32 vcc, %[thr], v237\n\t" \
    "s_cbranch_vccnz RAREF_%=\n\t" \
    "BACKF_%=:\n\t" \
    "s_waitcnt lgkmcnt(0)\n\t" \
    "v_mfma_f32_32x32x16_bf16 v[0:15], v[200:203], v[160:163], v[0:15]\n\t" \
    "ds_read_b128 v[168:171], v232 offset:512\n\t" \
    "ds_read_b128 v[172:175], v232 offset:2560\n\t" \
    "v_add_f32 v236, v236, v237\n\t" \
    "v_mfma_f32_32x32x16_bf16 v[16:31], v[204:207], v[160:163], v[16:31]\n\t" \
    "ds_read_b128 v[176:179], v232 offset:4608\n\t" \
    "ds_read_b128 v[180:183], v232 offset:6656\n\t" \
    "v_exp_f32 v144, v112\n\t" \
    "v_exp_f32 v145, v113\n\t" \
    "v_mfma_f32_32x32x16_bf16 v[32:47], v[208:211], v[160:163], v[32:47]\n\t" \
    "ds_read_b128 v[184:187], v232 offset:8704\n\t" \
    "ds_read_b128 v[188:191], v232 offset:10752\n\t" \
    "v_exp_f32 v146, v114\n\t" \
    "v_exp_f32 v147, v115\n\t" \
    "v_mfma_f32_32x32x16_bf16 v[48:63], v[212:215], v[160:163], v[48:63]\n\t" \
    "ds_read_b128 v[192:195], v232 offset:12800\n\t" \
    "ds_read_b128 v[196:199], v232 offset:14848\n\t" \
    "v_exp_f32 v148, v116\n\t" \
    "v_exp_f32 v149, v117\n\t" \
    "v_mfma_f32_32x32x16_bf16 v[0:15], v[216:219], v[164:167], v[0:15]\n\t" \
    "v_exp_f32 v150, v118\n\t" \
    "v_exp_f32 v151, v119\n\t" \
    "v_exp_f32 v152, v120\n\t" \
    "v_mfma_f32_32x32x16_bf16 v[16:31], v[220:223], v[164:167], v[16:31]\n\t" \
    "v_exp_f32 v153, v121\n\t" \
    "v_exp_f32 v154, v122\n\t" \
    "v_exp_f32 v155, v123\n\t" \
    "v_mfma_f32_32x32x16_bf16 v[32:47], v[224:227], v[164:167], v[32:47]\n\t" \
    "v_exp_f32 v156, v124\n\t" \
    "v_exp_f32 v157, v125\n\t" \
    "v_mfma_f32_32x32x16_bf16 v[48:63], v[228:231], v[164:167], v[48:63]\n\t" \
    "v_exp_f32 v158, v126\n\t" \
    "v_exp_f32 v159, v127\n\t" \
    "s_waitcnt lgkmcnt(0)\n\t" \
    "ds_read_b128 v[200:203], v234 offset:0\n\t" \
    "ds_read_b128 v[204:207], v234 offset:512\n\t" \
    "ds_read_b128 v[208:211], v234 offset:1024\n\t" \
    "ds_read_b128 v[212:215], v234 offset:1536\n\t" \
    "ds_read_b128 v[216:219], v234 offset:4096\n\t" \
    "ds_read_b128 v[220:223], v234 offset:4608\n\t" \
    "ds_read_b128 v[224:227], v234 offset:5120\n\t" \
    "ds_read_b128 v[228:231], v234 offset:5632\n\t" \
    "v_add_f32 v237, v144, v145\n\t" \
    "v_cvt_pk_bf16_f32 v160, v144, v145\n\t" \
    "v_add_f32 v237, v237, v146\n\t" \
    "v_add_f32 v237, v237, v147\n\t" \
    "v_cvt_pk_bf16_f32 v161, v146, v147\n\t" \
    "v_add_f32 v237, v237, v148\n\t" \
    "v_add_f32 v237, v237, v149\n\t" \
    "v_cvt_pk_bf16_f32 v162, v148, v149\n\t" \
    "v_add_f32 v237, v237, v150\n\t" \
    "v_add_f32 v237, v237, v151\n\t" \
    "v_cvt_pk_bf16_f32 v163, v150, v151\n\t" \
    "v_add_f32 v237, v237, v152\n\t" \
    "v_add_f32 v237, v237, v153\n\t" \
    "v_cvt_pk_bf16_f32 v164, v152, v153\n\t" \
    "v_add_f32 v237, v237, v154\n\t" \
    "v_add_f32 v237, v237, v155\n\t" \
    "v_cvt_pk_bf16_f32 v165, v154, v155\n\t" \
    "v_add_f32 v237, v237, v156\n\t" \
    "v_add_f32 v237, v237, v157\n\t" \
    "v_cvt_pk_bf16_f32 v166, v156, v157\n\t" \
    "v_add_f32 v237, v237, v158\n\t" \
    "v_add_f32 v237, v237, v159\n\t" \
    "v_cvt_pk_bf16_f32 v167, v158, v159\n\t" \
    "v_cmp_lt_f32 vcc, %[thr], v237\n\t" \
    "s_cbranch_vccnz RAREG_%=\n\t" \
    "BACKG_%=:\n\t" \
    "s_waitcnt lgkmcnt(0)\n\t" \
    "v_add_f32 v236, v236, v237\n\t" \
    "v_mfma_f32_32x32x16_bf16 v[0:15], v[200:203], v[160:163], v[0:15]\n\t" \
    "v_mfma_f32_32x32x16_bf16 v[16:31], v[204:207], v[160:163], v[16:31]\n\t" \
    "v_mfma_f32_32x32x16_bf16 v[32:47], v[208:211], v[160:163], v[32:47]\n\t" \
    "v_mfma_f32_32x32x16_bf16 v[48:63], v[212:215], v[160:163], v[48:63]\n\t" \
    "v_mfma_f32_32x32x16_bf16 v[0:15], v[216:219], v[164:167], v[0:15]\n\t" \
    "v_mfma_f32_32x32x16_bf16 v[16:31], v[220:223], v[164:167], v[16:31]\n\t" \
    "v_mfma_f32_32x32x16_bf16 v[32:47], v[224:227], v[164:167], v[32:47]\n\t" \
    "v_mfma_f32_32x32x16_bf16 v[48:63], v[228:231], v[164:167], v[48:63]\n\t" \
    "s_branch END_%=\n\t" \
    "PF_%=:\n\t" \
    "s_cmp_eq_u32 %[t], 8\n\t" \
    "s_cbranch_scc0 PF2_%=\n\t" \
    "global_load_dword v250, %[pfo], %[wo]\n\t" \
    "s_branch PFB_%=\n\t" \
    "PF2_%=:\n\t" \
    "global_load_dword v251, %[pfo2], %[w2]\n\t" \
    "s_branch PFB_%=\n\t" \
    "DMAL_%=:\n\t" \
    "s_add_u32 s60, %[t], 3\n\t" \
    "s_min_u32 s60, s60, %[ntm1]\n\t" \
    "s_lshl_b32 s60, s60, 14\n\t" \
    "s_add_u32 s62, %[khl], s60\n\t" \
    "s_addc_u32 s63, %[khh], 0\n\t" \
    "s_add_u32 s61, %[soff], 0xc000\n\t" \
    "s_and_b32 s61, s61, 0xffff\n\t" \
    "s_add_u32 m0, s61, %[kdst]\n\t" \
    "s_nop 0\n\t" \
    "global_load_lds_dwordx4 %[dvo], s[62:63] offset:0\n\t" \
    "global_load_lds_dwordx4 %[dvo], s[62:63] offset:1024\n\t" \
    "global_load_lds_dwordx4 %[dvo], s[62:63] offset:2048\n\t" \
    "global_load_lds_dwordx4 %[dvo], s[62:63] offset:3072\n\t" \
    "s_add_u32 s60, %[t], 2\n\t" \
    "s_min_u32 s60, s60, %[ntm1]\n\t" \
    "s_lshl_b32 s60, s60, 14\n\t" \
    "s_add_u32 s62, %[vhl], s60\n\t" \
    "s_addc_u32 s63, %[vhh], 0\n\t" \
    "s_add_u32 s61, %[soff], 0x8000\n\t" \
    "s_and_b32 s61, s61, 0xffff\n\t" \
    "s_add_u32 m0, s61, %[vdst]\n\t" \
    "s_nop 0\n\t" \
    "global_load_lds_dwordx4 %[dvo], s[62:63] offset:0\n\t" \
    "global_load_lds_dwordx4 %[dvo], s[62:63] offset:1024\n\t" \
    "global_load_lds_dwordx4 %[dvo], s[62:63] offset:2048\n\t" \
    "global_load_lds_dwordx4 %[dvo], s[62:63] offset:3072\n\t" \
    "s_branch DMABL_%=\n\t" \
    "DMAT_%=:\n\t" \
    "s_add_u32 s60, %[t], 3\n\t" \
    "s_min_u32 s60, s60, %[ntm1]\n\t" \
    "s_lshl_b32 s60, s60, 14\n\t" \
    "s_add_u32 s62, %[khl], s60\n\t" \
    "s_addc_u32 s63, %[khh], 0\n\t" \
    "s_add_u32 s61, %[soff], 0xc000\n\t" \
    "s_and_b32 s61, s61, 0xffff\n\t" \
    "s_add_u32 m0, s61, %[kdst]\n\t" \
    "s_nop 0\n\t" \
    "global_load_lds_dwordx4 %[dvo], s[62:63] offset:0\n\t" \
    "global_load_lds_dwordx4 %[dvo], s[62:63] offset:1024\n\t" \
    "global_load_lds_dwordx4 %[dvo], s[62:63] offset:2048\n\t" \
    "global_load_lds_dwordx4 %[dvo], s[62:63] offset:3072\n\t" \
    "s_add_u32 s60, %[t], 2\n\t" \
    "s_min_u32 s60, s60, %[ntm1]\n\t" \
    "s_lshl_b32 s60, s60, 14\n\t" \
    "s_add_u32 s62, %[vhl], s60\n\t" \
    "s_addc_u32 s63, %[vhh], 0\n\t" \
    "s_add_u32 s61, %[soff], 0x8000\n\t" \
    "s_and_b32 s61, s61, 0xffff\n\t" \
    "s_add_u32 m0, s61, %[vdst]\n\t" \
    "s_nop 0\n\t" \
    "global_load_lds_dwordx4 %[dvo], s[62:63] offset:0\n\t" \
    "global_load_lds_dwordx4 %[dvo], s[62:63] offset:1024\n\t" \
    "global_load_lds_dwordx4 %[dvo], s[62:63] offset:2048\n\t" \
    "global_load_lds_dwordx4 %[dvo], s[62:63] offset:3072\n\t" \
    "s_branch DMABT_%=\n\t" \
    "RAREA_%=:\n\t" \
    "s_nop 15\n\t" \
    "v_max3_f32 v239, v128, v129, v130\n\t" \
    "v_max3_f32 v239, v239, v131, v132\n\t" \
    "v_max3_f32 v239, v239, v133, v134\n\t" \
    "v_max3_f32 v239, v239, v135, v136\n\t" \
    "v_max3_f32 v239, v239, v137, v138\n\t" \
    "v_max3_f32 v239, v239, v139, v140\n\t" \
    "v_max3_f32 v239, v239, v141, v142\n\t" \
    "v_max_f32 v239, v239, v143\n\t" \
    "v_max_f32 v239, 0, v239\n\t" \
    "v_mov_b32 v240, v239\n\t" \
    "s_nop 1\n\t" \
    "v_permlane32_swap_b32 v239, v240\n\t" \
    "s_nop 1\n\t" \
    "v_max_f32 v239, v239, v240\n\t" \
    "v_add_f32 v238, v238, v239\n\t" \
    "v_exp_f32 v240, -v239\n\t" \
    "v_sub_f32 v241, 0, v238\n\t" \
    "v_sub_f32 v128, v128, v239\n\t" \
    "v_sub_f32 v112, v112, v239\n\t" \
    "v_mov_b32 v96, v241\n\t" \
    "v_sub_f32 v129, v129, v239\n\t" \
    "v_sub_f32 v113, v113, v239\n\t" \
    "v_mov_b32 v97, v241\n\t" \
    "v_sub_f32 v130, v130, v239\n\t" \
    "v_sub_f32 v114, v114, v239\n\t" \
    "v_mov_b32 v98, v241\n\t" \
    "v_sub_f32 v131, v131, v239\n\t" \
    "v_sub_f32 v115, v115, v239\n\t" \
    "v_mov_b32 v99, v241\n\t" \
    "v_sub_f32 v132, v132, v239\n\t" \
    "v_sub_f32 v116, v116, v239\n\t" \
    "v_mov_b32 v100, v241\n\t" \
    "v_sub_f32 v133, v133, v239\n\t" \
    "v_sub_f32 v117, v117, v239\n\t" \
    "v_mov_b32 v101, v241\n\t" \
    "v_sub_f32 v134, v134, v239\n\t" \
    "v_sub_f32 v118, v118, v239\n\t" \
    "v_mov_b32 v102, v241\n\t" \
    "v_sub_f32 v135, v135, v239\n\t" \
    "v_sub_f32 v119, v119, v239\n\t" \
    "v_mov_b32 v103, v241\n\t" \
    "v_sub_f32 v136, v136, v239\n\t" \
    "v_sub_f32 v120, v120, v239\n\t" \
    "v_mov_b32 v104, v241\n\t" \
    "v_sub_f32 v137, v137, v239\n\t" \
    "v_sub_f32 v121, v121, v239\n\t" \
    "v_mov_b32 v105, v241\n\t" \
    "v_sub_f32 v138, v138, v239\n\t" \
    "v_sub_f32 v122, v122, v239\n\t" \
    "v_mov_b32 v106, v241\n\t" \
    "v_sub_f32 v139, v139, v239\n\t" \
    "v_sub_f32 v123, v123, v239\n\t" \
    "v_mov_b32 v107, v241\n\t" \
    "v_sub_f32 v140, v140, v239\n\t" \
    "v_sub_f32 v124, v124, v239\n\t" \
    "v_mov_b32 v108, v241\n\t" \
    "v_sub_f32 v141, v141, v239\n\t" \
    "v_sub_f32 v125, v125, v239\n\t" \
    "v_mov_b32 v109, v241\n\t" \
    "v_sub_f32 v142, v142, v239\n\t" \
    "v_sub_f32 v126, v126, v239\n\t" \
    "v_mov_b32 v110, v241\n\t" \
    "v_sub_f32 v143, v143, v239\n\t" \
    "v_sub_f32 v127, v127, v239\n\t" \
    "v_mov_b32 v111, v241\n\t" \
    "v_exp_f32 v144, v128\n\t" \
    "v_exp_f32 v145, v129\n\t" \
    "v_exp_f32 v146, v130\n\t" \
    "v_exp_f32 v147, v131\n\t" \
    "v_exp_f32 v148, v132\n\t" \
    "v_exp_f32 v149, v133\n\t" \
    "v_exp_f32 v150, v134\n\t" \
    "v_exp_f32 v151, v135\n\t" \
    "v_exp_f32 v152, v136\n\t" \
    "v_exp_f32 v153, v137\n\t" \
    "v_exp_f32 v154, v138\n\t" \
    "v_exp_f32 v155, v139\n\t" \
    "v_exp_f32 v156, v140\n\t" \
    "v_exp_f32 v157, v141\n\t" \
    "v_exp_f32 v158, v142\n\t" \
    "v_exp_f32 v159, v143\n\t" \
    "v_mul_f32 v236, v236, v240\n\t" \
    "v_mul_f32 v0, v0, v240\n\t" \
    "v_mul_f32 v1, v1, v240\n\t" \
    "v_mul_f32 v2, v2, v240\n\t" \
    "v_mul_f32 v3, v3, v240\n\t" \
    "v_mul_f32 v4, v4, v240\n\t" \
    "v_mul_f32 v5, v5, v240\n\t" \
    "v_mul_f32 v6, v6, v240\n\t" \
    "v_mul_f32 v7, v7, v240\n\t" \
    "v_mul_f32 v8, v8, v240\n\t" \
    "v_mul_f32 v9, v9, v240\n\t" \
    "v_mul_f32 v10, v10, v240\n\t" \
    "v_mul_f32 v11, v11, v240\n\t" \
    "v_mul_f32 v12, v12, v240\n\t" \
    "v_mul_f32 v13, v13, v240\n\t" \
    "v_mul_f32 v14, v14, v240\n\t" \
    "v_mul_f32 v15, v15, v240\n\t" \
    "v_mul_f32 v16, v16, v240\n\t" \
    "v_mul_f32 v17, v17, v240\n\t" \
    "v_mul_f32 v18, v18, v240\n\t" \
    "v_mul_f32 v19, v19, v240\n\t" \
    "v_mul_f32 v20, v20, v240\n\t" \
    "v_mul_f32 v21, v21, v240\n\t" \
    "v_mul_f32 v22, v22, v240\n\t" \
    "v_mul_f32 v23, v23, v240\n\t" \
    "v_mul_f32 v24, v24, v240\n\t" \
    "v_mul_f32 v25, v25, v240\n\t" \
    "v_mul_f32 v26, v26, v240\n\t" \
    "v_mul_f32 v27, v27, v240\n\t" \
    "v_mul_f32 v28, v28, v240\n\t" \
    "v_mul_f32 v29, v29, v240\n\t" \
    "v_mul_f32 v30, v30, v240\n\t" \
    "v_mul_f32 v31, v31, v240\n\t" \
    "v_mul_f32 v32, v32, v240\n\t" \
    "v_mul_f32 v33, v33, v240\n\t" \
    "v_mul_f32 v34, v34, v240\n\t" \
    "v_mul_f32 v35, v35, v240\n\t" \
    "v_mul_f32 v36, v36, v240\n\t" \
    "v_mul_f32 v37, v37, v240\n\t" \
    "v_mul_f32 v38, v38, v240\n\t" \
    "v_mul_f32 v39, v39, v240\n\t" \
    "v_mul_f32 v40, v40, v240\n\t" \
    "v_mul_f32 v41, v41, v240\n\t" \
    "v_mul_f32 v42, v42, v240\n\t" \
    "v_mul_f32 v43, v43, v240\n\t" \
    "v_mul_f32 v44, v44, v240\n\t" \
    "v_mul_f32 v45, v45, v240\n\t" \
    "v_mul_f32 v46, v46, v240\n\t" \
    "v_mul_f32 v47, v47, v240\n\t" \
    "v_mul_f32 v48, v48, v240\n\t" \
    "v_mul_f32 v49, v49, v240\n\t" \
    "v_mul_f32 v50, v50, v240\n\t" \
    "v_mul_f32 v51, v51, v240\n\t" \
    "v_mul_f32 v52, v52, v240\n\t" \
    "v_mul_f32 v53, v53, v240\n\t" \
    "v_mul_f32 v54, v54, v240\n\t" \
    "v_mul_f32 v55, v55, v240\n\t" \
    "v_mul_f32 v56, v56, v240\n\t" \
    "v_mul_f32 v57, v57, v240\n\t" \
    "v_mul_f32 v58, v58, v240\n\t" \
    "v_mul_f32 v59, v59, v240\n\t" \
    "v_mul_f32 v60, v60, v240\n\t" \
    "v_mul_f32 v61, v61, v240\n\t" \
    "v_mul_f32 v62, v62, v240\n\t" \
    "v_mul_f32 v63, v63, v240\n\t" \
    "v_add_f32 v237, v144, v145\n\t" \
    "v_add_f32 v237, v237, v146\n\t" \
    "v_add_f32 v237, v237, v147\n\t" \
    "v_add_f32 v237, v237, v148\n\t" \
    "v_add_f32 v237, v237, v149\n\t" \
    "v_add_f32 v237, v237, v150\n\t" \
    "v_add_f32 v237, v237, v151\n\t" \
    "v_add_f32 v237, v237, v152\n\t" \
    "v_add_f32 v237, v237, v153\n\t" \
    "v_add_f32 v237, v237, v154\n\t" \
    "v_add_f32 v237, v237, v155\n\t" \
    "v_add_f32 v237, v237, v156\n\t" \
    "v_add_f32 v237, v237, v157\n\t" \
    "v_add_f32 v237, v237, v158\n\t" \
    "v_add_f32 v237, v237, v159\n\t" \
    "v_cvt_pk_bf16_f32 v160, v144, v145\n\t" \
    "v_cvt_pk_bf16_f32 v161, v146, v147\n\t" \
    "v_cvt_pk_bf16_f32 v162, v148, v149\n\t" \
    "v_cvt_pk_bf16_f32 v163, v150, v151\n\t" \
    "v_cvt_pk_bf16_f32 v164, v152, v153\n\t" \
    "v_cvt_pk_bf16_f32 v165, v154, v155\n\t" \
    "v_cvt_pk_bf16_f32 v166, v156, v157\n\t" \
    "v_cvt_pk_bf16_f32 v167, v158, v159\n\t" \
    "s_nop 1\n\t" \
    "s_branch BACKA_%=\n\t" \
    "RAREB_%=:\n\t" \
    "s_nop 15\n\t" \
    "v_max3_f32 v239, v112, v113, v114\n\t" \
    "v_max3_f32 v239, v239, v115, v116\n\t" \
    "v_max3_f32 v239, v239, v117, v118\n\t" \
    "v_max3_f32 v239, v239, v119, v120\n\t" \
    "v_max3_f32 v239, v239, v121, v122\n\t" \
    "v_max3_f32 v239, v239, v123, v124\n\t" \
    "v_max3_f32 v239, v239, v125, v126\n\t" \
    "v_max_f32 v239, v239, v127\n\t" \
    "v_max_f32 v239, 0, v239\n\t" \
    "v_mov_b32 v240, v239\n\t" \
    "s_nop 1\n\t" \
    "v_permlane32_swap_b32 v239, v240\n\t" \
    "s_nop 1\n\t" \
    "v_max_f32 v239, v239, v240\n\t" \
    "v_add_f32 v238, v238, v239\n\t" \
    "v_exp_f32 v240, -v239\n\t" \
    "v_sub_f32 v241, 0, v238\n\t" \
    "v_sub_f32 v112, v112, v239\n\t" \
    "v_sub_f32 v128, v128, v239\n\t" \
    "v_mov_b32 v96, v241\n\t" \
    "v_sub_f32 v113, v113, v239\n\t" \
    "v_sub_f32 v129, v129, v239\n\t" \
    "v_mov_b32 v97, v241\n\t" \
    "v_sub_f32 v114, v114, v239\n\t" \
    "v_sub_f32 v130, v130, v239\n\t" \
    "v_mov_b32 v98, v241\n\t" \
    "v_sub_f32 v115, v115, v239\n\t" \
    "v_sub_f32 v131, v131, v239\n\t" \
    "v_mov_b32 v99, v241\n\t" \
    "v_sub_f32 v116, v116, v239\n\t" \
    "v_sub_f32 v132, v132, v239\n\t" \
    "v_mov_b32 v100, v241\n\t" \
    "v_sub_f32 v117, v117, v239\n\t" \
    "v_sub_f32 v133, v133, v239\n\t" \
    "v_mov_b32 v101, v241\n\t" \
    "v_sub_f32 v118, v118, v239\n\t" \
    "v_sub_f32 v134, v134, v239\n\t" \
    "v_mov_b32 v102, v241\n\t" \
    "v_sub_f32 v119, v119, v239\n\t" \
    "v_sub_f32 v135, v135, v239\n\t" \
    "v_mov_b32 v103, v241\n\t" \
    "v_sub_f32 v120, v120, v239\n\t" \
    "v_sub_f32 v136, v136, v239\n\t" \
    "v_mov_b32 v104, v241\n\t" \
    "v_sub_f32 v121, v121, v239\n\t" \
    "v_sub_f32 v137, v137, v239\n\t" \
    "v_mov_b32 v105, v241\n\t" \
    "v_sub_f32 v122, v122, v239\n\t" \
    "v_sub_f32 v138, v138, v239\n\t" \
    "v_mov_b32 v106, v241\n\t" \
    "v_sub_f32 v123, v123, v239\n\t" \
    "v_sub_f32 v139, v139, v239\n\t" \
    "v_mov_b32 v107, v241\n\t" \
    "v_sub_f32 v124, v124, v239\n\t" \
    "v_sub_f32 v140, v140, v239\n\t" \
    "v_mov_b32 v108, v241\n\t" \
    "v_sub_f32 v125, v125, v239\n\t" \
    "v_sub_f32 v141, v141, v239\n\t" \
    "v_mov_b32 v109, v241\n\t" \
    "v_sub_f32 v126, v126, v239\n\t" \
    "v_sub_f32 v142, v142, v239\n\t" \
    "v_mov_b32 v110, v241\n\t" \
    "v_sub_f32 v127, v127, v239\n\t" \
    "v_sub_f32 v143, v143, v239\n\t" \
    "v_mov_b32 v111, v241\n\t" \
    "v_exp_f32 v144, v112\n\t" \
    "v_exp_f32 v145, v113\n\t" \
    "v_exp_f32 v146, v114\n\t" \
    "v_exp_f32 v147, v115\n\t" \
    "v_exp_f32 v148, v116\n\t" \
    "v_exp_f32 v149, v117\n\t" \
    "v_exp_f32 v150, v118\n\t" \
    "v_exp_f32 v151, v119\n\t" \
    "v_exp_f32 v152, v120\n\t" \
    "v_exp_f32 v153, v121\n\t" \
    "v_exp_f32 v154, v122\n\t" \
    "v_exp_f32 v155, v123\n\t" \
    "v_exp_f32 v156, v124\n\t" \
    "v_exp_f32 v157, v125\n\t" \
    "v_exp_f32 v158, v126\n\t" \
    "v_exp_f32 v159, v127\n\t" \
    "v_mul_f32 v236, v236, v240\n\t" \
    "v_mul_f32 v0, v0, v240\n\t" \
    "v_mul_f32 v1, v1, v240\n\t" \
    "v_mul_f32 v2, v2, v240\n\t" \
    "v_mul_f32 v3, v3, v240\n\t" \
    "v_mul_f32 v4, v4, v240\n\t" \
    "v_mul_f32 v5, v5, v240\n\t" \
    "v_mul_f32 v6, v6, v240\n\t" \
    "v_mul_f32 v7, v7, v240\n\t" \
    "v_mul_f32 v8, v8, v240\n\t" \
    "v_mul_f32 v9, v9, v240\n\t" \
    "v_mul_f32 v10, v10, v240\n\t" \
    "v_mul_f32 v11, v11, v240\n\t" \
    "v_mul_f32 v12, v12, v240\n\t" \
    "v_mul_f32 v13, v13, v240\n\t" \
    "v_mul_f32 v14, v14, v240\n\t" \
    "v_mul_f32 v15, v15, v240\n\t" \
    "v_mul_f32 v16, v16, v240\n\t" \
    "v_mul_f32 v17, v17, v240\n\t" \
    "v_mul_f32 v18, v18, v240\n\t" \
    "v_mul_f32 v19, v19, v240\n\t" \
    "v_mul_f32 v20, v20, v240\n\t" \
    "v_mul_f32 v21, v21, v240\n\t" \
    "v_mul_f32 v22, v22, v240\n\t" \
    "v_mul_f32 v23, v23, v240\n\t" \
    "v_mul_f32 v24, v24, v240\n\t" \
    "v_mul_f32 v25, v25, v240\n\t" \
    "v_mul_f32 v26, v26, v240\n\t" \
    "v_mul_f32 v27, v27, v240\n\t" \
    "v_mul_f32 v28, v28, v240\n\t" \
    "v_mul_f32 v29, v29, v240\n\t" \
    "v_mul_f32 v30, v30, v240\n\t" \
    "v_mul_f32 v31, v31, v240\n\t" \
    "v_mul_f32 v32, v32, v240\n\t" \
    "v_mul_f32 v33, v33, v240\n\t" \
    "v_mul_f32 v34, v34, v240\n\t" \
    "v_mul_f32 v35, v35, v240\n\t" \
    "v_mul_f32 v36, v36, v240\n\t" \
    "v_mul_f32 v37, v37, v240\n\t" \
    "v_mul_f32 v38, v38, v240\n\t" \
    "v_mul_f32 v39, v39, v240\n\t" \
    "v_mul_f32 v40, v40, v240\n\t" \
    "v_mul_f32 v41, v41, v240\n\t" \
    "v_mul_f32 v42, v42, v240\n\t" \
    "v_mul_f32 v43, v43, v240\n\t" \
    "v_mul_f32 v44, v44, v240\n\t" \
    "v_mul_f32 v45, v45, v240\n\t" \
    "v_mul_f32 v46, v46, v240\n\t" \
    "v_mul_f32 v47, v47, v240\n\t" \
    "v_mul_f32 v48, v48, v240\n\t" \
    "v_mul_f32 v49, v49, v240\n\t" \
    "v_mul_f32 v50, v50, v240\n\t" \
    "v_mul_f32 v51, v51, v240\n\t" \
    "v_mul_f32 v52, v52, v240\n\t" \
    "v_mul_f32 v53, v53, v240\n\t" \
    "v_mul_f32 v54, v54, v240\n\t" \
    "v_mul_f32 v55, v55, v240\n\t" \
    "v_mul_f32 v56, v56, v240\n\t" \
    "v_mul_f32 v57, v57, v240\n\t" \
    "v_mul_f32 v58, v58, v240\n\t" \
    "v_mul_f32 v59, v59, v240\n\t" \
    "v_mul_f32 v60, v60, v240\n\t" \
    "v_mul_f32 v61, v61, v240\n\t" \
    "v_mul_f32 v62, v62, v240\n\t" \
    "v_mul_f32 v63, v63, v240\n\t" \
    "v_add_f32 v237, v144, v145\n\t" \
    "v_add_f32 v237, v237, v146\n\t" \
    "v_add_f32 v237, v237, v147\n\t" \
    "v_add_f32 v237, v237, v148\n\t" \
    "v_add_f32 v237, v237, v149\n\t" \
    "v_add_f32 v237, v237, v150\n\t" \
    "v_add_f32 v237, v237, v151\n\t" \
    "v_add_f32 v237, v237, v152\n\t" \
    "v_add_f32 v237, v237, v153\n\t" \
    "v_add_f32 v237, v237, v154\n\t" \
    "v_add_f32 v237, v237, v155\n\t" \
    "v_add_f32 v237, v237, v156\n\t" \
    "v_add_f32 v237, v237, v157\n\t" \
    "v_add_f32 v237, v237, v158\n\t" \
    "v_add_f32 v237, v237, v159\n\t" \
    "v_cvt_pk_bf16_f32 v160, v144, v145\n\t" \
    "v_cvt_pk_bf16_f32 v161, v146, v147\n\t" \
    "v_cvt_pk_bf16_f32 v162, v148, v149\n\t" \
    "v_cvt_pk_bf16_f32 v163, v150, v151\n\t" \
    "v_cvt_pk_bf16_f32 v164, v152, v153\n\t" \
    "v_cvt_pk_bf16_f32 v165, v154, v155\n\t" \
    "v_cvt_pk_bf16_f32 v166, v156, v157\n\t" \
    "v_cvt_pk_bf16_f32 v167, v158, v159\n\t" \
    "s_nop 1\n\t" \
    "s_branch BACKB_%=\n\t" \
    "RAREC_%=:\n\t" \
    "s_nop 15\n\t" \
    "v_max3_f32 v239, v128, v129, v130\n\t" \
    "v_max3_f32 v239, v239, v131, v132\n\t" \
    "v_max3_f32 v239, v239, v133, v134\n\t" \
    "v_max3_f32 v239, v239, v135, v136\n\t" \
    "v_max3_f32 v239, v239, v137, v138\n\t" \
    "v_max3_f32 v239, v239, v139, v140\n\t" \
    "v_max3_f32 v239, v239, v141, v142\n\t" \
    "v_max_f32 v239, v239, v143\n\t" \
    "v_max_f32 v239, 0, v239\n\t" \
    "v_mov_b32 v240, v239\n\t" \
    "s_nop 1\n\t" \
    "v_permlane32_swap_b32 v239, v240\n\t" \
    "s_nop 1\n\t" \
    "v_max_f32 v239, v239, v240\n\t" \
    "v_add_f32 v238, v238, v239\n\t" \
    "v_exp_f32 v240, -v239\n\t" \
    "v_sub_f32 v241, 0, v238\n\t" \
    "v_sub_f32 v128, v128, v239\n\t" \
    "v_sub_f32 v112, v112, v239\n\t" \
    "v_mov_b32 v96, v241\n\t" \
    "v_sub_f32 v129, v129, v239\n\t" \
    "v_sub_f32 v113, v113, v239\n\t" \
    "v_mov_b32 v97, v241\n\t" \
    "v_sub_f32 v130, v130, v239\n\t" \
    "v_sub_f32 v114, v114, v239\n\t" \
    "v_mov_b32 v98, v241\n\t" \
    "v_sub_f32 v131, v131, v239\n\t" \
    "v_sub_f32 v115, v115, v239\n\t" \
    "v_mov_b32 v99, v241\n\t" \
    "v_sub_f32 v132, v132, v239\n\t" \
    "v_sub_f32 v116, v116, v239\n\t" \
    "v_mov_b32 v100, v241\n\t" \
    "v_sub_f32 v133, v133, v239\n\t" \
    "v_sub_f32 v117, v117, v239\n\t" \
    "v_mov_b32 v101, v241\n\t" \
    "v_sub_f32 v134, v134, v239\n\t" \
    "v_sub_f32 v118, v118, v239\n\t" \
    "v_mov_b32 v102, v241\n\t" \
    "v_sub_f32 v135, v135, v239\n\t" \
    "v_sub_f32 v119, v119, v239\n\t" \
    "v_mov_b32 v103, v241\n\t" \
    "v_sub_f32 v136, v136, v239\n\t" \
    "v_sub_f32 v120, v120, v239\n\t" \
    "v_mov_b32 v104, v241\n\t" \
    "v_sub_f32 v137, v137, v239\n\t" \
    "v_sub_f32 v121, v121, v239\n\t" \
    "v_mov_b32 v105, v241\n\t" \
    "v_sub_f32 v138, v138, v239\n\t" \
    "v_sub_f32 v122, v122, v239\n\t" \
    "v_mov_b32 v106, v241\n\t" \
    "v_sub_f32 v139, v139, v239\n\t" \
    "v_sub_f32 v123, v123, v239\n\t" \
    "v_mov_b32 v107, v241\n\t" \
    "v_sub_f32 v140, v140, v239\n\t" \
    "v_sub_f32 v124, v124, v239\n\t" \
    "v_mov_b32 v108, v241\n\t" \
    "v_sub_f32 v141, v141, v239\n\t" \
    "v_sub_f32 v125, v125, v239\n\t" \
    "v_mov_b32 v109, v241\n\t" \
    "v_sub_f32 v142, v142, v239\n\t" \
    "v_sub_f32 v126, v126, v239\n\t" \
    "v_mov_b32 v110, v241\n\t" \
    "v_sub_f32 v143, v143, v239\n\t" \
    "v_sub_f32 v127, v127, v239\n\t" \
    "v_mov_b32 v111, v241\n\t" \
    "v_exp_f32 v144, v128\n\t" \
    "v_exp_f32 v145, v129\n\t" \
    "v_exp_f32 v146, v130\n\t" \
    "v_exp_f32 v147, v131\n\t" \
    "v_exp_f32 v148, v132\n\t" \
    "v_exp_f32 v149, v133\n\t" \
    "v_exp_f32 v150, v134\n\t" \
    "v_exp_f32 v151, v135\n\t" \
    "v_exp_f32 v152, v136\n\t" \
    "v_exp_f32 v153, v137\n\t" \
    "v_exp_f32 v154, v138\n\t" \
    "v_exp_f32 v155, v139\n\t" \
    "v_exp_f32 v156, v140\n\t" \
    "v_exp_f32 v157, v141\n\t" \
    "v_exp_f32 v158, v142\n\t" \
    "v_exp_f32 v159, v143\n\t" \
    "v_mul_f32 v236, v236, v240\n\t" \
    "v_mul_f32 v0, v0, v240\n\t" \
    "v_mul_f32 v1, v1, v240\n\t" \
    "v_mul_f32 v2, v2, v240\n\t" \
    "v_mul_f32 v3, v3, v240\n\t" \
    "v_mul_f32 v4, v4, v240\n\t" \
    "v_mul_f32 v5, v5, v240\n\t" \
    "v_mul_f32 v6, v6, v240\n\t" \
    "v_mul_f32 v7, v7, v240\n\t" \
    "v_mul_f32 v8, v8, v240\n\t" \
    "v_mul_f32 v9, v9, v240\n\t" \
    "v_mul_f32 v10, v10, v240\n\t" \
    "v_mul_f32 v11, v11, v240\n\t" \
    "v_mul_f32 v12, v12, v240\n\t" \
    "v_mul_f32 v13, v13, v240\n\t" \
    "v_mul_f32 v14, v14, v240\n\t" \
    "v_mul_f32 v15, v15, v240\n\t" \
    "v_mul_f32 v16, v16, v240\n\t" \
    "v_mul_f32 v17, v17, v240\n\t" \
    "v_mul_f32 v18, v18, v240\n\t" \
    "v_mul_f32 v19, v19, v240\n\t" \
    "v_mul_f32 v20, v20, v240\n\t" \
    "v_mul_f32 v21, v21, v240\n\t" \
    "v_mul_f32 v22, v22, v240\n\t" \
    "v_mul_f32 v23, v23, v240\n\t" \
    "v_mul_f32 v24, v24, v240\n\t" \
    "v_mul_f32 v25, v25, v240\n\t" \
    "v_mul_f32 v26, v26, v240\n\t" \
    "v_mul_f32 v27, v27, v240\n\t" \
    "v_mul_f32 v28, v28, v240\n\t" \
    "v_mul_f32 v29, v29, v240\n\t" \
    "v_mul_f32 v30, v30, v240\n\t" \
    "v_mul_f32 v31, v31, v240\n\t" \
    "v_mul_f32 v32, v32, v240\n\t" \
    "v_mul_f32 v33, v33, v240\n\t" \
    "v_mul_f32 v34, v34, v240\n\t" \
    "v_mul_f32 v35, v35, v240\n\t" \
    "v_mul_f32 v36, v36, v240\n\t" \
    "v_mul_f32 v37, v37, v240\n\t" \
    "v_mul_f32 v38, v38, v240\n\t" \
    "v_mul_f32 v39, v39, v240\n\t" \
    "v_mul_f32 v40, v40, v240\n\t" \
    "v_mul_f32 v41, v41, v240\n\t" \
    "v_mul_f32 v42, v42, v240\n\t" \
    "v_mul_f32 v43, v43, v240\n\t" \
    "v_mul_f32 v44, v44, v240\n\t" \
    "v_mul_f32 v45, v45, v240\n\t" \
    "v_mul_f32 v46, v46, v240\n\t" \
    "v_mul_f32 v47, v47, v240\n\t" \
    "v_mul_f32 v48, v48, v240\n\t" \
    "v_mul_f32 v49, v49, v240\n\t" \
    "v_mul_f32 v50, v50, v240\n\t" \
    "v_mul_f32 v51, v51, v240\n\t" \
    "v_mul_f32 v52, v52, v240\n\t" \
    "v_mul_f32 v53, v53, v240\n\t" \
    "v_mul_f32 v54, v54, v240\n\t" \
    "v_mul_f32 v55, v55, v240\n\t" \
    "v_mul_f32 v56, v56, v240\n\t" \
    "v_mul_f32 v57, v57, v240\n\t" \
    "v_mul_f32 v58, v58, v240\n\t" \
    "v_mul_f32 v59, v59, v240\n\t" \
    "v_mul_f32 v60, v60, v240\n\t" \
    "v_mul_f32 v61, v61, v240\n\t" \
    "v_mul_f32 v62, v62, v240\n\t" \
    "v_mul_f32 v63, v63, v240\n\t" \
    "v_add_f32 v237, v144, v145\n\t" \
    "v_add_f32 v237, v237, v146\n\t" \
    "v_add_f32 v237, v237, v147\n\t" \
    "v_add_f32 v237, v237, v148\n\t" \
    "v_add_f32 v237, v237, v149\n\t" \
    "v_add_f32 v237, v237, v150\n\t" \
    "v_add_f32 v237, v237, v151\n\t" \
    "v_add_f32 v237, v237, v152\n\t" \
    "v_add_f32 v237, v237, v153\n\t" \
    "v_add_f32 v237, v237, v154\n\t" \
    "v_add_f32 v237, v237, v155\n\t" \
    "v_add_f32 v237, v237, v156\n\t" \
    "v_add_f32 v237, v237, v157\n\t" \
    "v_add_f32 v237, v237, v158\n\t" \
    "v_add_f32 v237, v237, v159\n\t" \
    "v_cvt_pk_bf16_f32 v160, v144, v145\n\t" \
    "v_cvt_pk_bf16_f32 v161, v146, v147\n\t" \
    "v_cvt_pk_bf16_f32 v162, v148, v149\n\t" \
    "v_cvt_pk_bf16_f32 v163, v150, v151\n\t" \
    "v_cvt_pk_bf16_f32 v164, v152, v153\n\t" \
    "v_cvt_pk_bf16_f32 v165, v154, v155\n\t" \
    "v_cvt_pk_bf16_f32 v166, v156, v157\n\t" \
    "v_cvt_pk_bf16_f32 v167, v158, v159\n\t" \
    "s_nop 1\n\t" \
    "s_branch BACKC_%=\n\t" \
    "RARED_%=:\n\t" \
    "s_nop 15\n\t" \
    "v_max3_f32 v239, v112, v113, v114\n\t" \
    "v_max3_f32 v239, v239, v115, v116\n\t" \
    "v_max3_f32 v239, v239, v117, v118\n\t" \
    "v_max3_f32 v239, v239, v119, v120\n\t" \
    "v_max3_f32 v239, v239, v121, v122\n\t" \
    "v_max3_f32 v239, v239, v123, v124\n\t" \
    "v_max3_f32 v239, v239, v125, v126\n\t" \
    "v_max_f32 v239, v239, v127\n\t" \
    "v_max_f32 v239, 0, v239\n\t" \
    "v_mov_b32 v240, v239\n\t" \
    "s_nop 1\n\t" \
    "v_permlane32_swap_b32 v239, v240\n\t" \
    "s_nop 1\n\t" \
    "v_max_f32 v239, v239, v240\n\t" \
    "v_add_f32 v238, v238, v239\n\t" \
    "v_exp_f32 v240, -v239\n\t" \
    "v_sub_f32 v241, 0, v238\n\t" \
    "v_sub_f32 v112, v112, v239\n\t" \
    "v_sub_f32 v128, v128, v239\n\t" \
    "v_mov_b32 v96, v241\n\t" \
    "v_sub_f32 v113, v113, v239\n\t" \
    "v_sub_f32 v129, v129, v239\n\t" \
    "v_mov_b32 v97, v241\n\t" \
    "v_sub_f32 v114, v114, v239\n\t" \
    "v_sub_f32 v130, v130, v239\n\t" \
    "v_mov_b32 v98, v241\n\t" \
    "v_sub_f32 v115, v115, v239\n\t" \
    "v_sub_f32 v131, v131, v239\n\t" \
    "v_mov_b32 v99, v241\n\t" \
    "v_sub_f32 v116, v116, v239\n\t" \
    "v_sub_f32 v132, v132, v239\n\t" \
    "v_mov_b32 v100, v241\n\t" \
    "v_sub_f32 v117, v117, v239\n\t" \
    "v_sub_f32 v133, v133, v239\n\t" \
    "v_mov_b32 v101, v241\n\t" \
    "v_sub_f32 v118, v118, v239\n\t" \
    "v_sub_f32 v134, v134, v239\n\t" \
    "v_mov_b32 v102, v241\n\t" \
    "v_sub_f32 v119, v119, v239\n\t" \
    "v_sub_f32 v135, v135, v239\n\t" \
    "v_mov_b32 v103, v241\n\t" \
    "v_sub_f32 v120, v120, v239\n\t" \
    "v_sub_f32 v136, v136, v239\n\t" \
    "v_mov_b32 v104, v241\n\t" \
    "v_sub_f32 v121, v121, v239\n\t" \
    "v_sub_f32 v137, v137, v239\n\t" \
    "v_mov_b32 v105, v241\n\t" \
    "v_sub_f32 v122, v122, v239\n\t" \
    "v_sub_f32 v138, v138, v239\n\t" \
    "v_mov_b32 v106, v241\n\t" \
    "v_sub_f32 v123, v123, v239\n\t" \
    "v_sub_f32 v139, v139, v239\n\t" \
    "v_mov_b32 v107, v241\n\t" \
    "v_sub_f32 v124, v124, v239\n\t" \
    "v_sub_f32 v140, v140, v239\n\t" \
    "v_mov_b32 v108, v241\n\t" \
    "v_sub_f32 v125, v125, v239\n\t" \
    "v_sub_f32 v141, v141, v239\n\t" \
    "v_mov_b32 v109, v241\n\t" \
    "v_sub_f32 v126, v126, v239\n\t" \
    "v_sub_f32 v142, v142, v239\n\t" \
    "v_mov_b32 v110, v241\n\t" \
    "v_sub_f32 v127, v127, v239\n\t" \
    "v_sub_f32 v143, v143, v239\n\t" \
    "v_mov_b32 v111, v241\n\t" \
    "v_exp_f32 v144, v112\n\t" \
    "v_exp_f32 v145, v113\n\t" \
    "v_exp_f32 v146, v114\n\t" \
    "v_exp_f32 v147, v115\n\t" \
    "v_exp_f32 v148, v116\n\t" \
    "v_exp_f32 v149, v117\n\t" \
    "v_exp_f32 v150, v118\n\t" \
    "v_exp_f32 v151, v119\n\t" \
    "v_exp_f32 v152, v120\n\t" \
    "v_exp_f32 v153, v121\n\t" \
    "v_exp_f32 v154, v122\n\t" \
    "v_exp_f32 v155, v123\n\t" \
    "v_exp_f32 v156, v124\n\t" \
    "v_exp_f32 v157, v125\n\t" \
    "v_exp_f32 v158, v126\n\t" \
    "v_exp_f32 v159, v127\n\t" \
    "v_mul_f32 v236, v236, v240\n\t" \
    "v_mul_f32 v0, v0, v240\n\t" \
    "v_mul_f32 v1, v1, v240\n\t" \
    "v_mul_f32 v2, v2, v240\n\t" \
    "v_mul_f32 v3, v3, v240\n\t" \
    "v_mul_f32 v4, v4, v240\n\t" \
    "v_mul_f32 v5, v5, v240\n\t" \
    "v_mul_f32 v6, v6, v240\n\t" \
    "v_mul_f32 v7, v7, v240\n\t" \
    "v_mul_f32 v8, v8, v240\n\t" \
    "v_mul_f32 v9, v9, v240\n\t" \
    "v_mul_f32 v10, v10, v240\n\t" \
    "v_mul_f32 v11, v11, v240\n\t" \
    "v_mul_f32 v12, v12, v240\n\t" \
    "v_mul_f32 v13, v13, v240\n\t" \
    "v_mul_f32 v14, v14, v240\n\t" \
    "v_mul_f32 v15, v15, v240\n\t" \
    "v_mul_f32 v16, v16, v240\n\t" \
    "v_mul_f32 v17, v17, v240\n\t" \
    "v_mul_f32 v18, v18, v240\n\t" \
    "v_mul_f32 v19, v19, v240\n\t" \
    "v_mul_f32 v20, v20, v240\n\t" \
    "v_mul_f32 v21, v21, v240\n\t" \
    "v_mul_f32 v22, v22, v240\n\t" \
    "v_mul_f32 v23, v23, v240\n\t" \
    "v_mul_f32 v24, v24, v240\n\t" \
    "v_mul_f32 v25, v25, v240\n\t" \
    "v_mul_f32 v26, v26, v240\n\t" \
    "v_mul_f32 v27, v27, v240\n\t" \
    "v_mul_f32 v28, v28, v240\n\t" \
    "v_mul_f32 v29, v29, v240\n\t" \
    "v_mul_f32 v30, v30, v240\n\t" \
    "v_mul_f32 v31, v31, v240\n\t" \
    "v_mul_f32 v32, v32, v240\n\t" \
    "v_mul_f32 v33, v33, v240\n\t" \
    "v_mul_f32 v34, v34, v240\n\t" \
    "v_mul_f32 v35, v35, v240\n\t" \
    "v_mul_f32 v36, v36, v240\n\t" \
    "v_mul_f32 v37, v37, v240\n\t" \
    "v_mul_f32 v38, v38, v240\n\t" \
    "v_mul_f32 v39, v39, v240\n\t" \
    "v_mul_f32 v40, v40, v240\n\t" \
    "v_mul_f32 v41, v41, v240\n\t" \
    "v_mul_f32 v42, v42, v240\n\t" \
    "v_mul_f32 v43, v43, v240\n\t" \
    "v_mul_f32 v44, v44, v240\n\t" \
    "v_mul_f32 v45, v45, v240\n\t" \
    "v_mul_f32 v46, v46, v240\n\t" \
    "v_mul_f32 v47, v47, v240\n\t" \
    "v_mul_f32 v48, v48, v240\n\t" \
    "v_mul_f32 v49, v49, v240\n\t" \
    "v_mul_f32 v50, v50, v240\n\t" \
    "v_mul_f32 v51, v51, v240\n\t" \
    "v_mul_f32 v52, v52, v240\n\t" \
    "v_mul_f32 v53, v53, v240\n\t" \
    "v_mul_f32 v54, v54, v240\n\t" \
    "v_mul_f32 v55, v55, v240\n\t" \
    "v_mul_f32 v56, v56, v240\n\t" \
    "v_mul_f32 v57, v57, v240\n\t" \
    "v_mul_f32 v58, v58, v240\n\t" \
    "v_mul_f32 v59, v59, v240\n\t" \
    "v_mul_f32 v60, v60, v240\n\t" \
    "v_mul_f32 v61, v61, v240\n\t" \
    "v_mul_f32 v62, v62, v240\n\t" \
    "v_mul_f32 v63, v63, v240\n\t" \
    "v_add_f32 v237, v144, v145\n\t" \
    "v_add_f32 v237, v237, v146\n\t" \
    "v_add_f32 v237, v237, v147\n\t" \
    "v_add_f32 v237, v237, v148\n\t" \
    "v_add_f32 v237, v237, v149\n\t" \
    "v_add_f32 v237, v237, v150\n\t" \
    "v_add_f32 v237, v237, v151\n\t" \
    "v_add_f32 v237, v237, v152\n\t" \
    "v_add_f32 v237, v237, v153\n\t" \
    "v_add_f32 v237, v237, v154\n\t" \
    "v_add_f32 v237, v237, v155\n\t" \
    "v_add_f32 v237, v237, v156\n\t" \
    "v_add_f32 v237, v237, v157\n\t" \
    "v_add_f32 v237, v237, v158\n\t" \
    "v_add_f32 v237, v237, v159\n\t" \
    "v_cvt_pk_bf16_f32 v160, v144, v145\n\t" \
    "v_cvt_pk_bf16_f32 v161, v146, v147\n\t" \
    "v_cvt_pk_bf16_f32 v162, v148, v149\n\t" \
    "v_cvt_pk_bf16_f32 v163, v150, v151\n\t" \
    "v_cvt_pk_bf16_f32 v164, v152, v153\n\t" \
    "v_cvt_pk_bf16_f32 v165, v154, v155\n\t" \
    "v_cvt_pk_bf16_f32 v166, v156, v157\n\t" \
    "v_cvt_pk_bf16_f32 v167, v158, v159\n\t" \
    "s_nop 1\n\t" \
    "s_branch BACKD_%=\n\t" \
    "RAREE_%=:\n\t" \
    "s_nop 15\n\t" \
    "v_max3_f32 v239, v128, v129, v130\n\t" \
    "v_max3_f32 v239, v239, v131, v132\n\t" \
    "v_max3_f32 v239, v239, v133, v134\n\t" \
    "v_max3_f32 v239, v239, v135, v136\n\t" \
    "v_max3_f32 v239, v239, v137, v138\n\t" \
    "v_max3_f32 v239, v239, v139, v140\n\t" \
    "v_max3_f32 v239, v239, v141, v142\n\t" \
    "v_max_f32 v239, v239, v143\n\t" \
    "v_max_f32 v239, 0, v239\n\t" \
    "v_mov_b32 v240, v239\n\t" \
    "s_nop 1\n\t" \
    "v_permlane32_swap_b32 v239, v240\n\t" \
    "s_nop 1\n\t" \
    "v_max_f32 v239, v239, v240\n\t" \
    "v_add_f32 v238, v238, v239\n\t" \
    "v_exp_f32 v240, -v239\n\t" \
    "v_sub_f32 v241, 0, v238\n\t" \
    "v_sub_f32 v128, v128, v239\n\t" \
    "v_mov_b32 v96, v241\n\t" \
    "v_sub_f32 v129, v129, v239\n\t" \
    "v_mov_b32 v97, v241\n\t" \
    "v_sub_f32 v130, v130, v239\n\t" \
    "v_mov_b32 v98, v241\n\t" \
    "v_sub_f32 v131, v131, v239\n\t" \
    "v_mov_b32 v99, v241\n\t" \
    "v_sub_f32 v132, v132, v239\n\t" \
    "v_mov_b32 v100, v241\n\t" \
    "v_sub_f32 v133, v133, v239\n\t" \
    "v_mov_b32 v101, v241\n\t" \
    "v_sub_f32 v134, v134, v239\n\t" \
    "v_mov_b32 v102, v241\n\t" \
    "v_sub_f32 v135, v135, v239\n\t" \
    "v_mov_b32 v103, v241\n\t" \
    "v_sub_f32 v136, v136, v239\n\t" \
    "v_mov_b32 v104, v241\n\t" \
    "v_sub_f32 v137, v137, v239\n\t" \
    "v_mov_b32 v105, v241\n\t" \
    "v_sub_f32 v138, v138, v239\n\t" \
    "v_mov_b32 v106, v241\n\t" \
    "v_sub_f32 v139, v139, v239\n\t" \
    "v_mov_b32 v107, v241\n\t" \
    "v_sub_f32 v140, v140, v239\n\t" \
    "v_mov_b32 v108, v241\n\t" \
    "v_sub_f32 v141, v141, v239\n\t" \
    "v_mov_b32 v109, v241\n\t" \
    "v_sub_f32 v142, v142, v239\n\t" \
    "v_mov_b32 v110, v241\n\t" \
    "v_sub_f32 v143, v143, v239\n\t" \
    "v_mov_b32 v111, v241\n\t" \
    "v_exp_f32 v144, v128\n\t" \
    "v_exp_f32 v145, v129\n\t" \
    "v_exp_f32 v146, v130\n\t" \
    "v_exp_f32 v147, v131\n\t" \
    "v_exp_f32 v148, v132\n\t" \
    "v_exp_f32 v149, v133\n\t" \
    "v_exp_f32 v150, v134\n\t" \
    "v_exp_f32 v151, v135\n\t" \
    "v_exp_f32 v152, v136\n\t" \
    "v_exp_f32 v153, v137\n\t" \
    "v_exp_f32 v154, v138\n\t" \
    "v_exp_f32 v155, v139\n\t" \
    "v_exp_f32 v156, v140\n\t" \
    "v_exp_f32 v157, v141\n\t" \
    "v_exp_f32 v158, v142\n\t" \
    "v_exp_f32 v159, v143\n\t" \
    "v_mul_f32 v236, v236, v240\n\t" \
    "v_mul_f32 v0, v0, v240\n\t" \
    "v_mul_f32 v1, v1, v240\n\t" \
    "v_mul_f32 v2, v2, v240\n\t" \
    "v_mul_f32 v3, v3, v240\n\t" \
    "v_mul_f32 v4, v4, v240\n\t" \
    "v_mul_f32 v5, v5, v240\n\t" \
    "v_mul_f32 v6, v6, v240\n\t" \
    "v_mul_f32 v7, v7, v240\n\t" \
    "v_mul_f32 v8, v8, v240\n\t" \
    "v_mul_f32 v9, v9, v240\n\t" \
    "v_mul_f32 v10, v10, v240\n\t" \
    "v_mul_f32 v11, v11, v240\n\t" \
    "v_mul_f32 v12, v12, v240\n\t" \
    "v_mul_f32 v13, v13, v240\n\t" \
    "v_mul_f32 v14, v14, v240\n\t" \
    "v_mul_f32 v15, v15, v240\n\t" \
    "v_mul_f32 v16, v16, v240\n\t" \
    "v_mul_f32 v17, v17, v240\n\t" \
    "v_mul_f32 v18, v18, v240\n\t" \
    "v_mul_f32 v19, v19, v240\n\t" \
    "v_mul_f32 v20, v20, v240\n\t" \
    "v_mul_f32 v21, v21, v240\n\t" \
    "v_mul_f32 v22, v22, v240\n\t" \
    "v_mul_f32 v23, v23, v240\n\t" \
    "v_mul_f32 v24, v24, v240\n\t" \
    "v_mul_f32 v25, v25, v240\n\t" \
    "v_mul_f32 v26, v26, v240\n\t" \
    "v_mul_f32 v27, v27, v240\n\t" \
    "v_mul_f32 v28, v28, v240\n\t" \
    "v_mul_f32 v29, v29, v240\n\t" \
    "v_mul_f32 v30, v30, v240\n\t" \
    "v_mul_f32 v31, v31, v240\n\t" \
    "v_mul_f32 v32, v32, v240\n\t" \
    "v_mul_f32 v33, v33, v240\n\t" \
    "v_mul_f32 v34, v34, v240\n\t" \
    "v_mul_f32 v35, v35, v240\n\t" \
    "v_mul_f32 v36, v36, v240\n\t" \
    "v_mul_f32 v37, v37, v240\n\t" \
    "v_mul_f32 v38, v38, v240\n\t" \
    "v_mul_f32 v39, v39, v240\n\t" \
    "v_mul_f32 v40, v40, v240\n\t" \
    "v_mul_f32 v41, v41, v240\n\t" \
    "v_mul_f32 v42, v42, v240\n\t" \
    "v_mul_f32 v43, v43, v240\n\t" \
    "v_mul_f32 v44, v44, v240\n\t" \
    "v_mul_f32 v45, v45, v240\n\t" \
    "v_mul_f32 v46, v46, v240\n\t" \
    "v_mul_f32 v47, v47, v240\n\t" \
    "v_mul_f32 v48, v48, v240\n\t" \
    "v_mul_f32 v49, v49, v240\n\t" \
    "v_mul_f32 v50, v50, v240\n\t" \
    "v_mul_f32 v51, v51, v240\n\t" \
    "v_mul_f32 v52, v52, v240\n\t" \
    "v_mul_f32 v53, v53, v240\n\t" \
    "v_mul_f32 v54, v54, v240\n\t" \
    "v_mul_f32 v55, v55, v240\n\t" \
    "v_mul_f32 v56, v56, v240\n\t" \
    "v_mul_f32 v57, v57, v240\n\t" \
    "v_mul_f32 v58, v58, v240\n\t" \
    "v_mul_f32 v59, v59, v240\n\t" \
    "v_mul_f32 v60, v60, v240\n\t" \
    "v_mul_f32 v61, v61, v240\n\t" \
    "v_mul_f32 v62, v62, v240\n\t" \
    "v_mul_f32 v63, v63, v240\n\t" \
    "v_add_f32 v237, v144, v145\n\t" \
    "v_add_f32 v237, v237, v146\n\t" \
    "v_add_f32 v237, v237, v147\n\t" \
    "v_add_f32 v237, v237, v148\n\t" \
    "v_add_f32 v237, v237, v149\n\t" \
    "v_add_f32 v237, v237, v150\n\t" \
    "v_add_f32 v237, v237, v151\n\t" \
    "v_add_f32 v237, v237, v152\n\t" \
    "v_add_f32 v237, v237, v153\n\t" \
    "v_add_f32 v237, v237, v154\n\t" \
    "v_add_f32 v237, v237, v155\n\t" \
    "v_add_f32 v237, v237, v156\n\t" \
    "v_add_f32 v237, v237, v157\n\t" \
    "v_add_f32 v237, v237, v158\n\t" \
    "v_add_f32 v237, v237, v159\n\t" \
    "v_cvt_pk_bf16_f32 v160, v144, v145\n\t" \
    "v_cvt_pk_bf16_f32 v161, v146, v147\n\t" \
    "v_cvt_pk_bf16_f32 v162, v148, v149\n\t" \
    "v_cvt_pk_bf16_f32 v163, v150, v151\n\t" \
    "v_cvt_pk_bf16_f32 v164, v152, v153\n\t" \
    "v_cvt_pk_bf16_f32 v165, v154, v155\n\t" \
    "v_cvt_pk_bf16_f32 v166, v156, v157\n\t" \
    "v_cvt_pk_bf16_f32 v167, v158, v159\n\t" \
    "s_nop 1\n\t" \
    "s_branch BACKE_%=\n\t" \
    "RAREF_%=:\n\t" \
    "s_nop 15\n\t" \
    "v_max3_f32 v239, v128, v129, v130\n\t" \
    "v_max3_f32 v239, v239, v131, v132\n\t" \
    "v_max3_f32 v239, v239, v133, v134\n\t" \
    "v_max3_f32 v239, v239, v135, v136\n\t" \
    "v_max3_f32 v239, v239, v137, v138\n\t" \
    "v_max3_f32 v239, v239, v139, v140\n\t" \
    "v_max3_f32 v239, v239, v141, v142\n\t" \
    "v_max_f32 v239, v239, v143\n\t" \
    "v_max_f32 v239, 0, v239\n\t" \
    "v_mov_b32 v240, v239\n\t" \
    "s_nop 1\n\t" \
    "v_permlane32_swap_b32 v239, v240\n\t" \
    "s_nop 1\n\t" \
    "v_max_f32 v239, v239, v240\n\t" \
    "v_add_f32 v238, v238, v239\n\t" \
    "v_exp_f32 v240, -v239\n\t" \
    "v_sub_f32 v241, 0, v238\n\t" \
    "v_sub_f32 v128, v128, v239\n\t" \
    "v_sub_f32 v112, v112, v239\n\t" \
    "v_mov_b32 v96, v241\n\t" \
    "v_sub_f32 v129, v129, v239\n\t" \
    "v_sub_f32 v113, v113, v239\n\t" \
    "v_mov_b32 v97, v241\n\t" \
    "v_sub_f32 v130, v130, v239\n\t" \
    "v_sub_f32 v114, v114, v239\n\t" \
    "v_mov_b32 v98, v241\n\t" \
    "v_sub_f32 v131, v131, v239\n\t" \
    "v_sub_f32 v115, v115, v239\n\t" \
    "v_mov_b32 v99, v241\n\t" \
    "v_sub_f32 v132, v132, v239\n\t" \
    "v_sub_f32 v116, v116, v239\n\t" \
    "v_mov_b32 v100, v241\n\t" \
    "v_sub_f32 v133, v133, v239\n\t" \
    "v_sub_f32 v117, v117, v239\n\t" \
    "v_mov_b32 v101, v241\n\t" \
    "v_sub_f32 v134, v134, v239\n\t" \
    "v_sub_f32 v118, v118, v239\n\t" \
    "v_mov_b32 v102, v241\n\t" \
    "v_sub_f32 v135, v135, v239\n\t" \
    "v_sub_f32 v119, v119, v239\n\t" \
    "v_mov_b32 v103, v241\n\t" \
    "v_sub_f32 v136, v136, v239\n\t" \
    "v_sub_f32 v120, v120, v239\n\t" \
    "v_mov_b32 v104, v241\n\t" \
    "v_sub_f32 v137, v137, v239\n\t" \
    "v_sub_f32 v121, v121, v239\n\t" \
    "v_mov_b32 v105, v241\n\t" \
    "v_sub_f32 v138, v138, v239\n\t" \
    "v_sub_f32 v122, v122, v239\n\t" \
    "v_mov_b32 v106, v241\n\t" \
    "v_sub_f32 v139, v139, v239\n\t" \
    "v_sub_f32 v123, v123, v239\n\t" \
    "v_mov_b32 v107, v241\n\t" \
    "v_sub_f32 v140, v140, v239\n\t" \
    "v_sub_f32 v124, v124, v239\n\t" \
    "v_mov_b32 v108, v241\n\t" \
    "v_sub_f32 v141, v141, v239\n\t" \
    "v_sub_f32 v125, v125, v239\n\t" \
    "v_mov_b32 v109, v241\n\t" \
    "v_sub_f32 v142, v142, v239\n\t" \
    "v_sub_f32 v126, v126, v239\n\t" \
    "v_mov_b32 v110, v241\n\t" \
    "v_sub_f32 v143, v143, v239\n\t" \
    "v_sub_f32 v127, v127, v239\n\t" \
    "v_mov_b32 v111, v241\n\t" \
    "v_exp_f32 v144, v128\n\t" \
    "v_exp_f32 v145, v129\n\t" \
    "v_exp_f32 v146, v130\n\t" \
    "v_exp_f32 v147, v131\n\t" \
    "v_exp_f32 v148, v132\n\t" \
    "v_exp_f32 v149, v133\n\t" \
    "v_exp_f32 v150, v134\n\t" \
    "v_exp_f32 v151, v135\n\t" \
    "v_exp_f32 v152, v136\n\t" \
    "v_exp_f32 v153, v137\n\t" \
    "v_exp_f32 v154, v138\n\t" \
    "v_exp_f32 v155, v139\n\t" \
    "v_exp_f32 v156, v140\n\t" \
    "v_exp_f32 v157, v141\n\t" \
    "v_exp_f32 v158, v142\n\t" \
    "v_exp_f32 v159, v143\n\t" \
    "v_mul_f32 v236, v236, v240\n\t" \
    "v_mul_f32 v0, v0, v240\n\t" \
    "v_mul_f32 v1, v1, v240\n\t" \
    "v_mul_f32 v2, v2, v240\n\t" \
    "v_mul_f32 v3, v3, v240\n\t" \
    "v_mul_f32 v4, v4, v240\n\t" \
    "v_mul_f32 v5, v5, v240\n\t" \
    "v_mul_f32 v6, v6, v240\n\t" \
    "v_mul_f32 v7, v7, v240\n\t" \
    "v_mul_f32 v8, v8, v240\n\t" \
    "v_mul_f32 v9, v9, v240\n\t" \
    "v_mul_f32 v10, v10, v240\n\t" \
    "v_mul_f32 v11, v11, v240\n\t" \
    "v_mul_f32 v12, v12, v240\n\t" \
    "v_mul_f32 v13, v13, v240\n\t" \
    "v_mul_f32 v14, v14, v240\n\t" \
    "v_mul_f32 v15, v15, v240\n\t" \
    "v_mul_f32 v16, v16, v240\n\t" \
    "v_mul_f32 v17, v17, v240\n\t" \
    "v_mul_f32 v18, v18, v240\n\t" \
    "v_mul_f32 v19, v19, v240\n\t" \
    "v_mul_f32 v20, v20, v240\n\t" \
    "v_mul_f32 v21, v21, v240\n\t" \
    "v_mul_f32 v22, v22, v240\n\t" \
    "v_mul_f32 v23, v23, v240\n\t" \
    "v_mul_f32 v24, v24, v240\n\t" \
    "v_mul_f32 v25, v25, v240\n\t" \
    "v_mul_f32 v26, v26, v240\n\t" \
    "v_mul_f32 v27, v27, v240\n\t" \
    "v_mul_f32 v28, v28, v240\n\t" \
    "v_mul_f32 v29, v29, v240\n\t" \
    "v_mul_f32 v30, v30, v240\n\t" \
    "v_mul_f32 v31, v31, v240\n\t" \
    "v_mul_f32 v32, v32, v240\n\t" \
    "v_mul_f32 v33, v33, v240\n\t" \
    "v_mul_f32 v34, v34, v240\n\t" \
    "v_mul_f32 v35, v35, v240\n\t" \
    "v_mul_f32 v36, v36, v240\n\t" \
    "v_mul_f32 v37, v37, v240\n\t" \
    "v_mul_f32 v38, v38, v240\n\t" \
    "v_mul_f32 v39, v39, v240\n\t" \
    "v_mul_f32 v40, v40, v240\n\t" \
    "v_mul_f32 v41, v41, v240\n\t" \
    "v_mul_f32 v42, v42, v240\n\t" \
    "v_mul_f32 v43, v43, v240\n\t" \
    "v_mul_f32 v44, v44, v240\n\t" \
    "v_mul_f32 v45, v45, v240\n\t" \
    "v_mul_f32 v46, v46, v240\n\t" \
    "v_mul_f32 v47, v47, v240\n\t" \
    "v_mul_f32 v48, v48, v240\n\t" \
    "v_mul_f32 v49, v49, v240\n\t" \
    "v_mul_f32 v50, v50, v240\n\t" \
    "v_mul_f32 v51, v51, v240\n\t" \
    "v_mul_f32 v52, v52, v240\n\t" \
    "v_mul_f32 v53, v53, v240\n\t" \
    "v_mul_f32 v54, v54, v240\n\t" \
    "v_mul_f32 v55, v55, v240\n\t" \
    "v_mul_f32 v56, v56, v240\n\t" \
    "v_mul_f32 v57, v57, v240\n\t" \
    "v_mul_f32 v58, v58, v240\n\t" \
    "v_mul_f32 v59, v59, v240\n\t" \
    "v_mul_f32 v60, v60, v240\n\t" \
    "v_mul_f32 v61, v61, v240\n\t" \
    "v_mul_f32 v62, v62, v240\n\t" \
    "v_mul_f32 v63, v63, v240\n\t" \
    "v_add_f32 v237, v144, v145\n\t" \
    "v_add_f32 v237, v237, v146\n\t" \
    "v_add_f32 v237, v237, v147\n\t" \
    "v_add_f32 v237, v237, v148\n\t" \
    "v_add_f32 v237, v237, v149\n\t" \
    "v_add_f32 v237, v237, v150\n\t" \
    "v_add_f32 v237, v237, v151\n\t" \
    "v_add_f32 v237, v237, v152\n\t" \
    "v_add_f32 v237, v237, v153\n\t" \
    "v_add_f32 v237, v237, v154\n\t" \
    "v_add_f32 v237, v237, v155\n\t" \
    "v_add_f32 v237, v237, v156\n\t" \
    "v_add_f32 v237, v237, v157\n\t" \
    "v_add_f32 v237, v237, v158\n\t" \
    "v_add_f32 v237, v237, v159\n\t" \
    "v_cvt_pk_bf16_f32 v160, v144, v145\n\t" \
    "v_cvt_pk_bf16_f32 v161, v146, v147\n\t" \
    "v_cvt_pk_bf16_f32 v162, v148, v149\n\t" \
    "v_cvt_pk_bf16_f32 v163, v150, v151\n\t" \
    "v_cvt_pk_bf16_f32 v164, v152, v153\n\t" \
    "v_cvt_pk_bf16_f32 v165, v154, v155\n\t" \
    "v_cvt_pk_bf16_f32 v166, v156, v157\n\t" \
    "v_cvt_pk_bf16_f32 v167, v158, v159\n\t" \
    "s_nop 1\n\t" \
    "s_branch BACKF_%=\n\t" \
    "RAREG_%=:\n\t" \
    "s_nop 15\n\t" \
    "v_max3_f32 v239, v112, v113, v114\n\t" \
    "v_max3_f32 v239, v239, v115, v116\n\t" \
    "v_max3_f32 v239, v239, v117, v118\n\t" \
    "v_max3_f32 v239, v239, v119, v120\n\t" \
    "v_max3_f32 v239, v239, v121, v122\n\t" \
    "v_max3_f32 v239, v239, v123, v124\n\t" \
    "v_max3_f32 v239, v239, v125, v126\n\t" \
    "v_max_f32 v239, v239, v127\n\t" \
    "v_max_f32 v239, 0, v239\n\t" \
    "v_mov_b32 v240, v239\n\t" \
    "s_nop 1\n\t" \
    "v_permlane32_swap_b32 v239, v240\n\t" \
    "s_nop 1\n\t" \
    "v_max_f32 v239, v239, v240\n\t" \
    "v_add_f32 v238, v238, v239\n\t" \
    "v_exp_f32 v240, -v239\n\t" \
    "v_sub_f32 v241, 0, v238\n\t" \
    "v_sub_f32 v112, v112, v239\n\t" \
    "v_mov_b32 v96, v241\n\t" \
    "v_sub_f32 v113, v113, v239\n\t" \
    "v_mov_b32 v97, v241\n\t" \
    "v_sub_f32 v114, v114, v239\n\t" \
    "v_mov_b32 v98, v241\n\t" \
    "v_sub_f32 v115, v115, v239\n\t" \
    "v_mov_b32 v99, v241\n\t" \
    "v_sub_f32 v116, v116, v239\n\t" \
    "v_mov_b32 v100, v241\n\t" \
    "v_sub_f32 v117, v117, v239\n\t" \
    "v_mov_b32 v101, v241\n\t" \
    "v_sub_f32 v118, v118, v239\n\t" \
    "v_mov_b32 v102, v241\n\t" \
    "v_sub_f32 v119, v119, v239\n\t" \
    "v_mov_b32 v103, v241\n\t" \
    "v_sub_f32 v120, v120, v239\n\t" \
    "v_mov_b32 v104, v241\n\t" \
    "v_sub_f32 v121, v121, v239\n\t" \
    "v_mov_b32 v105, v241\n\t" \
    "v_sub_f32 v122, v122, v239\n\t" \
    "v_mov_b32 v106, v241\n\t" \
    "v_sub_f32 v123, v123, v239\n\t" \
    "v_mov_b32 v107, v241\n\t" \
    "v_sub_f32 v124, v124, v239\n\t" \
    "v_mov_b32 v108, v241\n\t" \
    "v_sub_f32 v125, v125, v239\n\t" \
    "v_mov_b32 v109, v241\n\t" \
    "v_sub_f32 v126, v126, v239\n\t" \
    "v_mov_b32 v110, v241\n\t" \
    "v_sub_f32 v127, v127, v239\n\t" \
    "v_mov_b32 v111, v241\n\t" \
    "v_exp_f32 v144, v112\n\t" \
    "v_exp_f32 v145, v113\n\t" \
    "v_exp_f32 v146, v114\n\t" \
    "v_exp_f32 v147, v115\n\t" \
    "v_exp_f32 v148, v116\n\t" \
    "v_exp_f32 v149, v117\n\t" \
    "v_exp_f32 v150, v118\n\t" \
    "v_exp_f32 v151, v119\n\t" \
    "v_exp_f32 v152, v120\n\t" \
    "v_exp_f32 v153, v121\n\t" \
    "v_exp_f32 v154, v122\n\t" \
    "v_exp_f32 v155, v123\n\t" \
    "v_exp_f32 v156, v124\n\t" \
    "v_exp_f32 v157, v125\n\t" \
    "v_exp_f32 v158, v126\n\t" \
    "v_exp_f32 v159, v127\n\t" \
    "v_mul_f32 v236, v236, v240\n\t" \
    "v_mul_f32 v0, v0, v240\n\t" \
    "v_mul_f32 v1, v1, v240\n\t" \
    "v_mul_f32 v2, v2, v240\n\t" \
    "v_mul_f32 v3, v3, v240\n\t" \
    "v_mul_f32 v4, v4, v240\n\t" \
    "v_mul_f32 v5, v5, v240\n\t" \
    "v_mul_f32 v6, v6, v240\n\t" \
    "v_mul_f32 v7, v7, v240\n\t" \
    "v_mul_f32 v8, v8, v240\n\t" \
    "v_mul_f32 v9, v9, v240\n\t" \
    "v_mul_f32 v10, v10, v240\n\t" \
    "v_mul_f32 v11, v11, v240\n\t" \
    "v_mul_f32 v12, v12, v240\n\t" \
    "v_mul_f32 v13, v13, v240\n\t" \
    "v_mul_f32 v14, v14, v240\n\t" \
    "v_mul_f32 v15, v15, v240\n\t" \
    "v_mul_f32 v16, v16, v240\n\t" \
    "v_mul_f32 v17, v17, v240\n\t" \
    "v_mul_f32 v18, v18, v240\n\t" \
    "v_mul_f32 v19, v19, v240\n\t" \
    "v_mul_f32 v20, v20, v240\n\t" \
    "v_mul_f32 v21, v21, v240\n\t" \
    "v_mul_f32 v22, v22, v240\n\t" \
    "v_mul_f32 v23, v23, v240\n\t" \
    "v_mul_f32 v24, v24, v240\n\t" \
    "v_mul_f32 v25, v25, v240\n\t" \
    "v_mul_f32 v26, v26, v240\n\t" \
    "v_mul_f32 v27, v27, v240\n\t" \
    "v_mul_f32 v28, v28, v240\n\t" \
    "v_mul_f32 v29, v29, v240\n\t" \
    "v_mul_f32 v30, v30, v240\n\t" \
    "v_mul_f32 v31, v31, v240\n\t" \
    "v_mul_f32 v32, v32, v240\n\t" \
    "v_mul_f32 v33, v33, v240\n\t" \
    "v_mul_f32 v34, v34, v240\n\t" \
    "v_mul_f32 v35, v35, v240\n\t" \
    "v_mul_f32 v36, v36, v240\n\t" \
    "v_mul_f32 v37, v37, v240\n\t" \
    "v_mul_f32 v38, v38, v240\n\t" \
    "v_mul_f32 v39, v39, v240\n\t" \
    "v_mul_f32 v40, v40, v240\n\t" \
    "v_mul_f32 v41, v41, v240\n\t" \
    "v_mul_f32 v42, v42, v240\n\t" \
    "v_mul_f32 v43, v43, v240\n\t" \
    "v_mul_f32 v44, v44, v240\n\t" \
    "v_mul_f32 v45, v45, v240\n\t" \
    "v_mul_f32 v46, v46, v240\n\t" \
    "v_mul_f32 v47, v47, v240\n\t" \
    "v_mul_f32 v48, v48, v240\n\t" \
    "v_mul_f32 v49, v49, v240\n\t" \
    "v_mul_f32 v50, v50, v240\n\t" \
    "v_mul_f32 v51, v51, v240\n\t" \
    "v_mul_f32 v52, v52, v240\n\t" \
    "v_mul_f32 v53, v53, v240\n\t" \
    "v_mul_f32 v54, v54, v240\n\t" \
    "v_mul_f32 v55, v55, v240\n\t" \
    "v_mul_f32 v56, v56, v240\n\t" \
    "v_mul_f32 v57, v57, v240\n\t" \
    "v_mul_f32 v58, v58, v240\n\t" \
    "v_mul_f32 v59, v59, v240\n\t" \
    "v_mul_f32 v60, v60, v240\n\t" \
    "v_mul_f32 v61, v61, v240\n\t" \
    "v_mul_f32 v62, v62, v240\n\t" \
    "v_mul_f32 v63, v63, v240\n\t" \
    "v_add_f32 v237, v144, v145\n\t" \
    "v_add_f32 v237, v237, v146\n\t" \
    "v_add_f32 v237, v237, v147\n\t" \
    "v_add_f32 v237, v237, v148\n\t" \
    "v_add_f32 v237, v237, v149\n\t" \
    "v_add_f32 v237, v237, v150\n\t" \
    "v_add_f32 v237, v237, v151\n\t" \
    "v_add_f32 v237, v237, v152\n\t" \
    "v_add_f32 v237, v237, v153\n\t" \
    "v_add_f32 v237, v237, v154\n\t" \
    "v_add_f32 v237, v237, v155\n\t" \
    "v_add_f32 v237, v237, v156\n\t" \
    "v_add_f32 v237, v237, v157\n\t" \
    "v_add_f32 v237, v237, v158\n\t" \
    "v_add_f32 v237, v237, v159\n\t" \
    "v_cvt_pk_bf16_f32 v160, v144, v145\n\t" \
    "v_cvt_pk_bf16_f32 v161, v146, v147\n\t" \
    "v_cvt_pk_bf16_f32 v162, v148, v149\n\t" \
    "v_cvt_pk_bf16_f32 v163, v150, v151\n\t" \
    "v_cvt_pk_bf16_f32 v164, v152, v153\n\t" \
    "v_cvt_pk_bf16_f32 v165, v154, v155\n\t" \
    "v_cvt_pk_bf16_f32 v166, v156, v157\n\t" \
    "v_cvt_pk_bf16_f32 v167, v158, v159\n\t" \
    "s_nop 1\n\t" \
    "s_branch BACKG_%=\n\t" \
    "END_%=:\n\t" \
    "s_cmp_lg_u32 %[dmaw], 0\n\t" \
    "s_cbranch_scc1 END2_%=\n\t" \
    "s_waitcnt vmcnt(0)\n\t" \
    "END2_%=:\n\t" \
    "s_waitcnt lgkmcnt(0)\n\t" \
    "s_nop 15\n\t"
#define MX3(a, b, c) __builtin_fmaxf(__builtin_fmaxf((a), (b)), (c))
DI float max16(const f32x16& p) {
    float a = MX3(p[0], p[1], p[2]), b = MX3(p[3], p[4], p[5]);
    a = MX3(a, p[6], p[7]); b = MX3(b, p[8], p[9]); a = MX3(a, p[10], p[11]); b = MX3(b, p[12], p[13]); a = MX3(a, p[14], p[15]);
    return __builtin_fmaxf(a, b);
}
DI float halfmax(float m) { auto rr = __builtin_amdgcn_permlane32_swap(__float_as_uint(m), __float_as_uint(m), false, false); return __builtin_fmaxf(__uint_as_float(rr[0]), __uint_as_float(rr[1])); }
DI void cmask(f32x16& p, int r32, int hi) {
#pragma unroll
    for (int r = 0; r < 16; ++r) { const int kv = 4 * hi + (r & 3) + 8 * (r >> 2); if (kv > r32) p[r] = -INFINITY; }
}
#define WAITBAR(N) asm volatile("s_waitcnt vmcnt(" #N ")\n\ts_barrier" ::: "memory")
typedef unsigned u32x16 __attribute__((ext_vector_type(16)));

__global__ __launch_bounds__(512, 2) void k2_attn(const char* __restrict__ Q, const char* __restrict__ K, const char* __restrict__ VT, char* __restrict__ CTX, const char* __restrict__ wo, const char* __restrict__ w2) {
    extern __shared__ __attribute__((aligned(16))) unsigned char smem[];
    const lds_cptr lds = (lds_cptr)smem;
    const int tid = threadIdx.x, lane = tid & 63, r32 = lane & 31, hi = lane >> 5, wid = __builtin_amdgcn_readfirstlane(tid >> 6);
    const int xcd = blockIdx.x & 7, idx = blockIdx.x >> 3, h = xcd * 4 + (idx >> 3), ib = idx & 7;
    const int G = wid < 4 ? 4 * ib + wid : 63 - 4 * ib - (wid - 4);
    const int NT = 32 - 2 * ib;
    const char* Kh = K + (size_t)h * 32 * SLOT; const char* Vh = VT + (size_t)h * 32 * SLOT;
    const unsigned lds0 = (unsigned)(uintptr_t)smem;
    const unsigned dmaw = (unsigned)(wid - 4) >> 31;
    const unsigned dvo = (wid & 3) * 4096 + lane * 16, kdst = lds0 + LDS_K + (wid & 3) * 4096, vdst = lds0 + LDS_V + (wid & 3) * 4096;
#define DMA_T(base, dst, t, sl) do { const char* g_ = (base) + (size_t)(t) * SLOT; const unsigned l_ = (unsigned)__builtin_amdgcn_readfirstlane((dst) + ((sl) & 3) * SLOT); glds2(g_, dvo, l_); glds2(g_ + 2048, dvo, l_ + 2048); } while (0)
#define DMA_K(t, sl) DMA_T(Kh, kdst, t, sl)
#define DMA_V(t, sl) DMA_T(Vh, vdst, t, sl)
#define DMA_STEP(s) do { if (dmaw) { DMA_K(min((s) + 3, NT - 1), (s) + 3); DMA_V(min((s) + 2, NT - 1), (s) + 2); } } while (0)
    STAMP(2, 0);
    const unsigned cpf = code_pf(tid, 84);
    u32x16 qa, qb;
    { const char* qp = Q + ((size_t)h * 32 + (G >> 1)) * SLOT; const unsigned qo = hi * 1024 + ((G & 1) * 32 + r32) * 16;
      u32x4 q[8];
#define QLD(d) asm volatile("global_load_dwordx4 %0, %1, %2 offset:%c3" : "=&v"(q[d]) : "v"(qo + ((d) >> 1) * 4096), "s"(qp), "i"(((d) & 1) * 2048) : "memory")
      QLD(0); QLD(1); QLD(2); QLD(3); QLD(4); QLD(5); QLD(6); QLD(7);
#undef QLD
      if (dmaw) { DMA_K(0, 0); DMA_V(0, 0); DMA_K(1, 1); DMA_K(2, 2); DMA_V(1, 1); } else WAIT_V(0);
      asm volatile("s_waitcnt vmcnt(8)\n\ts_barrier" : "+v"(q[0]), "+v"(q[1]), "+v"(q[2]), "+v"(q[3]), "+v"(q[4]), "+v"(q[5]), "+v"(q[6]), "+v"(q[7]) :: "memory");
#pragma unroll
      for (int i = 0; i < 4; ++i)
#pragma unroll
          for (int e = 0; e < 4; ++e) { qa[4 * i + e] = q[i][e]; qb[4 * i + e] = q[4 + i][e]; }
    }
    STAMP(2, 1);
    CODE_PF_DONE(cpf);
    const unsigned kp0 = lds0 + LDS_K + hi * 1024 + r32 * 16, vp0 = lds0 + LDS_V + hi * 2048 + r32 * 16;
    f32x16 o0 = f32x16{}, o1 = f32x16{}, o2 = f32x16{}, o3 = f32x16{}, negm, cA, cB, P = f32x16{};
    u32x16 kfa, kfb;
    float l_reg = 0.f, mhat;
    {
        f32x16 s = f32x16{};
#pragma unroll
        for (int j = 0; j < 8; ++j) {
            const bf16x8 kf = lds_rd(lds + LDS_K + hi * 1024 + r32 * 16 + j * 2048); const u32x4 kw = __builtin_bit_cast(u32x4, kf);
#pragma unroll
            for (int e = 0; e < 4; ++e) { if (j < 4) kfa[4 * j + e] = kw[e]; else kfb[4 * (j - 4) + e] = kw[e]; }
            const u32x4 qw = j < 4 ? (u32x4){qa[4 * j], qa[4 * j + 1], qa[4 * j + 2], qa[4 * j + 3]} : (u32x4){qb[4 * j - 16], qb[4 * j - 15], qb[4 * j - 14], qb[4 * j - 13]};
            s = MFMA32(kf, __builtin_bit_cast(bf16x8, qw), s);
        }
        if (G == 0) cmask(s, r32, hi);
        mhat = halfmax(max16(s));
#pragma unroll
        for (int r = 0; r < 16; ++r) { negm[r] = -mhat; cB[r] = -INFINITY; cA[r] = -INFINITY; }
    }
    const float thr = 65536.0f;
    int t = 0; unsigned soff = 0;
    unsigned vk = kp0, vkn = kp0 + SLOT, vv = vp0, vvp = vp0;
#define K2_STATE [o0] "+&{v[0:15]}"(o0), [o1] "+&{v[16:31]}"(o1), [o2] "+&{v[32:47]}"(o2), [o3] "+&{v[48:63]}"(o3), [negm] "+&{v[96:111]}"(negm), [ca] "+&{v[112:127]}"(cA), [cb] "+&{v[128:143]}"(cB), \
        [p] "+&{v[144:159]}"(P), [kfa] "+&{v[168:183]}"(kfa), [kfb] "+&{v[184:199]}"(kfb), [vk] "+&{v232}"(vk), [vkn] "+&{v233}"(vkn), [vv] "+&{v234}"(vv), [vvp] "+&{v235}"(vvp), [l] "+&{v236}"(l_reg), [mh] "+&{v238}"(mhat)
#define K2_IN [qa] "{v[64:79]}"(qa), [qb] "{v[80:95]}"(qb), [thr] "s"(thr)
#define K2_CLOB "v160", "v161", "v162", "v163", "v164", "v165", "v166", "v167", "v200", "v201", "v202", "v203", "v204", "v205", "v206", "v207", "v208", "v209", "v210", "v211", "v212", "v213", "v214", "v215", \
        "v216", "v217", "v218", "v219", "v220", "v221", "v222", "v223", "v224", "v225", "v226", "v227", "v228", "v229", "v230", "v231", "v237", "v239", "v240", "v241", "vcc", "scc", "memory"
    const unsigned pfg = blockIdx.x * 256 + (tid & 255), pfo = pfg * 128u, pfo2 = (pfg < 57344u ? pfg : 0u) * 128u;
    const int nloop = G >> 1;
    asm volatile(K2_ASM : K2_STATE, [t] "+&s"(t), [soff] "+&s"(soff)
                 : K2_IN, [n] "s"(nloop), [odd] "s"(G & 1), [dmaw] "s"(dmaw), [ntm1] "s"(NT - 1), [khl] "s"((unsigned)(uintptr_t)Kh), [khh] "s"((unsigned)((uintptr_t)Kh >> 32)),
                   [vhl] "s"((unsigned)(uintptr_t)Vh), [vhh] "s"((unsigned)((uintptr_t)Vh >> 32)), [kdst] "s"(kdst), [vdst] "s"(vdst), [dvo] "v"(dvo), [kp0] "v"(kp0), [vp0] "v"(vp0), [dq] "v"(r32 - 4 * hi),
                   [pfo] "v"(pfo), [pfo2] "v"(pfo2), [wo] "s"(wo), [w2] "s"(w2)
                 : K2_CLOB, "s60", "s61", "s62", "s63", "v250", "v251");
    STAMP(2, 2);
    {
        auto rr = __builtin_amdgcn_permlane32_swap(__float_as_uint(l_reg), __float_as_uint(l_reg), false, false);
        const float inv = 1.0f / (__uint_as_float(rr[0]) + __uint_as_float(rr[1]));
        char* op = CTX + ((size_t)((G >> 2) * 64 + 2 * h) * 8 + hi) * 2048 + ((G & 3) * 32 + r32) * 16;
#define K2_ST(o, db) do { _Pragma("unroll") for (int g = 0; g < 4; g += 2) { \
            const unsigned x0 = cvtpk(o[4 * g] * inv, o[4 * g + 1] * inv), x1 = cvtpk(o[4 * g + 2] * inv, o[4 * g + 3] * inv); \
            const unsigned y0 = cvtpk(o[4 * g + 4] * inv, o[4 * g + 5] * inv), y1 = cvtpk(o[4 * g + 6] * inv, o[4 * g + 7] * inv); \
            const auto r0 = __builtin_amdgcn_permlane32_swap(x0, y0, false, false), r1 = __builtin_amdgcn_permlane32_swap(x1, y1, false, false); \
            ST16(op + (size_t)(((db) >> 1) * 8 + 4 * ((db) & 1) + g) * 2048, ((u32x4){r0[0], r1[0], r0[1], r1[1]})); } } while (0)
        K2_ST(o0, 0); K2_ST(o1, 1); K2_ST(o2, 2); K2_ST(o3, 3);
    }
    WAITBAR(16); ++t;
    if (t < NT) { DMA_STEP(t); WAITBAR(16); ++t;
        for (; t < NT; ++t) { DMA_STEP(t); WAITBAR(8); } }
    STAMP(2, 3);
    WAIT_V(0);
    STAMP(2, 4);
}
}

namespace k3 {
constexpr int STG = 49152, LDS_BYTES = 3 * STG;
typedef __attribute__((address_space(1))) unsigned gu32;
#define K3_ASM \
    "s_mov_b64 s[60:61], %[a1]\n\t" \
    "s_mov_b64 s[62:63], %[b1]\n\t" \
    "s_mov_b32 s67, %[nl]\n\t" \
    "s_mov_b32 s68, 0\n\t" \
    "s_add_u32 m0, %[mA], s68\n\t" \
    "s_add_u32 s66, %[mB], s68\n\t" \
    "global_load_lds_dwordx4 %[va], s[60:61] offset:0\n\t" \
    "global_load_lds_dwordx4 %[va], s[60:61] offset:1024\n\t" \
    "global_load_lds_dwordx4 %[va], s[60:61] offset:2048\n\t" \
    "global_load_lds_dwordx4 %[va], s[60:61] offset:3072\n\t" \
    "s_mov_b32 m0, s66\n\t" \
    "s_add_u32 s64, s62, 0x10000\n\t" \
    "s_addc_u32 s65, s63, 0\n\t" \
    "global_load_lds_dwordx4 %[vb], s[62:63]\n\t" \
    "s_add_u32 m0, s66, 8192\n\t" \
    "s_add_u32 s60, s60, 0x4000\n\t" \
    "global_load_lds_dwordx4 %[vb], s[64:65]\n\t" \
    "s_addc_u32 s61, s61, 0\n\t" \
    "s_add_u32 s62, s62, 0x20000\n\t" \
    "s_addc_u32 s63, s63, 0\n\t" \
    "s_mov_b32 s68, 49152\n\t" \
    "s_add_u32 m0, %[mA], s68\n\t" \
    "s_add_u32 s66, %[mB], s68\n\t" \
    "global_load_lds_dwordx4 %[va], s[60:61] offset:0\n\t" \
    "global_load_lds_dwordx4 %[va], s[60:61] offset:1024\n\t" \
    "global_load_lds_dwordx4 %[va], s[60:61] offset:2048\n\t" \
    "global_load_lds_dwordx4 %[va], s[60:61] offset:3072\n\t" \
    "s_mov_b32 m0, s66\n\t" \
    "s_add_u32 s64, s62, 0x10000\n\t" \
    "s_addc_u32 s65, s63, 0\n\t" \
    "global_load_lds_dwordx4 %[vb], s[62:63]\n\t" \
    "s_add_u32 m0, s66, 8192\n\t" \
    "s_add_u32 s60, s60, 0x4000\n\t" \
    "global_load_lds_dwordx4 %[vb], s[64:65]\n\t" \
    "s_addc_u32 s61, s61, 0\n\t" \
    "s_add_u32 s62, s62, 0x20000\n\t" \
    "s_addc_u32 s63, s63, 0\n\t" \
    "s_mov_b32 s68, 98304\n\t" \
    "s_add_u32 m0, %[mA], s68\n\t" \
    "s_add_u32 s66, %[mB], s68\n\t" \
    "global_load_lds_dwordx4 %[va], s[60:61] offset:0\n\t" \
    "global_load_lds_dwordx4 %[va], s[60:61] offset:1024\n\t" \
    "global_load_lds_dwordx4 %[va], s[60:61] offset:2048\n\t" \
    "global_load_lds_dwordx4 %[va], s[60:61] offset:3072\n\t" \
    "s_mov_b32 m0, s66\n\t" \
    "s_add_u32 s64, s62, 0x10000\n\t" \
    "s_addc_u32 s65, s63, 0\n\t" \
    "global_load_lds_dwordx4 %[vb], s[62:63]\n\t" \
    "s_add_u32 m0, s66, 8192\n\t" \
    "s_add_u32 s60, s60, 0x4000\n\t" \
    "global_load_lds_dwordx4 %[vb], s[64:65]\n\t" \
    "s_addc_u32 s61, s61, 0\n\t" \
    "s_add_u32 s62, s62, 0x20000\n\t" \
    "s_addc_u32 s63, s63, 0\n\t" \
    "s_mov_b32 s68, 0\n\t" \
    "s_mov_b32 s69, 98304\n\t" \
    "v_mov_b32 v128, v134\n\t" \
    "v_mov_b32 v129, v135\n\t" \
    "v_mov_b32 v130, v136\n\t" \
    "v_add_u32 v131, 49152, v134\n\t" \
    "v_add_u32 v132, 49152, v135\n\t" \
    "v_add_u32 v133, 49152, v136\n\t" \
    "s_waitcnt vmcnt(12)\n\t" \
    "s_barrier\n\t" \
    "ds_read_b128 v[64:67], v128 offset:0\n\t" \
    "ds_read_b64_tr_b16 v[80:81], v129 offset:0\n\t" \
    "ds_read_b64_tr_b16 v[82:83], v129 offset:256\n\t" \
    "ds_read_b64_tr_b16 v[84:85], v130 offset:0\n\t" \
    "ds_read_b64_tr_b16 v[86:87], v130 offset:256\n\t" \
    "ds_read_b128 v[68:71], v128 offset:256\n\t" \
    "ds_read_b64_tr_b16 v[88:89], v129 offset:512\n\t" \
    "ds_read_b64_tr_b16 v[90:91], v129 offset:768\n\t" \
    "ds_read_b64_tr_b16 v[92:93], v130 offset:512\n\t" \
    "ds_read_b64_tr_b16 v[94:95], v130 offset:768\n\t" \
    "ds_read_b128 v[72:75], v128 offset:512\n\t" \
    "ds_read_b128 v[76:79], v128 offset:768\n\t" \
    "LOOP_%=:\n\t" \
    "s_add_u32 m0, %[mA], s68\n\t" \
    "s_add_u32 s66, %[mB], s68\n\t" \
    "global_load_lds_dwordx4 %[va], s[60:61] offset:0\n\t" \
    "global_load_lds_dwordx4 %[va], s[60:61] offset:1024\n\t" \
    "global_load_lds_dwordx4 %[va], s[60:61] offset:2048\n\t" \
    "global_load_lds_dwordx4 %[va], s[60:61] offset:3072\n\t" \
    "s_mov_b32 m0, s66\n\t" \
    "s_add_u32 s64, s62, 0x10000\n\t" \
    "s_addc_u32 s65, s63, 0\n\t" \
    "global_load_lds_dwordx4 %[vb], s[62:63]\n\t" \
    "s_add_u32 m0, s66, 8192\n\t" \
    "s_add_u32 s60, s60, 0x4000\n\t" \
    "global_load_lds_dwordx4 %[vb], s[64:65]\n\t" \
    "s_addc_u32 s61, s61, 0\n\t" \
    "s_add_u32 s62, s62, 0x20000\n\t" \
    "s_addc_u32 s63, s63, 0\n\t" \
    "v_mfma_f32_16x16x32_bf16 v[0:3], v[112:115], v[96:99], v[0:3]\n\t" \
    "ds_read_b128 v[64:67], v131 offset:0\n\t" \
    "ds_read_b64_tr_b16 v[80:81], v132 offset:0\n\t" \
    "v_mfma_f32_16x16x32_bf16 v[4:7], v[116:119], v[96:99], v[4:7]\n\t" \
    "ds_read_b64_tr_b16 v[82:83], v132 offset:256\n\t" \
    "ds_read_b64_tr_b16 v[84:85], v133 offset:0\n\t" \
    "v_mfma_f32_16x16x32_bf16 v[8:11], v[120:123], v[96:99], v[8:11]\n\t" \
    "ds_read_b64_tr_b16 v[86:87], v133 offset:256\n\t" \
    "ds_read_b128 v[68:71], v131 offset:256\n\t" \
    "v_mfma_f32_16x16x32_bf16 v[12:15], v[124:127], v[96:99], v[12:15]\n\t" \
    "ds_read_b64_tr_b16 v[88:89], v132 offset:512\n\t" \
    "ds_read_b64_tr_b16 v[90:91], v132 offset:768\n\t" \
    "v_mfma_f32_16x16x32_bf16 v[16:19], v[112:115], v[100:103], v[16:19]\n\t" \
    "ds_read_b64_tr_b16 v[92:93], v133 offset:512\n\t" \
    "ds_read_b64_tr_b16 v[94:95], v133 offset:768\n\t" \
    "v_mfma_f32_16x16x32_bf16 v[20:23], v[116:119], v[100:103], v[20:23]\n\t" \
    "ds_read_b128 v[72:75], v131 offset:512\n\t" \
    "ds_read_b128 v[76:79], v131 offset:768\n\t" \
    "v_mfma_f32_16x16x32_bf16 v[24:27], v[120:123], v[100:103], v[24:27]\n\t" \
    "s_add_u32 m0, %[mA], s68\n\t" \
    "s_add_u32 s66, %[mB], s68\n\t" \
    "global_load_lds_dwordx4 %[va], s[60:61] offset:0\n\t" \
    "v_mfma_f32_16x16x32_bf16 v[28:31], v[124:127], v[100:103], v[28:31]\n\t" \
    "global_load_lds_dwordx4 %[va], s[60:61] offset:1024\n\t" \
    "global_load_lds_dwordx4 %[va], s[60:61] offset:2048\n\t" \
    "global_load_lds_dwordx4 %[va], s[60:61] offset:3072\n\t" \
    "v_mfma_f32_16x16x32_bf16 v[32:35], v[112:115], v[104:107], v[32:35]\n\t" \
    "s_mov_b32 m0, s66\n\t" \
    "s_add_u32 s64, s62, 0x10000\n\t" \
    "s_addc_u32 s65, s63, 0\n\t" \
    "v_mfma_f32_16x16x32_bf16 v[36:39], v[116:119], v[104:107], v[36:39]\n\t" \
    "global_load_lds_dwordx4 %[vb], s[62:63]\n\t" \
    "s_add_u32 m0, s66, 8192\n\t" \
    "s_add_u32 s60, s60, 0x4000\n\t" \
    "v_mfma_f32_16x16x32_bf16 v[40:43], v[120:123], v[104:107], v[40:43]\n\t" \
    "global_load_lds_dwordx4 %[vb], s[64:65]\n\t" \
    "s_addc_u32 s61, s61, 0\n\t" \
    "s_add_u32 s62, s62, 0x20000\n\t" \
    "v_mfma_f32_16x16x32_bf16 v[44:47], v[124:127], v[104:107], v[44:47]\n\t" \
    "s_addc_u32 s63, s63, 0\n\t" \
    "v_mov_b32 v128, v131\n\t" \
    "v_mov_b32 v129, v132\n\t" \
    "v_mfma_f32_16x16x32_bf16 v[48:51], v[112:115], v[108:111], v[48:51]\n\t" \
    "v_mov_b32 v130, v133\n\t" \
    "v_add_u32 v131, s69, v134\n\t" \
    "v_add_u32 v132, s69, v135\n\t" \
    "v_add_u32 v133, s69, v136\n\t" \
    "v_mfma_f32_16x16x32_bf16 v[52:55], v[116:119], v[108:111], v[52:55]\n\t" \
    "s_add_u32 s68, s68, 49152\n\t" \
    "s_cmp_eq_u32 s68, 147456\n\t" \
    "s_cselect_b32 s68, 0, s68\n\t" \
    "s_add_u32 s69, s69, 49152\n\t" \
    "v_mfma_f32_16x16x32_bf16 v[56:59], v[120:123], v[108:111], v[56:59]\n\t" \
    "s_cmp_eq_u32 s69, 147456\n\t" \
    "s_cselect_b32 s69, 0, s69\n\t" \
    "v_mfma_f32_16x16x32_bf16 v[60:63], v[124:127], v[108:111], v[60:63]\n\t" \
    "s_sub_u32 s67, s67, 1\n\t" \
    "s_cmp_lg_u32 s67, 0\n\t" \
    "s_cbranch_scc1 LOOP_%=\n\t" \
    "s_waitcnt lgkmcnt(0)\n\t" \
    "v_mfma_f32_16x16x32_bf16 v[0:3], v[80:83], v[64:67], v[0:3]\n\t" \
    "ds_read_b128 v[96:99], v128 offset:8192\n\t" \
    "ds_read_b64_tr_b16 v[112:113], v129 offset:8192\n\t" \
    "v_mfma_f32_16x16x32_bf16 v[4:7], v[84:87], v[64:67], v[4:7]\n\t" \
    "ds_read_b64_tr_b16 v[114:115], v129 offset:8448\n\t" \
    "ds_read_b64_tr_b16 v[116:117], v130 offset:8192\n\t" \
    "v_mfma_f32_16x16x32_bf16 v[8:11], v[88:91], v[64:67], v[8:11]\n\t" \
    "ds_read_b64_tr_b16 v[118:119], v130 offset:8448\n\t" \
    "ds_read_b128 v[100:103], v128 offset:8448\n\t" \
    "v_mfma_f32_16x16x32_bf16 v[12:15], v[92:95], v[64:67], v[12:15]\n\t" \
    "ds_read_b64_tr_b16 v[120:121], v129 offset:8704\n\t" \
    "ds_read_b64_tr_b16 v[122:123], v129 offset:8960\n\t" \
    "v_mfma_f32_16x16x32_bf16 v[16:19], v[80:83], v[68:71], v[16:19]\n\t" \
    "ds_read_b64_tr_b16 v[124:125], v130 offset:8704\n\t" \
    "ds_read_b64_tr_b16 v[126:127], v130 offset:8960\n\t" \
    "v_mfma_f32_16x16x32_bf16 v[20:23], v[84:87], v[68:71], v[20:23]\n\t" \
    "ds_read_b128 v[104:107], v128 offset:8704\n\t" \
    "ds_read_b128 v[108:111], v128 offset:8960\n\t" \
    "v_mfma_f32_16x16x32_bf16 v[24:27], v[88:91], v[68:71], v[24:27]\n\t" \
    "v_mfma_f32_16x16x32_bf16 v[28:31], v[92:95], v[68:71], v[28:31]\n\t" \
    "v_mfma_f32_16x16x32_bf16 v[32:35], v[80:83], v[72:75], v[32:35]\n\t" \
    "v_mfma_f32_16x16x32_bf16 v[36:39], v[84:87], v[72:75], v[36:39]\n\t" \
    "v_mfma_f32_16x16x32_bf16 v[40:43], v[88:91], v[72:75], v[40:43]\n\t" \
    "v_mfma_f32_16x16x32_bf16 v[44:47], v[92:95], v[72:75], v[44:47]\n\t" \
    "v_mfma_f32_16x16x32_bf16 v[48:51], v[80:83], v[76:79], v[48:51]\n\t" \
    "v_mfma_f32_16x16x32_bf16 v[52:55], v[84:87], v[76:79], v[52:55]\n\t" \
    "v_mfma_f32_16x16x32_bf16 v[56:59], v[88:91], v[76:79], v[56:59]\n\t" \
    "v_mfma_f32_16x16x32_bf16 v[60:63], v[92:95], v[76:79], v[60:63]\n\t" \
    "s_waitcnt vmcnt(6) lgkmcnt(0)\n\t" \
    "s_barrier\n\t" \
    "v_mfma_f32_16x16x32_bf16 v[0:3], v[112:115], v[96:99], v[0:3]\n\t" \
    "ds_read_b128 v[64:67], v131 offset:0\n\t" \
    "ds_read_b64_tr_b16 v[80:81], v132 offset:0\n\t" \
    "v_mfma_f32_16x16x32_bf16 v[4:7], v[116:119], v[96:99], v[4:7]\n\t" \
    "ds_read_b64_tr_b16 v[82:83], v132 offset:256\n\t" \
    "ds_read_b64_tr_b16 v[84:85], v133 offset:0\n\t" \
    "v_mfma_f32_16x16x32_bf16 v[8:11], v[120:123], v[96:99], v[8:11]\n\t" \
    "ds_read_b64_tr_b16 v[86:87], v133 offset:256\n\t" \
    "ds_read_b128 v[68:71], v131 offset:256\n\t" \
    "v_mfma_f32_16x16x32_bf16 v[12:15], v[124:127], v[96:99], v[12:15]\n\t" \
    "ds_read_b64_tr_b16 v[88:89], v132 offset:512\n\t" \
    "ds_read_b64_tr_b16 v[90:91], v132 offset:768\n\t" \
    "v_mfma_f32_16x16x32_bf16 v[16:19], v[112:115], v[100:103], v[16:19]\n\t" \
    "ds_read_b64_tr_b16 v[92:93], v133 offset:512\n\t" \
    "ds_read_b64_tr_b16 v[94:95], v133 offset:768\n\t" \
    "v_mfma_f32_16x16x32_bf16 v[20:23], v[116:119], v[100:103], v[20:23]\n\t" \
    "ds_read_b128 v[72:75], v131 offset:512\n\t" \
    "ds_read_b128 v[76:79], v131 offset:768\n\t" \
    "v_mfma_f32_16x16x32_bf16 v[24:27], v[120:123], v[100:103], v[24:27]\n\t" \
    "v_mov_b32 v128, v131\n\t" \
    "v_mov_b32 v129, v132\n\t" \
    "v_mov_b32 v130, v133\n\t" \
    "v_mfma_f32_16x16x32_bf16 v[28:31], v[124:127], v[100:103], v[28:31]\n\t" \
    "v_add_u32 v131, s69, v134\n\t" \
    "v_add_u32 v132, s69, v135\n\t" \
    "v_add_u32 v133, s69, v136\n\t" \
    "v_mfma_f32_16x16x32_bf16 v[32:35], v[112:115], v[104:107], v[32:35]\n\t" \
    "s_add_u32 s68, s68, 49152\n\t" \
    "s_cmp_eq_u32 s68, 147456\n\t" \
    "s_cselect_b32 s68, 0, s68\n\t" \
    "v_mfma_f32_16x16x32_bf16 v[36:39], v[116:119], v[104:107], v[36:39]\n\t" \
    "s_add_u32 s69, s69, 49152\n\t" \
    "s_cmp_eq_u32 s69, 147456\n\t" \
    "s_cselect_b32 s69, 0, s69\n\t" \
    "v_mfma_f32_16x16x32_bf16 v[40:43], v[120:123], v[104:107], v[40:43]\n\t" \
    "v_mfma_f32_16x16x32_bf16 v[44:47], v[124:127], v[104:107], v[44:47]\n\t" \
    "v_mfma_f32_16x16x32_bf16 v[48:51], v[112:115], v[108:111], v[48:51]\n\t" \
    "v_mfma_f32_16x16x32_bf16 v[52:55], v[116:119], v[108:111], v[52:55]\n\t" \
    "v_mfma_f32_16x16x32_bf16 v[56:59], v[120:123], v[108:111], v[56:59]\n\t" \
    "v_mfma_f32_16x16x32_bf16 v[60:63], v[124:127], v[108:111], v[60:63]\n\t" \
    "s_waitcnt lgkmcnt(0)\n\t" \
    "v_mfma_f32_16x16x32_bf16 v[0:3], v[80:83], v[64:67], v[0:3]\n\t" \
    "ds_read_b128 v[96:99], v128 offset:8192\n\t" \
    "ds_read_b64_tr_b16 v[112:113], v129 offset:8192\n\t" \
    "v_mfma_f32_16x16x32_bf16 v[4:7], v[84:87], v[64:67], v[4:7]\n\t" \
    "ds_read_b64_tr_b16 v[114:115], v129 offset:8448\n\t" \
    "ds_read_b64_tr_b16 v[116:117], v130 offset:8192\n\t" \
    "v_mfma_f32_16x16x32_bf16 v[8:11], v[88:91], v[64:67], v[8:11]\n\t" \
    "ds_read_b64_tr_b16 v[118:119], v130 offset:8448\n\t" \
    "ds_read_b128 v[100:103], v128 offset:8448\n\t" \
    "v_mfma_f32_16x16x32_bf16 v[12:15], v[92:95], v[64:67], v[12:15]\n\t" \
    "ds_read_b64_tr_b16 v[120:121], v129 offset:8704\n\t" \
    "ds_read_b64_tr_b16 v[122:123], v129 offset:8960\n\t" \
    "v_mfma_f32_16x16x32_bf16 v[16:19], v[80:83], v[68:71], v[16:19]\n\t" \
    "ds_read_b64_tr_b16 v[124:125], v130 offset:8704\n\t" \
    "ds_read_b64_tr_b16 v[126:127], v130 offset:8960\n\t" \
    "v_mfma_f32_16x16x32_bf16 v[20:23], v[84:87], v[68:71], v[20:23]\n\t" \
    "ds_read_b128 v[104:107], v128 offset:8704\n\t" \
    "ds_read_b128 v[108:111], v128 offset:8960\n\t" \
    "v_mfma_f32_16x16x32_bf16 v[24:27], v[88:91], v[68:71], v[24:27]\n\t" \
    "v_mfma_f32_16x16x32_bf16 v[28:31], v[92:95], v[68:71], v[28:31]\n\t" \
    "v_mfma_f32_16x16x32_bf16 v[32:35], v[80:83], v[72:75], v[32:35]\n\t" \
    "v_mfma_f32_16x16x32_bf16 v[36:39], v[84:87], v[72:75], v[36:39]\n\t" \
    "v_mfma_f32_16x16x32_bf16 v[40:43], v[88:91], v[72:75], v[40:43]\n\t" \
    "v_mfma_f32_16x16x32_bf16 v[44:47], v[92:95], v[72:75], v[44:47]\n\t" \
    "v_mfma_f32_16x16x32_bf16 v[48:51], v[80:83], v[76:79], v[48:51]\n\t" \
    "v_mfma_f32_16x16x32_bf16 v[52:55], v[84:87], v[76:79], v[52:55]\n\t" \
    "v_mfma_f32_16x16x32_bf16 v[56:59], v[88:91], v[76:79], v[56:59]\n\t" \
    "v_mfma_f32_16x16x32_bf16 v[60:63], v[92:95], v[76:79], v[60:63]\n\t" \
    "s_waitcnt vmcnt(0) lgkmcnt(0)\n\t" \
    "s_barrier\n\t" \
    "v_mfma_f32_16x16x32_bf16 v[0:3], v[112:115], v[96:99], v[0:3]\n\t" \
    "ds_read_b128 v[64:67], v131 offset:0\n\t" \
    "ds_read_b64_tr_b16 v[80:81], v132 offset:0\n\t" \
    "v_mfma_f32_16x16x32_bf16 v[4:7], v[116:119], v[96:99], v[4:7]\n\t" \
    "ds_read_b64_tr_b16 v[82:83], v132 offset:256\n\t" \
    "ds_read_b64_tr_b16 v[84:85], v133 offset:0\n\t" \
    "v_mfma_f32_16x16x32_bf16 v[8:11], v[120:123], v[96:99], v[8:11]\n\t" \
    "ds_read_b64_tr_b16 v[86:87], v133 offset:256\n\t" \
    "ds_read_b128 v[68:71], v131 offset:256\n\t" \
    "v_mfma_f32_16x16x32_bf16 v[12:15], v[124:127], v[96:99], v[12:15]\n\t" \
    "ds_read_b64_tr_b16 v[88:89], v132 offset:512\n\t" \
    "ds_read_b64_tr_b16 v[90:91], v132 offset:768\n\t" \
    "v_mfma_f32_16x16x32_bf16 v[16:19], v[112:115], v[100:103], v[16:19]\n\t" \
    "ds_read_b64_tr_b16 v[92:93], v133 offset:512\n\t" \
    "ds_read_b64_tr_b16 v[94:95], v133 offset:768\n\t" \
    "v_mfma_f32_16x16x32_bf16 v[20:23], v[116:119], v[100:103], v[20:23]\n\t" \
    "ds_read_b128 v[72:75], v131 offset:512\n\t" \
    "ds_read_b128 v[76:79], v131 offset:768\n\t" \
    "v_mfma_f32_16x16x32_bf16 v[24:27], v[120:123], v[100:103], v[24:27]\n\t" \
    "v_mov_b32 v128, v131\n\t" \
    "v_mov_b32 v129, v132\n\t" \
    "v_mov_b32 v130, v133\n\t" \
    "v_mfma_f32_16x16x32_bf16 v[28:31], v[124:127], v[100:103], v[28:31]\n\t" \
    "v_add_u32 v131, s69, v134\n\t" \
    "v_add_u32 v132, s69, v135\n\t" \
    "v_add_u32 v133, s69, v136\n\t" \
    "v_mfma_f32_16x16x32_bf16 v[32:35], v[112:115], v[104:107], v[32:35]\n\t" \
    "s_add_u32 s68, s68, 49152\n\t" \
    "s_cmp_eq_u32 s68, 147456\n\t" \
    "s_cselect_b32 s68, 0, s68\n\t" \
    "v_mfma_f32_16x16x32_bf16 v[36:39], v[116:119], v[104:107], v[36:39]\n\t" \
    "s_add_u32 s69, s69, 49152\n\t" \
    "s_cmp_eq_u32 s69, 147456\n\t" \
    "s_cselect_b32 s69, 0, s69\n\t" \
    "v_mfma_f32_16x16x32_bf16 v[40:43], v[120:123], v[104:107], v[40:43]\n\t" \
    "v_mfma_f32_16x16x32_bf16 v[44:47], v[124:127], v[104:107], v[44:47]\n\t" \
    "v_mfma_f32_16x16x32_bf16 v[48:51], v[112:115], v[108:111], v[48:51]\n\t" \
    "v_mfma_f32_16x16x32_bf16 v[52:55], v[116:119], v[108:111], v[52:55]\n\t" \
    "v_mfma_f32_16x16x32_bf16 v[56:59], v[120:123], v[108:111], v[56:59]\n\t" \
    "v_mfma_f32_16x16x32_bf16 v[60:63], v[124:127], v[108:111], v[60:63]\n\t" \
    "s_waitcnt lgkmcnt(0)\n\t" \
    "v_mfma_f32_16x16x32_bf16 v[0:3], v[80:83], v[64:67], v[0:3]\n\t" \
    "ds_read_b128 v[96:99], v128 offset:8192\n\t" \
    "ds_read_b64_tr_b16 v[112:113], v129 offset:8192\n\t" \
    "v_mfma_f32_16x16x32_bf16 v[4:7], v[84:87], v[64:67], v[4:7]\n\t" \
    "ds_read_b64_tr_b16 v[114:115], v129 offset:8448\n\t" \
    "ds_read_b64_tr_b16 v[116:117], v130 offset:8192\n\t" \
    "v_mfma_f32_16x16x32_bf16 v[8:11], v[88:91], v[64:67], v[8:11]\n\t" \
    "ds_read_b64_tr_b16 v[118:119], v130 offset:8448\n\t" \
    "ds_read_b128 v[100:103], v128 offset:8448\n\t" \
    "v_mfma_f32_16x16x32_bf16 v[12:15], v[92:95], v[64:67], v[12:15]\n\t" \
    "ds_read_b64_tr_b16 v[120:121], v129 offset:8704\n\t" \
    "ds_read_b64_tr_b16 v[122:123], v129 offset:8960\n\t" \
    "v_mfma_f32_16x16x32_bf16 v[16:19], v[80:83], v[68:71], v[16:19]\n\t" \
    "ds_read_b64_tr_b16 v[124:125], v130 offset:8704\n\t" \
    "ds_read_b64_tr_b16 v[126:127], v130 offset:8960\n\t" \
    "v_mfma_f32_16x16x32_bf16 v[20:23], v[84:87], v[68:71], v[20:23]\n\t" \
    "ds_read_b128 v[104:107], v128 offset:8704\n\t" \
    "ds_read_b128 v[108:111], v128 offset:8960\n\t" \
    "v_mfma_f32_16x16x32_bf16 v[24:27], v[88:91], v[68:71], v[24:27]\n\t" \
    "v_mfma_f32_16x16x32_bf16 v[28:31], v[92:95], v[68:71], v[28:31]\n\t" \
    "v_mfma_f32_16x16x32_bf16 v[32:35], v[80:83], v[72:75], v[32:35]\n\t" \
    "v_mfma_f32_16x16x32_bf16 v[36:39], v[84:87], v[72:75], v[36:39]\n\t" \
    "v_mfma_f32_16x16x32_bf16 v[40:43], v[88:91], v[72:75], v[40:43]\n\t" \
    "v_mfma_f32_16x16x32_bf16 v[44:47], v[92:95], v[72:75], v[44:47]\n\t" \
    "v_mfma_f32_16x16x32_bf16 v[48:51], v[80:83], v[76:79], v[48:51]\n\t" \
    "v_mfma_f32_16x16x32_bf16 v[52:55], v[84:87], v[76:79], v[52:55]\n\t" \
    "v_mfma_f32_16x16x32_bf16 v[56:59], v[88:91], v[76:79], v[56:59]\n\t" \
    "v_mfma_f32_16x16x32_bf16 v[60:63], v[92:95], v[76:79], v[60:63]\n\t" \
    "s_waitcnt vmcnt(0) lgkmcnt(0)\n\t" \
    "s_barrier\n\t" \
    "v_mfma_f32_16x16x32_bf16 v[0:3], v[112:115], v[96:99], v[0:3]\n\t" \
    "v_mov_b32 v128, v131\n\t" \
    "v_mov_b32 v129, v132\n\t" \
    "v_mfma_f32_16x16x32_bf16 v[4:7], v[116:119], v[96:99], v[4:7]\n\t" \
    "v_mov_b32 v130, v133\n\t" \
    "v_add_u32 v131, s69, v134\n\t" \
    "v_mfma_f32_16x16x32_bf16 v[8:11], v[120:123], v[96:99], v[8:11]\n\t" \
    "v_add_u32 v132, s69, v135\n\t" \
    "v_add_u32 v133, s69, v136\n\t" \
    "v_mfma_f32_16x16x32_bf16 v[12:15], v[124:127], v[96:99], v[12:15]\n\t" \
    "s_add_u32 s68, s68, 49152\n\t" \
    "s_cmp_eq_u32 s68, 147456\n\t" \
    "v_mfma_f32_16x16x32_bf16 v[16:19], v[112:115], v[100:103], v[16:19]\n\t" \
    "s_cselect_b32 s68, 0, s68\n\t" \
    "s_add_u32 s69, s69, 49152\n\t" \
    "v_mfma_f32_16x16x32_bf16 v[20:23], v[116:119], v[100:103], v[20:23]\n\t" \
    "s_cmp_eq_u32 s69, 147456\n\t" \
    "s_cselect_b32 s69, 0, s69\n\t" \
    "v_mfma_f32_16x16x32_bf16 v[24:27], v[120:123], v[100:103], v[24:27]\n\t" \
    "v_mfma_f32_16x16x32_bf16 v[28:31], v[124:127], v[100:103], v[28:31]\n\t" \
    "v_mfma_f32_16x16x32_bf16 v[32:35], v[112:115], v[104:107], v[32:35]\n\t" \
    "v_mfma_f32_16x16x32_bf16 v[36:39], v[116:119], v[104:107], v[36:39]\n\t" \
    "v_mfma_f32_16x16x32_bf16 v[40:43], v[120:123], v[104:107], v[40:43]\n\t" \
    "v_mfma_f32_16x16x32_bf16 v[44:47], v[124:127], v[104:107], v[44:47]\n\t" \
    "v_mfma_f32_16x16x32_bf16 v[48:51], v[112:115], v[108:111], v[48:51]\n\t" \
    "v_mfma_f32_16x16x32_bf16 v[52:55], v[116:119], v[108:111], v[52:55]\n\t" \
    "v_mfma_f32_16x16x32_bf16 v[56:59], v[120:123], v[108:111], v[56:59]\n\t" \
    "v_mfma_f32_16x16x32_bf16 v[60:63], v[124:127], v[108:111], v[60:63]\n\t" \
    "s_nop 15\n\t"
DI u32x4 pack8(const f32x16& c, int n) { return (u32x4){cvtpk(c[4 * n], c[4 * n + 1]), cvtpk(c[4 * n + 2], c[4 * n + 3]), cvtpk(c[4 * n + 4], c[4 * n + 5]), cvtpk(c[4 * n + 6], c[4 * n + 7])}; }
DI void add8(f32x16& c, int n, u32x4 w) {
#pragma unroll
    for (int i = 0; i < 4; ++i) { c[4 * n + 2 * i] += bflo(w[i]); c[4 * n + 2 * i + 1] += bfhi(w[i]); }
}
DI void poll(unsigned* flags, int first, int count, int lane) {
    gu32* f = (gu32*)(flags + (first + (lane % count)) * 32);
    for (int spin = 0; spin < (1 << 20); ++spin) {
        if (__all(__hip_atomic_load(f, __ATOMIC_RELAXED, __HIP_MEMORY_SCOPE_AGENT) == 1u)) break;
        __builtin_amdgcn_s_sleep(1);
    }
}
__global__ __launch_bounds__(512, 2) void k3_out(const char* __restrict__ ctx, const char* __restrict__ H, const bf16_t* __restrict__ wo, const bf16_t* __restrict__ w2,
                                                 const bf16_t* __restrict__ b2, const bf16_t* __restrict__ x, float* part, unsigned* flags, bf16_t* __restrict__ out) {
    extern __shared__ __attribute__((aligned(16))) unsigned char smem[];
    const int tid = threadIdx.x, wid = __builtin_amdgcn_readfirstlane(tid >> 6), lane = tid & 63, wr = wid >> 1, wc = wid & 1, fr = lane & 15, fq = lane >> 4;
    const int xcd = blockIdx.x & 7, idx = blockIdx.x >> 3, sl = xcd >> 1, mt = 4 * (xcd & 1) + (idx >> 3), ntile = idx & 7, tile = mt * 8 + ntile;
    const unsigned lds0 = (unsigned)(uintptr_t)smem;
    const size_t aw = (size_t)(wid & 3) * 4096, bw = (size_t)ntile * 256 + (size_t)(8 * (wid >> 1)) * (DM * 2) + 128 * (wid & 1);
    const int kt0 = sl < 2 ? 32 * sl : 28 * (sl - 2);
    const char* a1 = (sl < 2 ? ctx + (size_t)(2 * mt + (wid >> 2)) * 64 * 16384 : H + (size_t)(2 * mt + (wid >> 2)) * 56 * 16384) + aw + (size_t)kt0 * 16384;
    const char* b1 = (const char*)(sl < 2 ? wo : w2) + bw + (size_t)kt0 * (64 * DM * 2);
    const int nl = (sl < 2 ? 32 : 28) - 3;
    const unsigned va = lane * 16, vb = ((lane & 31) >> 2) * (DM * 2) + (32 * (lane >> 5) + 8 * ((lane & 3) ^ (wid & 2))) * 2;
    const unsigned mA = lds0 + (wid >> 2) * 16384 + (wid & 3) * 4096, mB = lds0 + 32768 + wid * 1024;
    const unsigned ra = lds0 + (wr >> 1) * 16384 + fq * 2048 + ((wr & 1) * 64 + fr) * 16;
    const unsigned rb0 = lds0 + 32768 + fq * 2048 + wc * 1024 + (fr >> 2) * 64 + (fq & 1) * 32 + (fr & 3) * 8, rb1 = rb0 ^ 32;
    f32x16 c0 = f32x16{}, c1 = f32x16{}, c2 = f32x16{}, c3 = f32x16{};
    u32x2 xb[4][4], bb[4];
    if (sl == 2) {
#pragma unroll
        for (int n = 0; n < 4; ++n) { bb[n] = *(const u32x2*)(b2 + ntile * 128 + wc * 64 + fq * 4 + n * 16);
#pragma unroll
            for (int m = 0; m < 4; ++m) xb[m][n] = *(const u32x2*)(x + (size_t)(mt * 256 + wr * 64 + m * 16 + fr) * DM + ntile * 128 + wc * 64 + fq * 4 + n * 16); }
    } else {
#pragma unroll
        for (int n = 0; n < 4; ++n) { bb[n] = (u32x2){0u, 0u};
#pragma unroll
            for (int m = 0; m < 4; ++m) xb[m][n] = (u32x2){0u, 0u}; }
    }
    STAMP(3, 0);
    const unsigned cpf = code_pf(tid, 52);
    asm volatile(K3_ASM
                 : "+&{v[0:15]}"(c0), "+&{v[16:31]}"(c1), "+&{v[32:47]}"(c2), "+&{v[48:63]}"(c3)
                 : "{v134}"(ra), "{v135}"(rb0), "{v136}"(rb1),
                   [va] "v"(va), [vb] "v"(vb), [a1] "s"(a1), [b1] "s"(b1), [nl] "s"(nl), [mA] "s"(mA), [mB] "s"(mB)
                 : "v64", "v65", "v66", "v67", "v68", "v69", "v70", "v71", "v72", "v73", "v74", "v75", "v76", "v77", "v78", "v79", "v80", "v81", "v82", "v83", "v84", "v85", "v86", "v87", "v88", "v89", "v90", "v91", "v92", "v93", "v94", "v95", "v96", "v97", "v98", "v99", "v100", "v101", "v102", "v103", "v104", "v105", "v106", "v107", "v108", "v109", "v110", "v111", "v112", "v113", "v114", "v115", "v116", "v117", "v118", "v119", "v120", "v121", "v122", "v123", "v124", "v125", "v126", "v127", "v128", "v129", "v130", "v131", "v132", "v133", "s60", "s61", "s62", "s63", "s64", "s65", "s66", "s67", "s68", "s69", "scc", "memory");
    STAMP(3, 1);
    CODE_PF_DONE(cpf);
    const auto rs = __builtin_amdgcn_make_buffer_rsrc((void*)((char*)part + (size_t)tile * 4 * 65536), 0, 4 * 65536, 0x00020000);
#define K3_SEND(c, m) do { __builtin_amdgcn_raw_buffer_store_b128(pack8(c, 0), rs, tid * 16 + (m) * 16384 + sl * 65536, 0, 16); __builtin_amdgcn_raw_buffer_store_b128(pack8(c, 2), rs, tid * 16 + (m) * 16384 + 8192 + sl * 65536, 0, 16); } while (0)
#define K3_GET(src, m, p) __builtin_amdgcn_raw_buffer_load_b128(rs, tid * 16 + (m) * 16384 + (p) * 8192, (src) * 65536, 16)
    gu32* myflag = (gu32*)(flags + (tile * 4 + sl) * 32);
    if (sl >= 2) {
#define K3_RES(c, m) do { _Pragma("unroll") for (int n = 0; n < 4; ++n) { \
            c[4 * n] += bflo(xb[m][n][0]) + bflo(bb[n][0]); c[4 * n + 1] += bfhi(xb[m][n][0]) + bfhi(bb[n][0]); c[4 * n + 2] += bflo(xb[m][n][1]) + bflo(bb[n][1]); c[4 * n + 3] += bfhi(xb[m][n][1]) + bfhi(bb[n][1]); } } while (0)
        K3_RES(c0, 0); K3_RES(c1, 1); K3_RES(c2, 2); K3_RES(c3, 3);
        K3_SEND(c0, 0); K3_SEND(c1, 1); K3_SEND(c2, 2); K3_SEND(c3, 3);
        WAIT_V(0);
        __syncthreads();
        if (tid == 0) __hip_atomic_store(myflag, 1u, __ATOMIC_RELAXED, __HIP_MEMORY_SCOPE_AGENT);
        STAMP(3, 2);
        return;
    }
    f32x16 f0, f1;
    if (sl == 0) { K3_SEND(c2, 2); K3_SEND(c3, 3); f0 = c0; f1 = c1; } else { K3_SEND(c0, 0); K3_SEND(c1, 1); f0 = c2; f1 = c3; }
    const int m0 = 2 * sl;
    WAIT_V(0);
    __syncthreads();
    if (tid == 0) __hip_atomic_store(myflag, 1u, __ATOMIC_RELAXED, __HIP_MEMORY_SCOPE_AGENT);
    STAMP(3, 2);
    if (wid == 0) poll(flags, tile * 4, 4, lane);
    asm volatile("" ::: "memory"); BAR(); asm volatile("" ::: "memory");
    STAMP(3, 3);
    u32x4 pa[3][2][2];
#pragma unroll
    for (int k = 0; k < 3; ++k)
#pragma unroll
        for (int m = 0; m < 2; ++m)
#pragma unroll
            for (int p = 0; p < 2; ++p) pa[k][m][p] = K3_GET(k == 0 ? 1 - sl : 1 + k, m0 + m, p);
#pragma unroll
    for (int k = 0; k < 3; ++k) { add8(f0, 0, pa[k][0][0]); add8(f0, 2, pa[k][0][1]); add8(f1, 0, pa[k][1][0]); add8(f1, 2, pa[k][1][1]); }
    const int row = mt * 256 + wr * 64 + m0 * 16 + fr;
    const int cg = ((fq & 1) << 1) | (fq >> 1);
    bf16_t* orow = out + (size_t)row * DM + ntile * 128 + wc * 64 + cg * 8;
#define K3_F4(c, n) (f32x4){c[4 * (n)], c[4 * (n) + 1], c[4 * (n) + 2], c[4 * (n) + 3]}
    *(u32x4*)(orow) = k1::pack_swap16(K3_F4(f0, 0), K3_F4(f0, 1)); *(u32x4*)(orow + 32) = k1::pack_swap16(K3_F4(f0, 2), K3_F4(f0, 3));
    *(u32x4*)(orow + 16 * DM) = k1::pack_swap16(K3_F4(f1, 0), K3_F4(f1, 1)); *(u32x4*)(orow + 16 * DM + 32) = k1::pack_swap16(K3_F4(f1, 2), K3_F4(f1, 3));
    STAMP(3, 4);
}
}

extern "C" void kernel_launch(void* const* d_in, const int* in_sizes, int n_in, void* d_out, int out_size, void* d_ws, size_t ws_size, hipStream_t stream) {
    const bf16_t* x = (const bf16_t*)d_in[0]; const bf16_t* ckv = (const bf16_t*)d_in[1];
    const bf16_t* wq = (const bf16_t*)d_in[2]; const bf16_t* wuk = (const bf16_t*)d_in[3]; const bf16_t* wuv = (const bf16_t*)d_in[4]; const bf16_t* wo = (const bf16_t*)d_in[5];
    const bf16_t* w1 = (const bf16_t*)d_in[7]; const bf16_t* b1 = (const bf16_t*)d_in[8]; const bf16_t* w2 = (const bf16_t*)d_in[9]; const bf16_t* b2 = (const bf16_t*)d_in[10];
    char* ws = (char*)d_ws;
    (void)hipFuncSetAttribute((const void*)k1::k1_proj, hipFuncAttributeMaxDynamicSharedMemorySize, k1::LDS_BYTES);
    (void)hipFuncSetAttribute((const void*)k2::k2_attn, hipFuncAttributeMaxDynamicSharedMemorySize, k2::LDS_BYTES);
    (void)hipFuncSetAttribute((const void*)k3::k3_out, hipFuncAttributeMaxDynamicSharedMemorySize, k3::LDS_BYTES);
    k0_ln<<<SEQ / 4, 256, 0, stream>>>(x, (bf16_t*)(ws + WS_XN), (unsigned*)(ws + WS_FLAG));
    k1::k1_proj<<<256, 512, k1::LDS_BYTES, stream>>>(ckv, ws + WS_XN, wuk, wuv, wq, w1, b1, ws + WS_Q, ws + WS_K, ws + WS_VT, ws + WS_H);
    k2::k2_attn<<<256, 512, k2::LDS_BYTES, stream>>>(ws + WS_Q, ws + WS_K, ws + WS_VT, ws + WS_CTX, (const char*)wo, (const char*)w2);
    k3::k3_out<<<256, 512, k3::LDS_BYTES, stream>>>(ws + WS_CTX, ws + WS_H, wo, w2, b2, x, (float*)(ws + WS_PART), (unsigned*)(ws + WS_FLAG), (bf16_t*)d_out);
}
```

```cpp
#include <hip/hip_runtime.h>
#include <cstdint>

#define STAMP(k, i) do {} while (0)
#define LAS __attribute__((address_space(3)))
#define DI __device__ __forceinline__
typedef unsigned short bf16_t;
typedef short bf16x8 __attribute__((ext_vector_type(8)));
typedef short s16x4 __attribute__((ext_vector_type(4)));
typedef short v4i16_t __attribute__((ext_vector_type(4)));
typedef float f32x2 __attribute__((ext_vector_type(2)));
typedef float f32x4 __attribute__((ext_vector_type(4)));
typedef float f32x16 __attribute__((ext_vector_type(16)));
typedef unsigned u32x2 __attribute__((ext_vector_type(2)));
typedef unsigned u32x4 __attribute__((ext_vector_type(4)));
typedef __bf16 bf16x2_t __attribute__((ext_vector_type(2)));
typedef LAS unsigned char* lds_ptr;
typedef LAS const unsigned char* lds_cptr;

constexpr int SEQ = 2048, DM = 1024, NH = 32, HD = 128, DC = 512, FF = 3584, NQ = NH * HD;
constexpr size_t MiB = 1u << 20;
constexpr size_t WS_FLAG = 0;
constexpr size_t WS_XN = 1 * MiB;
constexpr size_t WS_Q = 8 * MiB;
constexpr size_t WS_K = 24 * MiB;
constexpr size_t WS_VT = 40 * MiB;
constexpr size_t WS_CTX = 56 * MiB;
constexpr size_t WS_H = 72 * MiB;
constexpr size_t WS_PART = 88 * MiB;

DI unsigned cvtpk(float lo, float hi) { f32x2 v = {lo, hi}; bf16x2_t b = __builtin_convertvector(v, bf16x2_t); return __builtin_bit_cast(unsigned, b); }
DI float bflo(unsigned w) { return __uint_as_float(w << 16); }
DI float bfhi(unsigned w) { return __uint_as_float(w & 0xffff0000u); }
DI s16x4 lds_tr(lds_cptr p) { return __builtin_bit_cast(s16x4, __builtin_amdgcn_ds_read_tr16_b64_v4i16((LAS v4i16_t*)p)); }
DI bf16x8 lds_rd(lds_cptr p) { return *(const LAS bf16x8*)p; }
DI bf16x8 lds_rdv(lds_cptr p) { return *(const volatile LAS bf16x8*)p; }
DI bf16x8 cat8(s16x4 a, s16x4 b) { return __builtin_shufflevector(a, b, 0, 1, 2, 3, 4, 5, 6, 7); }
DI void st16_sc1(void* p, u32x4 v) { asm volatile("global_store_dwordx4 %0, %1, off sc1\n\ts_nop 1" :: "v"(p), "v"(v) : "memory"); }
#define ST16(p, v) st16_sc1((void*)(p), v)
template <bool WT> DI void st16(void* p, u32x4 v) { if constexpr (WT) st16_sc1(p, v); else *(u32x4*)p = v; }
DI unsigned code_pf(unsigned line, unsigned nlines) {
    unsigned long long pc; unsigned d;
    asm volatile("s_getpc_b64 %0" : "=s"(pc));
    asm volatile("global_load_dword %0, %1, %2" : "=v"(d) : "v"((line < nlines ? line : 0u) * 128u), "s"(pc) : "memory");
    return d;
}
#define CODE_PF_DONE(d) asm volatile("" :: "v"(d))
#define SBAR() __builtin_amdgcn_sched_barrier(0)
#define PIN(x) asm volatile("" : "+v"(x))
#define WAIT_V(n) asm volatile("s_waitcnt vmcnt(" #n ")" ::: "memory")
#define WAIT_L(n) asm volatile("s_waitcnt lgkmcnt(" #n ")" ::: "memory")
#define BAR() __builtin_amdgcn_s_barrier()
#define MFMA16(a, b, c) __builtin_amdgcn_mfma_f32_16x16x32_bf16(a, b, c, 0, 0, 0)
#define MFMA32(a, b, c) __builtin_amdgcn_mfma_f32_32x32x16_bf16(a, b, c, 0, 0, 0)
DI void glds(const char* sbase, unsigned voff, unsigned ldsaddr) {
    asm volatile("s_mov_b32 m0, %2\n\ts_nop 0\n\tglobal_load_lds_dwordx4 %0, %1" :: "v"(voff), "s"(sbase), "s"(ldsaddr) : "memory");
}
DI void glds2(const char* sbase, unsigned voff, unsigned ldsaddr) {
    asm volatile("s_mov_b32 m0, %2\n\ts_nop 0\n\tglobal_load_lds_dwordx4 %0, %1\n\tglobal_load_lds_dwordx4 %0, %1 offset:1024" :: "v"(voff), "s"(sbase), "s"(ldsaddr) : "memory");
}

__global__ __launch_bounds__(256) void k0_ln(const bf16_t* __restrict__ x, bf16_t* __restrict__ xn, unsigned* __restrict__ flags) {
    const int lane = threadIdx.x & 63, row = blockIdx.x * 4 + (threadIdx.x >> 6);
    STAMP(0, 0);
    unsigned cpf = 0; if (blockIdx.x < 16) cpf = code_pf(threadIdx.x + (blockIdx.x >> 3) * 256, 300);
    const u32x4* src = (const u32x4*)(x + (size_t)row * DM);
    const u32x4 v0 = src[lane], v1 = src[lane + 64];
    float f[16];
#pragma unroll
    for (int i = 0; i < 4; ++i) { f[2 * i] = bflo(v0[i]); f[2 * i + 1] = bfhi(v0[i]); f[8 + 2 * i] = bflo(v1[i]); f[8 + 2 * i + 1] = bfhi(v1[i]); }
    float s = 0.f;
#pragma unroll
    for (int i = 0; i < 16; ++i) s += f[i];
#pragma unroll
    for (int o = 32; o >= 1; o >>= 1) s += __shfl_xor(s, o);
    const float mean = s * (1.0f / DM);
    float q = 0.f;
#pragma unroll
    for (int i = 0; i < 16; ++i) { f[i] -= mean; q += f[i] * f[i]; }
#pragma unroll
    for (int o = 32; o >= 1; o >>= 1) q += __shfl_xor(q, o);
    const float rstd = 1.0f / sqrtf(q * (1.0f / DM) + 1e-5f);
    u32x4 w0, w1;
#pragma unroll
    for (int i = 0; i < 4; ++i) { w0[i] = cvtpk(f[2 * i] * rstd, f[2 * i + 1] * rstd); w1[i] = cvtpk(f[8 + 2 * i] * rstd, f[8 + 2 * i + 1] * rstd); }
    const int mh = row >> 7, r = row & 127, c = lane & 7, kt = lane >> 3, sw = (c ^ ((r >> 1) & 7)) * 16;
    char* base = (char*)xn + (size_t)mh * 16 * 16384 + r * 128 + sw;
    *(u32x4*)(base + (size_t)kt * 16384) = w0;
    *(u32x4*)(base + (size_t)(kt + 8) * 16384) = w1;
    STAMP(0, 1);
    WAIT_V(0); CODE_PF_DONE(cpf);
    if (blockIdx.x == 0) flags[threadIdx.x * 32] = 0u;
}

namespace k1 {
#define MID_WT false
constexpr int HTB = 16384, LDS_BYTES = 8 * HTB;
enum { T_K = 0, T_V = 1, T_Q = 2, T_H = 3 };
struct Src {
    const char* a; const char* b;
    unsigned aK, aH, aD, bK, bD;
    unsigned va, vb;
};
struct Frag { bf16x8 At[4][2], B0[2][2], B1[2][2]; };

DI f32x2 gelu_tanh2(f32x2 v) {
    const f32x2 z = v * (v * v * -0.10294324f + -2.3022082f);
    f32x2 e; e.x = __builtin_amdgcn_exp2f(z.x); e.y = __builtin_amdgcn_exp2f(z.y);
    e = e + 1.0f;
    f32x2 r; r.x = __builtin_amdgcn_rcpf(e.x); r.y = __builtin_amdgcn_rcpf(e.y);
    return v * r;
}

#define K1_SA(b, h) (((b) * 2 + (h)) * HTB)
#define K1_SB(b, h) ((4 + (b) * 2 + (h)) * HTB)
#define K1_STAGE(bufoff, gbase, voff, d2) do { glds((const char*)(gbase), voff, ldsw + (bufoff)); glds((const char*)(gbase) + (d2), voff, ldsw + (bufoff) + 8192); } while (0)
#define K1_LDA(dst, b, h) do { _Pragma("unroll") for (int m = 0; m < 4; ++m) { dst[m][0] = lds_rd(lds + K1_SA(b, h) + aoff0 + m * 2048); dst[m][1] = lds_rd(lds + K1_SA(b, h) + aoff1 + m * 2048); } } while (0)
#define K1_LDB(dst, b, h) do { _Pragma("unroll") for (int k = 0; k < 2; ++k) { \
        dst[0][k] = cat8(lds_tr(lds + K1_SB(b, h) + boff0 + k * 8192), lds_tr(lds + K1_SB(b, h) + boff0 + k * 8192 + 256)); \
        dst[1][k] = cat8(lds_tr(lds + K1_SB(b, h) + boff1 + k * 8192), lds_tr(lds + K1_SB(b, h) + boff1 + k * 8192 + 256)); } } while (0)
#define K1_MMA(ai, bj, At, Bt) do { __builtin_amdgcn_s_setprio(1); _Pragma("unroll") for (int m = 0; m < 4; ++m) _Pragma("unroll") for (int n = 0; n < 2; ++n) _Pragma("unroll") for (int k = 0; k < 2; ++k) \
        acc[ai][bj][m][n] = SWAP ? MFMA16(At[m][k], Bt[n][k], acc[ai][bj][m][n]) : MFMA16(Bt[n][k], At[m][k], acc[ai][bj][m][n]); __builtin_amdgcn_s_setprio(0); } while (0)

template <bool SWAP>
DI void kloop(lds_ptr lds, f32x4 (&acc)[2][2][4][2], const Src& cur, const Src& nxt, int nt, unsigned ldsw, int aoff0, int aoff1, int boff0, int boff1) {
    bf16x8 At[4][2], B0[2][2], B1[2][2];
    for (int t = 0; t < nt; t += 2) {
        const bool last = (t == nt - 2);
        const char* a1h = cur.a + (size_t)(t + 1) * cur.aK + cur.aH;
        const char* a2 = last ? nxt.a : cur.a + (size_t)(t + 2) * cur.aK; const char* b2 = last ? nxt.b : cur.b + (size_t)(t + 2) * cur.bK;
        const unsigned aK = last ? nxt.aK : cur.aK, aH = last ? nxt.aH : cur.aH, aD = last ? nxt.aD : cur.aD, bK = last ? nxt.bK : cur.bK, bD = last ? nxt.bD : cur.bD;
        const unsigned va = last ? nxt.va : cur.va, vb = last ? nxt.vb : cur.vb;
        const char* a3 = a2 + aK; const char* b3 = b2 + bK;
        K1_LDB(B0, 0, 0); K1_LDB(B1, 0, 1); SBAR(); K1_LDA(At, 0, 0); K1_STAGE(K1_SA(1, 1), a1h, cur.va, cur.aD);
        WAIT_V(8); WAIT_L(0); BAR(); K1_MMA(0, 0, At, B0); K1_MMA(0, 1, At, B1); BAR(); SBAR();
        K1_LDA(At, 0, 1); K1_STAGE(K1_SB(0, 0), b2, vb, bD); K1_STAGE(K1_SB(0, 1), b2 + 256, vb, bD); K1_STAGE(K1_SA(0, 0), a2, va, aD);
        WAIT_V(8); WAIT_L(0); BAR(); K1_MMA(1, 0, At, B0); K1_MMA(1, 1, At, B1); BAR(); SBAR();
        K1_LDB(B0, 1, 0); K1_LDB(B1, 1, 1); SBAR(); K1_LDA(At, 1, 0); K1_STAGE(K1_SA(0, 1), a2 + aH, va, aD);
        WAIT_V(8); WAIT_L(0); BAR(); K1_MMA(0, 0, At, B0); K1_MMA(0, 1, At, B1); BAR(); SBAR();
        K1_LDA(At, 1, 1); K1_STAGE(K1_SB(1, 0), b3, vb, bD); K1_STAGE(K1_SB(1, 1), b3 + 256, vb, bD); K1_STAGE(K1_SA(1, 0), a3, va, aD);
        WAIT_V(8); WAIT_L(0); BAR(); K1_MMA(1, 0, At, B0); K1_MMA(1, 1, At, B1); BAR(); SBAR();
    }
}

DI void zero_acc(f32x4 (&acc)[2][2][4][2]) {
#pragma unroll
    for (int a = 0; a < 2; ++a)
#pragma unroll
        for (int b = 0; b < 2; ++b)
#pragma unroll
            for (int m = 0; m < 4; ++m)
#pragma unroll
                for (int n = 0; n < 2; ++n) acc[a][b][m][n] = (f32x4){0.f, 0.f, 0.f, 0.f};
}
DI u32x4 pack_swap16(f32x4 v0, f32x4 v1) {
    const unsigned x0 = cvtpk(v0[0], v0[1]), x1 = cvtpk(v0[2], v0[3]), y0 = cvtpk(v1[0], v1[1]), y1 = cvtpk(v1[2], v1[3]);
    const auto r0 = __builtin_amdgcn_permlane16_swap(x0, y0, false, false), r1 = __builtin_amdgcn_permlane16_swap(x1, y1, false, false);
    return (u32x4){r0[0], r1[0], r0[1], r1[1]};
}
DI u32x4 pack_swap32(f32x4 v0, f32x4 v1) {
    const unsigned x0 = cvtpk(v0[0], v0[1]), x1 = cvtpk(v0[2], v0[3]), y0 = cvtpk(v1[0], v1[1]), y1 = cvtpk(v1[2], v1[3]);
    const auto r0 = __builtin_amdgcn_permlane32_swap(x0, y0, false, false), r1 = __builtin_amdgcn_permlane32_swap(x1, y1, false, false);
    return (u32x4){r0[0], r1[0], r0[1], r1[1]};
}
template <bool WT> DI void epi_qk(const f32x4 (&acc)[2][2][4][2], char* dst, int pm, int pn, float sc, int wr, int wc, int fr, int fq) {
    const int cg = ((fq & 1) << 1) | (fq >> 1);
    char* p = dst + (size_t)(2 * pn * 32 + 4 * pm + wr) * 16384 + (4 * wc + cg) * 1024 + fr * 16;
#pragma unroll
    for (int ai = 0; ai < 2; ++ai)
#pragma unroll
        for (int bj = 0; bj < 2; ++bj)
#pragma unroll
            for (int m = 0; m < 4; ++m)
                st16<WT>(p + (size_t)(bj * 32 + ai * 2) * 16384 + m * 256, pack_swap16(acc[ai][bj][m][0] * sc, acc[ai][bj][m][1] * sc));
}
DI void epi_v(const f32x4 (&acc)[2][2][4][2], char* dst, int pm, int pn, int wr, int wc, int fr, int fq) {
    char* p = dst + (size_t)(2 * pn * 32 + 4 * pm + wr) * 16384 + fq * 2048 + (32 * wc + fr) * 16;
#pragma unroll
    for (int ai = 0; ai < 2; ++ai)
#pragma unroll
        for (int bj = 0; bj < 2; ++bj)
#pragma unroll
            for (int m = 0; m < 4; m += 2)
#pragma unroll
                for (int n = 0; n < 2; ++n)
                    st16<MID_WT>(p + (size_t)(bj * 32 + ai * 2) * 16384 + m * 4096 + n * 256, pack_swap32(acc[ai][bj][m][n], acc[ai][bj][m + 1][n]));
}
DI void epi_h(const f32x4 (&acc)[2][2][4][2], char* dst, const bf16_t* b1, int pm, int pn, int wr, int wc, int fr, int fq) {
    const int cg = ((fq & 1) << 1) | (fq >> 1);
    f32x4 bv[2][2];
#pragma unroll
    for (int bj = 0; bj < 2; ++bj)
#pragma unroll
        for (int n = 0; n < 2; ++n) { const u32x2 w = *(const u32x2*)(b1 + pn * 256 + bj * 128 + wc * 32 + n * 16 + fq * 4); bv[bj][n] = (f32x4){bflo(w[0]), bfhi(w[0]), bflo(w[1]), bfhi(w[1])}; }
    char* p = dst + ((size_t)(2 * pm * 56 + 4 * pn + (wc >> 1)) * 8 + 4 * (wc & 1) + cg) * 2048 + (64 * wr + fr) * 16;
#pragma unroll
    for (int ai = 0; ai < 2; ++ai)
#pragma unroll
        for (int bj = 0; bj < 2; ++bj)
#pragma unroll
            for (int m = 0; m < 4; ++m) {
                f32x4 v0 = acc[ai][bj][m][0] + bv[bj][0], v1 = acc[ai][bj][m][1] + bv[bj][1];
                { const f32x2 a = gelu_tanh2((f32x2){v0[0], v0[1]}), b = gelu_tanh2((f32x2){v0[2], v0[3]}), c = gelu_tanh2((f32x2){v1[0], v1[1]}), d = gelu_tanh2((f32x2){v1[2], v1[3]});
                  v0 = (f32x4){a.x, a.y, b.x, b.y}; v1 = (f32x4){c.x, c.y, d.x, d.y}; }
                ST16(p + (size_t)(ai * 56 + bj * 2) * 16384 + m * 256, pack_swap16(v0, v1));
            }
}

__global__ __launch_bounds__(512, 2) void k1_proj(const bf16_t* __restrict__ ckv, const char* __restrict__ xn, const bf16_t* __restrict__ wuk, const bf16_t* __restrict__ wuv,
                                                  const bf16_t* __restrict__ wq, const bf16_t* __restrict__ w1, const bf16_t* __restrict__ b1,
                                                  char* __restrict__ Qo, char* __restrict__ Ko, char* __restrict__ VTo, char* __restrict__ Ho) {
    extern __shared__ __attribute__((aligned(16))) unsigned char smem[];
    lds_ptr lds = (lds_ptr)smem;
    const int tid = threadIdx.x, wid = __builtin_amdgcn_readfirstlane(tid >> 6), lane = tid & 63, wr = wid >> 2, wc = wid & 3, fr = lane & 15, fq = lane >> 4;
    const int xcd = blockIdx.x & 7, idx = blockIdx.x >> 3, pm = idx & 7, pj = xcd + 8 * (idx >> 3);
    const bool isV = pj >= 16, has2 = pj < 30, isH = pj >= 16;
    const unsigned ldsw = (unsigned)(uintptr_t)smem + (unsigned)wid * 1024u;
    const int aoff0 = (wr * 64 + fr) * 128 + ((fq ^ (fr >> 1)) * 16), aoff1 = aoff0 ^ 64;
    const int boff0 = fq * 2048 + wc * 512 + (fr >> 2) * 64 + (fq & 1) * 32 + (fr & 3) * 8, boff1 = boff0 ^ 32;
    const unsigned brow = 8 * (wid >> 1) + ((lane & 31) >> 2), bcol = (64 * (wid & 1) + 32 * (lane >> 5) + 8 * ((lane & 3) ^ (wid & 2))) * 2;
    Src u0, u1;
    u0.a = (const char*)ckv + (size_t)pm * 256 * DC * 2; u0.aK = 128; u0.aH = 128 * DC * 2; u0.aD = 64 * DC * 2;
    u0.va = (tid >> 3) * (DC * 2) + (((tid & 7) ^ ((tid >> 4) & 7)) * 16);
    u0.b = (const char*)(isV ? wuv : wuk) + (pj & 15) * 512; u0.bK = 64 * NQ * 2; u0.bD = 32 * NQ * 2; u0.vb = brow * (NQ * 2) + bcol;
    if (has2) {
        const unsigned ldb2 = isH ? FF * 2 : NQ * 2;
        u1.a = xn + (size_t)pm * 2 * 16 * 16384; u1.aK = 16384; u1.aH = 16 * 16384; u1.aD = 8192; u1.va = tid * 16;
        u1.b = (const char*)(isH ? w1 : wq) + (pj & 15) * 512; u1.bK = 64 * ldb2; u1.bD = 32 * ldb2; u1.vb = brow * ldb2 + bcol;
    } else u1 = u0;
    f32x4 acc[2][2][4][2];
    zero_acc(acc);
    STAMP(1, 0);
    const unsigned cpf = code_pf(tid, 136);
    K1_STAGE(K1_SB(0, 0), u0.b, u0.vb, u0.bD); K1_STAGE(K1_SB(0, 1), u0.b + 256, u0.vb, u0.bD); K1_STAGE(K1_SA(0, 0), u0.a, u0.va, u0.aD); K1_STAGE(K1_SA(0, 1), u0.a + u0.aH, u0.va, u0.aD);
    if (wr == 1) BAR();
    WAIT_V(2); BAR();
    CODE_PF_DONE(cpf);
    K1_STAGE(K1_SB(1, 0), u0.b + u0.bK, u0.vb, u0.bD); K1_STAGE(K1_SA(1, 0), u0.a + u0.aK, u0.va, u0.aD); K1_STAGE(K1_SB(1, 1), u0.b + u0.bK + 256, u0.vb, u0.bD);
    WAIT_V(6); BAR();
    STAMP(1, 1);
    if (isV) kloop<true>(lds, acc, u0, u1, DC / 64, ldsw, aoff0, aoff1, boff0, boff1);
    else     kloop<false>(lds, acc, u0, u1, DC / 64, ldsw, aoff0, aoff1, boff0, boff1);
    STAMP(1, 2);
    if (wr == 0) BAR();
    if (isV) epi_v(acc, VTo, pm, pj & 15, wr, wc, fr, fq); else epi_qk<MID_WT>(acc, Ko, pm, pj & 15, 1.0f, wr, wc, fr, fq);
    if (has2) {
        zero_acc(acc);
        STAMP(1, 3);
        if (wr == 1) BAR();
        kloop<false>(lds, acc, u1, u1, DM / 64, ldsw, aoff0, aoff1, boff0, boff1);
        STAMP(1, 4);
        if (wr == 0) BAR();
        if (isH) epi_h(acc, Ho, b1, pm, pj & 15, wr, wc, fr, fq); else epi_qk<true>(acc, Qo, pm, pj & 15, 0.12751743082459868f, wr, wc, fr, fq);
    }
    STAMP(1, 5);
    WAIT_V(0);
    STAMP(1, 6);
}
#undef K1_SA
#undef K1_SB
#undef K1_STAGE
#undef K1_LDA
#undef K1_LDB
#undef K1_MMA
}

namespace k2 {
constexpr int SLOT = 16384, LDS_K = 0, LDS_V = 4 * SLOT, LDS_BYTES = 8 * SLOT;
#define K2_ASM \
    "s_nop 1\n\t" \
    "s_cmp_eq_u32 %[n], 0\n\t" \
    "s_cbranch_scc1 TAIL_%=\n\t" \
    "LOOP_%=:\n\t" \
    "s_cmp_lg_u32 %[dmaw], 0\n\t" \
    "s_cbranch_scc1 DMAL_%=\n\t" \
    "DMABL_%=:\n\t" \
    "s_sub_u32 s60, %[t], 8\n\t" \
    "s_and_b32 s60, s60, 0xfffffffb\n\t" \
    "s_or_b32 s60, s60, %[dmaw]\n\t" \
    "s_cmp_eq_u32 s60, 0\n\t" \
    "s_cbranch_scc1 PF_%=\n\t" \
    "PFB_%=:\n\t" \
    "s_waitcnt lgkmcnt(0)\n\t" \
    "v_mfma_f32_32x32x16_bf16 v[112:127], v[168:171], v[64:67], v[96:111]\n\t" \
    "ds_read_b128 v[200:203], v235 offset:8192\n\t" \
    "ds_read_b128 v[204:207], v235 offset:8704\n\t" \
    "v_add_f32 v237, v144, v145\n\t" \
    "v_cvt_pk_bf16_f32 v160, v144, v145\n\t" \
    "v_mfma_f32_32x32x16_bf16 v[112:127], v[172:175], v[68:71], v[112:127]\n\t" \
    "ds_read_b128 v[208:211], v235 offset:9216\n\t" \
    "ds_read_b128 v[212:215], v235 offset:9728\n\t" \
    "v_add_f32 v237, v237, v146\n\t" \
    "v_add_f32 v237, v237, v147\n\t" \
    "v_cvt_pk_bf16_f32 v161, v146, v147\n\t" \
    "v_mfma_f32_32x32x16_bf16 v[112:127], v[176:179], v[72:75], v[112:127]\n\t" \
    "ds_read_b128 v[216:219], v235 offset:12288\n\t" \
    "ds_read_b128 v[220:223], v235 offset:12800\n\t" \
    "v_add_f32 v237, v237, v148\n\t" \
    "v_add_f32 v237, v237, v149\n\t" \
    "v_cvt_pk_bf16_f32 v162, v148, v149\n\t" \
    "v_mfma_f32_32x32x16_bf16 v[112:127], v[180:183], v[76:79], v[112:127]\n\t" \
    "ds_read_b128 v[224:227], v235 offset:13312\n\t" \
    "ds_read_b128 v[228:231], v235 offset:13824\n\t" \
    "v_add_f32 v237, v237, v150\n\t" \
    "v_add_f32 v237, v237, v151\n\t" \
    "v_cvt_pk_bf16_f32 v163, v150, v151\n\t" \
    "v_mfma_f32_32x32x16_bf16 v[112:127], v[184:187], v[80:83], v[112:127]\n\t" \
    "v_add_f32 v237, v237, v152\n\t" \
    "v_add_f32 v237, v237, v153\n\t" \
    "v_cvt_pk_bf16_f32 v164, v152, v153\n\t" \
    "v_mfma_f32_32x32x16_bf16 v[112:127], v[188:191], v[84:87], v[112:127]\n\t" \
    "v_add_f32 v237, v237, v154\n\t" \
    "v_add_f32 v237, v237, v155\n\t" \
    "v_cvt_pk_bf16_f32 v165, v154, v155\n\t" \
    "v_mfma_f32_32x32x16_bf16 v[112:127], v[192:195], v[88:91], v[112:127]\n\t" \
    "v_add_f32 v237, v237, v156\n\t" \
    "v_add_f32 v237, v237, v157\n\t" \
    "v_cvt_pk_bf16_f32 v166, v156, v157\n\t" \
    "v_mfma_f32_32x32x16_bf16 v[112:127], v[196:199], v[92:95], v[112:127]\n\t" \
    "v_add_f32 v237, v237, v158\n\t" \
    "v_add_f32 v237, v237, v159\n\t" \
    "v_cvt_pk_bf16_f32 v167, v158, v159\n\t" \
    "v_cmp_lt_f32 vcc, %[thr], v237\n\t" \
    "s_cbranch_vccnz RAREA_%=\n\t" \
    "BACKA_%=:\n\t" \
    "s_waitcnt lgkmcnt(0)\n\t" \
    "v_mfma_f32_32x32x16_bf16 v[0:15], v[200:203], v[160:163], v[0:15]\n\t" \
    "ds_read_b128 v[168:171], v232 offset:512\n\t" \
    "ds_read_b128 v[172:175], v232 offset:2560\n\t" \
    "v_add_f32 v236, v236, v237\n\t" \
    "v_mfma_f32_32x32x16_bf16 v[16:31], v[204:207], v[160:163], v[16:31]\n\t" \
    "ds_read_b128 v[176:179], v232 offset:4608\n\t" \
    "ds_read_b128 v[180:183], v232 offset:6656\n\t" \
    "v_exp_f32 v144, v112\n\t" \
    "v_exp_f32 v145, v113\n\t" \
    "v_mfma_f32_32x32x16_bf16 v[32:47], v[208:211], v[160:163], v[32:47]\n\t" \
    "ds_read_b128 v[184:187], v232 offset:8704\n\t" \
    "ds_read_b128 v[188:191], v232 offset:10752\n\t" \
    "v_exp_f32 v146, v114\n\t" \
    "v_exp_f32 v147, v115\n\t" \
    "v_mfma_f32_32x32x16_bf16 v[48:63], v[212:215], v[160:163], v[48:63]\n\t" \
    "ds_read_b128 v[192:195], v232 offset:12800\n\t" \
    "ds_read_b128 v[196:199], v232 offset:14848\n\t" \
    "v_exp_f32 v148, v116\n\t" \
    "v_exp_f32 v149, v117\n\t" \
    "v_mfma_f32_32x32x16_bf16 v[0:15], v[216:219], v[164:167], v[0:15]\n\t" \
    "v_exp_f32 v150, v118\n\t" \
    "v_exp_f32 v151, v119\n\t" \
    "v_exp_f32 v152, v120\n\t" \
    "v_mfma_f32_32x32x16_bf16 v[16:31], v[220:223], v[164:167], v[16:31]\n\t" \
    "v_exp_f32 v153, v121\n\t" \
    "v_exp_f32 v154, v122\n\t" \
    "v_exp_f32 v155, v123\n\t" \
    "v_mfma_f32_32x32x16_bf16 v[32:47], v[224:227], v[164:167], v[32:47]\n\t" \
    "v_exp_f32 v156, v124\n\t" \
    "v_exp_f32 v157, v125\n\t" \
    "v_mfma_f32_32x32x16_bf16 v[48:63], v[228:231], v[164:167], v[48:63]\n\t" \
    "v_exp_f32 v158, v126\n\t" \
    "v_exp_f32 v159, v127\n\t" \
    "s_waitcnt lgkmcnt(0)\n\t" \
    "v_mfma_f32_32x32x16_bf16 v[128:143], v[168:171], v[64:67], v[96:111]\n\t" \
    "ds_read_b128 v[200:203], v234 offset:0\n\t" \
    "ds_read_b128 v[204:207], v234 offset:512\n\t" \
    "v_add_f32 v237, v144, v145\n\t" \
    "v_cvt_pk_bf16_f32 v160, v144, v145\n\t" \
    "v_mfma_f32_32x32x16_bf16 v[128:143], v[172:175], v[68:71], v[128:143]\n\t" \
    "ds_read_b128 v[208:211], v234 offset:1024\n\t" \
    "ds_read_b128 v[212:215], v234 offset:1536\n\t" \
    "v_add_f32 v237, v237, v146\n\t" \
    "v_add_f32 v237, v237, v147\n\t" \
    "v_cvt_pk_bf16_f32 v161, v146, v147\n\t" \
    "v_mfma_f32_32x32x16_bf16 v[128:143], v[176:179], v[72:75], v[128:143]\n\t" \
    "ds_read_b128 v[216:219], v234 offset:4096\n\t" \
    "ds_read_b128 v[220:223], v234 offset:4608\n\t" \
    "v_add_f32 v237, v237, v148\n\t" \
    "v_add_f32 v237, v237, v149\n\t" \
    "v_cvt_pk_bf16_f32 v162, v148, v149\n\t" \
    "v_mfma_f32_32x32x16_bf16 v[128:143], v[180:183], v[76:79], v[128:143]\n\t" \
    "ds_read_b128 v[224:227], v234 offset:5120\n\t" \
    "ds_read_b128 v[228:231], v234 offset:5632\n\t" \
    "v_add_f32 v237, v237, v150\n\t" \
    "v_add_f32 v237, v237, v151\n\t" \
    "v_cvt_pk_bf16_f32 v163, v150, v151\n\t" \
    "v_mfma_f32_32x32x16_bf16 v[128:143], v[184:187], v[80:83], v[128:143]\n\t" \
    "v_add_f32 v237, v237, v152\n\t" \
    "v_add_f32 v237, v237, v153\n\t" \
    "v_cvt_pk_bf16_f32 v164, v152, v153\n\t" \
    "v_mfma_f32_32x32x16_bf16 v[128:143], v[188:191], v[84:87], v[128:143]\n\t" \
    "v_add_f32 v237, v237, v154\n\t" \
    "v_add_f32 v237, v237, v155\n\t" \
    "v_cvt_pk_bf16_f32 v165, v154, v155\n\t" \
    "v_mfma_f32_32x32x16_bf16 v[128:143], v[192:195], v[88:91], v[128:143]\n\t" \
    "v_add_f32 v237, v237, v156\n\t" \
    "v_add_f32 v237, v237, v157\n\t" \
    "v_cvt_pk_bf16_f32 v166, v156, v157\n\t" \
    "v_mfma_f32_32x32x16_bf16 v[128:143], v[196:199], v[92:95], v[128:143]\n\t" \
    "v_add_f32 v237, v237, v158\n\t" \
    "v_add_f32 v237, v237, v159\n\t" \
    "v_cvt_pk_bf16_f32 v167, v158, v159\n\t" \
    "v_cmp_lt_f32 vcc, %[thr], v237\n\t" \
    "s_cbranch_vccnz RAREB_%=\n\t" \
    "BACKB_%=:\n\t" \
    "s_waitcnt lgkmcnt(0)\n\t" \
    "v_mfma_f32_32x32x16_bf16 v[0:15], v[200:203], v[160:163], v[0:15]\n\t" \
    "ds_read_b128 v[168:171], v233 offset:0\n\t" \
    "ds_read_b128 v[172:175], v233 offset:2048\n\t" \
    "v_add_f32 v236, v236, v237\n\t" \
    "v_mfma_f32_32x32x16_bf16 v[16:31], v[204:207], v[160:163], v[16:31]\n\t" \
    "ds_read_b128 v[176:179], v233 offset:4096\n\t" \
    "ds_read_b128 v[180:183], v233 offset:6144\n\t" \
    "v_exp_f32 v144, v128\n\t" \
    "v_exp_f32 v145, v129\n\t" \
    "v_mfma_f32_32x32x16_bf16 v[32:47], v[208:211], v[160:163], v[32:47]\n\t" \
    "ds_read_b128 v[184:187], v233 offset:8192\n\t" \
    "ds_read_b128 v[188:191], v233 offset:10240\n\t" \
    "v_exp_f32 v146, v130\n\t" \
    "v_exp_f32 v147, v131\n\t" \
    "v_mfma_f32_32x32x16_bf16 v[48:63], v[212:215], v[160:163], v[48:63]\n\t" \
    "ds_read_b128 v[192:195], v233 offset:12288\n\t" \
    "ds_read_b128 v[196:199], v233 offset:14336\n\t" \
    "v_exp_f32 v148, v132\n\t" \
    "v_exp_f32 v149, v133\n\t" \
    "v_mfma_f32_32x32x16_bf16 v[0:15], v[216:219], v[164:167], v[0:15]\n\t" \
    "v_exp_f32 v150, v134\n\t" \
    "v_exp_f32 v151, v135\n\t" \
    "v_exp_f32 v152, v136\n\t" \
    "v_mfma_f32_32x32x16_bf16 v[16:31], v[220:223], v[164:167], v[16:31]\n\t" \
    "v_exp_f32 v153, v137\n\t" \
    "v_exp_f32 v154, v138\n\t" \
    "v_exp_f32 v155, v139\n\t" \
    "s_add_u32 %[soff], %[soff], 0x4000\n\t" \
    "s_and_b32 %[soff], %[soff], 0xffff\n\t" \
    "v_mfma_f32_32x32x16_bf16 v[32:47], v[224:227], v[164:167], v[32:47]\n\t" \
    "v_exp_f32 v156, v140\n\t" \
    "v_exp_f32 v157, v141\n\t" \
    "v_mov_b32 v235, v234\n\t" \
    "v_mov_b32 v232, v233\n\t" \
    "v_mfma_f32_32x32x16_bf16 v[48:63], v[228:231], v[164:167], v[48:63]\n\t" \
    "v_exp_f32 v158, v142\n\t" \
    "v_exp_f32 v159, v143\n\t" \
    "v_add_u32 v234, %[soff], %[vp0]\n\t" \
    "s_add_u32 s60, %[soff], 0x4000\n\t" \
    "s_and_b32 s60, s60, 0xffff\n\t" \
    "v_add_u32 v233, s60, %[kp0]\n\t" \
    "s_waitcnt vmcnt(8)\n\t" \
    "s_barrier\n\t" \
    "s_add_u32 %[t], %[t], 1\n\t" \
    "s_cmp_lt_u32 %[t], %[n]\n\t" \
    "s_cbranch_scc1 LOOP_%=\n\t" \
    "TAIL_%=:\n\t" \
    "s_cmp_lg_u32 %[dmaw], 0\n\t" \
    "s_cbranch_scc1 DMAT_%=\n\t" \
    "DMABT_%=:\n\t" \
    "s_cmp_eq_u32 %[odd], 0\n\t" \
    "s_cbranch_scc1 EVEN_%=\n\t" \
    "s_waitcnt lgkmcnt(0)\n\t" \
    "v_mfma_f32_32x32x16_bf16 v[112:127], v[168:171], v[64:67], v[96:111]\n\t" \
    "ds_read_b128 v[200:203], v235 offset:8192\n\t" \
    "ds_read_b128 v[204:207], v235 offset:8704\n\t" \
    "v_add_f32 v237, v144, v145\n\t" \
    "v_cvt_pk_bf16_f32 v160, v144, v145\n\t" \
    "v_mfma_f32_32x32x16_bf16 v[112:127], v[172:175], v[68:71], v[112:127]\n\t" \
    "ds_read_b128 v[208:211], v235 offset:9216\n\t" \
    "ds_read_b128 v[212:215], v235 offset:9728\n\t" \
    "v_add_f32 v237, v237, v146\n\t" \
    "v_add_f32 v237, v237, v147\n\t" \
    "v_cvt_pk_bf16_f32 v161, v146, v147\n\t" \
    "v_mfma_f32_32x32x16_bf16 v[112:127], v[176:179], v[72:75], v[112:127]\n\t" \
    "ds_read_b128 v[216:219], v235 offset:12288\n\t" \
    "ds_read_b128 v[220:223], v235 offset:12800\n\t" \
    "v_add_f32 v237, v237, v148\n\t" \
    "v_add_f32 v237, v237, v149\n\t" \
    "v_cvt_pk_bf16_f32 v162, v148, v149\n\t" \
    "v_mfma_f32_32x32x16_bf16 v[112:127], v[180:183], v[76:79], v[112:127]\n\t" \
    "ds_read_b128 v[224:227], v235 offset:13312\n\t" \
    "ds_read_b128 v[228:231], v235 offset:13824\n\t" \
    "v_add_f32 v237, v237, v150\n\t" \
    "v_add_f32 v237, v237, v151\n\t" \
    "v_cvt_pk_bf16_f32 v163, v150, v151\n\t" \
    "v_mfma_f32_32x32x16_bf16 v[112:127], v[184:187], v[80:83], v[112:127]\n\t" \
    "v_add_f32 v237, v237, v152\n\t" \
    "v_add_f32 v237, v237, v153\n\t" \
    "v_cvt_pk_bf16_f32 v164, v152, v153\n\t" \
    "v_mfma_f32_32x32x16_bf16 v[112:127], v[188:191], v[84:87], v[112:127]\n\t" \
    "v_add_f32 v237, v237, v154\n\t" \
    "v_add_f32 v237, v237, v155\n\t" \
    "v_cvt_pk_bf16_f32 v165, v154, v155\n\t" \
    "v_mfma_f32_32x32x16_bf16 v[112:127], v[192:195], v[88:91], v[112:127]\n\t" \
    "v_add_f32 v237, v237, v156\n\t" \
    "v_add_f32 v237, v237, v157\n\t" \
    "v_cvt_pk_bf16_f32 v166, v156, v157\n\t" \
    "v_mfma_f32_32x32x16_bf16 v[112:127], v[196:199], v[92:95], v[112:127]\n\t" \
    "v_add_f32 v237, v237, v158\n\t" \
    "v_add_f32 v237, v237, v159\n\t" \
    "v_cvt_pk_bf16_f32 v167, v158, v159\n\t" \
    "v_cmp_lt_f32 vcc, %[thr], v237\n\t" \
    "s_cbranch_vccnz RAREC_%=\n\t" \
    "BACKC_%=:\n\t" \
    "s_waitcnt lgkmcnt(0)\n\t" \
    "v_mfma_f32_32x32x16_bf16 v[0:15], v[200:203], v[160:163], v[0:15]\n\t" \
    "ds_read_b128 v[168:171], v232 offset:512\n\t" \
    "ds_read_b128 v[172:175], v232 offset:2560\n\t" \
    "v_add_f32 v236, v236, v237\n\t" \
    "v_mfma_f32_32x32x16_bf16 v[16:31], v[204:207], v[160:163], v[16:31]\n\t" \
    "ds_read_b128 v[176:179], v232 offset:4608\n\t" \
    "ds_read_b128 v[180:183], v232 offset:6656\n\t" \
    "v_exp_f32 v144, v112\n\t" \
    "v_exp_f32 v145, v113\n\t" \
    "v_mfma_f32_32x32x16_bf16 v[32:47], v[208:211], v[160:163], v[32:47]\n\t" \
    "ds_read_b128 v[184:187], v232 offset:8704\n\t" \
    "ds_read_b128 v[188:191], v232 offset:10752\n\t" \
    "v_exp_f32 v146, v114\n\t" \
    "v_exp_f32 v147, v115\n\t" \
    "v_mfma_f32_32x32x16_bf16 v[48:63], v[212:215], v[160:163], v[48:63]\n\t" \
    "ds_read_b128 v[192:195], v232 offset:12800\n\t" \
    "ds_read_b128 v[196:199], v232 offset:14848\n\t" \
    "v_exp_f32 v148, v116\n\t" \
    "v_exp_f32 v149, v117\n\t" \
    "v_mfma_f32_32x32x16_bf16 v[0:15], v[216:219], v[164:167], v[0:15]\n\t" \
    "v_exp_f32 v150, v118\n\t" \
    "v_exp_f32 v151, v119\n\t" \
    "v_exp_f32 v152, v120\n\t" \
    "v_mfma_f32_32x32x16_bf16 v[16:31], v[220:223], v[164:167], v[16:31]\n\t" \
    "v_exp_f32 v153, v121\n\t" \
    "v_exp_f32 v154, v122\n\t" \
    "v_exp_f32 v155, v123\n\t" \
    "v_mfma_f32_32x32x16_bf16 v[32:47], v[224:227], v[164:167], v[32:47]\n\t" \
    "v_exp_f32 v156, v124\n\t" \
    "v_exp_f32 v157, v125\n\t" \
    "v_mfma_f32_32x32x16_bf16 v[48:63], v[228:231], v[164:167], v[48:63]\n\t" \
    "v_exp_f32 v158, v126\n\t" \
    "v_exp_f32 v159, v127\n\t" \
    "v_mov_b32 v239, 0xff800000\n\t" \
    "v_cmp_gt_i32 vcc, 0, %[dq]\n\t" \
    "s_nop 1\n\t" \
    "v_cndmask_b32 v96, v96, v239, vcc\n\t" \
    "v_cmp_gt_i32 vcc, 1, %[dq]\n\t" \
    "s_nop 1\n\t" \
    "v_cndmask_b32 v97, v97, v239, vcc\n\t" \
    "v_cmp_gt_i32 vcc, 2, %[dq]\n\t" \
    "s_nop 1\n\t" \
    "v_cndmask_b32 v98, v98, v239, vcc\n\t" \
    "v_cmp_gt_i32 vcc, 3, %[dq]\n\t" \
    "s_nop 1\n\t" \
    "v_cndmask_b32 v99, v99, v239, vcc\n\t" \
    "v_cmp_gt_i32 vcc, 8, %[dq]\n\t" \
    "s_nop 1\n\t" \
    "v_cndmask_b32 v100, v100, v239, vcc\n\t" \
    "v_cmp_gt_i32 vcc, 9, %[dq]\n\t" \
    "s_nop 1\n\t" \
    "v_cndmask_b32 v101, v101, v239, vcc\n\t" \
    "v_cmp_gt_i32 vcc, 10, %[dq]\n\t" \
    "s_nop 1\n\t" \
    "v_cndmask_b32 v102, v102, v239, vcc\n\t" \
    "v_cmp_gt_i32 vcc, 11, %[dq]\n\t" \
    "s_nop 1\n\t" \
    "v_cndmask_b32 v103, v103, v239, vcc\n\t" \
    "v_cmp_gt_i32 vcc, 16, %[dq]\n\t" \
    "s_nop 1\n\t" \
    "v_cndmask_b32 v104, v104, v239, vcc\n\t" \
    "v_cmp_gt_i32 vcc, 17, %[dq]\n\t" \
    "s_nop 1\n\t" \
    "v_cndmask_b32 v105, v105, v239, vcc\n\t" \
    "v_cmp_gt_i32 vcc, 18, %[dq]\n\t" \
    "s_nop 1\n\t" \
    "v_cndmask_b32 v106, v106, v239, vcc\n\t" \
    "v_cmp_gt_i32 vcc, 19, %[dq]\n\t" \
    "s_nop 1\n\t" \
    "v_cndmask_b32 v107, v107, v239, vcc\n\t" \
    "v_cmp_gt_i32 vcc, 24, %[dq]\n\t" \
    "s_nop 1\n\t" \
    "v_cndmask_b32 v108, v108, v239, vcc\n\t" \
    "v_cmp_gt_i32 vcc, 25, %[dq]\n\t" \
    "s_nop 1\n\t" \
    "v_cndmask_b32 v109, v109, v239, vcc\n\t" \
    "v_cmp_gt_i32 vcc, 26, %[dq]\n\t" \
    "s_nop 1\n\t" \
    "v_cndmask_b32 v110, v110, v239, vcc\n\t" \
    "v_cmp_gt_i32 vcc, 27, %[dq]\n\t" \
    "s_nop 1\n\t" \
    "v_cndmask_b32 v111, v111, v239, vcc\n\t" \
    "s_nop 1\n\t" \
    "s_waitcnt lgkmcnt(0)\n\t" \
    "v_mfma_f32_32x32x16_bf16 v[128:143], v[168:171], v[64:67], v[96:111]\n\t" \
    "ds_read_b128 v[200:203], v234 offset:0\n\t" \
    "ds_read_b128 v[204:207], v234 offset:512\n\t" \
    "v_add_f32 v237, v144, v145\n\t" \
    "v_cvt_pk_bf16_f32 v160, v144, v145\n\t" \
    "v_mfma_f32_32x32x16_bf16 v[128:143], v[172:175], v[68:71], v[128:143]\n\t" \
    "ds_read_b128 v[208:211], v234 offset:1024\n\t" \
    "ds_read_b128 v[212:215], v234 offset:1536\n\t" \
    "v_add_f32 v237, v237, v146\n\t" \
    "v_add_f32 v237, v237, v147\n\t" \
    "v_cvt_pk_bf16_f32 v161, v146, v147\n\t" \
    "v_mfma_f32_32x32x16_bf16 v[128:143], v[176:179], v[72:75], v[128:143]\n\t" \
    "ds_read_b128 v[216:219], v234 offset:4096\n\t" \
    "ds_read_b128 v[220:223], v234 offset:4608\n\t" \
    "v_add_f32 v237, v237, v148\n\t" \
    "v_add_f32 v237, v237, v149\n\t" \
    "v_cvt_pk_bf16_f32 v162, v148, v149\n\t" \
    "v_mfma_f32_32x32x16_bf16 v[128:143], v[180:183], v[76:79], v[128:143]\n\t" \
    "ds_read_b128 v[224:227], v234 offset:5120\n\t" \
    "ds_read_b128 v[228:231], v234 offset:5632\n\t" \
    "v_add_f32 v237, v237, v150\n\t" \
    "v_add_f32 v237, v237, v151\n\t" \
    "v_cvt_pk_bf16_f32 v163, v150, v151\n\t" \
    "v_mfma_f32_32x32x16_bf16 v[128:143], v[184:187], v[80:83], v[128:143]\n\t" \
    "v_add_f32 v237, v237, v152\n\t" \
    "v_add_f32 v237, v237, v153\n\t" \
    "v_cvt_pk_bf16_f32 v164, v152, v153\n\t" \
    "v_mfma_f32_32x32x16_bf16 v[128:143], v[188:191], v[84:87], v[128:143]\n\t" \
    "v_add_f32 v237, v237, v154\n\t" \
    "v_add_f32 v237, v237, v155\n\t" \
    "v_cvt_pk_bf16_f32 v165, v154, v155\n\t" \
    "v_mfma_f32_32x32x16_bf16 v[128:143], v[192:195], v[88:91], v[128:143]\n\t" \
    "v_add_f32 v237, v237, v156\n\t" \
    "v_add_f32 v237, v237, v157\n\t" \
    "v_cvt_pk_bf16_f32 v166, v156, v157\n\t" \
    "v_mfma_f32_32x32x16_bf16 v[128:143], v[196:199], v[92:95], v[128:143]\n\t" \
    "v_add_f32 v237, v237, v158\n\t" \
    "v_add_f32 v237, v237, v159\n\t" \
    "v_cvt_pk_bf16_f32 v167, v158, v159\n\t" \
    "v_cmp_lt_f32 vcc, %[thr], v237\n\t" \
    "s_cbranch_vccnz RARED_%=\n\t" \
    "BACKD_%=:\n\t" \
    "s_waitcnt lgkmcnt(0)\n\t" \
    "v_mfma_f32_32x32x16_bf16 v[0:15], v[200:203], v[160:163], v[0:15]\n\t" \
    "ds_read_b128 v[168:171], v233 offset:0\n\t" \
    "ds_read_b128 v[172:175], v233 offset:2048\n\t" \
    "v_add_f32 v236, v236, v237\n\t" \
    "v_mfma_f32_32x32x16_bf16 v[16:31], v[204:207], v[160:163], v[16:31]\n\t" \
    "ds_read_b128 v[176:179], v233 offset:4096\n\t" \
    "ds_read_b128 v[180:183], v233 offset:6144\n\t" \
    "v_exp_f32 v144, v128\n\t" \
    "v_exp_f32 v145, v129\n\t" \
    "v_mfma_f32_32x32x16_bf16 v[32:47], v[208:211], v[160:163], v[32:47]\n\t" \
    "ds_read_b128 v[184:187], v233 offset:8192\n\t" \
    "ds_read_b128 v[188:191], v233 offset:10240\n\t" \
    "v_exp_f32 v146, v130\n\t" \
    "v_exp_f32 v147, v131\n\t" \
    "v_mfma_f32_32x32x16_bf16 v[48:63], v[212:215], v[160:163], v[48:63]\n\t" \
    "ds_read_b128 v[192:195], v233 offset:12288\n\t" \
    "ds_read_b128 v[196:199], v233 offset:14336\n\t" \
    "v_exp_f32 v148, v132\n\t" \
    "v_exp_f32 v149, v133\n\t" \
    "v_mfma_f32_32x32x16_bf16 v[0:15], v[216:219], v[164:167], v[0:15]\n\t" \
    "v_exp_f32 v150, v134\n\t" \
    "v_exp_f32 v151, v135\n\t" \
    "v_exp_f32 v152, v136\n\t" \
    "v_mfma_f32_32x32x16_bf16 v[16:31], v[220:223], v[164:167], v[16:31]\n\t" \
    "v_exp_f32 v153, v137\n\t" \
    "v_exp_f32 v154, v138\n\t" \
    "v_exp_f32 v155, v139\n\t" \
    "v_mfma_f32_32x32x16_bf16 v[32:47], v[224:227], v[164:167], v[32:47]\n\t" \
    "v_exp_f32 v156, v140\n\t" \
    "v_exp_f32 v157, v141\n\t" \
    "v_mfma_f32_32x32x16_bf16 v[48:63], v[228:231], v[164:167], v[48:63]\n\t" \
    "v_exp_f32 v158, v142\n\t" \
    "v_exp_f32 v159, v143\n\t" \
    "s_waitcnt lgkmcnt(0)\n\t" \
    "ds_read_b128 v[200:203], v234 offset:8192\n\t" \
    "ds_read_b128 v[204:207], v234 offset:8704\n\t" \
    "ds_read_b128 v[208:211], v234 offset:9216\n\t" \
    "ds_read_b128 v[212:215], v234 offset:9728\n\t" \
    "ds_read_b128 v[216:219], v234 offset:12288\n\t" \
    "ds_read_b128 v[220:223], v234 offset:12800\n\t" \
    "ds_read_b128 v[224:227], v234 offset:13312\n\t" \
    "ds_read_b128 v[228:231], v234 offset:13824\n\t" \
    "v_add_f32 v237, v144, v145\n\t" \
    "v_cvt_pk_bf16_f32 v160, v144, v145\n\t" \
    "v_add_f32 v237, v237, v146\n\t" \
    "v_add_f32 v237, v237, v147\n\t" \
    "v_cvt_pk_bf16_f32 v161, v146, v147\n\t" \
    "v_add_f32 v237, v237, v148\n\t" \
    "v_add_f32 v237, v237, v149\n\t" \
    "v_cvt_pk_bf16_f32 v162, v148, v149\n\t" \
    "v_add_f32 v237, v237, v150\n\t" \
    "v_add_f32 v237, v237, v151\n\t" \
    "v_cvt_pk_bf16_f32 v163, v150, v151\n\t" \
    "v_add_f32 v237, v237, v152\n\t" \
    "v_add_f32 v237, v237, v153\n\t" \
    "v_cvt_pk_bf16_f32 v164, v152, v153\n\t" \
    "v_add_f32 v237, v237, v154\n\t" \
    "v_add_f32 v237, v237, v155\n\t" \
    "v_cvt_pk_bf16_f32 v165, v154, v155\n\t" \
    "v_add_f32 v237, v237, v156\n\t" \
    "v_add_f32 v237, v237, v157\n\t" \
    "v_cvt_pk_bf16_f32 v166, v156, v157\n\t" \
    "v_add_f32 v237, v237, v158\n\t" \
    "v_add_f32 v237, v237, v159\n\t" \
    "v_cvt_pk_bf16_f32 v167, v158, v159\n\t" \
    "v_cmp_lt_f32 vcc, %[thr], v237\n\t" \
    "s_cbranch_vccnz RAREE_%=\n\t" \
    "BACKE_%=:\n\t" \
    "s_waitcnt lgkmcnt(0)\n\t" \
    "v_add_f32 v236, v236, v237\n\t" \
    "v_mfma_f32_32x32x16_bf16 v[0:15], v[200:203], v[160:163], v[0:15]\n\t" \
    "v_mfma_f32_32x32x16_bf16 v[16:31], v[204:207], v[160:163], v[16:31]\n\t" \
    "v_mfma_f32_32x32x16_bf16 v[32:47], v[208:211], v[160:163], v[32:47]\n\t" \
    "v_mfma_f32_32x32x16_bf16 v[48:63], v[212:215], v[160:163], v[48:63]\n\t" \
    "v_mfma_f32_32x32x16_bf16 v[0:15], v[216:219], v[164:167], v[0:15]\n\t" \
    "v_mfma_f32_32x32x16_bf16 v[16:31], v[220:223], v[164:167], v[16:31]\n\t" \
    "v_mfma_f32_32x32x16_bf16 v[32:47], v[224:227], v[164:167], v[32:47]\n\t" \
    "v_mfma_f32_32x32x16_bf16 v[48:63], v[228:231], v[164:167], v[48:63]\n\t" \
    "s_branch END_%=\n\t" \
    "EVEN_%=:\n\t" \
    "v_mov_b32 v239, 0xff800000\n\t" \
    "v_cmp_gt_i32 vcc, 0, %[dq]\n\t" \
    "s_nop 1\n\t" \
    "v_cndmask_b32 v96, v96, v239, vcc\n\t" \
    "v_cmp_gt_i32 vcc, 1, %[dq]\n\t" \
    "s_nop 1\n\t" \
    "v_cndmask_b32 v97, v97, v239, vcc\n\t" \
    "v_cmp_gt_i32 vcc, 2, %[dq]\n\t" \
    "s_nop 1\n\t" \
    "v_cndmask_b32 v98, v98, v239, vcc\n\t" \
    "v_cmp_gt_i32 vcc, 3, %[dq]\n\t" \
    "s_nop 1\n\t" \
    "v_cndmask_b32 v99, v99, v239, vcc\n\t" \
    "v_cmp_gt_i32 vcc, 8, %[dq]\n\t" \
    "s_nop 1\n\t" \
    "v_cndmask_b32 v100, v100, v239, vcc\n\t" \
    "v_cmp_gt_i32 vcc, 9, %[dq]\n\t" \
    "s_nop 1\n\t" \
    "v_cndmask_b32 v101, v101, v239, vcc\n\t" \
    "v_cmp_gt_i32 vcc, 10, %[dq]\n\t" \
    "s_nop 1\n\t" \
    "v_cndmask_b32 v102, v102, v239, vcc\n\t" \
    "v_cmp_gt_i32 vcc, 11, %[dq]\n\t" \
    "s_nop 1\n\t" \
    "v_cndmask_b32 v103, v103, v239, vcc\n\t" \
    "v_cmp_gt_i32 vcc, 16, %[dq]\n\t" \
    "s_nop 1\n\t" \
    "v_cndmask_b32 v104, v104, v239, vcc\n\t" \
    "v_cmp_gt_i32 vcc, 17, %[dq]\n\t" \
    "s_nop 1\n\t" \
    "v_cndmask_b32 v105, v105, v239, vcc\n\t" \
    "v_cmp_gt_i32 vcc, 18, %[dq]\n\t" \
    "s_nop 1\n\t" \
    "v_cndmask_b32 v106, v106, v239, vcc\n\t" \
    "v_cmp_gt_i32 vcc, 19, %[dq]\n\t" \
    "s_nop 1\n\t" \
    "v_cndmask_b32 v107, v107, v239, vcc\n\t" \
    "v_cmp_gt_i32 vcc, 24, %[dq]\n\t" \
    "s_nop 1\n\t" \
    "v_cndmask_b32 v108, v108, v239, vcc\n\t" \
    "v_cmp_gt_i32 vcc, 25, %[dq]\n\t" \
    "s_nop 1\n\t" \
    "v_cndmask_b32 v109, v109, v239, vcc\n\t" \
    "v_cmp_gt_i32 vcc, 26, %[dq]\n\t" \
    "s_nop 1\n\t" \
    "v_cndmask_b32 v110, v110, v239, vcc\n\t" \
    "v_cmp_gt_i32 vcc, 27, %[dq]\n\t" \
    "s_nop 1\n\t" \
    "v_cndmask_b32 v111, v111, v239, vcc\n\t" \
    "s_nop 1\n\t" \
    "s_waitcnt lgkmcnt(0)\n\t" \
    "v_mfma_f32_32x32x16_bf16 v[112:127], v[168:171], v[64:67], v[96:111]\n\t" \
    "ds_read_b128 v[200:203], v235 offset:8192\n\t" \
    "ds_read_b128 v[204:207], v235 offset:8704\n\t" \
    "v_add_f32 v237, v144, v145\n\t" \
    "v_cvt_pk_bf16_f32 v160, v144, v145\n\t" \
    "v_mfma_f32_32x32x16_bf16 v[112:127], v[172:175], v[68:71], v[112:127]\n\t" \
    "ds_read_b128 v[208:211], v235 offset:9216\n\t" \
    "ds_read_b128 v[212:215], v235 offset:9728\n\t" \
    "v_add_f32 v237, v237, v146\n\t" \
    "v_add_f32 v237, v237, v147\n\t" \
    "v_cvt_pk_bf16_f32 v161, v146, v147\n\t" \
    "v_mfma_f32_32x32x16_bf16 v[112:127], v[176:179], v[72:75], v[112:127]\n\t" \
    "ds_read_b128 v[216:219], v235 offset:12288\n\t" \
    "ds_read_b128 v[220:223], v235 offset:12800\n\t" \
    "v_add_f32 v237, v237, v148\n\t" \
    "v_add_f32 v237, v237, v149\n\t" \
    "v_cvt_pk_bf16_f32 v162, v148, v149\n\t" \
    "v_mfma_f32_32x32x16_bf16 v[112:127], v[180:183], v[76:79], v[112:127]\n\t" \
    "ds_read_b128 v[224:227], v235 offset:13312\n\t" \
    "ds_read_b128 v[228:231], v235 offset:13824\n\t" \
    "v_add_f32 v237, v237, v150\n\t" \
    "v_add_f32 v237, v237, v151\n\t" \
    "v_cvt_pk_bf16_f32 v163, v150, v151\n\t" \
    "v_mfma_f32_32x32x16_bf16 v[112:127], v[184:187], v[80:83], v[112:127]\n\t" \
    "v_add_f32 v237, v237, v152\n\t" \
    "v_add_f32 v237, v237, v153\n\t" \
    "v_cvt_pk_bf16_f32 v164, v152, v153\n\t" \
    "v_mfma_f32_32x32x16_bf16 v[112:127], v[188:191], v[84:87], v[112:127]\n\t" \
    "v_add_f32 v237, v237, v154\n\t" \
    "v_add_f32 v237, v237, v155\n\t" \
    "v_cvt_pk_bf16_f32 v165, v154, v155\n\t" \
    "v_mfma_f32_32x32x16_bf16 v[112:127], v[192:195], v[88:91], v[112:127]\n\t" \
    "v_add_f32 v237, v237, v156\n\t" \
    "v_add_f32 v237, v237, v157\n\t" \
    "v_cvt_pk_bf16_f32 v166, v156, v157\n\t" \
    "v_mfma_f32_32x32x16_bf16 v[112:127], v[196:199], v[92:95], v[112:127]\n\t" \
    "v_add_f32 v237, v237, v158\n\t" \
    "v_add_f32 v237, v237, v159\n\t" \
    "v_cvt_pk_bf16_f32 v167, v158, v159\n\t" \
    "v_cmp_lt_f32 vcc, %[thr], v237\n\t" \
    "s_cbranch_vccnz RAREF_%=\n\t" \
    "BACKF_%=:\n\t" \
    "s_waitcnt lgkmcnt(0)\n\t" \
    "v_mfma_f32_32x32x16_bf16 v[0:15], v[200:203], v[160:163], v[0:15]\n\t" \
    "ds_read_b128 v[168:171], v232 offset:512\n\t" \
    "ds_read_b128 v[172:175], v232 offset:2560\n\t" \
    "v_add_f32 v236, v236, v237\n\t" \
    "v_mfma_f32_32x32x16_bf16 v[16:31], v[204:207], v[160:163], v[16:31]\n\t" \
    "ds_read_b128 v[176:179], v232 offset:4608\n\t" \
    "ds_read_b128 v[180:183], v232 offset:6656\n\t" \
    "v_exp_f32 v144, v112\n\t" \
    "v_exp_f32 v145, v113\n\t" \
    "v_mfma_f32_32x32x16_bf16 v[32:47], v[208:211], v[160:163], v[32:47]\n\t" \
    "ds_read_b128 v[184:187], v232 offset:8704\n\t" \
    "ds_read_b128 v[188:191], v232 offset:10752\n\t" \
    "v_exp_f32 v146, v114\n\t" \
    "v_exp_f32 v147, v115\n\t" \
    "v_mfma_f32_32x32x16_bf16 v[48:63], v[212:215], v[160:163], v[48:63]\n\t" \
    "ds_read_b128 v[192:195], v232 offset:12800\n\t" \
    "ds_read_b128 v[196:199], v232 offset:14848\n\t" \
    "v_exp_f32 v148, v116\n\t" \
    "v_exp_f32 v149, v117\n\t" \
    "v_mfma_f32_32x32x16_bf16 v[0:15], v[216:219], v[164:167], v[0:15]\n\t" \
    "v_exp_f32 v150, v118\n\t" \
    "v_exp_f32 v151, v119\n\t" \
    "v_exp_f32 v152, v120\n\t" \
    "v_mfma_f32_32x32x16_bf16 v[16:31], v[220:223], v[164:167], v[16:31]\n\t" \
    "v_exp_f32 v153, v121\n\t" \
    "v_exp_f32 v154, v122\n\t" \
    "v_exp_f32 v155, v123\n\t" \
    "v_mfma_f32_32x32x16_bf16 v[32:47], v[224:227], v[164:167], v[32:47]\n\t" \
    "v_exp_f32 v156, v124\n\t" \
    "v_exp_f32 v157, v125\n\t" \
    "v_mfma_f32_32x32x16_bf16 v[48:63], v[228:231], v[164:167], v[48:63]\n\t" \
    "v_exp_f32 v158, v126\n\t" \
    "v_exp_f32 v159, v127\n\t" \
    "s_waitcnt lgkmcnt(0)\n\t" \
    "ds_read_b128 v[200:203], v234 offset:0\n\t" \
    "ds_read_b128 v[204:207], v234 offset:512\n\t" \
    "ds_read_b128 v[208:211], v234 offset:1024\n\t" \
    "ds_read_b128 v[212:215], v234 offset:1536\n\t" \
    "ds_read_b128 v[216:219], v234 offset:4096\n\t" \
    "ds_read_b128 v[220:223], v234 offset:4608\n\t" \
    "ds_read_b128 v[224:227], v234 offset:5120\n\t" \
    "ds_read_b128 v[228:231], v234 offset:5632\n\t" \
    "v_add_f32 v237, v144, v145\n\t" \
    "v_cvt_pk_bf16_f32 v160, v144, v145\n\t" \
    "v_add_f32 v237, v237, v146\n\t" \
    "v_add_f32 v237, v237, v147\n\t" \
    "v_cvt_pk_bf16_f32 v161, v146, v147\n\t" \
    "v_add_f32 v237, v237, v148\n\t" \
    "v_add_f32 v237, v237, v149\n\t" \
    "v_cvt_pk_bf16_f32 v162, v148, v149\n\t" \
    "v_add_f32 v237, v237, v150\n\t" \
    "v_add_f32 v237, v237, v151\n\t" \
    "v_cvt_pk_bf16_f32 v163, v150, v151\n\t" \
    "v_add_f32 v237, v237, v152\n\t" \
    "v_add_f32 v237, v237, v153\n\t" \
    "v_cvt_pk_bf16_f32 v164, v152, v153\n\t" \
    "v_add_f32 v237, v237, v154\n\t" \
    "v_add_f32 v237, v237, v155\n\t" \
    "v_cvt_pk_bf16_f32 v165, v154, v155\n\t" \
    "v_add_f32 v237, v237, v156\n\t" \
    "v_add_f32 v237, v237, v157\n\t" \
    "v_cvt_pk_bf16_f32 v166, v156, v157\n\t" \
    "v_add_f32 v237, v237, v158\n\t" \
    "v_add_f32 v237, v237, v159\n\t" \
    "v_cvt_pk_bf16_f32 v167, v158, v159\n\t" \
    "v_cmp_lt_f32 vcc, %[thr], v237\n\t" \
    "s_cbranch_vccnz RAREG_%=\n\t" \
    "BACKG_%=:\n\t" \
    "s_waitcnt lgkmcnt(0)\n\t" \
    "v_add_f32 v236, v236, v237\n\t" \
    "v_mfma_f32_32x32x16_bf16 v[0:15], v[200:203], v[160:163], v[0:15]\n\t" \
    "v_mfma_f32_32x32x16_bf16 v[16:31], v[204:207], v[160:163], v[16:31]\n\t" \
    "v_mfma_f32_32x32x16_bf16 v[32:47], v[208:211], v[160:163], v[32:47]\n\t" \
    "v_mfma_f32_32x32x16_bf16 v[48:63], v[212:215], v[160:163], v[48:63]\n\t" \
    "v_mfma_f32_32x32x16_bf16 v[0:15], v[216:219], v[164:167], v[0:15]\n\t" \
    "v_mfma_f32_32x32x16_bf16 v[16:31], v[220:223], v[164:167], v[16:31]\n\t" \
    "v_mfma_f32_32x32x16_bf16 v[32:47], v[224:227], v[164:167], v[32:47]\n\t" \
    "v_mfma_f32_32x32x16_bf16 v[48:63], v[228:231], v[164:167], v[48:63]\n\t" \
    "s_branch END_%=\n\t" \
    "PF_%=:\n\t" \
    "s_cmp_eq_u32 %[t], 8\n\t" \
    "s_cbranch_scc0 PF2_%=\n\t" \
    "global_load_dword v250, %[pfo], %[wo]\n\t" \
    "s_branch PFB_%=\n\t" \
    "PF2_%=:\n\t" \
    "global_load_dword v251, %[pfo2], %[w2]\n\t" \
    "s_branch PFB_%=\n\t" \
    "DMAL_%=:\n\t" \
    "s_add_u32 s60, %[t], 3\n\t" \
    "s_min_u32 s60, s60, %[ntm1]\n\t" \
    "s_lshl_b32 s60, s60, 14\n\t" \
    "s_add_u32 s62, %[khl], s60\n\t" \
    "s_addc_u32 s63, %[khh], 0\n\t" \
    "s_add_u32 s61, %[soff], 0xc000\n\t" \
    "s_and_b32 s61, s61, 0xffff\n\t" \
    "s_add_u32 m0, s61, %[kdst]\n\t" \
    "s_nop 0\n\t" \
    "global_load_lds_dwordx4 %[dvo], s[62:63] offset:0\n\t" \
    "global_load_lds_dwordx4 %[dvo], s[62:63] offset:1024\n\t" \
    "global_load_lds_dwordx4 %[dvo], s[62:63] offset:2048\n\t" \
    "global_load_lds_dwordx4 %[dvo], s[62:63] offset:3072\n\t" \
    "s_add_u32 s60, %[t], 2\n\t" \
    "s_min_u32 s60, s60, %[ntm1]\n\t" \
    "s_lshl_b32 s60, s60, 14\n\t" \
    "s_add_u32 s62, %[vhl], s60\n\t" \
    "s_addc_u32 s63, %[vhh], 0\n\t" \
    "s_add_u32 s61, %[soff], 0x8000\n\t" \
    "s_and_b32 s61, s61, 0xffff\n\t" \
    "s_add_u32 m0, s61, %[vdst]\n\t" \
    "s_nop 0\n\t" \
    "global_load_lds_dwordx4 %[dvo], s[62:63] offset:0\n\t" \
    "global_load_lds_dwordx4 %[dvo], s[62:63] offset:1024\n\t" \
    "global_load_lds_dwordx4 %[dvo], s[62:63] offset:2048\n\t" \
    "global_load_lds_dwordx4 %[dvo], s[62:63] offset:3072\n\t" \
    "s_branch DMABL_%=\n\t" \
    "DMAT_%=:\n\t" \
    "s_add_u32 s60, %[t], 3\n\t" \
    "s_min_u32 s60, s60, %[ntm1]\n\t" \
    "s_lshl_b32 s60, s60, 14\n\t" \
    "s_add_u32 s62, %[khl], s60\n\t" \
    "s_addc_u32 s63, %[khh], 0\n\t" \
    "s_add_u32 s61, %[soff], 0xc000\n\t" \
    "s_and_b32 s61, s61, 0xffff\n\t" \
    "s_add_u32 m0, s61, %[kdst]\n\t" \
    "s_nop 0\n\t" \
    "global_load_lds_dwordx4 %[dvo], s[62:63] offset:0\n\t" \
    "global_load_lds_dwordx4 %[dvo], s[62:63] offset:1024\n\t" \
    "global_load_lds_dwordx4 %[dvo], s[62:63] offset:2048\n\t" \
    "global_load_lds_dwordx4 %[dvo], s[62:63] offset:3072\n\t" \
    "s_add_u32 s60, %[t], 2\n\t" \
    "s_min_u32 s60, s60, %[ntm1]\n\t" \
    "s_lshl_b32 s60, s60, 14\n\t" \
    "s_add_u32 s62, %[vhl], s60\n\t" \
    "s_addc_u32 s63, %[vhh], 0\n\t" \
    "s_add_u32 s61, %[soff], 0x8000\n\t" \
    "s_and_b32 s61, s61, 0xffff\n\t" \
    "s_add_u32 m0, s61, %[vdst]\n\t" \
    "s_nop 0\n\t" \
    "global_load_lds_dwordx4 %[dvo], s[62:63] offset:0\n\t" \
    "global_load_lds_dwordx4 %[dvo], s[62:63] offset:1024\n\t" \
    "global_load_lds_dwordx4 %[dvo], s[62:63] offset:2048\n\t" \
    "global_load_lds_dwordx4 %[dvo], s[62:63] offset:3072\n\t" \
    "s_branch DMABT_%=\n\t" \
    "RAREA_%=:\n\t" \
    "s_nop 15\n\t" \
    "v_max3_f32 v239, v128, v129, v130\n\t" \
    "v_max3_f32 v239, v239, v131, v132\n\t" \
    "v_max3_f32 v239, v239, v133, v134\n\t" \
    "v_max3_f32 v239, v239, v135, v136\n\t" \
    "v_max3_f32 v239, v239, v137, v138\n\t" \
    "v_max3_f32 v239, v239, v139, v140\n\t" \
    "v_max3_f32 v239, v239, v141, v142\n\t" \
    "v_max_f32 v239, v239, v143\n\t" \
    "v_max_f32 v239, 0, v239\n\t" \
    "v_mov_b32 v240, v239\n\t" \
    "s_nop 1\n\t" \
    "v_permlane32_swap_b32 v239, v240\n\t" \
    "s_nop 1\n\t" \
    "v_max_f32 v239, v239, v240\n\t" \
    "v_add_f32 v238, v238, v239\n\t" \
    "v_exp_f32 v240, -v239\n\t" \
    "v_sub_f32 v241, 0, v238\n\t" \
    "v_sub_f32 v128, v128, v239\n\t" \
    "v_sub_f32 v112, v112, v239\n\t" \
    "v_mov_b32 v96, v241\n\t" \
    "v_sub_f32 v129, v129, v239\n\t" \
    "v_sub_f32 v113, v113, v239\n\t" \
    "v_mov_b32 v97, v241\n\t" \
    "v_sub_f32 v130, v130, v239\n\t" \
    "v_sub_f32 v114, v114, v239\n\t" \
    "v_mov_b32 v98, v241\n\t" \
    "v_sub_f32 v131, v131, v239\n\t" \
    "v_sub_f32 v115, v115, v239\n\t" \
    "v_mov_b32 v99, v241\n\t" \
    "v_sub_f32 v132, v132, v239\n\t" \
    "v_sub_f32 v116, v116, v239\n\t" \
    "v_mov_b32 v100, v241\n\t" \
    "v_sub_f32 v133, v133, v239\n\t" \
    "v_sub_f32 v117, v117, v239\n\t" \
    "v_mov_b32 v101, v241\n\t" \
    "v_sub_f32 v134, v134, v239\n\t" \
    "v_sub_f32 v118, v118, v239\n\t" \
    "v_mov_b32 v102, v241\n\t" \
    "v_sub_f32 v135, v135, v239\n\t" \
    "v_sub_f32 v119, v119, v239\n\t" \
    "v_mov_b32 v103, v241\n\t" \
    "v_sub_f32 v136, v136, v239\n\t" \
    "v_sub_f32 v120, v120, v239\n\t" \
    "v_mov_b32 v104, v241\n\t" \
    "v_sub_f32 v137, v137, v239\n\t" \
    "v_sub_f32 v121, v121, v239\n\t" \
    "v_mov_b32 v105, v241\n\t" \
    "v_sub_f32 v138, v138, v239\n\t" \
    "v_sub_f32 v122, v122, v239\n\t" \
    "v_mov_b32 v106, v241\n\t" \
    "v_sub_f32 v139, v139, v239\n\t" \
    "v_sub_f32 v123, v123, v239\n\t" \
    "v_mov_b32 v107, v241\n\t" \
    "v_sub_f32 v140, v140, v239\n\t" \
    "v_sub_f32 v124, v124, v239\n\t" \
    "v_mov_b32 v108, v241\n\t" \
    "v_sub_f32 v141, v141, v239\n\t" \
    "v_sub_f32 v125, v125, v239\n\t" \
    "v_mov_b32 v109, v241\n\t" \
    "v_sub_f32 v142, v142, v239\n\t" \
    "v_sub_f32 v126, v126, v239\n\t" \
    "v_mov_b32 v110, v241\n\t" \
    "v_sub_f32 v143, v143, v239\n\t" \
    "v_sub_f32 v127, v127, v239\n\t" \
    "v_mov_b32 v111, v241\n\t" \
    "v_exp_f32 v144, v128\n\t" \
    "v_exp_f32 v145, v129\n\t" \
    "v_exp_f32 v146, v130\n\t" \
    "v_exp_f32 v147, v131\n\t" \
    "v_exp_f32 v148, v132\n\t" \
    "v_exp_f32 v149, v133\n\t" \
    "v_exp_f32 v150, v134\n\t" \
    "v_exp_f32 v151, v135\n\t" \
    "v_exp_f32 v152, v136\n\t" \
    "v_exp_f32 v153, v137\n\t" \
    "v_exp_f32 v154, v138\n\t" \
    "v_exp_f32 v155, v139\n\t" \
    "v_exp_f32 v156, v140\n\t" \
    "v_exp_f32 v157, v141\n\t" \
    "v_exp_f32 v158, v142\n\t" \
    "v_exp_f32 v159, v143\n\t" \
    "v_mul_f32 v236, v236, v240\n\t" \
    "v_mul_f32 v0, v0, v240\n\t" \
    "v_mul_f32 v1, v1, v240\n\t" \
    "v_mul_f32 v2, v2, v240\n\t" \
    "v_mul_f32 v3, v3, v240\n\t" \
    "v_mul_f32 v4, v4, v240\n\t" \
    "v_mul_f32 v5, v5, v240\n\t" \
    "v_mul_f32 v6, v6, v240\n\t" \
    "v_mul_f32 v7, v7, v240\n\t" \
    "v_mul_f32 v8, v8, v240\n\t" \
    "v_mul_f32 v9, v9, v240\n\t" \
    "v_mul_f32 v10, v10, v240\n\t" \
    "v_mul_f32 v11, v11, v240\n\t" \
    "v_mul_f32 v12, v12, v240\n\t" \
    "v_mul_f32 v13, v13, v240\n\t" \
    "v_mul_f32 v14, v14, v240\n\t" \
    "v_mul_f32 v15, v15, v240\n\t" \
    "v_mul_f32 v16, v16, v240\n\t" \
    "v_mul_f32 v17, v17, v240\n\t" \
    "v_mul_f32 v18, v18, v240\n\t" \
    "v_mul_f32 v19, v19, v240\n\t" \
    "v_mul_f32 v20, v20, v240\n\t" \
    "v_mul_f32 v21, v21, v240\n\t" \
    "v_mul_f32 v22, v22, v240\n\t" \
    "v_mul_f32 v23, v23, v240\n\t" \
    "v_mul_f32 v24, v24, v240\n\t" \
    "v_mul_f32 v25, v25, v240\n\t" \
    "v_mul_f32 v26, v26, v240\n\t" \
    "v_mul_f32 v27, v27, v240\n\t" \
    "v_mul_f32 v28, v28, v240\n\t" \
    "v_mul_f32 v29, v29, v240\n\t" \
    "v_mul_f32 v30, v30, v240\n\t" \
    "v_mul_f32 v31, v31, v240\n\t" \
    "v_mul_f32 v32, v32, v240\n\t" \
    "v_mul_f32 v33, v33, v240\n\t" \
    "v_mul_f32 v34, v34, v240\n\t" \
    "v_mul_f32 v35, v35, v240\n\t" \
    "v_mul_f32 v36, v36, v240\n\t" \
    "v_mul_f32 v37, v37, v240\n\t" \
    "v_mul_f32 v38, v38, v240\n\t" \
    "v_mul_f32 v39, v39, v240\n\t" \
    "v_mul_f32 v40, v40, v240\n\t" \
    "v_mul_f32 v41, v41, v240\n\t" \
    "v_mul_f32 v42, v42, v240\n\t" \
    "v_mul_f32 v43, v43, v240\n\t" \
    "v_mul_f32 v44, v44, v240\n\t" \
    "v_mul_f32 v45, v45, v240\n\t" \
    "v_mul_f32 v46, v46, v240\n\t" \
    "v_mul_f32 v47, v47, v240\n\t" \
    "v_mul_f32 v48, v48, v240\n\t" \
    "v_mul_f32 v49, v49, v240\n\t" \
    "v_mul_f32 v50, v50, v240\n\t" \
    "v_mul_f32 v51, v51, v240\n\t" \
    "v_mul_f32 v52, v52, v240\n\t" \
    "v_mul_f32 v53, v53, v240\n\t" \
    "v_mul_f32 v54, v54, v240\n\t" \
    "v_mul_f32 v55, v55, v240\n\t" \
    "v_mul_f32 v56, v56, v240\n\t" \
    "v_mul_f32 v57, v57, v240\n\t" \
    "v_mul_f32 v58, v58, v240\n\t" \
    "v_mul_f32 v59, v59, v240\n\t" \
    "v_mul_f32 v60, v60, v240\n\t" \
    "v_mul_f32 v61, v61, v240\n\t" \
    "v_mul_f32 v62, v62, v240\n\t" \
    "v_mul_f32 v63, v63, v240\n\t" \
    "v_add_f32 v237, v144, v145\n\t" \
    "v_add_f32 v237, v237, v146\n\t" \
    "v_add_f32 v237, v237, v147\n\t" \
    "v_add_f32 v237, v237, v148\n\t" \
    "v_add_f32 v237, v237, v149\n\t" \
    "v_add_f32 v237, v237, v150\n\t" \
    "v_add_f32 v237, v237, v151\n\t" \
    "v_add_f32 v237, v237, v152\n\t" \
    "v_add_f32 v237, v237, v153\n\t" \
    "v_add_f32 v237, v237, v154\n\t" \
    "v_add_f32 v237, v237, v155\n\t" \
    "v_add_f32 v237, v237, v156\n\t" \
    "v_add_f32 v237, v237, v157\n\t" \
    "v_add_f32 v237, v237, v158\n\t" \
    "v_add_f32 v237, v237, v159\n\t" \
    "v_cvt_pk_bf16_f32 v160, v144, v145\n\t" \
    "v_cvt_pk_bf16_f32 v161, v146, v147\n\t" \
    "v_cvt_pk_bf16_f32 v162, v148, v149\n\t" \
    "v_cvt_pk_bf16_f32 v163, v150, v151\n\t" \
    "v_cvt_pk_bf16_f32 v164, v152, v153\n\t" \
    "v_cvt_pk_bf16_f32 v165, v154, v155\n\t" \
    "v_cvt_pk_bf16_f32 v166, v156, v157\n\t" \
    "v_cvt_pk_bf16_f32 v167, v158, v159\n\t" \
    "s_nop 1\n\t" \
    "s_branch BACKA_%=\n\t" \
    "RAREB_%=:\n\t" \
    "s_nop 15\n\t" \
    "v_max3_f32 v239, v112, v113, v114\n\t" \
    "v_max3_f32 v239, v239, v115, v116\n\t" \
    "v_max3_f32 v239, v239, v117, v118\n\t" \
    "v_max3_f32 v239, v239, v119, v120\n\t" \
    "v_max3_f32 v239, v239, v121, v122\n\t" \
    "v_max3_f32 v239, v239, v123, v124\n\t" \
    "v_max3_f32 v239, v239, v125, v126\n\t" \
    "v_max_f32 v239, v239, v127\n\t" \
    "v_max_f32 v239, 0, v239\n\t" \
    "v_mov_b32 v240, v239\n\t" \
    "s_nop 1\n\t" \
    "v_permlane32_swap_b32 v239, v240\n\t" \
    "s_nop 1\n\t" \
    "v_max_f32 v239, v239, v240\n\t" \
    "v_add_f32 v238, v238, v239\n\t" \
    "v_exp_f32 v240, -v239\n\t" \
    "v_sub_f32 v241, 0, v238\n\t" \
    "v_sub_f32 v112, v112, v239\n\t" \
    "v_sub_f32 v128, v128, v239\n\t" \
    "v_mov_b32 v96, v241\n\t" \
    "v_sub_f32 v113, v113, v239\n\t" \
    "v_sub_f32 v129, v129, v239\n\t" \
    "v_mov_b32 v97, v241\n\t" \
    "v_sub_f32 v114, v114, v239\n\t" \
    "v_sub_f32 v130, v130, v239\n\t" \
    "v_mov_b32 v98, v241\n\t" \
    "v_sub_f32 v115, v115, v239\n\t" \
    "v_sub_f32 v131, v131, v239\n\t" \
    "v_mov_b32 v99, v241\n\t" \
    "v_sub_f32 v116, v116, v239\n\t" \
    "v_sub_f32 v132, v132, v239\n\t" \
    "v_mov_b32 v100, v241\n\t" \
    "v_sub_f32 v117, v117, v239\n\t" \
    "v_sub_f32 v133, v133, v239\n\t" \
    "v_mov_b32 v101, v241\n\t" \
    "v_sub_f32 v118, v118, v239\n\t" \
    "v_sub_f32 v134, v134, v239\n\t" \
    "v_mov_b32 v102, v241\n\t" \
    "v_sub_f32 v119, v119, v239\n\t" \
    "v_sub_f32 v135, v135, v239\n\t" \
    "v_mov_b32 v103, v241\n\t" \
    "v_sub_f32 v120, v120, v239\n\t" \
    "v_sub_f32 v136, v136, v239\n\t" \
    "v_mov_b32 v104, v241\n\t" \
    "v_sub_f32 v121, v121, v239\n\t" \
    "v_sub_f32 v137, v137, v239\n\t" \
    "v_mov_b32 v105, v241\n\t" \
    "v_sub_f32 v122, v122, v239\n\t" \
    "v_sub_f32 v138, v138, v239\n\t" \
    "v_mov_b32 v106, v241\n\t" \
    "v_sub_f32 v123, v123, v239\n\t" \
    "v_sub_f32 v139, v139, v239\n\t" \
    "v_mov_b32 v107, v241\n\t" \
    "v_sub_f32 v124, v124, v239\n\t" \
    "v_sub_f32 v140, v140, v239\n\t" \
    "v_mov_b32 v108, v241\n\t" \
    "v_sub_f32 v125, v125, v239\n\t" \
    "v_sub_f32 v141, v141, v239\n\t" \
    "v_mov_b32 v109, v241\n\t" \
    "v_sub_f32 v126, v126, v239\n\t" \
    "v_sub_f32 v142, v142, v239\n\t" \
    "v_mov_b32 v110, v241\n\t" \
    "v_sub_f32 v127, v127, v239\n\t" \
    "v_sub_f32 v143, v143, v239\n\t" \
    "v_mov_b32 v111, v241\n\t" \
    "v_exp_f32 v144, v112\n\t" \
    "v_exp_f32 v145, v113\n\t" \
    "v_exp_f32 v146, v114\n\t" \
    "v_exp_f32 v147, v115\n\t" \
    "v_exp_f32 v148, v116\n\t" \
    "v_exp_f32 v149, v117\n\t" \
    "v_exp_f32 v150, v118\n\t" \
    "v_exp_f32 v151, v119\n\t" \
    "v_exp_f32 v152, v120\n\t" \
    "v_exp_f32 v153, v121\n\t" \
    "v_exp_f32 v154, v122\n\t" \
    "v_exp_f32 v155, v123\n\t" \
    "v_exp_f32 v156, v124\n\t" \
    "v_exp_f32 v157, v125\n\t" \
    "v_exp_f32 v158, v126\n\t" \
    "v_exp_f32 v159, v127\n\t" \
    "v_mul_f32 v236, v236, v240\n\t" \
    "v_mul_f32 v0, v0, v240\n\t" \
    "v_mul_f32 v1, v1, v240\n\t" \
    "v_mul_f32 v2, v2, v240\n\t" \
    "v_mul_f32 v3, v3, v240\n\t" \
    "v_mul_f32 v4, v4, v240\n\t" \
    "v_mul_f32 v5, v5, v240\n\t" \
    "v_mul_f32 v6, v6, v240\n\t" \
    "v_mul_f32 v7, v7, v240\n\t" \
    "v_mul_f32 v8, v8, v240\n\t" \
    "v_mul_f32 v9, v9, v240\n\t" \
    "v_mul_f32 v10, v10, v240\n\t" \
    "v_mul_f32 v11, v11, v240\n\t" \
    "v_mul_f32 v12, v12, v240\n\t" \
    "v_mul_f32 v13, v13, v240\n\t" \
    "v_mul_f32 v14, v14, v240\n\t" \
    "v_mul_f32 v15, v15, v240\n\t" \
    "v_mul_f32 v16, v16, v240\n\t" \
    "v_mul_f32 v17, v17, v240\n\t" \
    "v_mul_f32 v18, v18, v240\n\t" \
    "v_mul_f32 v19, v19, v240\n\t" \
    "v_mul_f32 v20, v20, v240\n\t" \
    "v_mul_f32 v21, v21, v240\n\t" \
    "v_mul_f32 v22, v22, v240\n\t" \
    "v_mul_f32 v23, v23, v240\n\t" \
    "v_mul_f32 v24, v24, v240\n\t" \
    "v_mul_f32 v25, v25, v240\n\t" \
    "v_mul_f32 v26, v26, v240\n\t" \
    "v_mul_f32 v27, v27, v240\n\t" \
    "v_mul_f32 v28, v28, v240\n\t" \
    "v_mul_f32 v29, v29, v240\n\t" \
    "v_mul_f32 v30, v30, v240\n\t" \
    "v_mul_f32 v31, v31, v240\n\t" \
    "v_mul_f32 v32, v32, v240\n\t" \
    "v_mul_f32 v33, v33, v240\n\t" \
    "v_mul_f32 v34, v34, v240\n\t" \
    "v_mul_f32 v35, v35, v240\n\t" \
    "v_mul_f32 v36, v36, v240\n\t" \
    "v_mul_f32 v37, v37, v240\n\t" \
    "v_mul_f32 v38, v38, v240\n\t" \
    "v_mul_f32 v39, v39, v240\n\t" \
    "v_mul_f32 v40, v40, v240\n\t" \
    "v_mul_f32 v41, v41, v240\n\t" \
    "v_mul_f32 v42, v42, v240\n\t" \
    "v_mul_f32 v43, v43, v240\n\t" \
    "v_mul_f32 v44, v44, v240\n\t" \
    "v_mul_f32 v45, v45, v240\n\t" \
    "v_mul_f32 v46, v46, v240\n\t" \
    "v_mul_f32 v47, v47, v240\n\t" \
    "v_mul_f32 v48, v48, v240\n\t" \
    "v_mul_f32 v49, v49, v240\n\t" \
    "v_mul_f32 v50, v50, v240\n\t" \
    "v_mul_f32 v51, v51, v240\n\t" \
    "v_mul_f32 v52, v52, v240\n\t" \
    "v_mul_f32 v53, v53, v240\n\t" \
    "v_mul_f32 v54, v54, v240\n\t" \
    "v_mul_f32 v55, v55, v240\n\t" \
    "v_mul_f32 v56, v56, v240\n\t" \
    "v_mul_f32 v57, v57, v240\n\t" \
    "v_mul_f32 v58, v58, v240\n\t" \
    "v_mul_f32 v59, v59, v240\n\t" \
    "v_mul_f32 v60, v60, v240\n\t" \
    "v_mul_f32 v61, v61, v240\n\t" \
    "v_mul_f32 v62, v62, v240\n\t" \
    "v_mul_f32 v63, v63, v240\n\t" \
    "v_add_f32 v237, v144, v145\n\t" \
    "v_add_f32 v237, v237, v146\n\t" \
    "v_add_f32 v237, v237, v147\n\t" \
    "v_add_f32 v237, v237, v148\n\t" \
    "v_add_f32 v237, v237, v149\n\t" \
    "v_add_f32 v237, v237, v150\n\t" \
    "v_add_f32 v237, v237, v151\n\t" \
    "v_add_f32 v237, v237, v152\n\t" \
    "v_add_f32 v237, v237, v153\n\t" \
    "v_add_f32 v237, v237, v154\n\t" \
    "v_add_f32 v237, v237, v155\n\t" \
    "v_add_f32 v237, v237, v156\n\t" \
    "v_add_f32 v237, v237, v157\n\t" \
    "v_add_f32 v237, v237, v158\n\t" \
    "v_add_f32 v237, v237, v159\n\t" \
    "v_cvt_pk_bf16_f32 v160, v144, v145\n\t" \
    "v_cvt_pk_bf16_f32 v161, v146, v147\n\t" \
    "v_cvt_pk_bf16_f32 v162, v148, v149\n\t" \
    "v_cvt_pk_bf16_f32 v163, v150, v151\n\t" \
    "v_cvt_pk_bf16_f32 v164, v152, v153\n\t" \
    "v_cvt_pk_bf16_f32 v165, v154, v155\n\t" \
    "v_cvt_pk_bf16_f32 v166, v156, v157\n\t" \
    "v_cvt_pk_bf16_f32 v167, v158, v159\n\t" \
    "s_nop 1\n\t" \
    "s_branch BACKB_%=\n\t" \
    "RAREC_%=:\n\t" \
    "s_nop 15\n\t" \
    "v_max3_f32 v239, v128, v129, v130\n\t" \
    "v_max3_f32 v239, v239, v131, v132\n\t" \
    "v_max3_f32 v239, v239, v133, v134\n\t" \
    "v_max3_f32 v239, v239, v135, v136\n\t" \
    "v_max3_f32 v239, v239, v137, v138\n\t" \
    "v_max3_f32 v239, v239, v139, v140\n\t" \
    "v_max3_f32 v239, v239, v141, v142\n\t" \
    "v_max_f32 v239, v239, v143\n\t" \
    "v_max_f32 v239, 0, v239\n\t" \
    "v_mov_b32 v240, v239\n\t" \
    "s_nop 1\n\t" \
    "v_permlane32_swap_b32 v239, v240\n\t" \
    "s_nop 1\n\t" \
    "v_max_f32 v239, v239, v240\n\t" \
    "v_add_f32 v238, v238, v239\n\t" \
    "v_exp_f32 v240, -v239\n\t" \
    "v_sub_f32 v241, 0, v238\n\t" \
    "v_sub_f32 v128, v128, v239\n\t" \
    "v_sub_f32 v112, v112, v239\n\t" \
    "v_mov_b32 v96, v241\n\t" \
    "v_sub_f32 v129, v129, v239\n\t" \
    "v_sub_f32 v113, v113, v239\n\t" \
    "v_mov_b32 v97, v241\n\t" \
    "v_sub_f32 v130, v130, v239\n\t" \
    "v_sub_f32 v114, v114, v239\n\t" \
    "v_mov_b32 v98, v241\n\t" \
    "v_sub_f32 v131, v131, v239\n\t" \
    "v_sub_f32 v115, v115, v239\n\t" \
    "v_mov_b32 v99, v241\n\t" \
    "v_sub_f32 v132, v132, v239\n\t" \
    "v_sub_f32 v116, v116, v239\n\t" \
    "v_mov_b32 v100, v241\n\t" \
    "v_sub_f32 v133, v133, v239\n\t" \
    "v_sub_f32 v117, v117, v239\n\t" \
    "v_mov_b32 v101, v241\n\t" \
    "v_sub_f32 v134, v134, v239\n\t" \
    "v_sub_f32 v118, v118, v239\n\t" \
    "v_mov_b32 v102, v241\n\t" \
    "v_sub_f32 v135, v135, v239\n\t" \
    "v_sub_f32 v119, v119, v239\n\t" \
    "v_mov_b32 v103, v241\n\t" \
    "v_sub_f32 v136, v136, v239\n\t" \
    "v_sub_f32 v120, v120, v239\n\t" \
    "v_mov_b32 v104, v241\n\t" \
    "v_sub_f32 v137, v137, v239\n\t" \
    "v_sub_f32 v121, v121, v239\n\t" \
    "v_mov_b32 v105, v241\n\t" \
    "v_sub_f32 v138, v138, v239\n\t" \
    "v_sub_f32 v122, v122, v239\n\t" \
    "v_mov_b32 v106, v241\n\t" \
    "v_sub_f32 v139, v139, v239\n\t" \
    "v_sub_f32 v123, v123, v239\n\t" \
    "v_mov_b32 v107, v241\n\t" \
    "v_sub_f32 v140, v140, v239\n\t" \
    "v_sub_f32 v124, v124, v239\n\t" \
    "v_mov_b32 v108, v241\n\t" \
    "v_sub_f32 v141, v141, v239\n\t" \
    "v_sub_f32 v125, v125, v239\n\t" \
    "v_mov_b32 v109, v241\n\t" \
    "v_sub_f32 v142, v142, v239\n\t" \
    "v_sub_f32 v126, v126, v239\n\t" \
    "v_mov_b32 v110, v241\n\t" \
    "v_sub_f32 v143, v143, v239\n\t" \
    "v_sub_f32 v127, v127, v239\n\t" \
    "v_mov_b32 v111, v241\n\t" \
    "v_exp_f32 v144, v128\n\t" \
    "v_exp_f32 v145, v129\n\t" \
    "v_exp_f32 v146, v130\n\t" \
    "v_exp_f32 v147, v131\n\t" \
    "v_exp_f32 v148, v132\n\t" \
    "v_exp_f32 v149, v133\n\t" \
    "v_exp_f32 v150, v134\n\t" \
    "v_exp_f32 v151, v135\n\t" \
    "v_exp_f32 v152, v136\n\t" \
    "v_exp_f32 v153, v137\n\t" \
    "v_exp_f32 v154, v138\n\t" \
    "v_exp_f32 v155, v139\n\t" \
    "v_exp_f32 v156, v140\n\t" \
    "v_exp_f32 v157, v141\n\t" \
    "v_exp_f32 v158, v142\n\t" \
    "v_exp_f32 v159, v143\n\t" \
    "v_mul_f32 v236, v236, v240\n\t" \
    "v_mul_f32 v0, v0, v240\n\t" \
    "v_mul_f32 v1, v1, v240\n\t" \
    "v_mul_f32 v2, v2, v240\n\t" \
    "v_mul_f32 v3, v3, v240\n\t" \
    "v_mul_f32 v4, v4, v240\n\t" \
    "v_mul_f32 v5, v5, v240\n\t" \
    "v_mul_f32 v6, v6, v240\n\t" \
    "v_mul_f32 v7, v7, v240\n\t" \
    "v_mul_f32 v8, v8, v240\n\t" \
    "v_mul_f32 v9, v9, v240\n\t" \
    "v_mul_f32 v10, v10, v240\n\t" \
    "v_mul_f32 v11, v11, v240\n\t" \
    "v_mul_f32 v12, v12, v240\n\t" \
    "v_mul_f32 v13, v13, v240\n\t" \
    "v_mul_f32 v14, v14, v240\n\t" \
    "v_mul_f32 v15, v15, v240\n\t" \
    "v_mul_f32 v16, v16, v240\n\t" \
    "v_mul_f32 v17, v17, v240\n\t" \
    "v_mul_f32 v18, v18, v240\n\t" \
    "v_mul_f32 v19, v19, v240\n\t" \
    "v_mul_f32 v20, v20, v240\n\t" \
    "v_mul_f32 v21, v21, v240\n\t" \
    "v_mul_f32 v22, v22, v240\n\t" \
    "v_mul_f32 v23, v23, v240\n\t" \
    "v_mul_f32 v24, v24, v240\n\t" \
    "v_mul_f32 v25, v25, v240\n\t" \
    "v_mul_f32 v26, v26, v240\n\t" \
    "v_mul_f32 v27, v27, v240\n\t" \
    "v_mul_f32 v28, v28, v240\n\t" \
    "v_mul_f32 v29, v29, v240\n\t" \
    "v_mul_f32 v30, v30, v240\n\t" \
    "v_mul_f32 v31, v31, v240\n\t" \
    "v_mul_f32 v32, v32, v240\n\t" \
    "v_mul_f32 v33, v33, v240\n\t" \
    "v_mul_f32 v34, v34, v240\n\t" \
    "v_mul_f32 v35, v35, v240\n\t" \
    "v_mul_f32 v36, v36, v240\n\t" \
    "v_mul_f32 v37, v37, v240\n\t" \
    "v_mul_f32 v38, v38, v240\n\t" \
    "v_mul_f32 v39, v39, v240\n\t" \
    "v_mul_f32 v40, v40, v240\n\t" \
    "v_mul_f32 v41, v41, v240\n\t" \
    "v_mul_f32 v42, v42, v240\n\t" \
    "v_mul_f32 v43, v43, v240\n\t" \
    "v_mul_f32 v44, v44, v240\n\t" \
    "v_mul_f32 v45, v45, v240\n\t" \
    "v_mul_f32 v46, v46, v240\n\t" \
    "v_mul_f32 v47, v47, v240\n\t" \
    "v_mul_f32 v48, v48, v240\n\t" \
    "v_mul_f32 v49, v49, v240\n\t" \
    "v_mul_f32 v50, v50, v240\n\t" \
    "v_mul_f32 v51, v51, v240\n\t" \
    "v_mul_f32 v52, v52, v240\n\t" \
    "v_mul_f32 v53, v53, v240\n\t" \
    "v_mul_f32 v54, v54, v240\n\t" \
    "v_mul_f32 v55, v55, v240\n\t" \
    "v_mul_f32 v56, v56, v240\n\t" \
    "v_mul_f32 v57, v57, v240\n\t" \
    "v_mul_f32 v58, v58, v240\n\t" \
    "v_mul_f32 v59, v59, v240\n\t" \
    "v_mul_f32 v60, v60, v240\n\t" \
    "v_mul_f32 v61, v61, v240\n\t" \
    "v_mul_f32 v62, v62, v240\n\t" \
    "v_mul_f32 v63, v63, v240\n\t" \
    "v_add_f32 v237, v144, v145\n\t" \
    "v_add_f32 v237, v237, v146\n\t" \
    "v_add_f32 v237, v237, v147\n\t" \
    "v_add_f32 v237, v237, v148\n\t" \
    "v_add_f32 v237, v237, v149\n\t" \
    "v_add_f32 v237, v237, v150\n\t" \
    "v_add_f32 v237, v237, v151\n\t" \
    "v_add_f32 v237, v237, v152\n\t" \
    "v_add_f32 v237, v237, v153\n\t" \
    "v_add_f32 v237, v237, v154\n\t" \
    "v_add_f32 v237, v237, v155\n\t" \
    "v_add_f32 v237, v237, v156\n\t" \
    "v_add_f32 v237, v237, v157\n\t" \
    "v_add_f32 v237, v237, v158\n\t" \
    "v_add_f32 v237, v237, v159\n\t" \
    "v_cvt_pk_bf16_f32 v160, v144, v145\n\t" \
    "v_cvt_pk_bf16_f32 v161, v146, v147\n\t" \
    "v_cvt_pk_bf16_f32 v162, v148, v149\n\t" \
    "v_cvt_pk_bf16_f32 v163, v150, v151\n\t" \
    "v_cvt_pk_bf16_f32 v164, v152, v153\n\t" \
    "v_cvt_pk_bf16_f32 v165, v154, v155\n\t" \
    "v_cvt_pk_bf16_f32 v166, v156, v157\n\t" \
    "v_cvt_pk_bf16_f32 v167, v158, v159\n\t" \
    "s_nop 1\n\t" \
    "s_branch BACKC_%=\n\t" \
    "RARED_%=:\n\t" \
    "s_nop 15\n\t" \
    "v_max3_f32 v239, v112, v113, v114\n\t" \
    "v_max3_f32 v239, v239, v115, v116\n\t" \
    "v_max3_f32 v239, v239, v117, v118\n\t" \
    "v_max3_f32 v239, v239, v119, v120\n\t" \
    "v_max3_f32 v239, v239, v121, v122\n\t" \
    "v_max3_f32 v239, v239, v123, v124\n\t" \
    "v_max3_f32 v239, v239, v125, v126\n\t" \
    "v_max_f32 v239, v239, v127\n\t" \
    "v_max_f32 v239, 0, v239\n\t" \
    "v_mov_b32 v240, v239\n\t" \
    "s_nop 1\n\t" \
    "v_permlane32_swap_b32 v239, v240\n\t" \
    "s_nop 1\n\t" \
    "v_max_f32 v239, v239, v240\n\t" \
    "v_add_f32 v238, v238, v239\n\t" \
    "v_exp_f32 v240, -v239\n\t" \
    "v_sub_f32 v241, 0, v238\n\t" \
    "v_sub_f32 v112, v112, v239\n\t" \
    "v_sub_f32 v128, v128, v239\n\t" \
    "v_mov_b32 v96, v241\n\t" \
    "v_sub_f32 v113, v113, v239\n\t" \
    "v_sub_f32 v129, v129, v239\n\t" \
    "v_mov_b32 v97, v241\n\t" \
    "v_sub_f32 v114, v114, v239\n\t" \
    "v_sub_f32 v130, v130, v239\n\t" \
    "v_mov_b32 v98, v241\n\t" \
    "v_sub_f32 v115, v115, v239\n\t" \
    "v_sub_f32 v131, v131, v239\n\t" \
    "v_mov_b32 v99, v241\n\t" \
    "v_sub_f32 v116, v116, v239\n\t" \
    "v_sub_f32 v132, v132, v239\n\t" \
    "v_mov_b32 v100, v241\n\t" \
    "v_sub_f32 v117, v117, v239\n\t" \
    "v_sub_f32 v133, v133, v239\n\t" \
    "v_mov_b32 v101, v241\n\t" \
    "v_sub_f32 v118, v118, v239\n\t" \
    "v_sub_f32 v134, v134, v239\n\t" \
    "v_mov_b32 v102, v241\n\t" \
    "v_sub_f32 v119, v119, v239\n\t" \
    "v_sub_f32 v135, v135, v239\n\t" \
    "v_mov_b32 v103, v241\n\t" \
    "v_sub_f32 v120, v120, v239\n\t" \
    "v_sub_f32 v136, v136, v239\n\t" \
    "v_mov_b32 v104, v241\n\t" \
    "v_sub_f32 v121, v121, v239\n\t" \
    "v_sub_f32 v137, v137, v239\n\t" \
    "v_mov_b32 v105, v241\n\t" \
    "v_sub_f32 v122, v122, v239\n\t" \
    "v_sub_f32 v138, v138, v239\n\t" \
    "v_mov_b32 v106, v241\n\t" \
    "v_sub_f32 v123, v123, v239\n\t" \
    "v_sub_f32 v139, v139, v239\n\t" \
    "v_mov_b32 v107, v241\n\t" \
    "v_sub_f32 v124, v124, v239\n\t" \
    "v_sub_f32 v140, v140, v239\n\t" \
    "v_mov_b32 v108, v241\n\t" \
    "v_sub_f32 v125, v125, v239\n\t" \
    "v_sub_f32 v141, v141, v239\n\t" \
    "v_mov_b32 v109, v241\n\t" \
    "v_sub_f32 v126, v126, v239\n\t" \
    "v_sub_f32 v142, v142, v239\n\t" \
    "v_mov_b32 v110, v241\n\t" \
    "v_sub_f32 v127, v127, v239\n\t" \
    "v_sub_f32 v143, v143, v239\n\t" \
    "v_mov_b32 v111, v241\n\t" \
    "v_exp_f32 v144, v112\n\t" \
    "v_exp_f32 v145, v113\n\t" \
    "v_exp_f32 v146, v114\n\t" \
    "v_exp_f32 v147, v115\n\t" \
    "v_exp_f32 v148, v116\n\t" \
    "v_exp_f32 v149, v117\n\t" \
    "v_exp_f32 v150, v118\n\t" \
    "v_exp_f32 v151, v119\n\t" \
    "v_exp_f32 v152, v120\n\t" \
    "v_exp_f32 v153, v121\n\t" \
    "v_exp_f32 v154, v122\n\t" \
    "v_exp_f32 v155, v123\n\t" \
    "v_exp_f32 v156, v124\n\t" \
    "v_exp_f32 v157, v125\n\t" \
    "v_exp_f32 v158, v126\n\t" \
    "v_exp_f32 v159, v127\n\t" \
    "v_mul_f32 v236, v236, v240\n\t" \
    "v_mul_f32 v0, v0, v240\n\t" \
    "v_mul_f32 v1, v1, v240\n\t" \
    "v_mul_f32 v2, v2, v240\n\t" \
    "v_mul_f32 v3, v3, v240\n\t" \
    "v_mul_f32 v4, v4, v240\n\t" \
    "v_mul_f32 v5, v5, v240\n\t" \
    "v_mul_f32 v6, v6, v240\n\t" \
    "v_mul_f32 v7, v7, v240\n\t" \
    "v_mul_f32 v8, v8, v240\n\t" \
    "v_mul_f32 v9, v9, v240\n\t" \
    "v_mul_f32 v10, v10, v240\n\t" \
    "v_mul_f32 v11, v11, v240\n\t" \
    "v_mul_f32 v12, v12, v240\n\t" \
    "v_mul_f32 v13, v13, v240\n\t" \
    "v_mul_f32 v14, v14, v240\n\t" \
    "v_mul_f32 v15, v15, v240\n\t" \
    "v_mul_f32 v16, v16, v240\n\t" \
    "v_mul_f32 v17, v17, v240\n\t" \
    "v_mul_f32 v18, v18, v240\n\t" \
    "v_mul_f32 v19, v19, v240\n\t" \
    "v_mul_f32 v20, v20, v240\n\t" \
    "v_mul_f32 v21, v21, v240\n\t" \
    "v_mul_f32 v22, v22, v240\n\t" \
    "v_mul_f32 v23, v23, v240\n\t" \
    "v_mul_f32 v24, v24, v240\n\t" \
    "v_mul_f32 v25, v25, v240\n\t" \
    "v_mul_f32 v26, v26, v240\n\t" \
    "v_mul_f32 v27, v27, v240\n\t" \
    "v_mul_f32 v28, v28, v240\n\t" \
    "v_mul_f32 v29, v29, v240\n\t" \
    "v_mul_f32 v30, v30, v240\n\t" \
    "v_mul_f32 v31, v31, v240\n\t" \
    "v_mul_f32 v32, v32, v240\n\t" \
    "v_mul_f32 v33, v33, v240\n\t" \
    "v_mul_f32 v34, v34, v240\n\t" \
    "v_mul_f32 v35, v35, v240\n\t" \
    "v_mul_f32 v36, v36, v240\n\t" \
    "v_mul_f32 v37, v37, v240\n\t" \
    "v_mul_f32 v38, v38, v240\n\t" \
    "v_mul_f32 v39, v39, v240\n\t" \
    "v_mul_f32 v40, v40, v240\n\t" \
    "v_mul_f32 v41, v41, v240\n\t" \
    "v_mul_f32 v42, v42, v240\n\t" \
    "v_mul_f32 v43, v43, v240\n\t" \
    "v_mul_f32 v44, v44, v240\n\t" \
    "v_mul_f32 v45, v45, v240\n\t" \
    "v_mul_f32 v46, v46, v240\n\t" \
    "v_mul_f32 v47, v47, v240\n\t" \
    "v_mul_f32 v48, v48, v240\n\t" \
    "v_mul_f32 v49, v49, v240\n\t" \
    "v_mul_f32 v50, v50, v240\n\t" \
    "v_mul_f32 v51, v51, v240\n\t" \
    "v_mul_f32 v52, v52, v240\n\t" \
    "v_mul_f32 v53, v53, v240\n\t" \
    "v_mul_f32 v54, v54, v240\n\t" \
    "v_mul_f32 v55, v55, v240\n\t" \
    "v_mul_f32 v56, v56, v240\n\t" \
    "v_mul_f32 v57, v57, v240\n\t" \
    "v_mul_f32 v58, v58, v240\n\t" \
    "v_mul_f32 v59, v59, v240\n\t" \
    "v_mul_f32 v60, v60, v240\n\t" \
    "v_mul_f32 v61, v61, v240\n\t" \
    "v_mul_f32 v62, v62, v240\n\t" \
    "v_mul_f32 v63, v63, v240\n\t" \
    "v_add_f32 v237, v144, v145\n\t" \
    "v_add_f32 v237, v237, v146\n\t" \
    "v_add_f32 v237, v237, v147\n\t" \
    "v_add_f32 v237, v237, v148\n\t" \
    "v_add_f32 v237, v237, v149\n\t" \
    "v_add_f32 v237, v237, v150\n\t" \
    "v_add_f32 v237, v237, v151\n\t" \
    "v_add_f32 v237, v237, v152\n\t" \
    "v_add_f32 v237, v237, v153\n\t" \
    "v_add_f32 v237, v237, v154\n\t" \
    "v_add_f32 v237, v237, v155\n\t" \
    "v_add_f32 v237, v237, v156\n\t" \
    "v_add_f32 v237, v237, v157\n\t" \
    "v_add_f32 v237, v237, v158\n\t" \
    "v_add_f32 v237, v237, v159\n\t" \
    "v_cvt_pk_bf16_f32 v160, v144, v145\n\t" \
    "v_cvt_pk_bf16_f32 v161, v146, v147\n\t" \
    "v_cvt_pk_bf16_f32 v162, v148, v149\n\t" \
    "v_cvt_pk_bf16_f32 v163, v150, v151\n\t" \
    "v_cvt_pk_bf16_f32 v164, v152, v153\n\t" \
    "v_cvt_pk_bf16_f32 v165, v154, v155\n\t" \
    "v_cvt_pk_bf16_f32 v166, v156, v157\n\t" \
    "v_cvt_pk_bf16_f32 v167, v158, v159\n\t" \
    "s_nop 1\n\t" \
    "s_branch BACKD_%=\n\t" \
    "RAREE_%=:\n\t" \
    "s_nop 15\n\t" \
    "v_max3_f32 v239, v128, v129, v130\n\t" \
    "v_max3_f32 v239, v239, v131, v132\n\t" \
    "v_max3_f32 v239, v239, v133, v134\n\t" \
    "v_max3_f32 v239, v239, v135, v136\n\t" \
    "v_max3_f32 v239, v239, v137, v138\n\t" \
    "v_max3_f32 v239, v239, v139, v140\n\t" \
    "v_max3_f32 v239, v239, v141, v142\n\t" \
    "v_max_f32 v239, v239, v143\n\t" \
    "v_max_f32 v239, 0, v239\n\t" \
    "v_mov_b32 v240, v239\n\t" \
    "s_nop 1\n\t" \
    "v_permlane32_swap_b32 v239, v240\n\t" \
    "s_nop 1\n\t" \
    "v_max_f32 v239, v239, v240\n\t" \
    "v_add_f32 v238, v238, v239\n\t" \
    "v_exp_f32 v240, -v239\n\t" \
    "v_sub_f32 v241, 0, v238\n\t" \
    "v_sub_f32 v128, v128, v239\n\t" \
    "v_mov_b32 v96, v241\n\t" \
    "v_sub_f32 v129, v129, v239\n\t" \
    "v_mov_b32 v97, v241\n\t" \
    "v_sub_f32 v130, v130, v239\n\t" \
    "v_mov_b32 v98, v241\n\t" \
    "v_sub_f32 v131, v131, v239\n\t" \
    "v_mov_b32 v99, v241\n\t" \
    "v_sub_f32 v132, v132, v239\n\t" \
    "v_mov_b32 v100, v241\n\t" \
    "v_sub_f32 v133, v133, v239\n\t" \
    "v_mov_b32 v101, v241\n\t" \
    "v_sub_f32 v134, v134, v239\n\t" \
    "v_mov_b32 v102, v241\n\t" \
    "v_sub_f32 v135, v135, v239\n\t" \
    "v_mov_b32 v103, v241\n\t" \
    "v_sub_f32 v136, v136, v239\n\t" \
    "v_mov_b32 v104, v241\n\t" \
    "v_sub_f32 v137, v137, v239\n\t" \
    "v_mov_b32 v105, v241\n\t" \
    "v_sub_f32 v138, v138, v239\n\t" \
    "v_mov_b32 v106, v241\n\t" \
    "v_sub_f32 v139, v139, v239\n\t" \
    "v_mov_b32 v107, v241\n\t" \
    "v_sub_f32 v140, v140, v239\n\t" \
    "v_mov_b32 v108, v241\n\t" \
    "v_sub_f32 v141, v141, v239\n\t" \
    "v_mov_b32 v109, v241\n\t" \
    "v_sub_f32 v142, v142, v239\n\t" \
    "v_mov_b32 v110, v241\n\t" \
    "v_sub_f32 v143, v143, v239\n\t" \
    "v_mov_b32 v111, v241\n\t" \
    "v_exp_f32 v144, v128\n\t" \
    "v_exp_f32 v145, v129\n\t" \
    "v_exp_f32 v146, v130\n\t" \
    "v_exp_f32 v147, v131\n\t" \
    "v_exp_f32 v148, v132\n\t" \
    "v_exp_f32 v149, v133\n\t" \
    "v_exp_f32 v150, v134\n\t" \
    "v_exp_f32 v151, v135\n\t" \
    "v_exp_f32 v152, v136\n\t" \
    "v_exp_f32 v153, v137\n\t" \
    "v_exp_f32 v154, v138\n\t" \
    "v_exp_f32 v155, v139\n\t" \
    "v_exp_f32 v156, v140\n\t" \
    "v_exp_f32 v157, v141\n\t" \
    "v_exp_f32 v158, v142\n\t" \
    "v_exp_f32 v159, v143\n\t" \
    "v_mul_f32 v236, v236, v240\n\t" \
    "v_mul_f32 v0, v0, v240\n\t" \
    "v_mul_f32 v1, v1, v240\n\t" \
    "v_mul_f32 v2, v2, v240\n\t" \
    "v_mul_f32 v3, v3, v240\n\t" \
    "v_mul_f32 v4, v4, v240\n\t" \
    "v_mul_f32 v5, v5, v240\n\t" \
    "v_mul_f32 v6, v6, v240\n\t" \
    "v_mul_f32 v7, v7, v240\n\t" \
    "v_mul_f32 v8, v8, v240\n\t" \
    "v_mul_f32 v9, v9, v240\n\t" \
    "v_mul_f32 v10, v10, v240\n\t" \
    "v_mul_f32 v11, v11, v240\n\t" \
    "v_mul_f32 v12, v12, v240\n\t" \
    "v_mul_f32 v13, v13, v240\n\t" \
    "v_mul_f32 v14, v14, v240\n\t" \
    "v_mul_f32 v15, v15, v240\n\t" \
    "v_mul_f32 v16, v16, v240\n\t" \
    "v_mul_f32 v17, v17, v240\n\t" \
    "v_mul_f32 v18, v18, v240\n\t" \
    "v_mul_f32 v19, v19, v240\n\t" \
    "v_mul_f32 v20, v20, v240\n\t" \
    "v_mul_f32 v21, v21, v240\n\t" \
    "v_mul_f32 v22, v22, v240\n\t" \
    "v_mul_f32 v23, v23, v240\n\t" \
    "v_mul_f32 v24, v24, v240\n\t" \
    "v_mul_f32 v25, v25, v240\n\t" \
    "v_mul_f32 v26, v26, v240\n\t" \
    "v_mul_f32 v27, v27, v240\n\t" \
    "v_mul_f32 v28, v28, v240\n\t" \
    "v_mul_f32 v29, v29, v240\n\t" \
    "v_mul_f32 v30, v30, v240\n\t" \
    "v_mul_f32 v31, v31, v240\n\t" \
    "v_mul_f32 v32, v32, v240\n\t" \
    "v_mul_f32 v33, v33, v240\n\t" \
    "v_mul_f32 v34, v34, v240\n\t" \
    "v_mul_f32 v35, v35, v240\n\t" \
    "v_mul_f32 v36, v36, v240\n\t" \
    "v_mul_f32 v37, v37, v240\n\t" \
    "v_mul_f32 v38, v38, v240\n\t" \
    "v_mul_f32 v39, v39, v240\n\t" \
    "v_mul_f32 v40, v40, v240\n\t" \
    "v_mul_f32 v41, v41, v240\n\t" \
    "v_mul_f32 v42, v42, v240\n\t" \
    "v_mul_f32 v43, v43, v240\n\t" \
    "v_mul_f32 v44, v44, v240\n\t" \
    "v_mul_f32 v45, v45, v240\n\t" \
    "v_mul_f32 v46, v46, v240\n\t" \
    "v_mul_f32 v47, v47, v240\n\t" \
    "v_mul_f32 v48, v48, v240\n\t" \
    "v_mul_f32 v49, v49, v240\n\t" \
    "v_mul_f32 v50, v50, v240\n\t" \
    "v_mul_f32 v51, v51, v240\n\t" \
    "v_mul_f32 v52, v52, v240\n\t" \
    "v_mul_f32 v53, v53, v240\n\t" \
    "v_mul_f32 v54, v54, v240\n\t" \
    "v_mul_f32 v55, v55, v240\n\t" \
    "v_mul_f32 v56, v56, v240\n\t" \
    "v_mul_f32 v57, v57, v240\n\t" \
    "v_mul_f32 v58, v58, v240\n\t" \
    "v_mul_f32 v59, v59, v240\n\t" \
    "v_mul_f32 v60, v60, v240\n\t" \
    "v_mul_f32 v61, v61, v240\n\t" \
    "v_mul_f32 v62, v62, v240\n\t" \
    "v_mul_f32 v63, v63, v240\n\t" \
    "v_add_f32 v237, v144, v145\n\t" \
    "v_add_f32 v237, v237, v146\n\t" \
    "v_add_f32 v237, v237, v147\n\t" \
    "v_add_f32 v237, v237, v148\n\t" \
    "v_add_f32 v237, v237, v149\n\t" \
    "v_add_f32 v237, v237, v150\n\t" \
    "v_add_f32 v237, v237, v151\n\t" \
    "v_add_f32 v237, v237, v152\n\t" \
    "v_add_f32 v237, v237, v153\n\t" \
    "v_add_f32 v237, v237, v154\n\t" \
    "v_add_f32 v237, v237, v155\n\t" \
    "v_add_f32 v237, v237, v156\n\t" \
    "v_add_f32 v237, v237, v157\n\t" \
    "v_add_f32 v237, v237, v158\n\t" \
    "v_add_f32 v237, v237, v159\n\t" \
    "v_cvt_pk_bf16_f32 v160, v144, v145\n\t" \
    "v_cvt_pk_bf16_f32 v161, v146, v147\n\t" \
    "v_cvt_pk_bf16_f32 v162, v148, v149\n\t" \
    "v_cvt_pk_bf16_f32 v163, v150, v151\n\t" \
    "v_cvt_pk_bf16_f32 v164, v152, v153\n\t" \
    "v_cvt_pk_bf16_f32 v165, v154, v155\n\t" \
    "v_cvt_pk_bf16_f32 v166, v156, v157\n\t" \
    "v_cvt_pk_bf16_f32 v167, v158, v159\n\t" \
    "s_nop 1\n\t" \
    "s_branch BACKE_%=\n\t" \
    "RAREF_%=:\n\t" \
    "s_nop 15\n\t" \
    "v_max3_f32 v239, v128, v129, v130\n\t" \
    "v_max3_f32 v239, v239, v131, v132\n\t" \
    "v_max3_f32 v239, v239, v133, v134\n\t" \
    "v_max3_f32 v239, v239, v135, v136\n\t" \
    "v_max3_f32 v239, v239, v137, v138\n\t" \
    "v_max3_f32 v239, v239, v139, v140\n\t" \
    "v_max3_f32 v239, v239, v141, v142\n\t" \
    "v_max_f32 v239, v239, v143\n\t" \
    "v_max_f32 v239, 0, v239\n\t" \
    "v_mov_b32 v240, v239\n\t" \
    "s_nop 1\n\t" \
    "v_permlane32_swap_b32 v239, v240\n\t" \
    "s_nop 1\n\t" \
    "v_max_f32 v239, v239, v240\n\t" \
    "v_add_f32 v238, v238, v239\n\t" \
    "v_exp_f32 v240, -v239\n\t" \
    "v_sub_f32 v241, 0, v238\n\t" \
    "v_sub_f32 v128, v128, v239\n\t" \
    "v_sub_f32 v112, v112, v239\n\t" \
    "v_mov_b32 v96, v241\n\t" \
    "v_sub_f32 v129, v129, v239\n\t" \
    "v_sub_f32 v113, v113, v239\n\t" \
    "v_mov_b32 v97, v241\n\t" \
    "v_sub_f32 v130, v130, v239\n\t" \
    "v_sub_f32 v114, v114, v239\n\t" \
    "v_mov_b32 v98, v241\n\t" \
    "v_sub_f32 v131, v131, v239\n\t" \
    "v_sub_f32 v115, v115, v239\n\t" \
    "v_mov_b32 v99, v241\n\t" \
    "v_sub_f32 v132, v132, v239\n\t" \
    "v_sub_f32 v116, v116, v239\n\t" \
    "v_mov_b32 v100, v241\n\t" \
    "v_sub_f32 v133, v133, v239\n\t" \
    "v_sub_f32 v117, v117, v239\n\t" \
    "v_mov_b32 v101, v241\n\t" \
    "v_sub_f32 v134, v134, v239\n\t" \
    "v_sub_f32 v118, v118, v239\n\t" \
    "v_mov_b32 v102, v241\n\t" \
    "v_sub_f32 v135, v135, v239\n\t" \
    "v_sub_f32 v119, v119, v239\n\t" \
    "v_mov_b32 v103, v241\n\t" \
    "v_sub_f32 v136, v136, v239\n\t" \
    "v_sub_f32 v120, v120, v239\n\t" \
    "v_mov_b32 v104, v241\n\t" \
    "v_sub_f32 v137, v137, v239\n\t" \
    "v_sub_f32 v121, v121, v239\n\t" \
    "v_mov_b32 v105, v241\n\t" \
    "v_sub_f32 v138, v138, v239\n\t" \
    "v_sub_f32 v122, v122, v239\n\t" \
    "v_mov_b32 v106, v241\n\t" \
    "v_sub_f32 v139, v139, v239\n\t" \
    "v_sub_f32 v123, v123, v239\n\t" \
    "v_mov_b32 v107, v241\n\t" \
    "v_sub_f32 v140, v140, v239\n\t" \
    "v_sub_f32 v124, v124, v239\n\t" \
    "v_mov_b32 v108, v241\n\t" \
    "v_sub_f32 v141, v141, v239\n\t" \
    "v_sub_f32 v125, v125, v239\n\t" \
    "v_mov_b32 v109, v241\n\t" \
    "v_sub_f32 v142, v142, v239\n\t" \
    "v_sub_f32 v126, v126, v239\n\t" \
    "v_mov_b32 v110, v241\n\t" \
    "v_sub_f32 v143, v143, v239\n\t" \
    "v_sub_f32 v127, v127, v239\n\t" \
    "v_mov_b32 v111, v241\n\t" \
    "v_exp_f32 v144, v128\n\t" \
    "v_exp_f32 v145, v129\n\t" \
    "v_exp_f32 v146, v130\n\t" \
    "v_exp_f32 v147, v131\n\t" \
    "v_exp_f32 v148, v132\n\t" \
    "v_exp_f32 v149, v133\n\t" \
    "v_exp_f32 v150, v134\n\t" \
    "v_exp_f32 v151, v135\n\t" \
    "v_exp_f32 v152, v136\n\t" \
    "v_exp_f32 v153, v137\n\t" \
    "v_exp_f32 v154, v138\n\t" \
    "v_exp_f32 v155, v139\n\t" \
    "v_exp_f32 v156, v140\n\t" \
    "v_exp_f32 v157, v141\n\t" \
    "v_exp_f32 v158, v142\n\t" \
    "v_exp_f32 v159, v143\n\t" \
    "v_mul_f32 v236, v236, v240\n\t" \
    "v_mul_f32 v0, v0, v240\n\t" \
    "v_mul_f32 v1, v1, v240\n\t" \
    "v_mul_f32 v2, v2, v240\n\t" \
    "v_mul_f32 v3, v3, v240\n\t" \
    "v_mul_f32 v4, v4, v240\n\t" \
    "v_mul_f32 v5, v5, v240\n\t" \
    "v_mul_f32 v6, v6, v240\n\t" \
    "v_mul_f32 v7, v7, v240\n\t" \
    "v_mul_f32 v8, v8, v240\n\t" \
    "v_mul_f32 v9, v9, v240\n\t" \
    "v_mul_f32 v10, v10, v240\n\t" \
    "v_mul_f32 v11, v11, v240\n\t" \
    "v_mul_f32 v12, v12, v240\n\t" \
    "v_mul_f32 v13, v13, v240\n\t" \
    "v_mul_f32 v14, v14, v240\n\t" \
    "v_mul_f32 v15, v15, v240\n\t" \
    "v_mul_f32 v16, v16, v240\n\t" \
    "v_mul_f32 v17, v17, v240\n\t" \
    "v_mul_f32 v18, v18, v240\n\t" \
    "v_mul_f32 v19, v19, v240\n\t" \
    "v_mul_f32 v20, v20, v240\n\t" \
    "v_mul_f32 v21, v21, v240\n\t" \
    "v_mul_f32 v22, v22, v240\n\t" \
    "v_mul_f32 v23, v23, v240\n\t" \
    "v_mul_f32 v24, v24, v240\n\t" \
    "v_mul_f32 v25, v25, v240\n\t" \
    "v_mul_f32 v26, v26, v240\n\t" \
    "v_mul_f32 v27, v27, v240\n\t" \
    "v_mul_f32 v28, v28, v240\n\t" \
    "v_mul_f32 v29, v29, v240\n\t" \
    "v_mul_f32 v30, v30, v240\n\t" \
    "v_mul_f32 v31, v31, v240\n\t" \
    "v_mul_f32 v32, v32, v240\n\t" \
    "v_mul_f32 v33, v33, v240\n\t" \
    "v_mul_f32 v34, v34, v240\n\t" \
    "v_mul_f32 v35, v35, v240\n\t" \
    "v_mul_f32 v36, v36, v240\n\t" \
    "v_mul_f32 v37, v37, v240\n\t" \
    "v_mul_f32 v38, v38, v240\n\t" \
    "v_mul_f32 v39, v39, v240\n\t" \
    "v_mul_f32 v40, v40, v240\n\t" \
    "v_mul_f32 v41, v41, v240\n\t" \
    "v_mul_f32 v42, v42, v240\n\t" \
    "v_mul_f32 v43, v43, v240\n\t" \
    "v_mul_f32 v44, v44, v240\n\t" \
    "v_mul_f32 v45, v45, v240\n\t" \
    "v_mul_f32 v46, v46, v240\n\t" \
    "v_mul_f32 v47, v47, v240\n\t" \
    "v_mul_f32 v48, v48, v240\n\t" \
    "v_mul_f32 v49, v49, v240\n\t" \
    "v_mul_f32 v50, v50, v240\n\t" \
    "v_mul_f32 v51, v51, v240\n\t" \
    "v_mul_f32 v52, v52, v240\n\t" \
    "v_mul_f32 v53, v53, v240\n\t" \
    "v_mul_f32 v54, v54, v240\n\t" \
    "v_mul_f32 v55, v55, v240\n\t" \
    "v_mul_f32 v56, v56, v240\n\t" \
    "v_mul_f32 v57, v57, v240\n\t" \
    "v_mul_f32 v58, v58, v240\n\t" \
    "v_mul_f32 v59, v59, v240\n\t" \
    "v_mul_f32 v60, v60, v240\n\t" \
    "v_mul_f32 v61, v61, v240\n\t" \
    "v_mul_f32 v62, v62, v240\n\t" \
    "v_mul_f32 v63, v63, v240\n\t" \
    "v_add_f32 v237, v144, v145\n\t" \
    "v_add_f32 v237, v237, v146\n\t" \
    "v_add_f32 v237, v237, v147\n\t" \
    "v_add_f32 v237, v237, v148\n\t" \
    "v_add_f32 v237, v237, v149\n\t" \
    "v_add_f32 v237, v237, v150\n\t" \
    "v_add_f32 v237, v237, v151\n\t" \
    "v_add_f32 v237, v237, v152\n\t" \
    "v_add_f32 v237, v237, v153\n\t" \
    "v_add_f32 v237, v237, v154\n\t" \
    "v_add_f32 v237, v237, v155\n\t" \
    "v_add_f32 v237, v237, v156\n\t" \
    "v_add_f32 v237, v237, v157\n\t" \
    "v_add_f32 v237, v237, v158\n\t" \
    "v_add_f32 v237, v237, v159\n\t" \
    "v_cvt_pk_bf16_f32 v160, v144, v145\n\t" \
    "v_cvt_pk_bf16_f32 v161, v146, v147\n\t" \
    "v_cvt_pk_bf16_f32 v162, v148, v149\n\t" \
    "v_cvt_pk_bf16_f32 v163, v150, v151\n\t" \
    "v_cvt_pk_bf16_f32 v164, v152, v153\n\t" \
    "v_cvt_pk_bf16_f32 v165, v154, v155\n\t" \
    "v_cvt_pk_bf16_f32 v166, v156, v157\n\t" \
    "v_cvt_pk_bf16_f32 v167, v158, v159\n\t" \
    "s_nop 1\n\t" \
    "s_branch BACKF_%=\n\t" \
    "RAREG_%=:\n\t" \
    "s_nop 15\n\t" \
    "v_max3_f32 v239, v112, v113, v114\n\t" \
    "v_max3_f32 v239, v239, v115, v116\n\t" \
    "v_max3_f32 v239, v239, v117, v118\n\t" \
    "v_max3_f32 v239, v239, v119, v120\n\t" \
    "v_max3_f32 v239, v239, v121, v122\n\t" \
    "v_max3_f32 v239, v239, v123, v124\n\t" \
    "v_max3_f32 v239, v239, v125, v126\n\t" \
    "v_max_f32 v239, v239, v127\n\t" \
    "v_max_f32 v239, 0, v239\n\t" \
    "v_mov_b32 v240, v239\n\t" \
    "s_nop 1\n\t" \
    "v_permlane32_swap_b32 v239, v240\n\t" \
    "s_nop 1\n\t" \
    "v_max_f32 v239, v239, v240\n\t" \
    "v_add_f32 v238, v238, v239\n\t" \
    "v_exp_f32 v240, -v239\n\t" \
    "v_sub_f32 v241, 0, v238\n\t" \
    "v_sub_f32 v112, v112, v239\n\t" \
    "v_mov_b32 v96, v241\n\t" \
    "v_sub_f32 v113, v113, v239\n\t" \
    "v_mov_b32 v97, v241\n\t" \
    "v_sub_f32 v114, v114, v239\n\t" \
    "v_mov_b32 v98, v241\n\t" \
    "v_sub_f32 v115, v115, v239\n\t" \
    "v_mov_b32 v99, v241\n\t" \
    "v_sub_f32 v116, v116, v239\n\t" \
    "v_mov_b32 v100, v241\n\t" \
    "v_sub_f32 v117, v117, v239\n\t" \
    "v_mov_b32 v101, v241\n\t" \
    "v_sub_f32 v118, v118, v239\n\t" \
    "v_mov_b32 v102, v241\n\t" \
    "v_sub_f32 v119, v119, v239\n\t" \
    "v_mov_b32 v103, v241\n\t" \
    "v_sub_f32 v120, v120, v239\n\t" \
    "v_mov_b32 v104, v241\n\t" \
    "v_sub_f32 v121, v121, v239\n\t" \
    "v_mov_b32 v105, v241\n\t" \
    "v_sub_f32 v122, v122, v239\n\t" \
    "v_mov_b32 v106, v241\n\t" \
    "v_sub_f32 v123, v123, v239\n\t" \
    "v_mov_b32 v107, v241\n\t" \
    "v_sub_f32 v124, v124, v239\n\t" \
    "v_mov_b32 v108, v241\n\t" \
    "v_sub_f32 v125, v125, v239\n\t" \
    "v_mov_b32 v109, v241\n\t" \
    "v_sub_f32 v126, v126, v239\n\t" \
    "v_mov_b32 v110, v241\n\t" \
    "v_sub_f32 v127, v127, v239\n\t" \
    "v_mov_b32 v111, v241\n\t" \
    "v_exp_f32 v144, v112\n\t" \
    "v_exp_f32 v145, v113\n\t" \
    "v_exp_f32 v146, v114\n\t" \
    "v_exp_f32 v147, v115\n\t" \
    "v_exp_f32 v148, v116\n\t" \
    "v_exp_f32 v149, v117\n\t" \
    "v_exp_f32 v150, v118\n\t" \
    "v_exp_f32 v151, v119\n\t" \
    "v_exp_f32 v152, v120\n\t" \
    "v_exp_f32 v153, v121\n\t" \
    "v_exp_f32 v154, v122\n\t" \
    "v_exp_f32 v155, v123\n\t" \
    "v_exp_f32 v156, v124\n\t" \
    "v_exp_f32 v157, v125\n\t" \
    "v_exp_f32 v158, v126\n\t" \
    "v_exp_f32 v159, v127\n\t" \
    "v_mul_f32 v236, v236, v240\n\t" \
    "v_mul_f32 v0, v0, v240\n\t" \
    "v_mul_f32 v1, v1, v240\n\t" \
    "v_mul_f32 v2, v2, v240\n\t" \
    "v_mul_f32 v3, v3, v240\n\t" \
    "v_mul_f32 v4, v4, v240\n\t" \
    "v_mul_f32 v5, v5, v240\n\t" \
    "v_mul_f32 v6, v6, v240\n\t" \
    "v_mul_f32 v7, v7, v240\n\t" \
    "v_mul_f32 v8, v8, v240\n\t" \
    "v_mul_f32 v9, v9, v240\n\t" \
    "v_mul_f32 v10, v10, v240\n\t" \
    "v_mul_f32 v11, v11, v240\n\t" \
    "v_mul_f32 v12, v12, v240\n\t" \
    "v_mul_f32 v13, v13, v240\n\t" \
    "v_mul_f32 v14, v14, v240\n\t" \
    "v_mul_f32 v15, v15, v240\n\t" \
    "v_mul_f32 v16, v16, v240\n\t" \
    "v_mul_f32 v17, v17, v240\n\t" \
    "v_mul_f32 v18, v18, v240\n\t" \
    "v_mul_f32 v19, v19, v240\n\t" \
    "v_mul_f32 v20, v20, v240\n\t" \
    "v_mul_f32 v21, v21, v240\n\t" \
    "v_mul_f32 v22, v22, v240\n\t" \
    "v_mul_f32 v23, v23, v240\n\t" \
    "v_mul_f32 v24, v24, v240\n\t" \
    "v_mul_f32 v25, v25, v240\n\t" \
    "v_mul_f32 v26, v26, v240\n\t" \
    "v_mul_f32 v27, v27, v240\n\t" \
    "v_mul_f32 v28, v28, v240\n\t" \
    "v_mul_f32 v29, v29, v240\n\t" \
    "v_mul_f32 v30, v30, v240\n\t" \
    "v_mul_f32 v31, v31, v240\n\t" \
    "v_mul_f32 v32, v32, v240\n\t" \
    "v_mul_f32 v33, v33, v240\n\t" \
    "v_mul_f32 v34, v34, v240\n\t" \
    "v_mul_f32 v35, v35, v240\n\t" \
    "v_mul_f32 v36, v36, v240\n\t" \
    "v_mul_f32 v37, v37, v240\n\t" \
    "v_mul_f32 v38, v38, v240\n\t" \
    "v_mul_f32 v39, v39, v240\n\t" \
    "v_mul_f32 v40, v40, v240\n\t" \
    "v_mul_f32 v41, v41, v240\n\t" \
    "v_mul_f32 v42, v42, v240\n\t" \
    "v_mul_f32 v43, v43, v240\n\t" \
    "v_mul_f32 v44, v44, v240\n\t" \
    "v_mul_f32 v45, v45, v240\n\t" \
    "v_mul_f32 v46, v46, v240\n\t" \
    "v_mul_f32 v47, v47, v240\n\t" \
    "v_mul_f32 v48, v48, v240\n\t" \
    "v_mul_f32 v49, v49, v240\n\t" \
    "v_mul_f32 v50, v50, v240\n\t" \
    "v_mul_f32 v51, v51, v240\n\t" \
    "v_mul_f32 v52, v52, v240\n\t" \
    "v_mul_f32 v53, v53, v240\n\t" \
    "v_mul_f32 v54, v54, v240\n\t" \
    "v_mul_f32 v55, v55, v240\n\t" \
    "v_mul_f32 v56, v56, v240\n\t" \
    "v_mul_f32 v57, v57, v240\n\t" \
    "v_mul_f32 v58, v58, v240\n\t" \
    "v_mul_f32 v59, v59, v240\n\t" \
    "v_mul_f32 v60, v60, v240\n\t" \
    "v_mul_f32 v61, v61, v240\n\t" \
    "v_mul_f32 v62, v62, v240\n\t" \
    "v_mul_f32 v63, v63, v240\n\t" \
    "v_add_f32 v237, v144, v145\n\t" \
    "v_add_f32 v237, v237, v146\n\t" \
    "v_add_f32 v237, v237, v147\n\t" \
    "v_add_f32 v237, v237, v148\n\t" \
    "v_add_f32 v237, v237, v149\n\t" \
    "v_add_f32 v237, v237, v150\n\t" \
    "v_add_f32 v237, v237, v151\n\t" \
    "v_add_f32 v237, v237, v152\n\t" \
    "v_add_f32 v237, v237, v153\n\t" \
    "v_add_f32 v237, v237, v154\n\t" \
    "v_add_f32 v237, v237, v155\n\t" \
    "v_add_f32 v237, v237, v156\n\t" \
    "v_add_f32 v237, v237, v157\n\t" \
    "v_add_f32 v237, v237, v158\n\t" \
    "v_add_f32 v237, v237, v159\n\t" \
    "v_cvt_pk_bf16_f32 v160, v144, v145\n\t" \
    "v_cvt_pk_bf16_f32 v161, v146, v147\n\t" \
    "v_cvt_pk_bf16_f32 v162, v148, v149\n\t" \
    "v_cvt_pk_bf16_f32 v163, v150, v151\n\t" \
    "v_cvt_pk_bf16_f32 v164, v152, v153\n\t" \
    "v_cvt_pk_bf16_f32 v165, v154, v155\n\t" \
    "v_cvt_pk_bf16_f32 v166, v156, v157\n\t" \
    "v_cvt_pk_bf16_f32 v167, v158, v159\n\t" \
    "s_nop 1\n\t" \
    "s_branch BACKG_%=\n\t" \
    "END_%=:\n\t" \
    "s_cmp_lg_u32 %[dmaw], 0\n\t" \
    "s_cbranch_scc1 END2_%=\n\t" \
    "s_waitcnt vmcnt(0)\n\t" \
    "END2_%=:\n\t" \
    "s_waitcnt lgkmcnt(0)\n\t" \
    "s_nop 15\n\t"
#define MX3(a, b, c) __builtin_fmaxf(__builtin_fmaxf((a), (b)), (c))
DI float max16(const f32x16& p) {
    float a = MX3(p[0], p[1], p[2]), b = MX3(p[3], p[4], p[5]);
    a = MX3(a, p[6], p[7]); b = MX3(b, p[8], p[9]); a = MX3(a, p[10], p[11]); b = MX3(b, p[12], p[13]); a = MX3(a, p[14], p[15]);
    return __builtin_fmaxf(a, b);
}
DI float halfmax(float m) { auto rr = __builtin_amdgcn_permlane32_swap(__float_as_uint(m), __float_as_uint(m), false, false); return __builtin_fmaxf(__uint_as_float(rr[0]), __uint_as_float(rr[1])); }
DI void cmask(f32x16& p, int r32, int hi) {
#pragma unroll
    for (int r = 0; r < 16; ++r) { const int kv = 4 * hi + (r & 3) + 8 * (r >> 2); if (kv > r32) p[r] = -INFINITY; }
}
#define WAITBAR(N) asm volatile("s_waitcnt vmcnt(" #N ")\n\ts_barrier" ::: "memory")
typedef unsigned u32x16 __attribute__((ext_vector_type(16)));

__global__ __launch_bounds__(512, 2) void k2_attn(const char* __restrict__ Q, const char* __restrict__ K, const char* __restrict__ VT, char* __restrict__ CTX, const char* __restrict__ wo, const char* __restrict__ w2) {
    extern __shared__ __attribute__((aligned(16))) unsigned char smem[];
    const lds_cptr lds = (lds_cptr)smem;
    const int tid = threadIdx.x, lane = tid & 63, r32 = lane & 31, hi = lane >> 5, wid = __builtin_amdgcn_readfirstlane(tid >> 6);
    const int xcd = blockIdx.x & 7, idx = blockIdx.x >> 3, h = xcd * 4 + (idx >> 3), ib = idx & 7;
    const int G = wid < 4 ? 4 * ib + wid : 63 - 4 * ib - (wid - 4);
    const int NT = 32 - 2 * ib;
    const char* Kh = K + (size_t)h * 32 * SLOT; const char* Vh = VT + (size_t)h * 32 * SLOT;
    const unsigned lds0 = (unsigned)(uintptr_t)smem;
    const unsigned dmaw = (unsigned)(wid - 4) >> 31;
    const unsigned dvo = (wid & 3) * 4096 + lane * 16, kdst = lds0 + LDS_K + (wid & 3) * 4096, vdst = lds0 + LDS_V + (wid & 3) * 4096;
#define DMA_T(base, dst, t, sl) do { const char* g_ = (base) + (size_t)(t) * SLOT; const unsigned l_ = (unsigned)__builtin_amdgcn_readfirstlane((dst) + ((sl) & 3) * SLOT); glds2(g_, dvo, l_); glds2(g_ + 2048, dvo, l_ + 2048); } while (0)
#define DMA_K(t, sl) DMA_T(Kh, kdst, t, sl)
#define DMA_V(t, sl) DMA_T(Vh, vdst, t, sl)
#define DMA_STEP(s) do { if (dmaw) { DMA_K(min((s) + 3, NT - 1), (s) + 3); DMA_V(min((s) + 2, NT - 1), (s) + 2); } } while (0)
    STAMP(2, 0);
    const unsigned cpf = code_pf(tid, 84);
    u32x16 qa, qb;
    { const char* qp = Q + ((size_t)h * 32 + (G >> 1)) * SLOT; const unsigned qo = hi * 1024 + ((G & 1) * 32 + r32) * 16;
      u32x4 q[8];
#define QLD(d) asm volatile("global_load_dwordx4 %0, %1, %2 offset:%c3" : "=&v"(q[d]) : "v"(qo + ((d) >> 1) * 4096), "s"(qp), "i"(((d) & 1) * 2048) : "memory")
      QLD(0); QLD(1); QLD(2); QLD(3); QLD(4); QLD(5); QLD(6); QLD(7);
#undef QLD
      if (dmaw) { DMA_K(0, 0); DMA_V(0, 0); DMA_K(1, 1); DMA_K(2, 2); DMA_V(1, 1); } else WAIT_V(0);
      asm volatile("s_waitcnt vmcnt(8)\n\ts_barrier" : "+v"(q[0]), "+v"(q[1]), "+v"(q[2]), "+v"(q[3]), "+v"(q[4]), "+v"(q[5]), "+v"(q[6]), "+v"(q[7]) :: "memory");
#pragma unroll
      for (int i = 0; i < 4; ++i)
#pragma unroll
          for (int e = 0; e < 4; ++e) { qa[4 * i + e] = q[i][e]; qb[4 * i + e] = q[4 + i][e]; }
    }
    STAMP(2, 1);
    CODE_PF_DONE(cpf);
    const unsigned kp0 = lds0 + LDS_K + hi * 1024 + r32 * 16, vp0 = lds0 + LDS_V + hi * 2048 + r32 * 16;
    f32x16 o0 = f32x16{}, o1 = f32x16{}, o2 = f32x16{}, o3 = f32x16{}, negm, cA, cB, P = f32x16{};
    u32x16 kfa, kfb;
    float l_reg = 0.f, mhat;
    {
        f32x16 s = f32x16{};
#pragma unroll
        for (int j = 0; j < 8; ++j) {
            const bf16x8 kf = lds_rd(lds + LDS_K + hi * 1024 + r32 * 16 + j * 2048); const u32x4 kw = __builtin_bit_cast(u32x4, kf);
#pragma unroll
            for (int e = 0; e < 4; ++e) { if (j < 4) kfa[4 * j + e] = kw[e]; else kfb[4 * (j - 4) + e] = kw[e]; }
            const u32x4 qw = j < 4 ? (u32x4){qa[4 * j], qa[4 * j + 1], qa[4 * j + 2], qa[4 * j + 3]} : (u32x4){qb[4 * j - 16], qb[4 * j - 15], qb[4 * j - 14], qb[4 * j - 13]};
            s = MFMA32(kf, __builtin_bit_cast(bf16x8, qw), s);
        }
        if (G == 0) cmask(s, r32, hi);
        mhat = halfmax(max16(s));
#pragma unroll
        for (int r = 0; r < 16; ++r) { negm[r] = -mhat; cB[r] = -INFINITY; cA[r] = -INFINITY; }
    }
    const float thr = 65536.0f;
    int t = 0; unsigned soff = 0;
    unsigned vk = kp0, vkn = kp0 + SLOT, vv = vp0, vvp = vp0;
#define K2_STATE [o0] "+&{v[0:15]}"(o0), [o1] "+&{v[16:31]}"(o1), [o2] "+&{v[32:47]}"(o2), [o3] "+&{v[48:63]}"(o3), [negm] "+&{v[96:111]}"(negm), [ca] "+&{v[112:127]}"(cA), [cb] "+&{v[128:143]}"(cB), \
        [p] "+&{v[144:159]}"(P), [kfa] "+&{v[168:183]}"(kfa), [kfb] "+&{v[184:199]}"(kfb), [vk] "+&{v232}"(vk), [vkn] "+&{v233}"(vkn), [vv] "+&{v234}"(vv), [vvp] "+&{v235}"(vvp), [l] "+&{v236}"(l_reg), [mh] "+&{v238}"(mhat)
#define K2_IN [qa] "{v[64:79]}"(qa), [qb] "{v[80:95]}"(qb), [thr] "s"(thr)
#define K2_CLOB "v160", "v161", "v162", "v163", "v164", "v165", "v166", "v167", "v200", "v201", "v202", "v203", "v204", "v205", "v206", "v207", "v208", "v209", "v210", "v211", "v212", "v213", "v214", "v215", \
        "v216", "v217", "v218", "v219", "v220", "v221", "v222", "v223", "v224", "v225", "v226", "v227", "v228", "v229", "v230", "v231", "v237", "v239", "v240", "v241", "vcc", "scc", "memory"
    const unsigned pfg = blockIdx.x * 256 + (tid & 255), pfo = pfg * 128u, pfo2 = (pfg < 57344u ? pfg : 0u) * 128u;
    const int nloop = G >> 1;
    asm volatile(K2_ASM : K2_STATE, [t] "+&s"(t), [soff] "+&s"(soff)
                 : K2_IN, [n] "s"(nloop), [odd] "s"(G & 1), [dmaw] "s"(dmaw), [ntm1] "s"(NT - 1), [khl] "s"((unsigned)(uintptr_t)Kh), [khh] "s"((unsigned)((uintptr_t)Kh >> 32)),
                   [vhl] "s"((unsigned)(uintptr_t)Vh), [vhh] "s"((unsigned)((uintptr_t)Vh >> 32)), [kdst] "s"(kdst), [vdst] "s"(vdst), [dvo] "v"(dvo), [kp0] "v"(kp0), [vp0] "v"(vp0), [dq] "v"(r32 - 4 * hi),
                   [pfo] "v"(pfo), [pfo2] "v"(pfo2), [wo] "s"(wo), [w2] "s"(w2)
                 : K2_CLOB, "s60", "s61", "s62", "s63", "v250", "v251");
    STAMP(2, 2);
    {
        auto rr = __builtin_amdgcn_permlane32_swap(__float_as_uint(l_reg), __float_as_uint(l_reg), false, false);
        const float inv = 1.0f / (__uint_as_float(rr[0]) + __uint_as_float(rr[1]));
        char* op = CTX + ((size_t)((G >> 2) * 64 + 2 * h) * 8 + hi) * 2048 + ((G & 3) * 32 + r32) * 16;
#define K2_ST(o, db) do { _Pragma("unroll") for (int g = 0; g < 4; g += 2) { \
            const unsigned x0 = cvtpk(o[4 * g] * inv, o[4 * g + 1] * inv), x1 = cvtpk(o[4 * g + 2] * inv, o[4 * g + 3] * inv); \
            const unsigned y0 = cvtpk(o[4 * g + 4] * inv, o[4 * g + 5] * inv), y1 = cvtpk(o[4 * g + 6] * inv, o[4 * g + 7] * inv); \
            const auto r0 = __builtin_amdgcn_permlane32_swap(x0, y0, false, false), r1 = __builtin_amdgcn_permlane32_swap(x1, y1, false, false); \
            ST16(op + (size_t)(((db) >> 1) * 8 + 4 * ((db) & 1) + g) * 2048, ((u32x4){r0[0], r1[0], r0[1], r1[1]})); } } while (0)
        K2_ST(o0, 0); K2_ST(o1, 1); K2_ST(o2, 2); K2_ST(o3, 3);
    }
    WAITBAR(16); ++t;
    if (t < NT) { DMA_STEP(t); WAITBAR(16); ++t;
        for (; t < NT; ++t) { DMA_STEP(t); WAITBAR(8); } }
    STAMP(2, 3);
    WAIT_V(0);
    STAMP(2, 4);
}
}

namespace k3 {
constexpr int STG = 49152, LDS_BYTES = 3 * STG;
typedef __attribute__((address_space(1))) unsigned gu32;
#define K3_ASM \
    "s_mov_b64 s[60:61], %[a1]\n\t" \
    "s_mov_b64 s[62:63], %[b1]\n\t" \
    "s_mov_b32 s67, %[nl]\n\t" \
    "s_mov_b32 s68, 0\n\t" \
    "s_add_u32 m0, %[mA], s68\n\t" \
    "s_add_u32 s66, %[mB], s68\n\t" \
    "global_load_lds_dwordx4 %[va], s[60:61] offset:0\n\t" \
    "global_load_lds_dwordx4 %[va], s[60:61] offset:1024\n\t" \
    "global_load_lds_dwordx4 %[va], s[60:61] offset:2048\n\t" \
    "global_load_lds_dwordx4 %[va], s[60:61] offset:3072\n\t" \
    "s_mov_b32 m0, s66\n\t" \
    "s_add_u32 s64, s62, 0x10000\n\t" \
    "s_addc_u32 s65, s63, 0\n\t" \
    "global_load_lds_dwordx4 %[vb], s[62:63]\n\t" \
    "s_add_u32 m0, s66, 8192\n\t" \
    "s_add_u32 s60, s60, 0x4000\n\t" \
    "global_load_lds_dwordx4 %[vb], s[64:65]\n\t" \
    "s_addc_u32 s61, s61, 0\n\t" \
    "s_add_u32 s62, s62, 0x20000\n\t" \
    "s_addc_u32 s63, s63, 0\n\t" \
    "s_mov_b32 s68, 49152\n\t" \
    "s_add_u32 m0, %[mA], s68\n\t" \
    "s_add_u32 s66, %[mB], s68\n\t" \
    "global_load_lds_dwordx4 %[va], s[60:61] offset:0\n\t" \
    "global_load_lds_dwordx4 %[va], s[60:61] offset:1024\n\t" \
    "global_load_lds_dwordx4 %[va], s[60:61] offset:2048\n\t" \
    "global_load_lds_dwordx4 %[va], s[60:61] offset:3072\n\t" \
    "s_mov_b32 m0, s66\n\t" \
    "s_add_u32 s64, s62, 0x10000\n\t" \
    "s_addc_u32 s65, s63, 0\n\t" \
    "global_load_lds_dwordx4 %[vb], s[62:63]\n\t" \
    "s_add_u32 m0, s66, 8192\n\t" \
    "s_add_u32 s60, s60, 0x4000\n\t" \
    "global_load_lds_dwordx4 %[vb], s[64:65]\n\t" \
    "s_addc_u32 s61, s61, 0\n\t" \
    "s_add_u32 s62, s62, 0x20000\n\t" \
    "s_addc_u32 s63, s63, 0\n\t" \
    "s_mov_b32 s68, 98304\n\t" \
    "s_add_u32 m0, %[mA], s68\n\t" \
    "s_add_u32 s66, %[mB], s68\n\t" \
    "global_load_lds_dwordx4 %[va], s[60:61] offset:0\n\t" \
    "global_load_lds_dwordx4 %[va], s[60:61] offset:1024\n\t" \
    "global_load_lds_dwordx4 %[va], s[60:61] offset:2048\n\t" \
    "global_load_lds_dwordx4 %[va], s[60:61] offset:3072\n\t" \
    "s_mov_b32 m0, s66\n\t" \
    "s_add_u32 s64, s62, 0x10000\n\t" \
    "s_addc_u32 s65, s63, 0\n\t" \
    "global_load_lds_dwordx4 %[vb], s[62:63]\n\t" \
    "s_add_u32 m0, s66, 8192\n\t" \
    "s_add_u32 s60, s60, 0x4000\n\t" \
    "global_load_lds_dwordx4 %[vb], s[64:65]\n\t" \
    "s_addc_u32 s61, s61, 0\n\t" \
    "s_add_u32 s62, s62, 0x20000\n\t" \
    "s_addc_u32 s63, s63, 0\n\t" \
    "s_mov_b32 s68, 0\n\t" \
    "s_mov_b32 s69, 98304\n\t" \
    "v_mov_b32 v128, v134\n\t" \
    "v_mov_b32 v129, v135\n\t" \
    "v_mov_b32 v130, v136\n\t" \
    "v_add_u32 v131, 49152, v134\n\t" \
    "v_add_u32 v132, 49152, v135\n\t" \
    "v_add_u32 v133, 49152, v136\n\t" \
    "s_waitcnt vmcnt(12)\n\t" \
    "s_barrier\n\t" \
    "ds_read_b128 v[64:67], v128 offset:0\n\t" \
    "ds_read_b64_tr_b16 v[80:81], v129 offset:0\n\t" \
    "ds_read_b64_tr_b16 v[82:83], v129 offset:256\n\t" \
    "ds_read_b64_tr_b16 v[84:85], v130 offset:0\n\t" \
    "ds_read_b64_tr_b16 v[86:87], v130 offset:256\n\t" \
    "ds_read_b128 v[68:71], v128 offset:256\n\t" \
    "ds_read_b64_tr_b16 v[88:89], v129 offset:512\n\t" \
    "ds_read_b64_tr_b16 v[90:91], v129 offset:768\n\t" \
    "ds_read_b64_tr_b16 v[92:93], v130 offset:512\n\t" \
    "ds_read_b64_tr_b16 v[94:95], v130 offset:768\n\t" \
    "ds_read_b128 v[72:75], v128 offset:512\n\t" \
    "ds_read_b128 v[76:79], v128 offset:768\n\t" \
    "LOOP_%=:\n\t" \
    "s_add_u32 m0, %[mA], s68\n\t" \
    "s_add_u32 s66, %[mB], s68\n\t" \
    "global_load_lds_dwordx4 %[va], s[60:61] offset:0\n\t" \
    "global_load_lds_dwordx4 %[va], s[60:61] offset:1024\n\t" \
    "global_load_lds_dwordx4 %[va], s[60:61] offset:2048\n\t" \
    "global_load_lds_dwordx4 %[va], s[60:61] offset:3072\n\t" \
    "s_mov_b32 m0, s66\n\t" \
    "s_add_u32 s64, s62, 0x10000\n\t" \
    "s_addc_u32 s65, s63, 0\n\t" \
    "global_load_lds_dwordx4 %[vb], s[62:63]\n\t" \
    "s_add_u32 m0, s66, 8192\n\t" \
    "s_add_u32 s60, s60, 0x4000\n\t" \
    "global_load_lds_dwordx4 %[vb], s[64:65]\n\t" \
    "s_addc_u32 s61, s61, 0\n\t" \
    "s_add_u32 s62, s62, 0x20000\n\t" \
    "s_addc_u32 s63, s63, 0\n\t" \
    "v_mfma_f32_16x16x32_bf16 v[0:3], v[112:115], v[96:99], v[0:3]\n\t" \
    "ds_read_b128 v[64:67], v131 offset:0\n\t" \
    "ds_read_b64_tr_b16 v[80:81], v132 offset:0\n\t" \
    "v_mfma_f32_16x16x32_bf16 v[4:7], v[116:119], v[96:99], v[4:7]\n\t" \
    "ds_read_b64_tr_b16 v[82:83], v132 offset:256\n\t" \
    "ds_read_b64_tr_b16 v[84:85], v133 offset:0\n\t" \
    "v_mfma_f32_16x16x32_bf16 v[8:11], v[120:123], v[96:99], v[8:11]\n\t" \
    "ds_read_b64_tr_b16 v[86:87], v133 offset:256\n\t" \
    "ds_read_b128 v[68:71], v131 offset:256\n\t" \
    "v_mfma_f32_16x16x32_bf16 v[12:15], v[124:127], v[96:99], v[12:15]\n\t" \
    "ds_read_b64_tr_b16 v[88:89], v132 offset:512\n\t" \
    "ds_read_b64_tr_b16 v[90:91], v132 offset:768\n\t" \
    "v_mfma_f32_16x16x32_bf16 v[16:19], v[112:115], v[100:103], v[16:19]\n\t" \
    "ds_read_b64_tr_b16 v[92:93], v133 offset:512\n\t" \
    "ds_read_b64_tr_b16 v[94:95], v133 offset:768\n\t" \
    "v_mfma_f32_16x16x32_bf16 v[20:23], v[116:119], v[100:103], v[20:23]\n\t" \
    "ds_read_b128 v[72:75], v131 offset:512\n\t" \
    "ds_read_b128 v[76:79], v131 offset:768\n\t" \
    "v_mfma_f32_16x16x32_bf16 v[24:27], v[120:123], v[100:103], v[24:27]\n\t" \
    "s_add_u32 m0, %[mA], s68\n\t" \
    "s_add_u32 s66, %[mB], s68\n\t" \
    "global_load_lds_dwordx4 %[va], s[60:61] offset:0\n\t" \
    "v_mfma_f32_16x16x32_bf16 v[28:31], v[124:127], v[100:103], v[28:31]\n\t" \
    "global_load_lds_dwordx4 %[va], s[60:61] offset:1024\n\t" \
    "global_load_lds_dwordx4 %[va], s[60:61] offset:2048\n\t" \
    "global_load_lds_dwordx4 %[va], s[60:61] offset:3072\n\t" \
    "v_mfma_f32_16x16x32_bf16 v[32:35], v[112:115], v[104:107], v[32:35]\n\t" \
    "s_mov_b32 m0, s66\n\t" \
    "s_add_u32 s64, s62, 0x10000\n\t" \
    "s_addc_u32 s65, s63, 0\n\t" \
    "v_mfma_f32_16x16x32_bf16 v[36:39], v[116:119], v[104:107], v[36:39]\n\t" \
    "global_load_lds_dwordx4 %[vb], s[62:63]\n\t" \
    "s_add_u32 m0, s66, 8192\n\t" \
    "s_add_u32 s60, s60, 0x4000\n\t" \
    "v_mfma_f32_16x16x32_bf16 v[40:43], v[120:123], v[104:107], v[40:43]\n\t" \
    "global_load_lds_dwordx4 %[vb], s[64:65]\n\t" \
    "s_addc_u32 s61, s61, 0\n\t" \
    "s_add_u32 s62, s62, 0x20000\n\t" \
    "v_mfma_f32_16x16x32_bf16 v[44:47], v[124:127], v[104:107], v[44:47]\n\t" \
    "s_addc_u32 s63, s63, 0\n\t" \
    "v_mov_b32 v128, v131\n\t" \
    "v_mov_b32 v129, v132\n\t" \
    "v_mfma_f32_16x16x32_bf16 v[48:51], v[112:115], v[108:111], v[48:51]\n\t" \
    "v_mov_b32 v130, v133\n\t" \
    "v_add_u32 v131, s69, v134\n\t" \
    "v_add_u32 v132, s69, v135\n\t" \
    "v_add_u32 v133, s69, v136\n\t" \
    "v_mfma_f32_16x16x32_bf16 v[52:55], v[116:119], v[108:111], v[52:55]\n\t" \
    "s_add_u32 s68, s68, 49152\n\t" \
    "s_cmp_eq_u32 s68, 147456\n\t" \
    "s_cselect_b32 s68, 0, s68\n\t" \
    "s_add_u32 s69, s69, 49152\n\t" \
    "v_mfma_f32_16x16x32_bf16 v[56:59], v[120:123], v[108:111], v[56:59]\n\t" \
    "s_cmp_eq_u32 s69, 147456\n\t" \
    "s_cselect_b32 s69, 0, s69\n\t" \
    "v_mfma_f32_16x16x32_bf16 v[60:63], v[124:127], v[108:111], v[60:63]\n\t" \
    "s_sub_u32 s67, s67, 1\n\t" \
    "s_cmp_lg_u32 s67, 0\n\t" \
    "s_cbranch_scc1 LOOP_%=\n\t" \
    "s_waitcnt lgkmcnt(0)\n\t" \
    "v_mfma_f32_16x16x32_bf16 v[0:3], v[80:83], v[64:67], v[0:3]\n\t" \
    "ds_read_b128 v[96:99], v128 offset:8192\n\t" \
    "ds_read_b64_tr_b16 v[112:113], v129 offset:8192\n\t" \
    "v_mfma_f32_16x16x32_bf16 v[4:7], v[84:87], v[64:67], v[4:7]\n\t" \
    "ds_read_b64_tr_b16 v[114:115], v129 offset:8448\n\t" \
    "ds_read_b64_tr_b16 v[116:117], v130 offset:8192\n\t" \
    "v_mfma_f32_16x16x32_bf16 v[8:11], v[88:91], v[64:67], v[8:11]\n\t" \
    "ds_read_b64_tr_b16 v[118:119], v130 offset:8448\n\t" \
    "ds_read_b128 v[100:103], v128 offset:8448\n\t" \
    "v_mfma_f32_16x16x32_bf16 v[12:15], v[92:95], v[64:67], v[12:15]\n\t" \
    "ds_read_b64_tr_b16 v[120:121], v129 offset:8704\n\t" \
    "ds_read_b64_tr_b16 v[122:123], v129 offset:8960\n\t" \
    "v_mfma_f32_16x16x32_bf16 v[16:19], v[80:83], v[68:71], v[16:19]\n\t" \
    "ds_read_b64_tr_b16 v[124:125], v130 offset:8704\n\t" \
    "ds_read_b64_tr_b16 v[126:127], v130 offset:8960\n\t" \
    "v_mfma_f32_16x16x32_bf16 v[20:23], v[84:87], v[68:71], v[20:23]\n\t" \
    "ds_read_b128 v[104:107], v128 offset:8704\n\t" \
    "ds_read_b128 v[108:111], v128 offset:8960\n\t" \
    "v_mfma_f32_16x16x32_bf16 v[24:27], v[88:91], v[68:71], v[24:27]\n\t" \
    "v_mfma_f32_16x16x32_bf16 v[28:31], v[92:95], v[68:71], v[28:31]\n\t" \
    "v_mfma_f32_16x16x32_bf16 v[32:35], v[80:83], v[72:75], v[32:35]\n\t" \
    "v_mfma_f32_16x16x32_bf16 v[36:39], v[84:87], v[72:75], v[36:39]\n\t" \
    "v_mfma_f32_16x16x32_bf16 v[40:43], v[88:91], v[72:75], v[40:43]\n\t" \
    "v_mfma_f32_16x16x32_bf16 v[44:47], v[92:95], v[72:75], v[44:47]\n\t" \
    "v_mfma_f32_16x16x32_bf16 v[48:51], v[80:83], v[76:79], v[48:51]\n\t" \
    "v_mfma_f32_16x16x32_bf16 v[52:55], v[84:87], v[76:79], v[52:55]\n\t" \
    "v_mfma_f32_16x16x32_bf16 v[56:59], v[88:91], v[76:79], v[56:59]\n\t" \
    "v_mfma_f32_16x16x32_bf16 v[60:63], v[92:95], v[76:79], v[60:63]\n\t" \
    "s_waitcnt vmcnt(6) lgkmcnt(0)\n\t" \
    "s_barrier\n\t" \
    "v_mfma_f32_16x16x32_bf16 v[0:3], v[112:115], v[96:99], v[0:3]\n\t" \
    "ds_read_b128 v[64:67], v131 offset:0\n\t" \
    "ds_read_b64_tr_b16 v[80:81], v132 offset:0\n\t" \
    "v_mfma_f32_16x16x32_bf16 v[4:7], v[116:119], v[96:99], v[4:7]\n\t" \
    "ds_read_b64_tr_b16 v[82:83], v132 offset:256\n\t" \
    "ds_read_b64_tr_b16 v[84:85], v133 offset:0\n\t" \
    "v_mfma_f32_16x16x32_bf16 v[8:11], v[120:123], v[96:99], v[8:11]\n\t" \
    "ds_read_b64_tr_b16 v[86:87], v133 offset:256\n\t" \
    "ds_read_b128 v[68:71], v131 offset:256\n\t" \
    "v_mfma_f32_16x16x32_bf16 v[12:15], v[124:127], v[96:99], v[12:15]\n\t" \
    "ds_read_b64_tr_b16 v[88:89], v132 offset:512\n\t" \
    "ds_read_b64_tr_b16 v[90:91], v132 offset:768\n\t" \
    "v_mfma_f32_16x16x32_bf16 v[16:19], v[112:115], v[100:103], v[16:19]\n\t" \
    "ds_read_b64_tr_b16 v[92:93], v133 offset:512\n\t" \
    "ds_read_b64_tr_b16 v[94:95], v133 offset:768\n\t" \
    "v_mfma_f32_16x16x32_bf16 v[20:23], v[116:119], v[100:103], v[20:23]\n\t" \
    "ds_read_b128 v[72:75], v131 offset:512\n\t" \
    "ds_read_b128 v[76:79], v131 offset:768\n\t" \
    "v_mfma_f32_16x16x32_bf16 v[24:27], v[120:123], v[100:103], v[24:27]\n\t" \
    "v_mov_b32 v128, v131\n\t" \
    "v_mov_b32 v129, v132\n\t" \
    "v_mov_b32 v130, v133\n\t" \
    "v_mfma_f32_16x16x32_bf16 v[28:31], v[124:127], v[100:103], v[28:31]\n\t" \
    "v_add_u32 v131, s69, v134\n\t" \
    "v_add_u32 v132, s69, v135\n\t" \
    "v_add_u32 v133, s69, v136\n\t" \
    "v_mfma_f32_16x16x32_bf16 v[32:35], v[112:115], v[104:107], v[32:35]\n\t" \
    "s_add_u32 s68, s68, 49152\n\t" \
    "s_cmp_eq_u32 s68, 147456\n\t" \
    "s_cselect_b32 s68, 0, s68\n\t" \
    "v_mfma_f32_16x16x32_bf16 v[36:39], v[116:119], v[104:107], v[36:39]\n\t" \
    "s_add_u32 s69, s69, 49152\n\t" \
    "s_cmp_eq_u32 s69, 147456\n\t" \
    "s_cselect_b32 s69, 0, s69\n\t" \
    "v_mfma_f32_16x16x32_bf16 v[40:43], v[120:123], v[104:107], v[40:43]\n\t" \
    "v_mfma_f32_16x16x32_bf16 v[44:47], v[124:127], v[104:107], v[44:47]\n\t" \
    "v_mfma_f32_16x16x32_bf16 v[48:51], v[112:115], v[108:111], v[48:51]\n\t" \
    "v_mfma_f32_16x16x32_bf16 v[52:55], v[116:119], v[108:111], v[52:55]\n\t" \
    "v_mfma_f32_16x16x32_bf16 v[56:59], v[120:123], v[108:111], v[56:59]\n\t" \
    "v_mfma_f32_16x16x32_bf16 v[60:63], v[124:127], v[108:111], v[60:63]\n\t" \
    "s_waitcnt lgkmcnt(0)\n\t" \
    "v_mfma_f32_16x16x32_bf16 v[0:3], v[80:83], v[64:67], v[0:3]\n\t" \
    "ds_read_b128 v[96:99], v128 offset:8192\n\t" \
    "ds_read_b64_tr_b16 v[112:113], v129 offset:8192\n\t" \
    "v_mfma_f32_16x16x32_bf16 v[4:7], v[84:87], v[64:67], v[4:7]\n\t" \
    "ds_read_b64_tr_b16 v[114:115], v129 offset:8448\n\t" \
    "ds_read_b64_tr_b16 v[116:117], v130 offset:8192\n\t" \
    "v_mfma_f32_16x16x32_bf16 v[8:11], v[88:91], v[64:67], v[8:11]\n\t" \
    "ds_read_b64_tr_b16 v[118:119], v130 offset:8448\n\t" \
    "ds_read_b128 v[100:103], v128 offset:8448\n\t" \
    "v_mfma_f32_16x16x32_bf16 v[12:15], v[92:95], v[64:67], v[12:15]\n\t" \
    "ds_read_b64_tr_b16 v[120:121], v129 offset:8704\n\t" \
    "ds_read_b64_tr_b16 v[122:123], v129 offset:8960\n\t" \
    "v_mfma_f32_16x16x32_bf16 v[16:19], v[80:83], v[68:71], v[16:19]\n\t" \
    "ds_read_b64_tr_b16 v[124:125], v130 offset:8704\n\t" \
    "ds_read_b64_tr_b16 v[126:127], v130 offset:8960\n\t" \
    "v_mfma_f32_16x16x32_bf16 v[20:23], v[84:87], v[68:71], v[20:23]\n\t" \
    "ds_read_b128 v[104:107], v128 offset:8704\n\t" \
    "ds_read_b128 v[108:111], v128 offset:8960\n\t" \
    "v_mfma_f32_16x16x32_bf16 v[24:27], v[88:91], v[68:71], v[24:27]\n\t" \
    "v_mfma_f32_16x16x32_bf16 v[28:31], v[92:95], v[68:71], v[28:31]\n\t" \
    "v_mfma_f32_16x16x32_bf16 v[32:35], v[80:83], v[72:75], v[32:35]\n\t" \
    "v_mfma_f32_16x16x32_bf16 v[36:39], v[84:87], v[72:75], v[36:39]\n\t" \
    "v_mfma_f32_16x16x32_bf16 v[40:43], v[88:91], v[72:75], v[40:43]\n\t" \
    "v_mfma_f32_16x16x32_bf16 v[44:47], v[92:95], v[72:75], v[44:47]\n\t" \
    "v_mfma_f32_16x16x32_bf16 v[48:51], v[80:83], v[76:79], v[48:51]\n\t" \
    "v_mfma_f32_16x16x32_bf16 v[52:55], v[84:87], v[76:79], v[52:55]\n\t" \
    "v_mfma_f32_16x16x32_bf16 v[56:59], v[88:91], v[76:79], v[56:59]\n\t" \
    "v_mfma_f32_16x16x32_bf16 v[60:63], v[92:95], v[76:79], v[60:63]\n\t" \
    "s_waitcnt vmcnt(0) lgkmcnt(0)\n\t" \
    "s_barrier\n\t" \
    "v_mfma_f32_16x16x32_bf16 v[0:3], v[112:115], v[96:99], v[0:3]\n\t" \
    "ds_read_b128 v[64:67], v131 offset:0\n\t" \
    "ds_read_b64_tr_b16 v[80:81], v132 offset:0\n\t" \
    "v_mfma_f32_16x16x32_bf16 v[4:7], v[116:119], v[96:99], v[4:7]\n\t" \
    "ds_read_b64_tr_b16 v[82:83], v132 offset:256\n\t" \
    "ds_read_b64_tr_b16 v[84:85], v133 offset:0\n\t" \
    "v_mfma_f32_16x16x32_bf16 v[8:11], v[120:123], v[96:99], v[8:11]\n\t" \
    "ds_read_b64_tr_b16 v[86:87], v133 offset:256\n\t" \
    "ds_read_b128 v[68:71], v131 offset:256\n\t" \
    "v_mfma_f32_16x16x32_bf16 v[12:15], v[124:127], v[96:99], v[12:15]\n\t" \
    "ds_read_b64_tr_b16 v[88:89], v132 offset:512\n\t" \
    "ds_read_b64_tr_b16 v[90:91], v132 offset:768\n\t" \
    "v_mfma_f32_16x16x32_bf16 v[16:19], v[112:115], v[100:103], v[16:19]\n\t" \
    "ds_read_b64_tr_b16 v[92:93], v133 offset:512\n\t" \
    "ds_read_b64_tr_b16 v[94:95], v133 offset:768\n\t" \
    "v_mfma_f32_16x16x32_bf16 v[20:23], v[116:119], v[100:103], v[20:23]\n\t" \
    "ds_read_b128 v[72:75], v131 offset:512\n\t" \
    "ds_read_b128 v[76:79], v131 offset:768\n\t" \
    "v_mfma_f32_16x16x32_bf16 v[24:27], v[120:123], v[100:103], v[24:27]\n\t" \
    "v_mov_b32 v128, v131\n\t" \
    "v_mov_b32 v129, v132\n\t" \
    "v_mov_b32 v130, v133\n\t" \
    "v_mfma_f32_16x16x32_bf16 v[28:31], v[124:127], v[100:103], v[28:31]\n\t" \
    "v_add_u32 v131, s69, v134\n\t" \
    "v_add_u32 v132, s69, v135\n\t" \
    "v_add_u32 v133, s69, v136\n\t" \
    "v_mfma_f32_16x16x32_bf16 v[32:35], v[112:115], v[104:107], v[32:35]\n\t" \
    "s_add_u32 s68, s68, 49152\n\t" \
    "s_cmp_eq_u32 s68, 147456\n\t" \
    "s_cselect_b32 s68, 0, s68\n\t" \
    "v_mfma_f32_16x16x32_bf16 v[36:39], v[116:119], v[104:107], v[36:39]\n\t" \
    "s_add_u32 s69, s69, 49152\n\t" \
    "s_cmp_eq_u32 s69, 147456\n\t" \
    "s_cselect_b32 s69, 0, s69\n\t" \
    "v_mfma_f32_16x16x32_bf16 v[40:43], v[120:123], v[104:107], v[40:43]\n\t" \
    "v_mfma_f32_16x16x32_bf16 v[44:47], v[124:127], v[104:107], v[44:47]\n\t" \
    "v_mfma_f32_16x16x32_bf16 v[48:51], v[112:115], v[108:111], v[48:51]\n\t" \
    "v_mfma_f32_16x16x32_bf16 v[52:55], v[116:119], v[108:111], v[52:55]\n\t" \
    "v_mfma_f32_16x16x32_bf16 v[56:59], v[120:123], v[108:111], v[56:59]\n\t" \
    "v_mfma_f32_16x16x32_bf16 v[60:63], v[124:127], v[108:111], v[60:63]\n\t" \
    "s_waitcnt lgkmcnt(0)\n\t" \
    "v_mfma_f32_16x16x32_bf16 v[0:3], v[80:83], v[64:67], v[0:3]\n\t" \
    "ds_read_b128 v[96:99], v128 offset:8192\n\t" \
    "ds_read_b64_tr_b16 v[112:113], v129 offset:8192\n\t" \
    "v_mfma_f32_16x16x32_bf16 v[4:7], v[84:87], v[64:67], v[4:7]\n\t" \
    "ds_read_b64_tr_b16 v[114:115], v129 offset:8448\n\t" \
    "ds_read_b64_tr_b16 v[116:117], v130 offset:8192\n\t" \
    "v_mfma_f32_16x16x32_bf16 v[8:11], v[88:91], v[64:67], v[8:11]\n\t" \
    "ds_read_b64_tr_b16 v[118:119], v130 offset:8448\n\t" \
    "ds_read_b128 v[100:103], v128 offset:8448\n\t" \
    "v_mfma_f32_16x16x32_bf16 v[12:15], v[92:95], v[64:67], v[12:15]\n\t" \
    "ds_read_b64_tr_b16 v[120:121], v129 offset:8704\n\t" \
    "ds_read_b64_tr_b16 v[122:123], v129 offset:8960\n\t" \
    "v_mfma_f32_16x16x32_bf16 v[16:19], v[80:83], v[68:71], v[16:19]\n\t" \
    "ds_read_b64_tr_b16 v[124:125], v130 offset:8704\n\t" \
    "ds_read_b64_tr_b16 v[126:127], v130 offset:8960\n\t" \
    "v_mfma_f32_16x16x32_bf16 v[20:23], v[84:87], v[68:71], v[20:23]\n\t" \
    "ds_read_b128 v[104:107], v128 offset:8704\n\t" \
    "ds_read_b128 v[108:111], v128 offset:8960\n\t" \
    "v_mfma_f32_16x16x32_bf16 v[24:27], v[88:91], v[68:71], v[24:27]\n\t" \
    "v_mfma_f32_16x16x32_bf16 v[28:31], v[92:95], v[68:71], v[28:31]\n\t" \
    "v_mfma_f32_16x16x32_bf16 v[32:35], v[80:83], v[72:75], v[32:35]\n\t" \
    "v_mfma_f32_16x16x32_bf16 v[36:39], v[84:87], v[72:75], v[36:39]\n\t" \
    "v_mfma_f32_16x16x32_bf16 v[40:43], v[88:91], v[72:75], v[40:43]\n\t" \
    "v_mfma_f32_16x16x32_bf16 v[44:47], v[92:95], v[72:75], v[44:47]\n\t" \
    "v_mfma_f32_16x16x32_bf16 v[48:51], v[80:83], v[76:79], v[48:51]\n\t" \
    "v_mfma_f32_16x16x32_bf16 v[52:55], v[84:87], v[76:79], v[52:55]\n\t" \
    "v_mfma_f32_16x16x32_bf16 v[56:59], v[88:91], v[76:79], v[56:59]\n\t" \
    "v_mfma_f32_16x16x32_bf16 v[60:63], v[92:95], v[76:79], v[60:63]\n\t" \
    "s_waitcnt vmcnt(0) lgkmcnt(0)\n\t" \
    "s_barrier\n\t" \
    "v_mfma_f32_16x16x32_bf16 v[0:3], v[112:115], v[96:99], v[0:3]\n\t" \
    "v_mov_b32 v128, v131\n\t" \
    "v_mov_b32 v129, v132\n\t" \
    "v_mfma_f32_16x16x32_bf16 v[4:7], v[116:119], v[96:99], v[4:7]\n\t" \
    "v_mov_b32 v130, v133\n\t" \
    "v_add_u32 v131, s69, v134\n\t" \
    "v_mfma_f32_16x16x32_bf16 v[8:11], v[120:123], v[96:99], v[8:11]\n\t" \
    "v_add_u32 v132, s69, v135\n\t" \
    "v_add_u32 v133, s69, v136\n\t" \
    "v_mfma_f32_16x16x32_bf16 v[12:15], v[124:127], v[96:99], v[12:15]\n\t" \
    "s_add_u32 s68, s68, 49152\n\t" \
    "s_cmp_eq_u32 s68, 147456\n\t" \
    "v_mfma_f32_16x16x32_bf16 v[16:19], v[112:115], v[100:103], v[16:19]\n\t" \
    "s_cselect_b32 s68, 0, s68\n\t" \
    "s_add_u32 s69, s69, 49152\n\t" \
    "v_mfma_f32_16x16x32_bf16 v[20:23], v[116:119], v[100:103], v[20:23]\n\t" \
    "s_cmp_eq_u32 s69, 147456\n\t" \
    "s_cselect_b32 s69, 0, s69\n\t" \
    "v_mfma_f32_16x16x32_bf16 v[24:27], v[120:123], v[100:103], v[24:27]\n\t" \
    "v_mfma_f32_16x16x32_bf16 v[28:31], v[124:127], v[100:103], v[28:31]\n\t" \
    "v_mfma_f32_16x16x32_bf16 v[32:35], v[112:115], v[104:107], v[32:35]\n\t" \
    "v_mfma_f32_16x16x32_bf16 v[36:39], v[116:119], v[104:107], v[36:39]\n\t" \
    "v_mfma_f32_16x16x32_bf16 v[40:43], v[120:123], v[104:107], v[40:43]\n\t" \
    "v_mfma_f32_16x16x32_bf16 v[44:47], v[124:127], v[104:107], v[44:47]\n\t" \
    "v_mfma_f32_16x16x32_bf16 v[48:51], v[112:115], v[108:111], v[48:51]\n\t" \
    "v_mfma_f32_16x16x32_bf16 v[52:55], v[116:119], v[108:111], v[52:55]\n\t" \
    "v_mfma_f32_16x16x32_bf16 v[56:59], v[120:123], v[108:111], v[56:59]\n\t" \
    "v_mfma_f32_16x16x32_bf16 v[60:63], v[124:127], v[108:111], v[60:63]\n\t" \
    "s_nop 15\n\t"
DI u32x4 pack8(const f32x16& c, int n) { return (u32x4){cvtpk(c[4 * n], c[4 * n + 1]), cvtpk(c[4 * n + 2], c[4 * n + 3]), cvtpk(c[4 * n + 4], c[4 * n + 5]), cvtpk(c[4 * n + 6], c[4 * n + 7])}; }
DI void add8(f32x16& c, int n, u32x4 w) {
#pragma unroll
    for (int i = 0; i < 4; ++i) { c[4 * n + 2 * i] += bflo(w[i]); c[4 * n + 2 * i + 1] += bfhi(w[i]); }
}
DI void poll(unsigned* flags, int first, int count, int lane) {
    gu32* f = (gu32*)(flags + (first + (lane % count)) * 32);
    for (int spin = 0; spin < (1 << 20); ++spin) {
        if (__all(__hip_atomic_load(f, __ATOMIC_RELAXED, __HIP_MEMORY_SCOPE_AGENT) == 1u)) break;
        __builtin_amdgcn_s_sleep(1);
    }
}
__global__ __launch_bounds__(512, 2) void k3_out(const char* __restrict__ ctx, const char* __restrict__ H, const bf16_t* __restrict__ wo, const bf16_t* __restrict__ w2,
                                                 const bf16_t* __restrict__ b2, const bf16_t* __restrict__ x, float* part, unsigned* flags, bf16_t* __restrict__ out) {
    extern __shared__ __attribute__((aligned(16))) unsigned char smem[];
    const int tid = threadIdx.x, wid = __builtin_amdgcn_readfirstlane(tid >> 6), lane = tid & 63, wr = wid >> 1, wc = wid & 1, fr = lane & 15, fq = lane >> 4;
    const int xcd = blockIdx.x & 7, idx = blockIdx.x >> 3, sl = xcd >> 1, mt = 4 * (xcd & 1) + (idx >> 3), ntile = idx & 7, tile = mt * 8 + ntile;
    const unsigned lds0 = (unsigned)(uintptr_t)smem;
    const size_t aw = (size_t)(wid & 3) * 4096, bw = (size_t)ntile * 256 + (size_t)(8 * (wid >> 1)) * (DM * 2) + 128 * (wid & 1);
    const int kt0 = sl < 2 ? 32 * sl : 28 * (sl - 2);
    const char* a1 = (sl < 2 ? ctx + (size_t)(2 * mt + (wid >> 2)) * 64 * 16384 : H + (size_t)(2 * mt + (wid >> 2)) * 56 * 16384) + aw + (size_t)kt0 * 16384;
    const char* b1 = (const char*)(sl < 2 ? wo : w2) + bw + (size_t)kt0 * (64 * DM * 2);
    const int nl = (sl < 2 ? 32 : 28) - 3;
    const unsigned va = lane * 16, vb = ((lane & 31) >> 2) * (DM * 2) + (32 * (lane >> 5) + 8 * ((lane & 3) ^ (wid & 2))) * 2;
    const unsigned mA = lds0 + (wid >> 2) * 16384 + (wid & 3) * 4096, mB = lds0 + 32768 + wid * 1024;
    const unsigned ra = lds0 + (wr >> 1) * 16384 + fq * 2048 + ((wr & 1) * 64 + fr) * 16;
    const unsigned rb0 = lds0 + 32768 + fq * 2048 + wc * 1024 + (fr >> 2) * 64 + (fq & 1) * 32 + (fr & 3) * 8, rb1 = rb0 ^ 32;
    f32x16 c0 = f32x16{}, c1 = f32x16{}, c2 = f32x16{}, c3 = f32x16{};
    u32x2 xb[2][4], bb[4];
    if (sl >= 2) {
#pragma unroll
        for (int n = 0; n < 4; ++n) { bb[n] = *(const u32x2*)(b2 + ntile * 128 + wc * 64 + fq * 4 + n * 16);
#pragma unroll
            for (int m = 0; m < 2; ++m) xb[m][n] = *(const u32x2*)(x + (size_t)(mt * 256 + wr * 64 + (2 * (sl - 2) + m) * 16 + fr) * DM + ntile * 128 + wc * 64 + fq * 4 + n * 16); }
    } else {
#pragma unroll
        for (int n = 0; n < 4; ++n) { bb[n] = (u32x2){0u, 0u}; xb[0][n] = (u32x2){0u, 0u}; xb[1][n] = (u32x2){0u, 0u}; }
    }
    STAMP(3, 0);
    const unsigned cpf = code_pf(tid, 52);
    asm volatile(K3_ASM
                 : "+&{v[0:15]}"(c0), "+&{v[16:31]}"(c1), "+&{v[32:47]}"(c2), "+&{v[48:63]}"(c3)
                 : "{v134}"(ra), "{v135}"(rb0), "{v136}"(rb1),
                   [va] "v"(va), [vb] "v"(vb), [a1] "s"(a1), [b1] "s"(b1), [nl] "s"(nl), [mA] "s"(mA), [mB] "s"(mB)
                 : "v64", "v65", "v66", "v67", "v68", "v69", "v70", "v71", "v72", "v73", "v74", "v75", "v76", "v77", "v78", "v79", "v80", "v81", "v82", "v83", "v84", "v85", "v86", "v87", "v88", "v89", "v90", "v91", "v92", "v93", "v94", "v95", "v96", "v97", "v98", "v99", "v100", "v101", "v102", "v103", "v104", "v105", "v106", "v107", "v108", "v109", "v110", "v111", "v112", "v113", "v114", "v115", "v116", "v117", "v118", "v119", "v120", "v121", "v122", "v123", "v124", "v125", "v126", "v127", "v128", "v129", "v130", "v131", "v132", "v133", "s60", "s61", "s62", "s63", "s64", "s65", "s66", "s67", "s68", "s69", "scc", "memory");
    STAMP(3, 1);
    CODE_PF_DONE(cpf);
    const auto rs = __builtin_amdgcn_make_buffer_rsrc((void*)((char*)part + (size_t)tile * 4 * 65536), 0, 4 * 65536, 0x00020000);
#define K3_SEND(c, m) do { __builtin_amdgcn_raw_buffer_store_b128(pack8(c, 0), rs, tid * 16 + (m) * 16384 + sl * 65536, 0, 16); __builtin_amdgcn_raw_buffer_store_b128(pack8(c, 2), rs, tid * 16 + (m) * 16384 + 8192 + sl * 65536, 0, 16); } while (0)
#define K3_GET(src, m, p) __builtin_amdgcn_raw_buffer_load_b128(rs, tid * 16 + (m) * 16384 + (p) * 8192, (src) * 65536, 16)
    gu32* myflag = (gu32*)(flags + (tile * 4 + sl) * 32);
    if (sl < 2) {
        K3_SEND(c0, 0); K3_SEND(c1, 1); K3_SEND(c2, 2); K3_SEND(c3, 3);
        WAIT_V(0);
        __syncthreads();
        if (tid == 0) __hip_atomic_store(myflag, 1u, __ATOMIC_RELAXED, __HIP_MEMORY_SCOPE_AGENT);
        STAMP(3, 2);
        return;
    }
    f32x16 f0, f1;
    const int m0 = 2 * (sl - 2);
    if (sl == 2) { K3_SEND(c2, 2); K3_SEND(c3, 3); f0 = c0; f1 = c1; } else { K3_SEND(c0, 0); K3_SEND(c1, 1); f0 = c2; f1 = c3; }
    WAIT_V(0);
    __syncthreads();
    if (tid == 0) __hip_atomic_store(myflag, 1u, __ATOMIC_RELAXED, __HIP_MEMORY_SCOPE_AGENT);
    STAMP(3, 2);
#pragma unroll
    for (int n = 0; n < 4; ++n) {
        const float b0 = bflo(bb[n][0]), b1_ = bfhi(bb[n][0]), b2_ = bflo(bb[n][1]), b3 = bfhi(bb[n][1]);
        f0[4 * n] += bflo(xb[0][n][0]) + b0; f0[4 * n + 1] += bfhi(xb[0][n][0]) + b1_; f0[4 * n + 2] += bflo(xb[0][n][1]) + b2_; f0[4 * n + 3] += bfhi(xb[0][n][1]) + b3;
        f1[4 * n] += bflo(xb[1][n][0]) + b0; f1[4 * n + 1] += bfhi(xb[1][n][0]) + b1_; f1[4 * n + 2] += bflo(xb[1][n][1]) + b2_; f1[4 * n + 3] += bfhi(xb[1][n][1]) + b3;
    }
    if (wid == 0) poll(flags, tile * 4 + (5 - sl), 1, lane);
    asm volatile("" ::: "memory"); BAR(); asm volatile("" ::: "memory");
    { const u32x4 q0 = K3_GET(5 - sl, m0, 0), q1 = K3_GET(5 - sl, m0, 1), q2 = K3_GET(5 - sl, m0 + 1, 0), q3 = K3_GET(5 - sl, m0 + 1, 1);
      add8(f0, 0, q0); add8(f0, 2, q1); add8(f1, 0, q2); add8(f1, 2, q3); }
    STAMP(3, 3);
    if (wid == 0) poll(flags, tile * 4, 2, lane);
    asm volatile("" ::: "memory"); BAR(); asm volatile("" ::: "memory");
    STAMP(3, 4);
    u32x4 pa[2][2][2];
#pragma unroll
    for (int k = 0; k < 2; ++k)
#pragma unroll
        for (int m = 0; m < 2; ++m)
#pragma unroll
            for (int p = 0; p < 2; ++p) pa[k][m][p] = K3_GET(k, m0 + m, p);
#pragma unroll
    for (int k = 0; k < 2; ++k) { add8(f0, 0, pa[k][0][0]); add8(f0, 2, pa[k][0][1]); add8(f1, 0, pa[k][1][0]); add8(f1, 2, pa[k][1][1]); }
    const int row = mt * 256 + wr * 64 + m0 * 16 + fr;
    const int cg = ((fq & 1) << 1) | (fq >> 1);
    bf16_t* orow = out + (size_t)row * DM + ntile * 128 + wc * 64 + cg * 8;
#define K3_F4(c, n) (f32x4){c[4 * (n)], c[4 * (n) + 1], c[4 * (n) + 2], c[4 * (n) + 3]}
    *(u32x4*)(orow) = k1::pack_swap16(K3_F4(f0, 0), K3_F4(f0, 1)); *(u32x4*)(orow + 32) = k1::pack_swap16(K3_F4(f0, 2), K3_F4(f0, 3));
    *(u32x4*)(orow + 16 * DM) = k1::pack_swap16(K3_F4(f1, 0), K3_F4(f1, 1)); *(u32x4*)(orow + 16 * DM + 32) = k1::pack_swap16(K3_F4(f1, 2), K3_F4(f1, 3));
    STAMP(3, 5);
}
}

extern "C" void kernel_launch(void* const* d_in, const int* in_sizes, int n_in, void* d_out, int out_size, void* d_ws, size_t ws_size, hipStream_t stream) {
    const bf16_t* x = (const bf16_t*)d_in[0]; const bf16_t* ckv = (const bf16_t*)d_in[1];
    const bf16_t* wq = (const bf16_t*)d_in[2]; const bf16_t* wuk = (const bf16_t*)d_in[3]; const bf16_t* wuv = (const bf16_t*)d_in[4]; const bf16_t* wo = (const bf16_t*)d_in[5];
    const bf16_t* w1 = (const bf16_t*)d_in[7]; const bf16_t* b1 = (const bf16_t*)d_in[8]; const bf16_t* w2 = (const bf16_t*)d_in[9]; const bf16_t* b2 = (const bf16_t*)d_in[10];
    char* ws = (char*)d_ws;
    (void)hipFuncSetAttribute((const void*)k1::k1_proj, hipFuncAttributeMaxDynamicSharedMemorySize, k1::LDS_BYTES);
    (void)hipFuncSetAttribute((const void*)k2::k2_attn, hipFuncAttributeMaxDynamicSharedMemorySize, k2::LDS_BYTES);
    (void)hipFuncSetAttribute((const void*)k3::k3_out, hipFuncAttributeMaxDynamicSharedMemorySize, k3::LDS_BYTES);
    k0_ln<<<SEQ / 4, 256, 0, stream>>>(x, (bf16_t*)(ws + WS_XN), (unsigned*)(ws + WS_FLAG));
    k1::k1_proj<<<256, 512, k1::LDS_BYTES, stream>>>(ckv, ws + WS_XN, wuk, wuv, wq, w1, b1, ws + WS_Q, ws + WS_K, ws + WS_VT, ws + WS_H);
    k2::k2_attn<<<256, 512, k2::LDS_BYTES, stream>>>(ws + WS_Q, ws + WS_K, ws + WS_VT, ws + WS_CTX, (const char*)wo, (const char*)w2);
    k3::k3_out<<<256, 512, k3::LDS_BYTES, stream>>>(ws + WS_CTX, ws + WS_H, wo, w2, b2, x, (float*)(ws + WS_PART), (unsigned*)(ws + WS_FLAG), (bf16_t*)d_out);
}
```

```cpp
#include <hip/hip_runtime.h>
#include <cstdint>

#define STAMP(k, i) do {} while (0)
#define LAS __attribute__((address_space(3)))
#define DI __device__ __forceinline__
typedef unsigned short bf16_t;
typedef short bf16x8 __attribute__((ext_vector_type(8)));
typedef short s16x4 __attribute__((ext_vector_type(4)));
typedef short v4i16_t __attribute__((ext_vector_type(4)));
typedef float f32x2 __attribute__((ext_vector_type(2)));
typedef float f32x4 __attribute__((ext_vector_type(4)));
typedef float f32x16 __attribute__((ext_vector_type(16)));
typedef unsigned u32x2 __attribute__((ext_vector_type(2)));
typedef unsigned u32x4 __attribute__((ext_vector_type(4)));
typedef __bf16 bf16x2_t __attribute__((ext_vector_type(2)));
typedef LAS unsigned char* lds_ptr;
typedef LAS const unsigned char* lds_cptr;

constexpr int SEQ = 2048, DM = 1024, NH = 32, HD = 128, DC = 512, FF = 3584, NQ = NH * HD;
constexpr size_t MiB = 1u << 20;
constexpr size_t WS_FLAG = 0;
constexpr size_t WS_XN = 1 * MiB;
constexpr size_t WS_K = 24 * MiB;
constexpr size_t WS_VT = 40 * MiB;
constexpr size_t WS_CTX = 56 * MiB;
constexpr size_t WS_H = 72 * MiB;
constexpr size_t WS_PART = 88 * MiB;

DI unsigned cvtpk(float lo, float hi) { f32x2 v = {lo, hi}; bf16x2_t b = __builtin_convertvector(v, bf16x2_t); return __builtin_bit_cast(unsigned, b); }
DI float bflo(unsigned w) { return __uint_as_float(w << 16); }
DI float bfhi(unsigned w) { return __uint_as_float(w & 0xffff0000u); }
DI s16x4 lds_tr(lds_cptr p) { return __builtin_bit_cast(s16x4, __builtin_amdgcn_ds_read_tr16_b64_v4i16((LAS v4i16_t*)p)); }
DI bf16x8 lds_rd(lds_cptr p) { return *(const LAS bf16x8*)p; }
DI bf16x8 lds_rdv(lds_cptr p) { return *(const volatile LAS bf16x8*)p; }
DI bf16x8 cat8(s16x4 a, s16x4 b) { return __builtin_shufflevector(a, b, 0, 1, 2, 3, 4, 5, 6, 7); }
DI void st16_sc1(void* p, u32x4 v) { asm volatile("global_store_dwordx4 %0, %1, off sc1\n\ts_nop 1" :: "v"(p), "v"(v) : "memory"); }
#define ST16(p, v) st16_sc1((void*)(p), v)
template <bool WT> DI void st16(void* p, u32x4 v) { if constexpr (WT) st16_sc1(p, v); else *(u32x4*)p = v; }
DI unsigned code_pf(unsigned line, unsigned nlines) {
    unsigned long long pc; unsigned d;
    asm volatile("s_getpc_b64 %0" : "=s"(pc));
    asm volatile("global_load_dword %0, %1, %2" : "=v"(d) : "v"((line < nlines ? line : 0u) * 128u), "s"(pc) : "memory");
    return d;
}
#define CODE_PF_DONE(d) asm volatile("" :: "v"(d))
#define SBAR() __builtin_amdgcn_sched_barrier(0)
#define PIN(x) asm volatile("" : "+v"(x))
#define WAIT_V(n) asm volatile("s_waitcnt vmcnt(" #n ")" ::: "memory")
#define WAIT_L(n) asm volatile("s_waitcnt lgkmcnt(" #n ")" ::: "memory")
#define BAR() __builtin_amdgcn_s_barrier()
#define MFMA16(a, b, c) __builtin_amdgcn_mfma_f32_16x16x32_bf16(a, b, c, 0, 0, 0)
#define MFMA32(a, b, c) __builtin_amdgcn_mfma_f32_32x32x16_bf16(a, b, c, 0, 0, 0)
DI void glds(const char* sbase, unsigned voff, unsigned ldsaddr) {
    asm volatile("s_mov_b32 m0, %2\n\ts_nop 0\n\tglobal_load_lds_dwordx4 %0, %1" :: "v"(voff), "s"(sbase), "s"(ldsaddr) : "memory");
}
DI void glds2(const char* sbase, unsigned voff, unsigned ldsaddr) {
    asm volatile("s_mov_b32 m0, %2\n\ts_nop 0\n\tglobal_load_lds_dwordx4 %0, %1\n\tglobal_load_lds_dwordx4 %0, %1 offset:1024" :: "v"(voff), "s"(sbase), "s"(ldsaddr) : "memory");
}

DI float wave_sum(float v) {
    v += __builtin_bit_cast(float, __builtin_amdgcn_update_dpp(0, __builtin_bit_cast(int, v), 0x128, 0xf, 0xf, false));
    v += __builtin_bit_cast(float, __builtin_amdgcn_update_dpp(0, __builtin_bit_cast(int, v), 0x124, 0xf, 0xf, false));
    v += __builtin_bit_cast(float, __builtin_amdgcn_update_dpp(0, __builtin_bit_cast(int, v), 0x122, 0xf, 0xf, false));
    v += __builtin_bit_cast(float, __builtin_amdgcn_update_dpp(0, __builtin_bit_cast(int, v), 0x121, 0xf, 0xf, false));
    { const auto r = __builtin_amdgcn_permlane16_swap(__float_as_uint(v), __float_as_uint(v), false, false); v = __uint_as_float(r[0]) + __uint_as_float(r[1]); }
    { const auto r = __builtin_amdgcn_permlane32_swap(__float_as_uint(v), __float_as_uint(v), false, false); v = __uint_as_float(r[0]) + __uint_as_float(r[1]); }
    return v;
}
__global__ __launch_bounds__(512) void k0_ln(const bf16_t* __restrict__ x, char* __restrict__ xn, unsigned* __restrict__ flags) {
    __shared__ u32x4 tile[8][129];
    const int lane = threadIdx.x & 63, wv = threadIdx.x >> 6, row = blockIdx.x * 8 + wv;
    STAMP(0, 0);
    unsigned cpf = 0; if (blockIdx.x < 8) cpf = code_pf(threadIdx.x, 290);
    const u32x4* src = (const u32x4*)(x + (size_t)row * DM);
    const u32x4 v0 = src[lane], v1 = src[lane + 64];
    float f[16];
#pragma unroll
    for (int i = 0; i < 4; ++i) { f[2 * i] = bflo(v0[i]); f[2 * i + 1] = bfhi(v0[i]); f[8 + 2 * i] = bflo(v1[i]); f[8 + 2 * i + 1] = bfhi(v1[i]); }
    float s = 0.f;
#pragma unroll
    for (int i = 0; i < 16; ++i) s += f[i];
    s = wave_sum(s);
    const float mean = s * (1.0f / DM);
    float q = 0.f;
#pragma unroll
    for (int i = 0; i < 16; ++i) { f[i] -= mean; q += f[i] * f[i]; }
    q = wave_sum(q);
    const float rstd = 1.0f / sqrtf(q * (1.0f / DM) + 1e-5f);
    u32x4 w0, w1;
#pragma unroll
    for (int i = 0; i < 4; ++i) { w0[i] = cvtpk(f[2 * i] * rstd, f[2 * i + 1] * rstd); w1[i] = cvtpk(f[8 + 2 * i] * rstd, f[8 + 2 * i + 1] * rstd); }
    tile[wv][lane] = w0; tile[wv][lane + 64] = w1;
    __syncthreads();
    const int r0 = (blockIdx.x * 8) & 127, mh = blockIdx.x >> 4, r = threadIdx.x & 7;
#pragma unroll
    for (int j = 0; j < 2; ++j) { const int c128 = (threadIdx.x >> 3) + 64 * j; *(u32x4*)(xn + ((size_t)mh * 128 + c128) * 2048 + (r0 + r) * 16) = tile[r][c128]; }
    STAMP(0, 1);
    WAIT_V(0); CODE_PF_DONE(cpf);
    if (blockIdx.x == 0 && threadIdx.x < 256) flags[threadIdx.x * 32] = 0u;
}

namespace k1 {
constexpr int HTB = 16384, LDS_BYTES = 8 * HTB;
enum { T_K = 0, T_V = 1, T_Q = 2, T_H = 3 };
struct Src {
    const char* a; const char* b;
    unsigned aK, aH, aD, bK, bD;
    unsigned va, vb;
};
struct Frag { bf16x8 At[4][2], B0[2][2], B1[2][2]; };

DI f32x2 gelu_tanh2(f32x2 v) {
    const f32x2 z = v * (v * v * -0.10294324f + -2.3022082f);
    f32x2 e; e.x = __builtin_amdgcn_exp2f(z.x); e.y = __builtin_amdgcn_exp2f(z.y);
    e = e + 1.0f;
    f32x2 r; r.x = __builtin_amdgcn_rcpf(e.x); r.y = __builtin_amdgcn_rcpf(e.y);
    return v * r;
}

#define K1_SA(b, h) (((b) * 2 + (h)) * HTB)
#define K1_SB(b, h) ((4 + (b) * 2 + (h)) * HTB)
#define K1_STAGE(bufoff, gbase, voff, d2) do { glds((const char*)(gbase), voff, ldsw + (bufoff)); glds((const char*)(gbase) + (d2), voff, ldsw + (bufoff) + 8192); } while (0)
#define K1_LDA(dst, b, h) do { _Pragma("unroll") for (int m = 0; m < 4; ++m) { dst[m][0] = lds_rd(lds + K1_SA(b, h) + aoff0 + m * MSTEP); dst[m][1] = lds_rd(lds + K1_SA(b, h) + aoff1 + m * MSTEP); } } while (0)
#define K1_LDB(dst, b, h) do { _Pragma("unroll") for (int k = 0; k < 2; ++k) { \
        dst[0][k] = cat8(lds_tr(lds + K1_SB(b, h) + boff0 + k * 8192), lds_tr(lds + K1_SB(b, h) + boff0 + k * 8192 + 256)); \
        dst[1][k] = cat8(lds_tr(lds + K1_SB(b, h) + boff1 + k * 8192), lds_tr(lds + K1_SB(b, h) + boff1 + k * 8192 + 256)); } } while (0)
#define K1_MMA(ai, bj, At, Bt) do { __builtin_amdgcn_s_setprio(1); _Pragma("unroll") for (int m = 0; m < 4; ++m) _Pragma("unroll") for (int n = 0; n < 2; ++n) _Pragma("unroll") for (int k = 0; k < 2; ++k) \
        acc[ai][bj][m][n] = SWAP ? MFMA16(At[m][k], Bt[n][k], acc[ai][bj][m][n]) : MFMA16(Bt[n][k], At[m][k], acc[ai][bj][m][n]); __builtin_amdgcn_s_setprio(0); } while (0)

template <bool SWAP, int MSTEP>
DI void kloop(lds_ptr lds, f32x4 (&acc)[2][2][4][2], const Src& cur, const Src& nxt, int nt, unsigned ldsw, int aoff0, int aoff1, int boff0, int boff1) {
    bf16x8 At[4][2], B0[2][2], B1[2][2];
    for (int t = 0; t < nt; t += 2) {
        const bool last = (t == nt - 2);
        const char* a1h = cur.a + (size_t)(t + 1) * cur.aK + cur.aH;
        const char* a2 = last ? nxt.a : cur.a + (size_t)(t + 2) * cur.aK; const char* b2 = last ? nxt.b : cur.b + (size_t)(t + 2) * cur.bK;
        const unsigned aK = last ? nxt.aK : cur.aK, aH = last ? nxt.aH : cur.aH, aD = last ? nxt.aD : cur.aD, bK = last ? nxt.bK : cur.bK, bD = last ? nxt.bD : cur.bD;
        const unsigned va = last ? nxt.va : cur.va, vb = last ? nxt.vb : cur.vb;
        const char* a3 = a2 + aK; const char* b3 = b2 + bK;
        K1_LDB(B0, 0, 0); K1_LDB(B1, 0, 1); SBAR(); K1_LDA(At, 0, 0); K1_STAGE(K1_SA(1, 1), a1h, cur.va, cur.aD);
        WAIT_V(8); WAIT_L(0); BAR(); K1_MMA(0, 0, At, B0); K1_MMA(0, 1, At, B1); BAR(); SBAR();
        K1_LDA(At, 0, 1); K1_STAGE(K1_SB(0, 0), b2, vb, bD); K1_STAGE(K1_SB(0, 1), b2 + 256, vb, bD); K1_STAGE(K1_SA(0, 0), a2, va, aD);
        WAIT_V(8); WAIT_L(0); BAR(); K1_MMA(1, 0, At, B0); K1_MMA(1, 1, At, B1); BAR(); SBAR();
        K1_LDB(B0, 1, 0); K1_LDB(B1, 1, 1); SBAR(); K1_LDA(At, 1, 0); K1_STAGE(K1_SA(0, 1), a2 + aH, va, aD);
        WAIT_V(8); WAIT_L(0); BAR(); K1_MMA(0, 0, At, B0); K1_MMA(0, 1, At, B1); BAR(); SBAR();
        K1_LDA(At, 1, 1); K1_STAGE(K1_SB(1, 0), b3, vb, bD); K1_STAGE(K1_SB(1, 1), b3 + 256, vb, bD); K1_STAGE(K1_SA(1, 0), a3, va, aD);
        WAIT_V(8); WAIT_L(0); BAR(); K1_MMA(1, 0, At, B0); K1_MMA(1, 1, At, B1); BAR(); SBAR();
    }
}

DI void zero_acc(f32x4 (&acc)[2][2][4][2]) {
#pragma unroll
    for (int a = 0; a < 2; ++a)
#pragma unroll
        for (int b = 0; b < 2; ++b)
#pragma unroll
            for (int m = 0; m < 4; ++m)
#pragma unroll
                for (int n = 0; n < 2; ++n) acc[a][b][m][n] = (f32x4){0.f, 0.f, 0.f, 0.f};
}
DI u32x4 pack_swap16(f32x4 v0, f32x4 v1) {
    const unsigned x0 = cvtpk(v0[0], v0[1]), x1 = cvtpk(v0[2], v0[3]), y0 = cvtpk(v1[0], v1[1]), y1 = cvtpk(v1[2], v1[3]);
    const auto r0 = __builtin_amdgcn_permlane16_swap(x0, y0, false, false), r1 = __builtin_amdgcn_permlane16_swap(x1, y1, false, false);
    return (u32x4){r0[0], r1[0], r0[1], r1[1]};
}
DI u32x4 pack_swap32(f32x4 v0, f32x4 v1) {
    const unsigned x0 = cvtpk(v0[0], v0[1]), x1 = cvtpk(v0[2], v0[3]), y0 = cvtpk(v1[0], v1[1]), y1 = cvtpk(v1[2], v1[3]);
    const auto r0 = __builtin_amdgcn_permlane32_swap(x0, y0, false, false), r1 = __builtin_amdgcn_permlane32_swap(x1, y1, false, false);
    return (u32x4){r0[0], r1[0], r0[1], r1[1]};
}
template <bool WT> DI void epi_qk(const f32x4 (&acc)[2][2][4][2], char* dst, int pm, int pn, float sc, int wr, int wc, int fr, int fq) {
    const int cg = ((fq & 1) << 1) | (fq >> 1);
    char* p = dst + (size_t)(2 * pn * 32 + 4 * pm + wr) * 16384 + (4 * wc + cg) * 1024 + fr * 16;
#pragma unroll
    for (int ai = 0; ai < 2; ++ai)
#pragma unroll
        for (int bj = 0; bj < 2; ++bj)
#pragma unroll
            for (int m = 0; m < 4; ++m)
                st16<WT>(p + (size_t)(bj * 32 + ai * 2) * 16384 + m * 256, pack_swap16(acc[ai][bj][m][0] * sc, acc[ai][bj][m][1] * sc));
}
template <bool WT> DI void epi_v(const f32x4 (&acc)[2][2][4][2], char* dst, int pm, int pn, int wr, int wc, int fr, int fq) {
    char* p = dst + (size_t)(2 * pn * 32 + 4 * pm + wr) * 16384 + fq * 2048 + (32 * wc + fr) * 16;
#pragma unroll
    for (int ai = 0; ai < 2; ++ai)
#pragma unroll
        for (int bj = 0; bj < 2; ++bj)
#pragma unroll
            for (int m = 0; m < 4; m += 2)
#pragma unroll
                for (int n = 0; n < 2; ++n)
                    st16<WT>(p + (size_t)(bj * 32 + ai * 2) * 16384 + m * 4096 + n * 256, pack_swap32(acc[ai][bj][m][n], acc[ai][bj][m + 1][n]));
}
DI void epi_h(const f32x4 (&acc)[2][2][4][2], char* dst, const bf16_t* b1, int pm, int pn, int wr, int wc, int fr, int fq) {
    const int cg = ((fq & 1) << 1) | (fq >> 1);
    f32x4 bv[2][2];
#pragma unroll
    for (int bj = 0; bj < 2; ++bj)
#pragma unroll
        for (int n = 0; n < 2; ++n) { const u32x2 w = *(const u32x2*)(b1 + pn * 256 + bj * 128 + wc * 32 + n * 16 + fq * 4); bv[bj][n] = (f32x4){bflo(w[0]), bfhi(w[0]), bflo(w[1]), bfhi(w[1])}; }
    char* p = dst + ((size_t)(2 * pm * 56 + 4 * pn + (wc >> 1)) * 8 + 4 * (wc & 1) + cg) * 2048 + (64 * wr + fr) * 16;
#pragma unroll
    for (int ai = 0; ai < 2; ++ai)
#pragma unroll
        for (int bj = 0; bj < 2; ++bj)
#pragma unroll
            for (int m = 0; m < 4; ++m) {
                f32x4 v0 = acc[ai][bj][m][0] + bv[bj][0], v1 = acc[ai][bj][m][1] + bv[bj][1];
                { const f32x2 a = gelu_tanh2((f32x2){v0[0], v0[1]}), b = gelu_tanh2((f32x2){v0[2], v0[3]}), c = gelu_tanh2((f32x2){v1[0], v1[1]}), d = gelu_tanh2((f32x2){v1[2], v1[3]});
                  v0 = (f32x4){a.x, a.y, b.x, b.y}; v1 = (f32x4){c.x, c.y, d.x, d.y}; }
                ST16(p + (size_t)(ai * 56 + bj * 2) * 16384 + m * 256, pack_swap16(v0, v1));
            }
}

__global__ __launch_bounds__(512, 2) void k1_proj(const bf16_t* __restrict__ ckv, const char* __restrict__ xn, const bf16_t* __restrict__ wuk, const bf16_t* __restrict__ wuv,
                                                  const bf16_t* __restrict__ w1, const bf16_t* __restrict__ b1,
                                                  char* __restrict__ Ko, char* __restrict__ VTo, char* __restrict__ Ho) {
    extern __shared__ __attribute__((aligned(16))) unsigned char smem[];
    lds_ptr lds = (lds_ptr)smem;
    const int tid = threadIdx.x, wid = __builtin_amdgcn_readfirstlane(tid >> 6), lane = tid & 63, wr = wid >> 2, wc = wid & 3, fr = lane & 15, fq = lane >> 4;
    const int xcd = blockIdx.x & 7, idx = blockIdx.x >> 3;
    if (idx >= 30) return;
    const bool isH = idx >= 16;
    const int kk = 16 * xcd + idx, jj = 14 * xcd + (idx - 16);
    const int pn = isH ? jj >> 3 : kk >> 2, pm0 = isH ? jj & 7 : 2 * (kk & 3);
    const bool isV = !isH && pn >= 16;
    const unsigned ldsw = (unsigned)(uintptr_t)smem + (unsigned)wid * 1024u;
    const int aoff0 = (wr * 64 + fr) * 128 + ((fq ^ (fr >> 1)) * 16), aoff1 = aoff0 ^ 64;
    const int boff0 = fq * 2048 + wc * 512 + (fr >> 2) * 64 + (fq & 1) * 32 + (fr & 3) * 8, boff1 = boff0 ^ 32;
    const unsigned brow = 8 * (wid >> 1) + ((lane & 31) >> 2), bcol = (64 * (wid & 1) + 32 * (lane >> 5) + 8 * ((lane & 3) ^ (wid & 2))) * 2;
    Src u0, u1;
    if (isH) {
        u0.a = xn + (size_t)pm0 * 2 * 16 * 16384; u0.aK = 16384; u0.aH = 16 * 16384; u0.aD = 8192; u0.va = tid * 16;
        u0.b = (const char*)w1 + pn * 512; u0.bK = 64 * FF * 2; u0.bD = 32 * FF * 2; u0.vb = brow * (FF * 2) + bcol;
        u1 = u0;
    } else {
        u0.a = (const char*)ckv + (size_t)pm0 * 256 * DC * 2; u0.aK = 128; u0.aH = 128 * DC * 2; u0.aD = 64 * DC * 2;
        u0.va = (tid >> 3) * (DC * 2) + (((tid & 7) ^ ((tid >> 4) & 7)) * 16);
        u0.b = (const char*)(isV ? wuv : wuk) + (pn & 15) * 512; u0.bK = 64 * NQ * 2; u0.bD = 32 * NQ * 2; u0.vb = brow * (NQ * 2) + bcol;
        u1 = u0; u1.a += (size_t)256 * DC * 2;
    }
    f32x4 acc[2][2][4][2];
    zero_acc(acc);
    STAMP(1, 0);
    const unsigned cpf = code_pf(tid, 120);
    K1_STAGE(K1_SB(0, 0), u0.b, u0.vb, u0.bD); K1_STAGE(K1_SB(0, 1), u0.b + 256, u0.vb, u0.bD); K1_STAGE(K1_SA(0, 0), u0.a, u0.va, u0.aD); K1_STAGE(K1_SA(0, 1), u0.a + u0.aH, u0.va, u0.aD);
    if (wr == 1) BAR();
    WAIT_V(2); BAR();
    CODE_PF_DONE(cpf);
    K1_STAGE(K1_SB(1, 0), u0.b + u0.bK, u0.vb, u0.bD); K1_STAGE(K1_SA(1, 0), u0.a + u0.aK, u0.va, u0.aD); K1_STAGE(K1_SB(1, 1), u0.b + u0.bK + 256, u0.vb, u0.bD);
    WAIT_V(6); BAR();
    STAMP(1, 1);
    if (isH) {
        const int hoff = fq * 2048 + (wr * 64 + fr) * 16;
        kloop<false, 256>(lds, acc, u0, u1, DM / 64, ldsw, hoff, hoff + 8192, boff0, boff1);
        STAMP(1, 2);
        if (wr == 0) BAR();
        STAMP(1, 3); STAMP(1, 4);
        epi_h(acc, Ho, b1, pm0, pn, wr, wc, fr, fq);
    } else if (isV) {
        kloop<true, 2048>(lds, acc, u0, u1, DC / 64, ldsw, aoff0, aoff1, boff0, boff1);
        STAMP(1, 2);
        if (wr == 0) BAR();
        epi_v<false>(acc, VTo, pm0, pn & 15, wr, wc, fr, fq);
        zero_acc(acc);
        STAMP(1, 3);
        if (wr == 1) BAR();
        kloop<true, 2048>(lds, acc, u1, u1, DC / 64, ldsw, aoff0, aoff1, boff0, boff1);
        STAMP(1, 4);
        if (wr == 0) BAR();
        epi_v<true>(acc, VTo, pm0 + 1, pn & 15, wr, wc, fr, fq);
    } else {
        kloop<false, 2048>(lds, acc, u0, u1, DC / 64, ldsw, aoff0, aoff1, boff0, boff1);
        STAMP(1, 2);
        if (wr == 0) BAR();
        epi_qk<false>(acc, Ko, pm0, pn, 1.0f, wr, wc, fr, fq);
        zero_acc(acc);
        STAMP(1, 3);
        if (wr == 1) BAR();
        kloop<false, 2048>(lds, acc, u1, u1, DC / 64, ldsw, aoff0, aoff1, boff0, boff1);
        STAMP(1, 4);
        if (wr == 0) BAR();
        epi_qk<true>(acc, Ko, pm0 + 1, pn, 1.0f, wr, wc, fr, fq);
    }
    STAMP(1, 5);
    WAIT_V(0);
    STAMP(1, 6);
}
#undef K1_SA
#undef K1_SB
#undef K1_STAGE
#undef K1_LDA
#undef K1_LDB
#undef K1_MMA
}

namespace k2 {
#define K2_HEAD (2 * (xcd + 8 * (idx >> 4)) + ((idx >> 3) & 1))
constexpr int SLOT = 16384, LDS_K = 0, LDS_V = 4 * SLOT, LDS_BYTES = 9 * SLOT;
#define K2_ASM \
    "s_nop 1\n\t" \
    "s_cmp_eq_u32 %[n], 0\n\t" \
    "s_cbranch_scc1 TAIL_%=\n\t" \
    "LOOP_%=:\n\t" \
    "s_cmp_lg_u32 %[dmaw], 0\n\t" \
    "s_cbranch_scc1 DMAL_%=\n\t" \
    "DMABL_%=:\n\t" \
    "s_sub_u32 s60, %[t], 8\n\t" \
    "s_and_b32 s60, s60, 0xfffffffb\n\t" \
    "s_or_b32 s60, s60, %[dmaw]\n\t" \
    "s_cmp_eq_u32 s60, 0\n\t" \
    "s_cbranch_scc1 PF_%=\n\t" \
    "PFB_%=:\n\t" \
    "s_waitcnt lgkmcnt(0)\n\t" \
    "v_mfma_f32_32x32x16_bf16 v[112:127], v[168:171], v[64:67], v[96:111]\n\t" \
    "ds_read_b128 v[200:203], v235 offset:8192\n\t" \
    "ds_read_b128 v[204:207], v235 offset:8704\n\t" \
    "v_add_f32 v237, v144, v145\n\t" \
    "v_cvt_pk_bf16_f32 v160, v144, v145\n\t" \
    "v_mfma_f32_32x32x16_bf16 v[112:127], v[172:175], v[68:71], v[112:127]\n\t" \
    "ds_read_b128 v[208:211], v235 offset:9216\n\t" \
    "ds_read_b128 v[212:215], v235 offset:9728\n\t" \
    "v_add_f32 v237, v237, v146\n\t" \
    "v_add_f32 v237, v237, v147\n\t" \
    "v_cvt_pk_bf16_f32 v161, v146, v147\n\t" \
    "v_mfma_f32_32x32x16_bf16 v[112:127], v[176:179], v[72:75], v[112:127]\n\t" \
    "ds_read_b128 v[216:219], v235 offset:12288\n\t" \
    "ds_read_b128 v[220:223], v235 offset:12800\n\t" \
    "v_add_f32 v237, v237, v148\n\t" \
    "v_add_f32 v237, v237, v149\n\t" \
    "v_cvt_pk_bf16_f32 v162, v148, v149\n\t" \
    "v_mfma_f32_32x32x16_bf16 v[112:127], v[180:183], v[76:79], v[112:127]\n\t" \
    "ds_read_b128 v[224:227], v235 offset:13312\n\t" \
    "ds_read_b128 v[228:231], v235 offset:13824\n\t" \
    "v_add_f32 v237, v237, v150\n\t" \
    "v_add_f32 v237, v237, v151\n\t" \
    "v_cvt_pk_bf16_f32 v163, v150, v151\n\t" \
    "v_mfma_f32_32x32x16_bf16 v[112:127], v[184:187], v[80:83], v[112:127]\n\t" \
    "v_add_f32 v237, v237, v152\n\t" \
    "v_add_f32 v237, v237, v153\n\t" \
    "v_cvt_pk_bf16_f32 v164, v152, v153\n\t" \
    "v_mfma_f32_32x32x16_bf16 v[112:127], v[188:191], v[84:87], v[112:127]\n\t" \
    "v_add_f32 v237, v237, v154\n\t" \
    "v_add_f32 v237, v237, v155\n\t" \
    "v_cvt_pk_bf16_f32 v165, v154, v155\n\t" \
    "v_mfma_f32_32x32x16_bf16 v[112:127], v[192:195], v[88:91], v[112:127]\n\t" \
    "v_add_f32 v237, v237, v156\n\t" \
    "v_add_f32 v237, v237, v157\n\t" \
    "v_cvt_pk_bf16_f32 v166, v156, v157\n\t" \
    "v_mfma_f32_32x32x16_bf16 v[112:127], v[196:199], v[92:95], v[112:127]\n\t" \
    "v_add_f32 v237, v237, v158\n\t" \
    "v_add_f32 v237, v237, v159\n\t" \
    "v_cvt_pk_bf16_f32 v167, v158, v159\n\t" \
    "v_cmp_lt_f32 vcc, %[thr], v237\n\t" \
    "s_cbranch_vccnz RAREA_%=\n\t" \
    "BACKA_%=:\n\t" \
    "s_waitcnt lgkmcnt(0)\n\t" \
    "v_mfma_f32_32x32x16_bf16 v[0:15], v[200:203], v[160:163], v[0:15]\n\t" \
    "ds_read_b128 v[168:171], v232 offset:512\n\t" \
    "ds_read_b128 v[172:175], v232 offset:2560\n\t" \
    "v_add_f32 v236, v236, v237\n\t" \
    "v_mfma_f32_32x32x16_bf16 v[16:31], v[204:207], v[160:163], v[16:31]\n\t" \
    "ds_read_b128 v[176:179], v232 offset:4608\n\t" \
    "ds_read_b128 v[180:183], v232 offset:6656\n\t" \
    "v_exp_f32 v144, v112\n\t" \
    "v_exp_f32 v145, v113\n\t" \
    "v_mfma_f32_32x32x16_bf16 v[32:47], v[208:211], v[160:163], v[32:47]\n\t" \
    "ds_read_b128 v[184:187], v232 offset:8704\n\t" \
    "ds_read_b128 v[188:191], v232 offset:10752\n\t" \
    "v_exp_f32 v146, v114\n\t" \
    "v_exp_f32 v147, v115\n\t" \
    "v_mfma_f32_32x32x16_bf16 v[48:63], v[212:215], v[160:163], v[48:63]\n\t" \
    "ds_read_b128 v[192:195], v232 offset:12800\n\t" \
    "ds_read_b128 v[196:199], v232 offset:14848\n\t" \
    "v_exp_f32 v148, v116\n\t" \
    "v_exp_f32 v149, v117\n\t" \
    "v_mfma_f32_32x32x16_bf16 v[0:15], v[216:219], v[164:167], v[0:15]\n\t" \
    "v_exp_f32 v150, v118\n\t" \
    "v_exp_f32 v151, v119\n\t" \
    "v_exp_f32 v152, v120\n\t" \
    "v_mfma_f32_32x32x16_bf16 v[16:31], v[220:223], v[164:167], v[16:31]\n\t" \
    "v_exp_f32 v153, v121\n\t" \
    "v_exp_f32 v154, v122\n\t" \
    "v_exp_f32 v155, v123\n\t" \
    "v_mfma_f32_32x32x16_bf16 v[32:47], v[224:227], v[164:167], v[32:47]\n\t" \
    "v_exp_f32 v156, v124\n\t" \
    "v_exp_f32 v157, v125\n\t" \
    "v_mfma_f32_32x32x16_bf16 v[48:63], v[228:231], v[164:167], v[48:63]\n\t" \
    "v_exp_f32 v158, v126\n\t" \
    "v_exp_f32 v159, v127\n\t" \
    "s_waitcnt lgkmcnt(0)\n\t" \
    "v_mfma_f32_32x32x16_bf16 v[128:143], v[168:171], v[64:67], v[96:111]\n\t" \
    "ds_read_b128 v[200:203], v234 offset:0\n\t" \
    "ds_read_b128 v[204:207], v234 offset:512\n\t" \
    "v_add_f32 v237, v144, v145\n\t" \
    "v_cvt_pk_bf16_f32 v160, v144, v145\n\t" \
    "v_mfma_f32_32x32x16_bf16 v[128:143], v[172:175], v[68:71], v[128:143]\n\t" \
    "ds_read_b128 v[208:211], v234 offset:1024\n\t" \
    "ds_read_b128 v[212:215], v234 offset:1536\n\t" \
    "v_add_f32 v237, v237, v146\n\t" \
    "v_add_f32 v237, v237, v147\n\t" \
    "v_cvt_pk_bf16_f32 v161, v146, v147\n\t" \
    "v_mfma_f32_32x32x16_bf16 v[128:143], v[176:179], v[72:75], v[128:143]\n\t" \
    "ds_read_b128 v[216:219], v234 offset:4096\n\t" \
    "ds_read_b128 v[220:223], v234 offset:4608\n\t" \
    "v_add_f32 v237, v237, v148\n\t" \
    "v_add_f32 v237, v237, v149\n\t" \
    "v_cvt_pk_bf16_f32 v162, v148, v149\n\t" \
    "v_mfma_f32_32x32x16_bf16 v[128:143], v[180:183], v[76:79], v[128:143]\n\t" \
    "ds_read_b128 v[224:227], v234 offset:5120\n\t" \
    "ds_read_b128 v[228:231], v234 offset:5632\n\t" \
    "v_add_f32 v237, v237, v150\n\t" \
    "v_add_f32 v237, v237, v151\n\t" \
    "v_cvt_pk_bf16_f32 v163, v150, v151\n\t" \
    "v_mfma_f32_32x32x16_bf16 v[128:143], v[184:187], v[80:83], v[128:143]\n\t" \
    "v_add_f32 v237, v237, v152\n\t" \
    "v_add_f32 v237, v237, v153\n\t" \
    "v_cvt_pk_bf16_f32 v164, v152, v153\n\t" \
    "v_mfma_f32_32x32x16_bf16 v[128:143], v[188:191], v[84:87], v[128:143]\n\t" \
    "v_add_f32 v237, v237, v154\n\t" \
    "v_add_f32 v237, v237, v155\n\t" \
    "v_cvt_pk_bf16_f32 v165, v154, v155\n\t" \
    "v_mfma_f32_32x32x16_bf16 v[128:143], v[192:195], v[88:91], v[128:143]\n\t" \
    "v_add_f32 v237, v237, v156\n\t" \
    "v_add_f32 v237, v237, v157\n\t" \
    "v_cvt_pk_bf16_f32 v166, v156, v157\n\t" \
    "v_mfma_f32_32x32x16_bf16 v[128:143], v[196:199], v[92:95], v[128:143]\n\t" \
    "v_add_f32 v237, v237, v158\n\t" \
    "v_add_f32 v237, v237, v159\n\t" \
    "v_cvt_pk_bf16_f32 v167, v158, v159\n\t" \
    "v_cmp_lt_f32 vcc, %[thr], v237\n\t" \
    "s_cbranch_vccnz RAREB_%=\n\t" \
    "BACKB_%=:\n\t" \
    "s_waitcnt lgkmcnt(0)\n\t" \
    "v_mfma_f32_32x32x16_bf16 v[0:15], v[200:203], v[160:163], v[0:15]\n\t" \
    "ds_read_b128 v[168:171], v233 offset:0\n\t" \
    "ds_read_b128 v[172:175], v233 offset:2048\n\t" \
    "v_add_f32 v236, v236, v237\n\t" \
    "v_mfma_f32_32x32x16_bf16 v[16:31], v[204:207], v[160:163], v[16:31]\n\t" \
    "ds_read_b128 v[176:179], v233 offset:4096\n\t" \
    "ds_read_b128 v[180:183], v233 offset:6144\n\t" \
    "v_exp_f32 v144, v128\n\t" \
    "v_exp_f32 v145, v129\n\t" \
    "v_mfma_f32_32x32x16_bf16 v[32:47], v[208:211], v[160:163], v[32:47]\n\t" \
    "ds_read_b128 v[184:187], v233 offset:8192\n\t" \
    "ds_read_b128 v[188:191], v233 offset:10240\n\t" \
    "v_exp_f32 v146, v130\n\t" \
    "v_exp_f32 v147, v131\n\t" \
    "v_mfma_f32_32x32x16_bf16 v[48:63], v[212:215], v[160:163], v[48:63]\n\t" \
    "ds_read_b128 v[192:195], v233 offset:12288\n\t" \
    "ds_read_b128 v[196:199], v233 offset:14336\n\t" \
    "v_exp_f32 v148, v132\n\t" \
    "v_exp_f32 v149, v133\n\t" \
    "v_mfma_f32_32x32x16_bf16 v[0:15], v[216:219], v[164:167], v[0:15]\n\t" \
    "v_exp_f32 v150, v134\n\t" \
    "v_exp_f32 v151, v135\n\t" \
    "v_exp_f32 v152, v136\n\t" \
    "v_mfma_f32_32x32x16_bf16 v[16:31], v[220:223], v[164:167], v[16:31]\n\t" \
    "v_exp_f32 v153, v137\n\t" \
    "v_exp_f32 v154, v138\n\t" \
    "v_exp_f32 v155, v139\n\t" \
    "s_add_u32 %[soff], %[soff], 0x4000\n\t" \
    "s_and_b32 %[soff], %[soff], 0xffff\n\t" \
    "v_mfma_f32_32x32x16_bf16 v[32:47], v[224:227], v[164:167], v[32:47]\n\t" \
    "v_exp_f32 v156, v140\n\t" \
    "v_exp_f32 v157, v141\n\t" \
    "v_mov_b32 v235, v234\n\t" \
    "v_mov_b32 v232, v233\n\t" \
    "v_mfma_f32_32x32x16_bf16 v[48:63], v[228:231], v[164:167], v[48:63]\n\t" \
    "v_exp_f32 v158, v142\n\t" \
    "v_exp_f32 v159, v143\n\t" \
    "v_add_u32 v234, %[soff], %[vp0]\n\t" \
    "s_add_u32 s60, %[soff], 0x4000\n\t" \
    "s_and_b32 s60, s60, 0xffff\n\t" \
    "v_add_u32 v233, s60, %[kp0]\n\t" \
    "s_waitcnt vmcnt(8)\n\t" \
    "s_barrier\n\t" \
    "s_add_u32 %[t], %[t], 1\n\t" \
    "s_cmp_lt_u32 %[t], %[n]\n\t" \
    "s_cbranch_scc1 LOOP_%=\n\t" \
    "TAIL_%=:\n\t" \
    "s_cmp_lg_u32 %[dmaw], 0\n\t" \
    "s_cbranch_scc1 DMAT_%=\n\t" \
    "DMABT_%=:\n\t" \
    "s_cmp_eq_u32 %[odd], 0\n\t" \
    "s_cbranch_scc1 EVEN_%=\n\t" \
    "s_waitcnt lgkmcnt(0)\n\t" \
    "v_mfma_f32_32x32x16_bf16 v[112:127], v[168:171], v[64:67], v[96:111]\n\t" \
    "ds_read_b128 v[200:203], v235 offset:8192\n\t" \
    "ds_read_b128 v[204:207], v235 offset:8704\n\t" \
    "v_add_f32 v237, v144, v145\n\t" \
    "v_cvt_pk_bf16_f32 v160, v144, v145\n\t" \
    "v_mfma_f32_32x32x16_bf16 v[112:127], v[172:175], v[68:71], v[112:127]\n\t" \
    "ds_read_b128 v[208:211], v235 offset:9216\n\t" \
    "ds_read_b128 v[212:215], v235 offset:9728\n\t" \
    "v_add_f32 v237, v237, v146\n\t" \
    "v_add_f32 v237, v237, v147\n\t" \
    "v_cvt_pk_bf16_f32 v161, v146, v147\n\t" \
    "v_mfma_f32_32x32x16_bf16 v[112:127], v[176:179], v[72:75], v[112:127]\n\t" \
    "ds_read_b128 v[216:219], v235 offset:12288\n\t" \
    "ds_read_b128 v[220:223], v235 offset:12800\n\t" \
    "v_add_f32 v237, v237, v148\n\t" \
    "v_add_f32 v237, v237, v149\n\t" \
    "v_cvt_pk_bf16_f32 v162, v148, v149\n\t" \
    "v_mfma_f32_32x32x16_bf16 v[112:127], v[180:183], v[76:79], v[112:127]\n\t" \
    "ds_read_b128 v[224:227], v235 offset:13312\n\t" \
    "ds_read_b128 v[228:231], v235 offset:13824\n\t" \
    "v_add_f32 v237, v237, v150\n\t" \
    "v_add_f32 v237, v237, v151\n\t" \
    "v_cvt_pk_bf16_f32 v163, v150, v151\n\t" \
    "v_mfma_f32_32x32x16_bf16 v[112:127], v[184:187], v[80:83], v[112:127]\n\t" \
    "v_add_f32 v237, v237, v152\n\t" \
    "v_add_f32 v237, v237, v153\n\t" \
    "v_cvt_pk_bf16_f32 v164, v152, v153\n\t" \
    "v_mfma_f32_32x32x16_bf16 v[112:127], v[188:191], v[84:87], v[112:127]\n\t" \
    "v_add_f32 v237, v237, v154\n\t" \
    "v_add_f32 v237, v237, v155\n\t" \
    "v_cvt_pk_bf16_f32 v165, v154, v155\n\t" \
    "v_mfma_f32_32x32x16_bf16 v[112:127], v[192:195], v[88:91], v[112:127]\n\t" \
    "v_add_f32 v237, v237, v156\n\t" \
    "v_add_f32 v237, v237, v157\n\t" \
    "v_cvt_pk_bf16_f32 v166, v156, v157\n\t" \
    "v_mfma_f32_32x32x16_bf16 v[112:127], v[196:199], v[92:95], v[112:127]\n\t" \
    "v_add_f32 v237, v237, v158\n\t" \
    "v_add_f32 v237, v237, v159\n\t" \
    "v_cvt_pk_bf16_f32 v167, v158, v159\n\t" \
    "v_cmp_lt_f32 vcc, %[thr], v237\n\t" \
    "s_cbranch_vccnz RAREC_%=\n\t" \
    "BACKC_%=:\n\t" \
    "s_waitcnt lgkmcnt(0)\n\t" \
    "v_mfma_f32_32x32x16_bf16 v[0:15], v[200:203], v[160:163], v[0:15]\n\t" \
    "ds_read_b128 v[168:171], v232 offset:512\n\t" \
    "ds_read_b128 v[172:175], v232 offset:2560\n\t" \
    "v_add_f32 v236, v236, v237\n\t" \
    "v_mfma_f32_32x32x16_bf16 v[16:31], v[204:207], v[160:163], v[16:31]\n\t" \
    "ds_read_b128 v[176:179], v232 offset:4608\n\t" \
    "ds_read_b128 v[180:183], v232 offset:6656\n\t" \
    "v_exp_f32 v144, v112\n\t" \
    "v_exp_f32 v145, v113\n\t" \
    "v_mfma_f32_32x32x16_bf16 v[32:47], v[208:211], v[160:163], v[32:47]\n\t" \
    "ds_read_b128 v[184:187], v232 offset:8704\n\t" \
    "ds_read_b128 v[188:191], v232 offset:10752\n\t" \
    "v_exp_f32 v146, v114\n\t" \
    "v_exp_f32 v147, v115\n\t" \
    "v_mfma_f32_32x32x16_bf16 v[48:63], v[212:215], v[160:163], v[48:63]\n\t" \
    "ds_read_b128 v[192:195], v232 offset:12800\n\t" \
    "ds_read_b128 v[196:199], v232 offset:14848\n\t" \
    "v_exp_f32 v148, v116\n\t" \
    "v_exp_f32 v149, v117\n\t" \
    "v_mfma_f32_32x32x16_bf16 v[0:15], v[216:219], v[164:167], v[0:15]\n\t" \
    "v_exp_f32 v150, v118\n\t" \
    "v_exp_f32 v151, v119\n\t" \
    "v_exp_f32 v152, v120\n\t" \
    "v_mfma_f32_32x32x16_bf16 v[16:31], v[220:223], v[164:167], v[16:31]\n\t" \
    "v_exp_f32 v153, v121\n\t" \
    "v_exp_f32 v154, v122\n\t" \
    "v_exp_f32 v155, v123\n\t" \
    "v_mfma_f32_32x32x16_bf16 v[32:47], v[224:227], v[164:167], v[32:47]\n\t" \
    "v_exp_f32 v156, v124\n\t" \
    "v_exp_f32 v157, v125\n\t" \
    "v_mfma_f32_32x32x16_bf16 v[48:63], v[228:231], v[164:167], v[48:63]\n\t" \
    "v_exp_f32 v158, v126\n\t" \
    "v_exp_f32 v159, v127\n\t" \
    "v_mov_b32 v239, 0xff800000\n\t" \
    "v_cmp_gt_i32 vcc, 0, %[dq]\n\t" \
    "s_nop 1\n\t" \
    "v_cndmask_b32 v96, v96, v239, vcc\n\t" \
    "v_cmp_gt_i32 vcc, 1, %[dq]\n\t" \
    "s_nop 1\n\t" \
    "v_cndmask_b32 v97, v97, v239, vcc\n\t" \
    "v_cmp_gt_i32 vcc, 2, %[dq]\n\t" \
    "s_nop 1\n\t" \
    "v_cndmask_b32 v98, v98, v239, vcc\n\t" \
    "v_cmp_gt_i32 vcc, 3, %[dq]\n\t" \
    "s_nop 1\n\t" \
    "v_cndmask_b32 v99, v99, v239, vcc\n\t" \
    "v_cmp_gt_i32 vcc, 8, %[dq]\n\t" \
    "s_nop 1\n\t" \
    "v_cndmask_b32 v100, v100, v239, vcc\n\t" \
    "v_cmp_gt_i32 vcc, 9, %[dq]\n\t" \
    "s_nop 1\n\t" \
    "v_cndmask_b32 v101, v101, v239, vcc\n\t" \
    "v_cmp_gt_i32 vcc, 10, %[dq]\n\t" \
    "s_nop 1\n\t" \
    "v_cndmask_b32 v102, v102, v239, vcc\n\t" \
    "v_cmp_gt_i32 vcc, 11, %[dq]\n\t" \
    "s_nop 1\n\t" \
    "v_cndmask_b32 v103, v103, v239, vcc\n\t" \
    "v_cmp_gt_i32 vcc, 16, %[dq]\n\t" \
    "s_nop 1\n\t" \
    "v_cndmask_b32 v104, v104, v239, vcc\n\t" \
    "v_cmp_gt_i32 vcc, 17, %[dq]\n\t" \
    "s_nop 1\n\t" \
    "v_cndmask_b32 v105, v105, v239, vcc\n\t" \
    "v_cmp_gt_i32 vcc, 18, %[dq]\n\t" \
    "s_nop 1\n\t" \
    "v_cndmask_b32 v106, v106, v239, vcc\n\t" \
    "v_cmp_gt_i32 vcc, 19, %[dq]\n\t" \
    "s_nop 1\n\t" \
    "v_cndmask_b32 v107, v107, v239, vcc\n\t" \
    "v_cmp_gt_i32 vcc, 24, %[dq]\n\t" \
    "s_nop 1\n\t" \
    "v_cndmask_b32 v108, v108, v239, vcc\n\t" \
    "v_cmp_gt_i32 vcc, 25, %[dq]\n\t" \
    "s_nop 1\n\t" \
    "v_cndmask_b32 v109, v109, v239, vcc\n\t" \
    "v_cmp_gt_i32 vcc, 26, %[dq]\n\t" \
    "s_nop 1\n\t" \
    "v_cndmask_b32 v110, v110, v239, vcc\n\t" \
    "v_cmp_gt_i32 vcc, 27, %[dq]\n\t" \
    "s_nop 1\n\t" \
    "v_cndmask_b32 v111, v111, v239, vcc\n\t" \
    "s_nop 1\n\t" \
    "s_waitcnt lgkmcnt(0)\n\t" \
    "v_mfma_f32_32x32x16_bf16 v[128:143], v[168:171], v[64:67], v[96:111]\n\t" \
    "ds_read_b128 v[200:203], v234 offset:0\n\t" \
    "ds_read_b128 v[204:207], v234 offset:512\n\t" \
    "v_add_f32 v237, v144, v145\n\t" \
    "v_cvt_pk_bf16_f32 v160, v144, v145\n\t" \
    "v_mfma_f32_32x32x16_bf16 v[128:143], v[172:175], v[68:71], v[128:143]\n\t" \
    "ds_read_b128 v[208:211], v234 offset:1024\n\t" \
    "ds_read_b128 v[212:215], v234 offset:1536\n\t" \
    "v_add_f32 v237, v237, v146\n\t" \
    "v_add_f32 v237, v237, v147\n\t" \
    "v_cvt_pk_bf16_f32 v161, v146, v147\n\t" \
    "v_mfma_f32_32x32x16_bf16 v[128:143], v[176:179], v[72:75], v[128:143]\n\t" \
    "ds_read_b128 v[216:219], v234 offset:4096\n\t" \
    "ds_read_b128 v[220:223], v234 offset:4608\n\t" \
    "v_add_f32 v237, v237, v148\n\t" \
    "v_add_f32 v237, v237, v149\n\t" \
    "v_cvt_pk_bf16_f32 v162, v148, v149\n\t" \
    "v_mfma_f32_32x32x16_bf16 v[128:143], v[180:183], v[76:79], v[128:143]\n\t" \
    "ds_read_b128 v[224:227], v234 offset:5120\n\t" \
    "ds_read_b128 v[228:231], v234 offset:5632\n\t" \
    "v_add_f32 v237, v237, v150\n\t" \
    "v_add_f32 v237, v237, v151\n\t" \
    "v_cvt_pk_bf16_f32 v163, v150, v151\n\t" \
    "v_mfma_f32_32x32x16_bf16 v[128:143], v[184:187], v[80:83], v[128:143]\n\t" \
    "v_add_f32 v237, v237, v152\n\t" \
    "v_add_f32 v237, v237, v153\n\t" \
    "v_cvt_pk_bf16_f32 v164, v152, v153\n\t" \
    "v_mfma_f32_32x32x16_bf16 v[128:143], v[188:191], v[84:87], v[128:143]\n\t" \
    "v_add_f32 v237, v237, v154\n\t" \
    "v_add_f32 v237, v237, v155\n\t" \
    "v_cvt_pk_bf16_f32 v165, v154, v155\n\t" \
    "v_mfma_f32_32x32x16_bf16 v[128:143], v[192:195], v[88:91], v[128:143]\n\t" \
    "v_add_f32 v237, v237, v156\n\t" \
    "v_add_f32 v237, v237, v157\n\t" \
    "v_cvt_pk_bf16_f32 v166, v156, v157\n\t" \
    "v_mfma_f32_32x32x16_bf16 v[128:143], v[196:199], v[92:95], v[128:143]\n\t" \
    "v_add_f32 v237, v237, v158\n\t" \
    "v_add_f32 v237, v237, v159\n\t" \
    "v_cvt_pk_bf16_f32 v167, v158, v159\n\t" \
    "v_cmp_lt_f32 vcc, %[thr], v237\n\t" \
    "s_cbranch_vccnz RARED_%=\n\t" \
    "BACKD_%=:\n\t" \
    "s_waitcnt lgkmcnt(0)\n\t" \
    "v_mfma_f32_32x32x16_bf16 v[0:15], v[200:203], v[160:163], v[0:15]\n\t" \
    "ds_read_b128 v[168:171], v233 offset:0\n\t" \
    "ds_read_b128 v[172:175], v233 offset:2048\n\t" \
    "v_add_f32 v236, v236, v237\n\t" \
    "v_mfma_f32_32x32x16_bf16 v[16:31], v[204:207], v[160:163], v[16:31]\n\t" \
    "ds_read_b128 v[176:179], v233 offset:4096\n\t" \
    "ds_read_b128 v[180:183], v233 offset:6144\n\t" \
    "v_exp_f32 v144, v128\n\t" \
    "v_exp_f32 v145, v129\n\t" \
    "v_mfma_f32_32x32x16_bf16 v[32:47], v[208:211], v[160:163], v[32:47]\n\t" \
    "ds_read_b128 v[184:187], v233 offset:8192\n\t" \
    "ds_read_b128 v[188:191], v233 offset:10240\n\t" \
    "v_exp_f32 v146, v130\n\t" \
    "v_exp_f32 v147, v131\n\t" \
    "v_mfma_f32_32x32x16_bf16 v[48:63], v[212:215], v[160:163], v[48:63]\n\t" \
    "ds_read_b128 v[192:195], v233 offset:12288\n\t" \
    "ds_read_b128 v[196:199], v233 offset:14336\n\t" \
    "v_exp_f32 v148, v132\n\t" \
    "v_exp_f32 v149, v133\n\t" \
    "v_mfma_f32_32x32x16_bf16 v[0:15], v[216:219], v[164:167], v[0:15]\n\t" \
    "v_exp_f32 v150, v134\n\t" \
    "v_exp_f32 v151, v135\n\t" \
    "v_exp_f32 v152, v136\n\t" \
    "v_mfma_f32_32x32x16_bf16 v[16:31], v[220:223], v[164:167], v[16:31]\n\t" \
    "v_exp_f32 v153, v137\n\t" \
    "v_exp_f32 v154, v138\n\t" \
    "v_exp_f32 v155, v139\n\t" \
    "v_mfma_f32_32x32x16_bf16 v[32:47], v[224:227], v[164:167], v[32:47]\n\t" \
    "v_exp_f32 v156, v140\n\t" \
    "v_exp_f32 v157, v141\n\t" \
    "v_mfma_f32_32x32x16_bf16 v[48:63], v[228:231], v[164:167], v[48:63]\n\t" \
    "v_exp_f32 v158, v142\n\t" \
    "v_exp_f32 v159, v143\n\t" \
    "s_waitcnt lgkmcnt(0)\n\t" \
    "ds_read_b128 v[200:203], v234 offset:8192\n\t" \
    "ds_read_b128 v[204:207], v234 offset:8704\n\t" \
    "ds_read_b128 v[208:211], v234 offset:9216\n\t" \
    "ds_read_b128 v[212:215], v234 offset:9728\n\t" \
    "ds_read_b128 v[216:219], v234 offset:12288\n\t" \
    "ds_read_b128 v[220:223], v234 offset:12800\n\t" \
    "ds_read_b128 v[224:227], v234 offset:13312\n\t" \
    "ds_read_b128 v[228:231], v234 offset:13824\n\t" \
    "v_add_f32 v237, v144, v145\n\t" \
    "v_cvt_pk_bf16_f32 v160, v144, v145\n\t" \
    "v_add_f32 v237, v237, v146\n\t" \
    "v_add_f32 v237, v237, v147\n\t" \
    "v_cvt_pk_bf16_f32 v161, v146, v147\n\t" \
    "v_add_f32 v237, v237, v148\n\t" \
    "v_add_f32 v237, v237, v149\n\t" \
    "v_cvt_pk_bf16_f32 v162, v148, v149\n\t" \
    "v_add_f32 v237, v237, v150\n\t" \
    "v_add_f32 v237, v237, v151\n\t" \
    "v_cvt_pk_bf16_f32 v163, v150, v151\n\t" \
    "v_add_f32 v237, v237, v152\n\t" \
    "v_add_f32 v237, v237, v153\n\t" \
    "v_cvt_pk_bf16_f32 v164, v152, v153\n\t" \
    "v_add_f32 v237, v237, v154\n\t" \
    "v_add_f32 v237, v237, v155\n\t" \
    "v_cvt_pk_bf16_f32 v165, v154, v155\n\t" \
    "v_add_f32 v237, v237, v156\n\t" \
    "v_add_f32 v237, v237, v157\n\t" \
    "v_cvt_pk_bf16_f32 v166, v156, v157\n\t" \
    "v_add_f32 v237, v237, v158\n\t" \
    "v_add_f32 v237, v237, v159\n\t" \
    "v_cvt_pk_bf16_f32 v167, v158, v159\n\t" \
    "v_cmp_lt_f32 vcc, %[thr], v237\n\t" \
    "s_cbranch_vccnz RAREE_%=\n\t" \
    "BACKE_%=:\n\t" \
    "s_waitcnt lgkmcnt(0)\n\t" \
    "v_add_f32 v236, v236, v237\n\t" \
    "v_mfma_f32_32x32x16_bf16 v[0:15], v[200:203], v[160:163], v[0:15]\n\t" \
    "v_mfma_f32_32x32x16_bf16 v[16:31], v[204:207], v[160:163], v[16:31]\n\t" \
    "v_mfma_f32_32x32x16_bf16 v[32:47], v[208:211], v[160:163], v[32:47]\n\t" \
    "v_mfma_f32_32x32x16_bf16 v[48:63], v[212:215], v[160:163], v[48:63]\n\t" \
    "v_mfma_f32_32x32x16_bf16 v[0:15], v[216:219], v[164:167], v[0:15]\n\t" \
    "v_mfma_f32_32x32x16_bf16 v[16:31], v[220:223], v[164:167], v[16:31]\n\t" \
    "v_mfma_f32_32x32x16_bf16 v[32:47], v[224:227], v[164:167], v[32:47]\n\t" \
    "v_mfma_f32_32x32x16_bf16 v[48:63], v[228:231], v[164:167], v[48:63]\n\t" \
    "s_branch END_%=\n\t" \
    "EVEN_%=:\n\t" \
    "v_mov_b32 v239, 0xff800000\n\t" \
    "v_cmp_gt_i32 vcc, 0, %[dq]\n\t" \
    "s_nop 1\n\t" \
    "v_cndmask_b32 v96, v96, v239, vcc\n\t" \
    "v_cmp_gt_i32 vcc, 1, %[dq]\n\t" \
    "s_nop 1\n\t" \
    "v_cndmask_b32 v97, v97, v239, vcc\n\t" \
    "v_cmp_gt_i32 vcc, 2, %[dq]\n\t" \
    "s_nop 1\n\t" \
    "v_cndmask_b32 v98, v98, v239, vcc\n\t" \
    "v_cmp_gt_i32 vcc, 3, %[dq]\n\t" \
    "s_nop 1\n\t" \
    "v_cndmask_b32 v99, v99, v239, vcc\n\t" \
    "v_cmp_gt_i32 vcc, 8, %[dq]\n\t" \
    "s_nop 1\n\t" \
    "v_cndmask_b32 v100, v100, v239, vcc\n\t" \
    "v_cmp_gt_i32 vcc, 9, %[dq]\n\t" \
    "s_nop 1\n\t" \
    "v_cndmask_b32 v101, v101, v239, vcc\n\t" \
    "v_cmp_gt_i32 vcc, 10, %[dq]\n\t" \
    "s_nop 1\n\t" \
    "v_cndmask_b32 v102, v102, v239, vcc\n\t" \
    "v_cmp_gt_i32 vcc, 11, %[dq]\n\t" \
    "s_nop 1\n\t" \
    "v_cndmask_b32 v103, v103, v239, vcc\n\t" \
    "v_cmp_gt_i32 vcc, 16, %[dq]\n\t" \
    "s_nop 1\n\t" \
    "v_cndmask_b32 v104, v104, v239, vcc\n\t" \
    "v_cmp_gt_i32 vcc, 17, %[dq]\n\t" \
    "s_nop 1\n\t" \
    "v_cndmask_b32 v105, v105, v239, vcc\n\t" \
    "v_cmp_gt_i32 vcc, 18, %[dq]\n\t" \
    "s_nop 1\n\t" \
    "v_cndmask_b32 v106, v106, v239, vcc\n\t" \
    "v_cmp_gt_i32 vcc, 19, %[dq]\n\t" \
    "s_nop 1\n\t" \
    "v_cndmask_b32 v107, v107, v239, vcc\n\t" \
    "v_cmp_gt_i32 vcc, 24, %[dq]\n\t" \
    "s_nop 1\n\t" \
    "v_cndmask_b32 v108, v108, v239, vcc\n\t" \
    "v_cmp_gt_i32 vcc, 25, %[dq]\n\t" \
    "s_nop 1\n\t" \
    "v_cndmask_b32 v109, v109, v239, vcc\n\t" \
    "v_cmp_gt_i32 vcc, 26, %[dq]\n\t" \
    "s_nop 1\n\t" \
    "v_cndmask_b32 v110, v110, v239, vcc\n\t" \
    "v_cmp_gt_i32 vcc, 27, %[dq]\n\t" \
    "s_nop 1\n\t" \
    "v_cndmask_b32 v111, v111, v239, vcc\n\t" \
    "s_nop 1\n\t" \
    "s_waitcnt lgkmcnt(0)\n\t" \
    "v_mfma_f32_32x32x16_bf16 v[112:127], v[168:171], v[64:67], v[96:111]\n\t" \
    "ds_read_b128 v[200:203], v235 offset:8192\n\t" \
    "ds_read_b128 v[204:207], v235 offset:8704\n\t" \
    "v_add_f32 v237, v144, v145\n\t" \
    "v_cvt_pk_bf16_f32 v160, v144, v145\n\t" \
    "v_mfma_f32_32x32x16_bf16 v[112:127], v[172:175], v[68:71], v[112:127]\n\t" \
    "ds_read_b128 v[208:211], v235 offset:9216\n\t" \
    "ds_read_b128 v[212:215], v235 offset:9728\n\t" \
    "v_add_f32 v237, v237, v146\n\t" \
    "v_add_f32 v237, v237, v147\n\t" \
    "v_cvt_pk_bf16_f32 v161, v146, v147\n\t" \
    "v_mfma_f32_32x32x16_bf16 v[112:127], v[176:179], v[72:75], v[112:127]\n\t" \
    "ds_read_b128 v[216:219], v235 offset:12288\n\t" \
    "ds_read_b128 v[220:223], v235 offset:12800\n\t" \
    "v_add_f32 v237, v237, v148\n\t" \
    "v_add_f32 v237, v237, v149\n\t" \
    "v_cvt_pk_bf16_f32 v162, v148, v149\n\t" \
    "v_mfma_f32_32x32x16_bf16 v[112:127], v[180:183], v[76:79], v[112:127]\n\t" \
    "ds_read_b128 v[224:227], v235 offset:13312\n\t" \
    "ds_read_b128 v[228:231], v235 offset:13824\n\t" \
    "v_add_f32 v237, v237, v150\n\t" \
    "v_add_f32 v237, v237, v151\n\t" \
    "v_cvt_pk_bf16_f32 v163, v150, v151\n\t" \
    "v_mfma_f32_32x32x16_bf16 v[112:127], v[184:187], v[80:83], v[112:127]\n\t" \
    "v_add_f32 v237, v237, v152\n\t" \
    "v_add_f32 v237, v237, v153\n\t" \
    "v_cvt_pk_bf16_f32 v164, v152, v153\n\t" \
    "v_mfma_f32_32x32x16_bf16 v[112:127], v[188:191], v[84:87], v[112:127]\n\t" \
    "v_add_f32 v237, v237, v154\n\t" \
    "v_add_f32 v237, v237, v155\n\t" \
    "v_cvt_pk_bf16_f32 v165, v154, v155\n\t" \
    "v_mfma_f32_32x32x16_bf16 v[112:127], v[192:195], v[88:91], v[112:127]\n\t" \
    "v_add_f32 v237, v237, v156\n\t" \
    "v_add_f32 v237, v237, v157\n\t" \
    "v_cvt_pk_bf16_f32 v166, v156, v157\n\t" \
    "v_mfma_f32_32x32x16_bf16 v[112:127], v[196:199], v[92:95], v[112:127]\n\t" \
    "v_add_f32 v237, v237, v158\n\t" \
    "v_add_f32 v237, v237, v159\n\t" \
    "v_cvt_pk_bf16_f32 v167, v158, v159\n\t" \
    "v_cmp_lt_f32 vcc, %[thr], v237\n\t" \
    "s_cbranch_vccnz RAREF_%=\n\t" \
    "BACKF_%=:\n\t" \
    "s_waitcnt lgkmcnt(0)\n\t" \
    "v_mfma_f32_32x32x16_bf16 v[0:15], v[200:203], v[160:163], v[0:15]\n\t" \
    "ds_read_b128 v[168:171], v232 offset:512\n\t" \
    "ds_read_b128 v[172:175], v232 offset:2560\n\t" \
    "v_add_f32 v236, v236, v237\n\t" \
    "v_mfma_f32_32x32x16_bf16 v[16:31], v[204:207], v[160:163], v[16:31]\n\t" \
    "ds_read_b128 v[176:179], v232 offset:4608\n\t" \
    "ds_read_b128 v[180:183], v232 offset:6656\n\t" \
    "v_exp_f32 v144, v112\n\t" \
    "v_exp_f32 v145, v113\n\t" \
    "v_mfma_f32_32x32x16_bf16 v[32:47], v[208:211], v[160:163], v[32:47]\n\t" \
    "ds_read_b128 v[184:187], v232 offset:8704\n\t" \
    "ds_read_b128 v[188:191], v232 offset:10752\n\t" \
    "v_exp_f32 v146, v114\n\t" \
    "v_exp_f32 v147, v115\n\t" \
    "v_mfma_f32_32x32x16_bf16 v[48:63], v[212:215], v[160:163], v[48:63]\n\t" \
    "ds_read_b128 v[192:195], v232 offset:12800\n\t" \
    "ds_read_b128 v[196:199], v232 offset:14848\n\t" \
    "v_exp_f32 v148, v116\n\t" \
    "v_exp_f32 v149, v117\n\t" \
    "v_mfma_f32_32x32x16_bf16 v[0:15], v[216:219], v[164:167], v[0:15]\n\t" \
    "v_exp_f32 v150, v118\n\t" \
    "v_exp_f32 v151, v119\n\t" \
    "v_exp_f32 v152, v120\n\t" \
    "v_mfma_f32_32x32x16_bf16 v[16:31], v[220:223], v[164:167], v[16:31]\n\t" \
    "v_exp_f32 v153, v121\n\t" \
    "v_exp_f32 v154, v122\n\t" \
    "v_exp_f32 v155, v123\n\t" \
    "v_mfma_f32_32x32x16_bf16 v[32:47], v[224:227], v[164:167], v[32:47]\n\t" \
    "v_exp_f32 v156, v124\n\t" \
    "v_exp_f32 v157, v125\n\t" \
    "v_mfma_f32_32x32x16_bf16 v[48:63], v[228:231], v[164:167], v[48:63]\n\t" \
    "v_exp_f32 v158, v126\n\t" \
    "v_exp_f32 v159, v127\n\t" \
    "s_waitcnt lgkmcnt(0)\n\t" \
    "ds_read_b128 v[200:203], v234 offset:0\n\t" \
    "ds_read_b128 v[204:207], v234 offset:512\n\t" \
    "ds_read_b128 v[208:211], v234 offset:1024\n\t" \
    "ds_read_b128 v[212:215], v234 offset:1536\n\t" \
    "ds_read_b128 v[216:219], v234 offset:4096\n\t" \
    "ds_read_b128 v[220:223], v234 offset:4608\n\t" \
    "ds_read_b128 v[224:227], v234 offset:5120\n\t" \
    "ds_read_b128 v[228:231], v234 offset:5632\n\t" \
    "v_add_f32 v237, v144, v145\n\t" \
    "v_cvt_pk_bf16_f32 v160, v144, v145\n\t" \
    "v_add_f32 v237, v237, v146\n\t" \
    "v_add_f32 v237, v237, v147\n\t" \
    "v_cvt_pk_bf16_f32 v161, v146, v147\n\t" \
    "v_add_f32 v237, v237, v148\n\t" \
    "v_add_f32 v237, v237, v149\n\t" \
    "v_cvt_pk_bf16_f32 v162, v148, v149\n\t" \
    "v_add_f32 v237, v237, v150\n\t" \
    "v_add_f32 v237, v237, v151\n\t" \
    "v_cvt_pk_bf16_f32 v163, v150, v151\n\t" \
    "v_add_f32 v237, v237, v152\n\t" \
    "v_add_f32 v237, v237, v153\n\t" \
    "v_cvt_pk_bf16_f32 v164, v152, v153\n\t" \
    "v_add_f32 v237, v237, v154\n\t" \
    "v_add_f32 v237, v237, v155\n\t" \
    "v_cvt_pk_bf16_f32 v165, v154, v155\n\t" \
    "v_add_f32 v237, v237, v156\n\t" \
    "v_add_f32 v237, v237, v157\n\t" \
    "v_cvt_pk_bf16_f32 v166, v156, v157\n\t" \
    "v_add_f32 v237, v237, v158\n\t" \
    "v_add_f32 v237, v237, v159\n\t" \
    "v_cvt_pk_bf16_f32 v167, v158, v159\n\t" \
    "v_cmp_lt_f32 vcc, %[thr], v237\n\t" \
    "s_cbranch_vccnz RAREG_%=\n\t" \
    "BACKG_%=:\n\t" \
    "s_waitcnt lgkmcnt(0)\n\t" \
    "v_add_f32 v236, v236, v237\n\t" \
    "v_mfma_f32_32x32x16_bf16 v[0:15], v[200:203], v[160:163], v[0:15]\n\t" \
    "v_mfma_f32_32x32x16_bf16 v[16:31], v[204:207], v[160:163], v[16:31]\n\t" \
    "v_mfma_f32_32x32x16_bf16 v[32:47], v[208:211], v[160:163], v[32:47]\n\t" \
    "v_mfma_f32_32x32x16_bf16 v[48:63], v[212:215], v[160:163], v[48:63]\n\t" \
    "v_mfma_f32_32x32x16_bf16 v[0:15], v[216:219], v[164:167], v[0:15]\n\t" \
    "v_mfma_f32_32x32x16_bf16 v[16:31], v[220:223], v[164:167], v[16:31]\n\t" \
    "v_mfma_f32_32x32x16_bf16 v[32:47], v[224:227], v[164:167], v[32:47]\n\t" \
    "v_mfma_f32_32x32x16_bf16 v[48:63], v[228:231], v[164:167], v[48:63]\n\t" \
    "s_branch END_%=\n\t" \
    "PF_%=:\n\t" \
    "s_cmp_eq_u32 %[t], 8\n\t" \
    "s_cbranch_scc0 PF2_%=\n\t" \
    "global_load_dword v250, %[pfo], %[wo]\n\t" \
    "s_branch PFB_%=\n\t" \
    "PF2_%=:\n\t" \
    "global_load_dword v251, %[pfo2], %[w2]\n\t" \
    "s_branch PFB_%=\n\t" \
    "DMAL_%=:\n\t" \
    "s_add_u32 s60, %[t], 3\n\t" \
    "s_min_u32 s60, s60, %[ntm1]\n\t" \
    "s_lshl_b32 s60, s60, 14\n\t" \
    "s_add_u32 s62, %[khl], s60\n\t" \
    "s_addc_u32 s63, %[khh], 0\n\t" \
    "s_add_u32 s61, %[soff], 0xc000\n\t" \
    "s_and_b32 s61, s61, 0xffff\n\t" \
    "s_add_u32 m0, s61, %[kdst]\n\t" \
    "s_nop 0\n\t" \
    "global_load_lds_dwordx4 %[dvo], s[62:63] offset:0\n\t" \
    "global_load_lds_dwordx4 %[dvo], s[62:63] offset:1024\n\t" \
    "global_load_lds_dwordx4 %[dvo], s[62:63] offset:2048\n\t" \
    "global_load_lds_dwordx4 %[dvo], s[62:63] offset:3072\n\t" \
    "s_add_u32 s60, %[t], 2\n\t" \
    "s_min_u32 s60, s60, %[ntm1]\n\t" \
    "s_lshl_b32 s60, s60, 14\n\t" \
    "s_add_u32 s62, %[vhl], s60\n\t" \
    "s_addc_u32 s63, %[vhh], 0\n\t" \
    "s_add_u32 s61, %[soff], 0x8000\n\t" \
    "s_and_b32 s61, s61, 0xffff\n\t" \
    "s_add_u32 m0, s61, %[vdst]\n\t" \
    "s_nop 0\n\t" \
    "global_load_lds_dwordx4 %[dvo], s[62:63] offset:0\n\t" \
    "global_load_lds_dwordx4 %[dvo], s[62:63] offset:1024\n\t" \
    "global_load_lds_dwordx4 %[dvo], s[62:63] offset:2048\n\t" \
    "global_load_lds_dwordx4 %[dvo], s[62:63] offset:3072\n\t" \
    "s_branch DMABL_%=\n\t" \
    "DMAT_%=:\n\t" \
    "s_add_u32 s60, %[t], 3\n\t" \
    "s_min_u32 s60, s60, %[ntm1]\n\t" \
    "s_lshl_b32 s60, s60, 14\n\t" \
    "s_add_u32 s62, %[khl], s60\n\t" \
    "s_addc_u32 s63, %[khh], 0\n\t" \
    "s_add_u32 s61, %[soff], 0xc000\n\t" \
    "s_and_b32 s61, s61, 0xffff\n\t" \
    "s_add_u32 m0, s61, %[kdst]\n\t" \
    "s_nop 0\n\t" \
    "global_load_lds_dwordx4 %[dvo], s[62:63] offset:0\n\t" \
    "global_load_lds_dwordx4 %[dvo], s[62:63] offset:1024\n\t" \
    "global_load_lds_dwordx4 %[dvo], s[62:63] offset:2048\n\t" \
    "global_load_lds_dwordx4 %[dvo], s[62:63] offset:3072\n\t" \
    "s_add_u32 s60, %[t], 2\n\t" \
    "s_min_u32 s60, s60, %[ntm1]\n\t" \
    "s_lshl_b32 s60, s60, 14\n\t" \
    "s_add_u32 s62, %[vhl], s60\n\t" \
    "s_addc_u32 s63, %[vhh], 0\n\t" \
    "s_add_u32 s61, %[soff], 0x8000\n\t" \
    "s_and_b32 s61, s61, 0xffff\n\t" \
    "s_add_u32 m0, s61, %[vdst]\n\t" \
    "s_nop 0\n\t" \
    "global_load_lds_dwordx4 %[dvo], s[62:63] offset:0\n\t" \
    "global_load_lds_dwordx4 %[dvo], s[62:63] offset:1024\n\t" \
    "global_load_lds_dwordx4 %[dvo], s[62:63] offset:2048\n\t" \
    "global_load_lds_dwordx4 %[dvo], s[62:63] offset:3072\n\t" \
    "s_branch DMABT_%=\n\t" \
    "RAREA_%=:\n\t" \
    "s_nop 15\n\t" \
    "v_max3_f32 v239, v128, v129, v130\n\t" \
    "v_max3_f32 v239, v239, v131, v132\n\t" \
    "v_max3_f32 v239, v239, v133, v134\n\t" \
    "v_max3_f32 v239, v239, v135, v136\n\t" \
    "v_max3_f32 v239, v239, v137, v138\n\t" \
    "v_max3_f32 v239, v239, v139, v140\n\t" \
    "v_max3_f32 v239, v239, v141, v142\n\t" \
    "v_max_f32 v239, v239, v143\n\t" \
    "v_max_f32 v239, 0, v239\n\t" \
    "v_mov_b32 v240, v239\n\t" \
    "s_nop 1\n\t" \
    "v_permlane32_swap_b32 v239, v240\n\t" \
    "s_nop 1\n\t" \
    "v_max_f32 v239, v239, v240\n\t" \
    "v_add_f32 v238, v238, v239\n\t" \
    "v_exp_f32 v240, -v239\n\t" \
    "v_sub_f32 v241, 0, v238\n\t" \
    "v_sub_f32 v128, v128, v239\n\t" \
    "v_sub_f32 v112, v112, v239\n\t" \
    "v_mov_b32 v96, v241\n\t" \
    "v_sub_f32 v129, v129, v239\n\t" \
    "v_sub_f32 v113, v113, v239\n\t" \
    "v_mov_b32 v97, v241\n\t" \
    "v_sub_f32 v130, v130, v239\n\t" \
    "v_sub_f32 v114, v114, v239\n\t" \
    "v_mov_b32 v98, v241\n\t" \
    "v_sub_f32 v131, v131, v239\n\t" \
    "v_sub_f32 v115, v115, v239\n\t" \
    "v_mov_b32 v99, v241\n\t" \
    "v_sub_f32 v132, v132, v239\n\t" \
    "v_sub_f32 v116, v116, v239\n\t" \
    "v_mov_b32 v100, v241\n\t" \
    "v_sub_f32 v133, v133, v239\n\t" \
    "v_sub_f32 v117, v117, v239\n\t" \
    "v_mov_b32 v101, v241\n\t" \
    "v_sub_f32 v134, v134, v239\n\t" \
    "v_sub_f32 v118, v118, v239\n\t" \
    "v_mov_b32 v102, v241\n\t" \
    "v_sub_f32 v135, v135, v239\n\t" \
    "v_sub_f32 v119, v119, v239\n\t" \
    "v_mov_b32 v103, v241\n\t" \
    "v_sub_f32 v136, v136, v239\n\t" \
    "v_sub_f32 v120, v120, v239\n\t" \
    "v_mov_b32 v104, v241\n\t" \
    "v_sub_f32 v137, v137, v239\n\t" \
    "v_sub_f32 v121, v121, v239\n\t" \
    "v_mov_b32 v105, v241\n\t" \
    "v_sub_f32 v138, v138, v239\n\t" \
    "v_sub_f32 v122, v122, v239\n\t" \
    "v_mov_b32 v106, v241\n\t" \
    "v_sub_f32 v139, v139, v239\n\t" \
    "v_sub_f32 v123, v123, v239\n\t" \
    "v_mov_b32 v107, v241\n\t" \
    "v_sub_f32 v140, v140, v239\n\t" \
    "v_sub_f32 v124, v124, v239\n\t" \
    "v_mov_b32 v108, v241\n\t" \
    "v_sub_f32 v141, v141, v239\n\t" \
    "v_sub_f32 v125, v125, v239\n\t" \
    "v_mov_b32 v109, v241\n\t" \
    "v_sub_f32 v142, v142, v239\n\t" \
    "v_sub_f32 v126, v126, v239\n\t" \
    "v_mov_b32 v110, v241\n\t" \
    "v_sub_f32 v143, v143, v239\n\t" \
    "v_sub_f32 v127, v127, v239\n\t" \
    "v_mov_b32 v111, v241\n\t" \
    "v_exp_f32 v144, v128\n\t" \
    "v_exp_f32 v145, v129\n\t" \
    "v_exp_f32 v146, v130\n\t" \
    "v_exp_f32 v147, v131\n\t" \
    "v_exp_f32 v148, v132\n\t" \
    "v_exp_f32 v149, v133\n\t" \
    "v_exp_f32 v150, v134\n\t" \
    "v_exp_f32 v151, v135\n\t" \
    "v_exp_f32 v152, v136\n\t" \
    "v_exp_f32 v153, v137\n\t" \
    "v_exp_f32 v154, v138\n\t" \
    "v_exp_f32 v155, v139\n\t" \
    "v_exp_f32 v156, v140\n\t" \
    "v_exp_f32 v157, v141\n\t" \
    "v_exp_f32 v158, v142\n\t" \
    "v_exp_f32 v159, v143\n\t" \
    "v_mul_f32 v236, v236, v240\n\t" \
    "v_mul_f32 v0, v0, v240\n\t" \
    "v_mul_f32 v1, v1, v240\n\t" \
    "v_mul_f32 v2, v2, v240\n\t" \
    "v_mul_f32 v3, v3, v240\n\t" \
    "v_mul_f32 v4, v4, v240\n\t" \
    "v_mul_f32 v5, v5, v240\n\t" \
    "v_mul_f32 v6, v6, v240\n\t" \
    "v_mul_f32 v7, v7, v240\n\t" \
    "v_mul_f32 v8, v8, v240\n\t" \
    "v_mul_f32 v9, v9, v240\n\t" \
    "v_mul_f32 v10, v10, v240\n\t" \
    "v_mul_f32 v11, v11, v240\n\t" \
    "v_mul_f32 v12, v12, v240\n\t" \
    "v_mul_f32 v13, v13, v240\n\t" \
    "v_mul_f32 v14, v14, v240\n\t" \
    "v_mul_f32 v15, v15, v240\n\t" \
    "v_mul_f32 v16, v16, v240\n\t" \
    "v_mul_f32 v17, v17, v240\n\t" \
    "v_mul_f32 v18, v18, v240\n\t" \
    "v_mul_f32 v19, v19, v240\n\t" \
    "v_mul_f32 v20, v20, v240\n\t" \
    "v_mul_f32 v21, v21, v240\n\t" \
    "v_mul_f32 v22, v22, v240\n\t" \
    "v_mul_f32 v23, v23, v240\n\t" \
    "v_mul_f32 v24, v24, v240\n\t" \
    "v_mul_f32 v25, v25, v240\n\t" \
    "v_mul_f32 v26, v26, v240\n\t" \
    "v_mul_f32 v27, v27, v240\n\t" \
    "v_mul_f32 v28, v28, v240\n\t" \
    "v_mul_f32 v29, v29, v240\n\t" \
    "v_mul_f32 v30, v30, v240\n\t" \
    "v_mul_f32 v31, v31, v240\n\t" \
    "v_mul_f32 v32, v32, v240\n\t" \
    "v_mul_f32 v33, v33, v240\n\t" \
    "v_mul_f32 v34, v34, v240\n\t" \
    "v_mul_f32 v35, v35, v240\n\t" \
    "v_mul_f32 v36, v36, v240\n\t" \
    "v_mul_f32 v37, v37, v240\n\t" \
    "v_mul_f32 v38, v38, v240\n\t" \
    "v_mul_f32 v39, v39, v240\n\t" \
    "v_mul_f32 v40, v40, v240\n\t" \
    "v_mul_f32 v41, v41, v240\n\t" \
    "v_mul_f32 v42, v42, v240\n\t" \
    "v_mul_f32 v43, v43, v240\n\t" \
    "v_mul_f32 v44, v44, v240\n\t" \
    "v_mul_f32 v45, v45, v240\n\t" \
    "v_mul_f32 v46, v46, v240\n\t" \
    "v_mul_f32 v47, v47, v240\n\t" \
    "v_mul_f32 v48, v48, v240\n\t" \
    "v_mul_f32 v49, v49, v240\n\t" \
    "v_mul_f32 v50, v50, v240\n\t" \
    "v_mul_f32 v51, v51, v240\n\t" \
    "v_mul_f32 v52, v52, v240\n\t" \
    "v_mul_f32 v53, v53, v240\n\t" \
    "v_mul_f32 v54, v54, v240\n\t" \
    "v_mul_f32 v55, v55, v240\n\t" \
    "v_mul_f32 v56, v56, v240\n\t" \
    "v_mul_f32 v57, v57, v240\n\t" \
    "v_mul_f32 v58, v58, v240\n\t" \
    "v_mul_f32 v59, v59, v240\n\t" \
    "v_mul_f32 v60, v60, v240\n\t" \
    "v_mul_f32 v61, v61, v240\n\t" \
    "v_mul_f32 v62, v62, v240\n\t" \
    "v_mul_f32 v63, v63, v240\n\t" \
    "v_add_f32 v237, v144, v145\n\t" \
    "v_add_f32 v237, v237, v146\n\t" \
    "v_add_f32 v237, v237, v147\n\t" \
    "v_add_f32 v237, v237, v148\n\t" \
    "v_add_f32 v237, v237, v149\n\t" \
    "v_add_f32 v237, v237, v150\n\t" \
    "v_add_f32 v237, v237, v151\n\t" \
    "v_add_f32 v237, v237, v152\n\t" \
    "v_add_f32 v237, v237, v153\n\t" \
    "v_add_f32 v237, v237, v154\n\t" \
    "v_add_f32 v237, v237, v155\n\t" \
    "v_add_f32 v237, v237, v156\n\t" \
    "v_add_f32 v237, v237, v157\n\t" \
    "v_add_f32 v237, v237, v158\n\t" \
    "v_add_f32 v237, v237, v159\n\t" \
    "v_cvt_pk_bf16_f32 v160, v144, v145\n\t" \
    "v_cvt_pk_bf16_f32 v161, v146, v147\n\t" \
    "v_cvt_pk_bf16_f32 v162, v148, v149\n\t" \
    "v_cvt_pk_bf16_f32 v163, v150, v151\n\t" \
    "v_cvt_pk_bf16_f32 v164, v152, v153\n\t" \
    "v_cvt_pk_bf16_f32 v165, v154, v155\n\t" \
    "v_cvt_pk_bf16_f32 v166, v156, v157\n\t" \
    "v_cvt_pk_bf16_f32 v167, v158, v159\n\t" \
    "s_nop 1\n\t" \
    "s_branch BACKA_%=\n\t" \
    "RAREB_%=:\n\t" \
    "s_nop 15\n\t" \
    "v_max3_f32 v239, v112, v113, v114\n\t" \
    "v_max3_f32 v239, v239, v115, v116\n\t" \
    "v_max3_f32 v239, v239, v117, v118\n\t" \
    "v_max3_f32 v239, v239, v119, v120\n\t" \
    "v_max3_f32 v239, v239, v121, v122\n\t" \
    "v_max3_f32 v239, v239, v123, v124\n\t" \
    "v_max3_f32 v239, v239, v125, v126\n\t" \
    "v_max_f32 v239, v239, v127\n\t" \
    "v_max_f32 v239, 0, v239\n\t" \
    "v_mov_b32 v240, v239\n\t" \
    "s_nop 1\n\t" \
    "v_permlane32_swap_b32 v239, v240\n\t" \
    "s_nop 1\n\t" \
    "v_max_f32 v239, v239, v240\n\t" \
    "v_add_f32 v238, v238, v239\n\t" \
    "v_exp_f32 v240, -v239\n\t" \
    "v_sub_f32 v241, 0, v238\n\t" \
    "v_sub_f32 v112, v112, v239\n\t" \
    "v_sub_f32 v128, v128, v239\n\t" \
    "v_mov_b32 v96, v241\n\t" \
    "v_sub_f32 v113, v113, v239\n\t" \
    "v_sub_f32 v129, v129, v239\n\t" \
    "v_mov_b32 v97, v241\n\t" \
    "v_sub_f32 v114, v114, v239\n\t" \
    "v_sub_f32 v130, v130, v239\n\t" \
    "v_mov_b32 v98, v241\n\t" \
    "v_sub_f32 v115, v115, v239\n\t" \
    "v_sub_f32 v131, v131, v239\n\t" \
    "v_mov_b32 v99, v241\n\t" \
    "v_sub_f32 v116, v116, v239\n\t" \
    "v_sub_f32 v132, v132, v239\n\t" \
    "v_mov_b32 v100, v241\n\t" \
    "v_sub_f32 v117, v117, v239\n\t" \
    "v_sub_f32 v133, v133, v239\n\t" \
    "v_mov_b32 v101, v241\n\t" \
    "v_sub_f32 v118, v118, v239\n\t" \
    "v_sub_f32 v134, v134, v239\n\t" \
    "v_mov_b32 v102, v241\n\t" \
    "v_sub_f32 v119, v119, v239\n\t" \
    "v_sub_f32 v135, v135, v239\n\t" \
    "v_mov_b32 v103, v241\n\t" \
    "v_sub_f32 v120, v120, v239\n\t" \
    "v_sub_f32 v136, v136, v239\n\t" \
    "v_mov_b32 v104, v241\n\t" \
    "v_sub_f32 v121, v121, v239\n\t" \
    "v_sub_f32 v137, v137, v239\n\t" \
    "v_mov_b32 v105, v241\n\t" \
    "v_sub_f32 v122, v122, v239\n\t" \
    "v_sub_f32 v138, v138, v239\n\t" \
    "v_mov_b32 v106, v241\n\t" \
    "v_sub_f32 v123, v123, v239\n\t" \
    "v_sub_f32 v139, v139, v239\n\t" \
    "v_mov_b32 v107, v241\n\t" \
    "v_sub_f32 v124, v124, v239\n\t" \
    "v_sub_f32 v140, v140, v239\n\t" \
    "v_mov_b32 v108, v241\n\t" \
    "v_sub_f32 v125, v125, v239\n\t" \
    "v_sub_f32 v141, v141, v239\n\t" \
    "v_mov_b32 v109, v241\n\t" \
    "v_sub_f32 v126, v126, v239\n\t" \
    "v_sub_f32 v142, v142, v239\n\t" \
    "v_mov_b32 v110, v241\n\t" \
    "v_sub_f32 v127, v127, v239\n\t" \
    "v_sub_f32 v143, v143, v239\n\t" \
    "v_mov_b32 v111, v241\n\t" \
    "v_exp_f32 v144, v112\n\t" \
    "v_exp_f32 v145, v113\n\t" \
    "v_exp_f32 v146, v114\n\t" \
    "v_exp_f32 v147, v115\n\t" \
    "v_exp_f32 v148, v116\n\t" \
    "v_exp_f32 v149, v117\n\t" \
    "v_exp_f32 v150, v118\n\t" \
    "v_exp_f32 v151, v119\n\t" \
    "v_exp_f32 v152, v120\n\t" \
    "v_exp_f32 v153, v121\n\t" \
    "v_exp_f32 v154, v122\n\t" \
    "v_exp_f32 v155, v123\n\t" \
    "v_exp_f32 v156, v124\n\t" \
    "v_exp_f32 v157, v125\n\t" \
    "v_exp_f32 v158, v126\n\t" \
    "v_exp_f32 v159, v127\n\t" \
    "v_mul_f32 v236, v236, v240\n\t" \
    "v_mul_f32 v0, v0, v240\n\t" \
    "v_mul_f32 v1, v1, v240\n\t" \
    "v_mul_f32 v2, v2, v240\n\t" \
    "v_mul_f32 v3, v3, v240\n\t" \
    "v_mul_f32 v4, v4, v240\n\t" \
    "v_mul_f32 v5, v5, v240\n\t" \
    "v_mul_f32 v6, v6, v240\n\t" \
    "v_mul_f32 v7, v7, v240\n\t" \
    "v_mul_f32 v8, v8, v240\n\t" \
    "v_mul_f32 v9, v9, v240\n\t" \
    "v_mul_f32 v10, v10, v240\n\t" \
    "v_mul_f32 v11, v11, v240\n\t" \
    "v_mul_f32 v12, v12, v240\n\t" \
    "v_mul_f32 v13, v13, v240\n\t" \
    "v_mul_f32 v14, v14, v240\n\t" \
    "v_mul_f32 v15, v15, v240\n\t" \
    "v_mul_f32 v16, v16, v240\n\t" \
    "v_mul_f32 v17, v17, v240\n\t" \
    "v_mul_f32 v18, v18, v240\n\t" \
    "v_mul_f32 v19, v19, v240\n\t" \
    "v_mul_f32 v20, v20, v240\n\t" \
    "v_mul_f32 v21, v21, v240\n\t" \
    "v_mul_f32 v22, v22, v240\n\t" \
    "v_mul_f32 v23, v23, v240\n\t" \
    "v_mul_f32 v24, v24, v240\n\t" \
    "v_mul_f32 v25, v25, v240\n\t" \
    "v_mul_f32 v26, v26, v240\n\t" \
    "v_mul_f32 v27, v27, v240\n\t" \
    "v_mul_f32 v28, v28, v240\n\t" \
    "v_mul_f32 v29, v29, v240\n\t" \
    "v_mul_f32 v30, v30, v240\n\t" \
    "v_mul_f32 v31, v31, v240\n\t" \
    "v_mul_f32 v32, v32, v240\n\t" \
    "v_mul_f32 v33, v33, v240\n\t" \
    "v_mul_f32 v34, v34, v240\n\t" \
    "v_mul_f32 v35, v35, v240\n\t" \
    "v_mul_f32 v36, v36, v240\n\t" \
    "v_mul_f32 v37, v37, v240\n\t" \
    "v_mul_f32 v38, v38, v240\n\t" \
    "v_mul_f32 v39, v39, v240\n\t" \
    "v_mul_f32 v40, v40, v240\n\t" \
    "v_mul_f32 v41, v41, v240\n\t" \
    "v_mul_f32 v42, v42, v240\n\t" \
    "v_mul_f32 v43, v43, v240\n\t" \
    "v_mul_f32 v44, v44, v240\n\t" \
    "v_mul_f32 v45, v45, v240\n\t" \
    "v_mul_f32 v46, v46, v240\n\t" \
    "v_mul_f32 v47, v47, v240\n\t" \
    "v_mul_f32 v48, v48, v240\n\t" \
    "v_mul_f32 v49, v49, v240\n\t" \
    "v_mul_f32 v50, v50, v240\n\t" \
    "v_mul_f32 v51, v51, v240\n\t" \
    "v_mul_f32 v52, v52, v240\n\t" \
    "v_mul_f32 v53, v53, v240\n\t" \
    "v_mul_f32 v54, v54, v240\n\t" \
    "v_mul_f32 v55, v55, v240\n\t" \
    "v_mul_f32 v56, v56, v240\n\t" \
    "v_mul_f32 v57, v57, v240\n\t" \
    "v_mul_f32 v58, v58, v240\n\t" \
    "v_mul_f32 v59, v59, v240\n\t" \
    "v_mul_f32 v60, v60, v240\n\t" \
    "v_mul_f32 v61, v61, v240\n\t" \
    "v_mul_f32 v62, v62, v240\n\t" \
    "v_mul_f32 v63, v63, v240\n\t" \
    "v_add_f32 v237, v144, v145\n\t" \
    "v_add_f32 v237, v237, v146\n\t" \
    "v_add_f32 v237, v237, v147\n\t" \
    "v_add_f32 v237, v237, v148\n\t" \
    "v_add_f32 v237, v237, v149\n\t" \
    "v_add_f32 v237, v237, v150\n\t" \
    "v_add_f32 v237, v237, v151\n\t" \
    "v_add_f32 v237, v237, v152\n\t" \
    "v_add_f32 v237, v237, v153\n\t" \
    "v_add_f32 v237, v237, v154\n\t" \
    "v_add_f32 v237, v237, v155\n\t" \
    "v_add_f32 v237, v237, v156\n\t" \
    "v_add_f32 v237, v237, v157\n\t" \
    "v_add_f32 v237, v237, v158\n\t" \
    "v_add_f32 v237, v237, v159\n\t" \
    "v_cvt_pk_bf16_f32 v160, v144, v145\n\t" \
    "v_cvt_pk_bf16_f32 v161, v146, v147\n\t" \
    "v_cvt_pk_bf16_f32 v162, v148, v149\n\t" \
    "v_cvt_pk_bf16_f32 v163, v150, v151\n\t" \
    "v_cvt_pk_bf16_f32 v164, v152, v153\n\t" \
    "v_cvt_pk_bf16_f32 v165, v154, v155\n\t" \
    "v_cvt_pk_bf16_f32 v166, v156, v157\n\t" \
    "v_cvt_pk_bf16_f32 v167, v158, v159\n\t" \
    "s_nop 1\n\t" \
    "s_branch BACKB_%=\n\t" \
    "RAREC_%=:\n\t" \
    "s_nop 15\n\t" \
    "v_max3_f32 v239, v128, v129, v130\n\t" \
    "v_max3_f32 v239, v239, v131, v132\n\t" \
    "v_max3_f32 v239, v239, v133, v134\n\t" \
    "v_max3_f32 v239, v239, v135, v136\n\t" \
    "v_max3_f32 v239, v239, v137, v138\n\t" \
    "v_max3_f32 v239, v239, v139, v140\n\t" \
    "v_max3_f32 v239, v239, v141, v142\n\t" \
    "v_max_f32 v239, v239, v143\n\t" \
    "v_max_f32 v239, 0, v239\n\t" \
    "v_mov_b32 v240, v239\n\t" \
    "s_nop 1\n\t" \
    "v_permlane32_swap_b32 v239, v240\n\t" \
    "s_nop 1\n\t" \
    "v_max_f32 v239, v239, v240\n\t" \
    "v_add_f32 v238, v238, v239\n\t" \
    "v_exp_f32 v240, -v239\n\t" \
    "v_sub_f32 v241, 0, v238\n\t" \
    "v_sub_f32 v128, v128, v239\n\t" \
    "v_sub_f32 v112, v112, v239\n\t" \
    "v_mov_b32 v96, v241\n\t" \
    "v_sub_f32 v129, v129, v239\n\t" \
    "v_sub_f32 v113, v113, v239\n\t" \
    "v_mov_b32 v97, v241\n\t" \
    "v_sub_f32 v130, v130, v239\n\t" \
    "v_sub_f32 v114, v114, v239\n\t" \
    "v_mov_b32 v98, v241\n\t" \
    "v_sub_f32 v131, v131, v239\n\t" \
    "v_sub_f32 v115, v115, v239\n\t" \
    "v_mov_b32 v99, v241\n\t" \
    "v_sub_f32 v132, v132, v239\n\t" \
    "v_sub_f32 v116, v116, v239\n\t" \
    "v_mov_b32 v100, v241\n\t" \
    "v_sub_f32 v133, v133, v239\n\t" \
    "v_sub_f32 v117, v117, v239\n\t" \
    "v_mov_b32 v101, v241\n\t" \
    "v_sub_f32 v134, v134, v239\n\t" \
    "v_sub_f32 v118, v118, v239\n\t" \
    "v_mov_b32 v102, v241\n\t" \
    "v_sub_f32 v135, v135, v239\n\t" \
    "v_sub_f32 v119, v119, v239\n\t" \
    "v_mov_b32 v103, v241\n\t" \
    "v_sub_f32 v136, v136, v239\n\t" \
    "v_sub_f32 v120, v120, v239\n\t" \
    "v_mov_b32 v104, v241\n\t" \
    "v_sub_f32 v137, v137, v239\n\t" \
    "v_sub_f32 v121, v121, v239\n\t" \
    "v_mov_b32 v105, v241\n\t" \
    "v_sub_f32 v138, v138, v239\n\t" \
    "v_sub_f32 v122, v122, v239\n\t" \
    "v_mov_b32 v106, v241\n\t" \
    "v_sub_f32 v139, v139, v239\n\t" \
    "v_sub_f32 v123, v123, v239\n\t" \
    "v_mov_b32 v107, v241\n\t" \
    "v_sub_f32 v140, v140, v239\n\t" \
    "v_sub_f32 v124, v124, v239\n\t" \
    "v_mov_b32 v108, v241\n\t" \
    "v_sub_f32 v141, v141, v239\n\t" \
    "v_sub_f32 v125, v125, v239\n\t" \
    "v_mov_b32 v109, v241\n\t" \
    "v_sub_f32 v142, v142, v239\n\t" \
    "v_sub_f32 v126, v126, v239\n\t" \
    "v_mov_b32 v110, v241\n\t" \
    "v_sub_f32 v143, v143, v239\n\t" \
    "v_sub_f32 v127, v127, v239\n\t" \
    "v_mov_b32 v111, v241\n\t" \
    "v_exp_f32 v144, v128\n\t" \
    "v_exp_f32 v145, v129\n\t" \
    "v_exp_f32 v146, v130\n\t" \
    "v_exp_f32 v147, v131\n\t" \
    "v_exp_f32 v148, v132\n\t" \
    "v_exp_f32 v149, v133\n\t" \
    "v_exp_f32 v150, v134\n\t" \
    "v_exp_f32 v151, v135\n\t" \
    "v_exp_f32 v152, v136\n\t" \
    "v_exp_f32 v153, v137\n\t" \
    "v_exp_f32 v154, v138\n\t" \
    "v_exp_f32 v155, v139\n\t" \
    "v_exp_f32 v156, v140\n\t" \
    "v_exp_f32 v157, v141\n\t" \
    "v_exp_f32 v158, v142\n\t" \
    "v_exp_f32 v159, v143\n\t" \
    "v_mul_f32 v236, v236, v240\n\t" \
    "v_mul_f32 v0, v0, v240\n\t" \
    "v_mul_f32 v1, v1, v240\n\t" \
    "v_mul_f32 v2, v2, v240\n\t" \
    "v_mul_f32 v3, v3, v240\n\t" \
    "v_mul_f32 v4, v4, v240\n\t" \
    "v_mul_f32 v5, v5, v240\n\t" \
    "v_mul_f32 v6, v6, v240\n\t" \
    "v_mul_f32 v7, v7, v240\n\t" \
    "v_mul_f32 v8, v8, v240\n\t" \
    "v_mul_f32 v9, v9, v240\n\t" \
    "v_mul_f32 v10, v10, v240\n\t" \
    "v_mul_f32 v11, v11, v240\n\t" \
    "v_mul_f32 v12, v12, v240\n\t" \
    "v_mul_f32 v13, v13, v240\n\t" \
    "v_mul_f32 v14, v14, v240\n\t" \
    "v_mul_f32 v15, v15, v240\n\t" \
    "v_mul_f32 v16, v16, v240\n\t" \
    "v_mul_f32 v17, v17, v240\n\t" \
    "v_mul_f32 v18, v18, v240\n\t" \
    "v_mul_f32 v19, v19, v240\n\t" \
    "v_mul_f32 v20, v20, v240\n\t" \
    "v_mul_f32 v21, v21, v240\n\t" \
    "v_mul_f32 v22, v22, v240\n\t" \
    "v_mul_f32 v23, v23, v240\n\t" \
    "v_mul_f32 v24, v24, v240\n\t" \
    "v_mul_f32 v25, v25, v240\n\t" \
    "v_mul_f32 v26, v26, v240\n\t" \
    "v_mul_f32 v27, v27, v240\n\t" \
    "v_mul_f32 v28, v28, v240\n\t" \
    "v_mul_f32 v29, v29, v240\n\t" \
    "v_mul_f32 v30, v30, v240\n\t" \
    "v_mul_f32 v31, v31, v240\n\t" \
    "v_mul_f32 v32, v32, v240\n\t" \
    "v_mul_f32 v33, v33, v240\n\t" \
    "v_mul_f32 v34, v34, v240\n\t" \
    "v_mul_f32 v35, v35, v240\n\t" \
    "v_mul_f32 v36, v36, v240\n\t" \
    "v_mul_f32 v37, v37, v240\n\t" \
    "v_mul_f32 v38, v38, v240\n\t" \
    "v_mul_f32 v39, v39, v240\n\t" \
    "v_mul_f32 v40, v40, v240\n\t" \
    "v_mul_f32 v41, v41, v240\n\t" \
    "v_mul_f32 v42, v42, v240\n\t" \
    "v_mul_f32 v43, v43, v240\n\t" \
    "v_mul_f32 v44, v44, v240\n\t" \
    "v_mul_f32 v45, v45, v240\n\t" \
    "v_mul_f32 v46, v46, v240\n\t" \
    "v_mul_f32 v47, v47, v240\n\t" \
    "v_mul_f32 v48, v48, v240\n\t" \
    "v_mul_f32 v49, v49, v240\n\t" \
    "v_mul_f32 v50, v50, v240\n\t" \
    "v_mul_f32 v51, v51, v240\n\t" \
    "v_mul_f32 v52, v52, v240\n\t" \
    "v_mul_f32 v53, v53, v240\n\t" \
    "v_mul_f32 v54, v54, v240\n\t" \
    "v_mul_f32 v55, v55, v240\n\t" \
    "v_mul_f32 v56, v56, v240\n\t" \
    "v_mul_f32 v57, v57, v240\n\t" \
    "v_mul_f32 v58, v58, v240\n\t" \
    "v_mul_f32 v59, v59, v240\n\t" \
    "v_mul_f32 v60, v60, v240\n\t" \
    "v_mul_f32 v61, v61, v240\n\t" \
    "v_mul_f32 v62, v62, v240\n\t" \
    "v_mul_f32 v63, v63, v240\n\t" \
    "v_add_f32 v237, v144, v145\n\t" \
    "v_add_f32 v237, v237, v146\n\t" \
    "v_add_f32 v237, v237, v147\n\t" \
    "v_add_f32 v237, v237, v148\n\t" \
    "v_add_f32 v237, v237, v149\n\t" \
    "v_add_f32 v237, v237, v150\n\t" \
    "v_add_f32 v237, v237, v151\n\t" \
    "v_add_f32 v237, v237, v152\n\t" \
    "v_add_f32 v237, v237, v153\n\t" \
    "v_add_f32 v237, v237, v154\n\t" \
    "v_add_f32 v237, v237, v155\n\t" \
    "v_add_f32 v237, v237, v156\n\t" \
    "v_add_f32 v237, v237, v157\n\t" \
    "v_add_f32 v237, v237, v158\n\t" \
    "v_add_f32 v237, v237, v159\n\t" \
    "v_cvt_pk_bf16_f32 v160, v144, v145\n\t" \
    "v_cvt_pk_bf16_f32 v161, v146, v147\n\t" \
    "v_cvt_pk_bf16_f32 v162, v148, v149\n\t" \
    "v_cvt_pk_bf16_f32 v163, v150, v151\n\t" \
    "v_cvt_pk_bf16_f32 v164, v152, v153\n\t" \
    "v_cvt_pk_bf16_f32 v165, v154, v155\n\t" \
    "v_cvt_pk_bf16_f32 v166, v156, v157\n\t" \
    "v_cvt_pk_bf16_f32 v167, v158, v159\n\t" \
    "s_nop 1\n\t" \
    "s_branch BACKC_%=\n\t" \
    "RARED_%=:\n\t" \
    "s_nop 15\n\t" \
    "v_max3_f32 v239, v112, v113, v114\n\t" \
    "v_max3_f32 v239, v239, v115, v116\n\t" \
    "v_max3_f32 v239, v239, v117, v118\n\t" \
    "v_max3_f32 v239, v239, v119, v120\n\t" \
    "v_max3_f32 v239, v239, v121, v122\n\t" \
    "v_max3_f32 v239, v239, v123, v124\n\t" \
    "v_max3_f32 v239, v239, v125, v126\n\t" \
    "v_max_f32 v239, v239, v127\n\t" \
    "v_max_f32 v239, 0, v239\n\t" \
    "v_mov_b32 v240, v239\n\t" \
    "s_nop 1\n\t" \
    "v_permlane32_swap_b32 v239, v240\n\t" \
    "s_nop 1\n\t" \
    "v_max_f32 v239, v239, v240\n\t" \
    "v_add_f32 v238, v238, v239\n\t" \
    "v_exp_f32 v240, -v239\n\t" \
    "v_sub_f32 v241, 0, v238\n\t" \
    "v_sub_f32 v112, v112, v239\n\t" \
    "v_sub_f32 v128, v128, v239\n\t" \
    "v_mov_b32 v96, v241\n\t" \
    "v_sub_f32 v113, v113, v239\n\t" \
    "v_sub_f32 v129, v129, v239\n\t" \
    "v_mov_b32 v97, v241\n\t" \
    "v_sub_f32 v114, v114, v239\n\t" \
    "v_sub_f32 v130, v130, v239\n\t" \
    "v_mov_b32 v98, v241\n\t" \
    "v_sub_f32 v115, v115, v239\n\t" \
    "v_sub_f32 v131, v131, v239\n\t" \
    "v_mov_b32 v99, v241\n\t" \
    "v_sub_f32 v116, v116, v239\n\t" \
    "v_sub_f32 v132, v132, v239\n\t" \
    "v_mov_b32 v100, v241\n\t" \
    "v_sub_f32 v117, v117, v239\n\t" \
    "v_sub_f32 v133, v133, v239\n\t" \
    "v_mov_b32 v101, v241\n\t" \
    "v_sub_f32 v118, v118, v239\n\t" \
    "v_sub_f32 v134, v134, v239\n\t" \
    "v_mov_b32 v102, v241\n\t" \
    "v_sub_f32 v119, v119, v239\n\t" \
    "v_sub_f32 v135, v135, v239\n\t" \
    "v_mov_b32 v103, v241\n\t" \
    "v_sub_f32 v120, v120, v239\n\t" \
    "v_sub_f32 v136, v136, v239\n\t" \
    "v_mov_b32 v104, v241\n\t" \
    "v_sub_f32 v121, v121, v239\n\t" \
    "v_sub_f32 v137, v137, v239\n\t" \
    "v_mov_b32 v105, v241\n\t" \
    "v_sub_f32 v122, v122, v239\n\t" \
    "v_sub_f32 v138, v138, v239\n\t" \
    "v_mov_b32 v106, v241\n\t" \
    "v_sub_f32 v123, v123, v239\n\t" \
    "v_sub_f32 v139, v139, v239\n\t" \
    "v_mov_b32 v107, v241\n\t" \
    "v_sub_f32 v124, v124, v239\n\t" \
    "v_sub_f32 v140, v140, v239\n\t" \
    "v_mov_b32 v108, v241\n\t" \
    "v_sub_f32 v125, v125, v239\n\t" \
    "v_sub_f32 v141, v141, v239\n\t" \
    "v_mov_b32 v109, v241\n\t" \
    "v_sub_f32 v126, v126, v239\n\t" \
    "v_sub_f32 v142, v142, v239\n\t" \
    "v_mov_b32 v110, v241\n\t" \
    "v_sub_f32 v127, v127, v239\n\t" \
    "v_sub_f32 v143, v143, v239\n\t" \
    "v_mov_b32 v111, v241\n\t" \
    "v_exp_f32 v144, v112\n\t" \
    "v_exp_f32 v145, v113\n\t" \
    "v_exp_f32 v146, v114\n\t" \
    "v_exp_f32 v147, v115\n\t" \
    "v_exp_f32 v148, v116\n\t" \
    "v_exp_f32 v149, v117\n\t" \
    "v_exp_f32 v150, v118\n\t" \
    "v_exp_f32 v151, v119\n\t" \
    "v_exp_f32 v152, v120\n\t" \
    "v_exp_f32 v153, v121\n\t" \
    "v_exp_f32 v154, v122\n\t" \
    "v_exp_f32 v155, v123\n\t" \
    "v_exp_f32 v156, v124\n\t" \
    "v_exp_f32 v157, v125\n\t" \
    "v_exp_f32 v158, v126\n\t" \
    "v_exp_f32 v159, v127\n\t" \
    "v_mul_f32 v236, v236, v240\n\t" \
    "v_mul_f32 v0, v0, v240\n\t" \
    "v_mul_f32 v1, v1, v240\n\t" \
    "v_mul_f32 v2, v2, v240\n\t" \
    "v_mul_f32 v3, v3, v240\n\t" \
    "v_mul_f32 v4, v4, v240\n\t" \
    "v_mul_f32 v5, v5, v240\n\t" \
    "v_mul_f32 v6, v6, v240\n\t" \
    "v_mul_f32 v7, v7, v240\n\t" \
    "v_mul_f32 v8, v8, v240\n\t" \
    "v_mul_f32 v9, v9, v240\n\t" \
    "v_mul_f32 v10, v10, v240\n\t" \
    "v_mul_f32 v11, v11, v240\n\t" \
    "v_mul_f32 v12, v12, v240\n\t" \
    "v_mul_f32 v13, v13, v240\n\t" \
    "v_mul_f32 v14, v14, v240\n\t" \
    "v_mul_f32 v15, v15, v240\n\t" \
    "v_mul_f32 v16, v16, v240\n\t" \
    "v_mul_f32 v17, v17, v240\n\t" \
    "v_mul_f32 v18, v18, v240\n\t" \
    "v_mul_f32 v19, v19, v240\n\t" \
    "v_mul_f32 v20, v20, v240\n\t" \
    "v_mul_f32 v21, v21, v240\n\t" \
    "v_mul_f32 v22, v22, v240\n\t" \
    "v_mul_f32 v23, v23, v240\n\t" \
    "v_mul_f32 v24, v24, v240\n\t" \
    "v_mul_f32 v25, v25, v240\n\t" \
    "v_mul_f32 v26, v26, v240\n\t" \
    "v_mul_f32 v27, v27, v240\n\t" \
    "v_mul_f32 v28, v28, v240\n\t" \
    "v_mul_f32 v29, v29, v240\n\t" \
    "v_mul_f32 v30, v30, v240\n\t" \
    "v_mul_f32 v31, v31, v240\n\t" \
    "v_mul_f32 v32, v32, v240\n\t" \
    "v_mul_f32 v33, v33, v240\n\t" \
    "v_mul_f32 v34, v34, v240\n\t" \
    "v_mul_f32 v35, v35, v240\n\t" \
    "v_mul_f32 v36, v36, v240\n\t" \
    "v_mul_f32 v37, v37, v240\n\t" \
    "v_mul_f32 v38, v38, v240\n\t" \
    "v_mul_f32 v39, v39, v240\n\t" \
    "v_mul_f32 v40, v40, v240\n\t" \
    "v_mul_f32 v41, v41, v240\n\t" \
    "v_mul_f32 v42, v42, v240\n\t" \
    "v_mul_f32 v43, v43, v240\n\t" \
    "v_mul_f32 v44, v44, v240\n\t" \
    "v_mul_f32 v45, v45, v240\n\t" \
    "v_mul_f32 v46, v46, v240\n\t" \
    "v_mul_f32 v47, v47, v240\n\t" \
    "v_mul_f32 v48, v48, v240\n\t" \
    "v_mul_f32 v49, v49, v240\n\t" \
    "v_mul_f32 v50, v50, v240\n\t" \
    "v_mul_f32 v51, v51, v240\n\t" \
    "v_mul_f32 v52, v52, v240\n\t" \
    "v_mul_f32 v53, v53, v240\n\t" \
    "v_mul_f32 v54, v54, v240\n\t" \
    "v_mul_f32 v55, v55, v240\n\t" \
    "v_mul_f32 v56, v56, v240\n\t" \
    "v_mul_f32 v57, v57, v240\n\t" \
    "v_mul_f32 v58, v58, v240\n\t" \
    "v_mul_f32 v59, v59, v240\n\t" \
    "v_mul_f32 v60, v60, v240\n\t" \
    "v_mul_f32 v61, v61, v240\n\t" \
    "v_mul_f32 v62, v62, v240\n\t" \
    "v_mul_f32 v63, v63, v240\n\t" \
    "v_add_f32 v237, v144, v145\n\t" \
    "v_add_f32 v237, v237, v146\n\t" \
    "v_add_f32 v237, v237, v147\n\t" \
    "v_add_f32 v237, v237, v148\n\t" \
    "v_add_f32 v237, v237, v149\n\t" \
    "v_add_f32 v237, v237, v150\n\t" \
    "v_add_f32 v237, v237, v151\n\t" \
    "v_add_f32 v237, v237, v152\n\t" \
    "v_add_f32 v237, v237, v153\n\t" \
    "v_add_f32 v237, v237, v154\n\t" \
    "v_add_f32 v237, v237, v155\n\t" \
    "v_add_f32 v237, v237, v156\n\t" \
    "v_add_f32 v237, v237, v157\n\t" \
    "v_add_f32 v237, v237, v158\n\t" \
    "v_add_f32 v237, v237, v159\n\t" \
    "v_cvt_pk_bf16_f32 v160, v144, v145\n\t" \
    "v_cvt_pk_bf16_f32 v161, v146, v147\n\t" \
    "v_cvt_pk_bf16_f32 v162, v148, v149\n\t" \
    "v_cvt_pk_bf16_f32 v163, v150, v151\n\t" \
    "v_cvt_pk_bf16_f32 v164, v152, v153\n\t" \
    "v_cvt_pk_bf16_f32 v165, v154, v155\n\t" \
    "v_cvt_pk_bf16_f32 v166, v156, v157\n\t" \
    "v_cvt_pk_bf16_f32 v167, v158, v159\n\t" \
    "s_nop 1\n\t" \
    "s_branch BACKD_%=\n\t" \
    "RAREE_%=:\n\t" \
    "s_nop 15\n\t" \
    "v_max3_f32 v239, v128, v129, v130\n\t" \
    "v_max3_f32 v239, v239, v131, v132\n\t" \
    "v_max3_f32 v239, v239, v133, v134\n\t" \
    "v_max3_f32 v239, v239, v135, v136\n\t" \
    "v_max3_f32 v239, v239, v137, v138\n\t" \
    "v_max3_f32 v239, v239, v139, v140\n\t" \
    "v_max3_f32 v239, v239, v141, v142\n\t" \
    "v_max_f32 v239, v239, v143\n\t" \
    "v_max_f32 v239, 0, v239\n\t" \
    "v_mov_b32 v240, v239\n\t" \
    "s_nop 1\n\t" \
    "v_permlane32_swap_b32 v239, v240\n\t" \
    "s_nop 1\n\t" \
    "v_max_f32 v239, v239, v240\n\t" \
    "v_add_f32 v238, v238, v239\n\t" \
    "v_exp_f32 v240, -v239\n\t" \
    "v_sub_f32 v241, 0, v238\n\t" \
    "v_sub_f32 v128, v128, v239\n\t" \
    "v_mov_b32 v96, v241\n\t" \
    "v_sub_f32 v129, v129, v239\n\t" \
    "v_mov_b32 v97, v241\n\t" \
    "v_sub_f32 v130, v130, v239\n\t" \
    "v_mov_b32 v98, v241\n\t" \
    "v_sub_f32 v131, v131, v239\n\t" \
    "v_mov_b32 v99, v241\n\t" \
    "v_sub_f32 v132, v132, v239\n\t" \
    "v_mov_b32 v100, v241\n\t" \
    "v_sub_f32 v133, v133, v239\n\t" \
    "v_mov_b32 v101, v241\n\t" \
    "v_sub_f32 v134, v134, v239\n\t" \
    "v_mov_b32 v102, v241\n\t" \
    "v_sub_f32 v135, v135, v239\n\t" \
    "v_mov_b32 v103, v241\n\t" \
    "v_sub_f32 v136, v136, v239\n\t" \
    "v_mov_b32 v104, v241\n\t" \
    "v_sub_f32 v137, v137, v239\n\t" \
    "v_mov_b32 v105, v241\n\t" \
    "v_sub_f32 v138, v138, v239\n\t" \
    "v_mov_b32 v106, v241\n\t" \
    "v_sub_f32 v139, v139, v239\n\t" \
    "v_mov_b32 v107, v241\n\t" \
    "v_sub_f32 v140, v140, v239\n\t" \
    "v_mov_b32 v108, v241\n\t" \
    "v_sub_f32 v141, v141, v239\n\t" \
    "v_mov_b32 v109, v241\n\t" \
    "v_sub_f32 v142, v142, v239\n\t" \
    "v_mov_b32 v110, v241\n\t" \
    "v_sub_f32 v143, v143, v239\n\t" \
    "v_mov_b32 v111, v241\n\t" \
    "v_exp_f32 v144, v128\n\t" \
    "v_exp_f32 v145, v129\n\t" \
    "v_exp_f32 v146, v130\n\t" \
    "v_exp_f32 v147, v131\n\t" \
    "v_exp_f32 v148, v132\n\t" \
    "v_exp_f32 v149, v133\n\t" \
    "v_exp_f32 v150, v134\n\t" \
    "v_exp_f32 v151, v135\n\t" \
    "v_exp_f32 v152, v136\n\t" \
    "v_exp_f32 v153, v137\n\t" \
    "v_exp_f32 v154, v138\n\t" \
    "v_exp_f32 v155, v139\n\t" \
    "v_exp_f32 v156, v140\n\t" \
    "v_exp_f32 v157, v141\n\t" \
    "v_exp_f32 v158, v142\n\t" \
    "v_exp_f32 v159, v143\n\t" \
    "v_mul_f32 v236, v236, v240\n\t" \
    "v_mul_f32 v0, v0, v240\n\t" \
    "v_mul_f32 v1, v1, v240\n\t" \
    "v_mul_f32 v2, v2, v240\n\t" \
    "v_mul_f32 v3, v3, v240\n\t" \
    "v_mul_f32 v4, v4, v240\n\t" \
    "v_mul_f32 v5, v5, v240\n\t" \
    "v_mul_f32 v6, v6, v240\n\t" \
    "v_mul_f32 v7, v7, v240\n\t" \
    "v_mul_f32 v8, v8, v240\n\t" \
    "v_mul_f32 v9, v9, v240\n\t" \
    "v_mul_f32 v10, v10, v240\n\t" \
    "v_mul_f32 v11, v11, v240\n\t" \
    "v_mul_f32 v12, v12, v240\n\t" \
    "v_mul_f32 v13, v13, v240\n\t" \
    "v_mul_f32 v14, v14, v240\n\t" \
    "v_mul_f32 v15, v15, v240\n\t" \
    "v_mul_f32 v16, v16, v240\n\t" \
    "v_mul_f32 v17, v17, v240\n\t" \
    "v_mul_f32 v18, v18, v240\n\t" \
    "v_mul_f32 v19, v19, v240\n\t" \
    "v_mul_f32 v20, v20, v240\n\t" \
    "v_mul_f32 v21, v21, v240\n\t" \
    "v_mul_f32 v22, v22, v240\n\t" \
    "v_mul_f32 v23, v23, v240\n\t" \
    "v_mul_f32 v24, v24, v240\n\t" \
    "v_mul_f32 v25, v25, v240\n\t" \
    "v_mul_f32 v26, v26, v240\n\t" \
    "v_mul_f32 v27, v27, v240\n\t" \
    "v_mul_f32 v28, v28, v240\n\t" \
    "v_mul_f32 v29, v29, v240\n\t" \
    "v_mul_f32 v30, v30, v240\n\t" \
    "v_mul_f32 v31, v31, v240\n\t" \
    "v_mul_f32 v32, v32, v240\n\t" \
    "v_mul_f32 v33, v33, v240\n\t" \
    "v_mul_f32 v34, v34, v240\n\t" \
    "v_mul_f32 v35, v35, v240\n\t" \
    "v_mul_f32 v36, v36, v240\n\t" \
    "v_mul_f32 v37, v37, v240\n\t" \
    "v_mul_f32 v38, v38, v240\n\t" \
    "v_mul_f32 v39, v39, v240\n\t" \
    "v_mul_f32 v40, v40, v240\n\t" \
    "v_mul_f32 v41, v41, v240\n\t" \
    "v_mul_f32 v42, v42, v240\n\t" \
    "v_mul_f32 v43, v43, v240\n\t" \
    "v_mul_f32 v44, v44, v240\n\t" \
    "v_mul_f32 v45, v45, v240\n\t" \
    "v_mul_f32 v46, v46, v240\n\t" \
    "v_mul_f32 v47, v47, v240\n\t" \
    "v_mul_f32 v48, v48, v240\n\t" \
    "v_mul_f32 v49, v49, v240\n\t" \
    "v_mul_f32 v50, v50, v240\n\t" \
    "v_mul_f32 v51, v51, v240\n\t" \
    "v_mul_f32 v52, v52, v240\n\t" \
    "v_mul_f32 v53, v53, v240\n\t" \
    "v_mul_f32 v54, v54, v240\n\t" \
    "v_mul_f32 v55, v55, v240\n\t" \
    "v_mul_f32 v56, v56, v240\n\t" \
    "v_mul_f32 v57, v57, v240\n\t" \
    "v_mul_f32 v58, v58, v240\n\t" \
    "v_mul_f32 v59, v59, v240\n\t" \
    "v_mul_f32 v60, v60, v240\n\t" \
    "v_mul_f32 v61, v61, v240\n\t" \
    "v_mul_f32 v62, v62, v240\n\t" \
    "v_mul_f32 v63, v63, v240\n\t" \
    "v_add_f32 v237, v144, v145\n\t" \
    "v_add_f32 v237, v237, v146\n\t" \
    "v_add_f32 v237, v237, v147\n\t" \
    "v_add_f32 v237, v237, v148\n\t" \
    "v_add_f32 v237, v237, v149\n\t" \
    "v_add_f32 v237, v237, v150\n\t" \
    "v_add_f32 v237, v237, v151\n\t" \
    "v_add_f32 v237, v237, v152\n\t" \
    "v_add_f32 v237, v237, v153\n\t" \
    "v_add_f32 v237, v237, v154\n\t" \
    "v_add_f32 v237, v237, v155\n\t" \
    "v_add_f32 v237, v237, v156\n\t" \
    "v_add_f32 v237, v237, v157\n\t" \
    "v_add_f32 v237, v237, v158\n\t" \
    "v_add_f32 v237, v237, v159\n\t" \
    "v_cvt_pk_bf16_f32 v160, v144, v145\n\t" \
    "v_cvt_pk_bf16_f32 v161, v146, v147\n\t" \
    "v_cvt_pk_bf16_f32 v162, v148, v149\n\t" \
    "v_cvt_pk_bf16_f32 v163, v150, v151\n\t" \
    "v_cvt_pk_bf16_f32 v164, v152, v153\n\t" \
    "v_cvt_pk_bf16_f32 v165, v154, v155\n\t" \
    "v_cvt_pk_bf16_f32 v166, v156, v157\n\t" \
    "v_cvt_pk_bf16_f32 v167, v158, v159\n\t" \
    "s_nop 1\n\t" \
    "s_branch BACKE_%=\n\t" \
    "RAREF_%=:\n\t" \
    "s_nop 15\n\t" \
    "v_max3_f32 v239, v128, v129, v130\n\t" \
    "v_max3_f32 v239, v239, v131, v132\n\t" \
    "v_max3_f32 v239, v239, v133, v134\n\t" \
    "v_max3_f32 v239, v239, v135, v136\n\t" \
    "v_max3_f32 v239, v239, v137, v138\n\t" \
    "v_max3_f32 v239, v239, v139, v140\n\t" \
    "v_max3_f32 v239, v239, v141, v142\n\t" \
    "v_max_f32 v239, v239, v143\n\t" \
    "v_max_f32 v239, 0, v239\n\t" \
    "v_mov_b32 v240, v239\n\t" \
    "s_nop 1\n\t" \
    "v_permlane32_swap_b32 v239, v240\n\t" \
    "s_nop 1\n\t" \
    "v_max_f32 v239, v239, v240\n\t" \
    "v_add_f32 v238, v238, v239\n\t" \
    "v_exp_f32 v240, -v239\n\t" \
    "v_sub_f32 v241, 0, v238\n\t" \
    "v_sub_f32 v128, v128, v239\n\t" \
    "v_sub_f32 v112, v112, v239\n\t" \
    "v_mov_b32 v96, v241\n\t" \
    "v_sub_f32 v129, v129, v239\n\t" \
    "v_sub_f32 v113, v113, v239\n\t" \
    "v_mov_b32 v97, v241\n\t" \
    "v_sub_f32 v130, v130, v239\n\t" \
    "v_sub_f32 v114, v114, v239\n\t" \
    "v_mov_b32 v98, v241\n\t" \
    "v_sub_f32 v131, v131, v239\n\t" \
    "v_sub_f32 v115, v115, v239\n\t" \
    "v_mov_b32 v99, v241\n\t" \
    "v_sub_f32 v132, v132, v239\n\t" \
    "v_sub_f32 v116, v116, v239\n\t" \
    "v_mov_b32 v100, v241\n\t" \
    "v_sub_f32 v133, v133, v239\n\t" \
    "v_sub_f32 v117, v117, v239\n\t" \
    "v_mov_b32 v101, v241\n\t" \
    "v_sub_f32 v134, v134, v239\n\t" \
    "v_sub_f32 v118, v118, v239\n\t" \
    "v_mov_b32 v102, v241\n\t" \
    "v_sub_f32 v135, v135, v239\n\t" \
    "v_sub_f32 v119, v119, v239\n\t" \
    "v_mov_b32 v103, v241\n\t" \
    "v_sub_f32 v136, v136, v239\n\t" \
    "v_sub_f32 v120, v120, v239\n\t" \
    "v_mov_b32 v104, v241\n\t" \
    "v_sub_f32 v137, v137, v239\n\t" \
    "v_sub_f32 v121, v121, v239\n\t" \
    "v_mov_b32 v105, v241\n\t" \
    "v_sub_f32 v138, v138, v239\n\t" \
    "v_sub_f32 v122, v122, v239\n\t" \
    "v_mov_b32 v106, v241\n\t" \
    "v_sub_f32 v139, v139, v239\n\t" \
    "v_sub_f32 v123, v123, v239\n\t" \
    "v_mov_b32 v107, v241\n\t" \
    "v_sub_f32 v140, v140, v239\n\t" \
    "v_sub_f32 v124, v124, v239\n\t" \
    "v_mov_b32 v108, v241\n\t" \
    "v_sub_f32 v141, v141, v239\n\t" \
    "v_sub_f32 v125, v125, v239\n\t" \
    "v_mov_b32 v109, v241\n\t" \
    "v_sub_f32 v142, v142, v239\n\t" \
    "v_sub_f32 v126, v126, v239\n\t" \
    "v_mov_b32 v110, v241\n\t" \
    "v_sub_f32 v143, v143, v239\n\t" \
    "v_sub_f32 v127, v127, v239\n\t" \
    "v_mov_b32 v111, v241\n\t" \
    "v_exp_f32 v144, v128\n\t" \
    "v_exp_f32 v145, v129\n\t" \
    "v_exp_f32 v146, v130\n\t" \
    "v_exp_f32 v147, v131\n\t" \
    "v_exp_f32 v148, v132\n\t" \
    "v_exp_f32 v149, v133\n\t" \
    "v_exp_f32 v150, v134\n\t" \
    "v_exp_f32 v151, v135\n\t" \
    "v_exp_f32 v152, v136\n\t" \
    "v_exp_f32 v153, v137\n\t" \
    "v_exp_f32 v154, v138\n\t" \
    "v_exp_f32 v155, v139\n\t" \
    "v_exp_f32 v156, v140\n\t" \
    "v_exp_f32 v157, v141\n\t" \
    "v_exp_f32 v158, v142\n\t" \
    "v_exp_f32 v159, v143\n\t" \
    "v_mul_f32 v236, v236, v240\n\t" \
    "v_mul_f32 v0, v0, v240\n\t" \
    "v_mul_f32 v1, v1, v240\n\t" \
    "v_mul_f32 v2, v2, v240\n\t" \
    "v_mul_f32 v3, v3, v240\n\t" \
    "v_mul_f32 v4, v4, v240\n\t" \
    "v_mul_f32 v5, v5, v240\n\t" \
    "v_mul_f32 v6, v6, v240\n\t" \
    "v_mul_f32 v7, v7, v240\n\t" \
    "v_mul_f32 v8, v8, v240\n\t" \
    "v_mul_f32 v9, v9, v240\n\t" \
    "v_mul_f32 v10, v10, v240\n\t" \
    "v_mul_f32 v11, v11, v240\n\t" \
    "v_mul_f32 v12, v12, v240\n\t" \
    "v_mul_f32 v13, v13, v240\n\t" \
    "v_mul_f32 v14, v14, v240\n\t" \
    "v_mul_f32 v15, v15, v240\n\t" \
    "v_mul_f32 v16, v16, v240\n\t" \
    "v_mul_f32 v17, v17, v240\n\t" \
    "v_mul_f32 v18, v18, v240\n\t" \
    "v_mul_f32 v19, v19, v240\n\t" \
    "v_mul_f32 v20, v20, v240\n\t" \
    "v_mul_f32 v21, v21, v240\n\t" \
    "v_mul_f32 v22, v22, v240\n\t" \
    "v_mul_f32 v23, v23, v240\n\t" \
    "v_mul_f32 v24, v24, v240\n\t" \
    "v_mul_f32 v25, v25, v240\n\t" \
    "v_mul_f32 v26, v26, v240\n\t" \
    "v_mul_f32 v27, v27, v240\n\t" \
    "v_mul_f32 v28, v28, v240\n\t" \
    "v_mul_f32 v29, v29, v240\n\t" \
    "v_mul_f32 v30, v30, v240\n\t" \
    "v_mul_f32 v31, v31, v240\n\t" \
    "v_mul_f32 v32, v32, v240\n\t" \
    "v_mul_f32 v33, v33, v240\n\t" \
    "v_mul_f32 v34, v34, v240\n\t" \
    "v_mul_f32 v35, v35, v240\n\t" \
    "v_mul_f32 v36, v36, v240\n\t" \
    "v_mul_f32 v37, v37, v240\n\t" \
    "v_mul_f32 v38, v38, v240\n\t" \
    "v_mul_f32 v39, v39, v240\n\t" \
    "v_mul_f32 v40, v40, v240\n\t" \
    "v_mul_f32 v41, v41, v240\n\t" \
    "v_mul_f32 v42, v42, v240\n\t" \
    "v_mul_f32 v43, v43, v240\n\t" \
    "v_mul_f32 v44, v44, v240\n\t" \
    "v_mul_f32 v45, v45, v240\n\t" \
    "v_mul_f32 v46, v46, v240\n\t" \
    "v_mul_f32 v47, v47, v240\n\t" \
    "v_mul_f32 v48, v48, v240\n\t" \
    "v_mul_f32 v49, v49, v240\n\t" \
    "v_mul_f32 v50, v50, v240\n\t" \
    "v_mul_f32 v51, v51, v240\n\t" \
    "v_mul_f32 v52, v52, v240\n\t" \
    "v_mul_f32 v53, v53, v240\n\t" \
    "v_mul_f32 v54, v54, v240\n\t" \
    "v_mul_f32 v55, v55, v240\n\t" \
    "v_mul_f32 v56, v56, v240\n\t" \
    "v_mul_f32 v57, v57, v240\n\t" \
    "v_mul_f32 v58, v58, v240\n\t" \
    "v_mul_f32 v59, v59, v240\n\t" \
    "v_mul_f32 v60, v60, v240\n\t" \
    "v_mul_f32 v61, v61, v240\n\t" \
    "v_mul_f32 v62, v62, v240\n\t" \
    "v_mul_f32 v63, v63, v240\n\t" \
    "v_add_f32 v237, v144, v145\n\t" \
    "v_add_f32 v237, v237, v146\n\t" \
    "v_add_f32 v237, v237, v147\n\t" \
    "v_add_f32 v237, v237, v148\n\t" \
    "v_add_f32 v237, v237, v149\n\t" \
    "v_add_f32 v237, v237, v150\n\t" \
    "v_add_f32 v237, v237, v151\n\t" \
    "v_add_f32 v237, v237, v152\n\t" \
    "v_add_f32 v237, v237, v153\n\t" \
    "v_add_f32 v237, v237, v154\n\t" \
    "v_add_f32 v237, v237, v155\n\t" \
    "v_add_f32 v237, v237, v156\n\t" \
    "v_add_f32 v237, v237, v157\n\t" \
    "v_add_f32 v237, v237, v158\n\t" \
    "v_add_f32 v237, v237, v159\n\t" \
    "v_cvt_pk_bf16_f32 v160, v144, v145\n\t" \
    "v_cvt_pk_bf16_f32 v161, v146, v147\n\t" \
    "v_cvt_pk_bf16_f32 v162, v148, v149\n\t" \
    "v_cvt_pk_bf16_f32 v163, v150, v151\n\t" \
    "v_cvt_pk_bf16_f32 v164, v152, v153\n\t" \
    "v_cvt_pk_bf16_f32 v165, v154, v155\n\t" \
    "v_cvt_pk_bf16_f32 v166, v156, v157\n\t" \
    "v_cvt_pk_bf16_f32 v167, v158, v159\n\t" \
    "s_nop 1\n\t" \
    "s_branch BACKF_%=\n\t" \
    "RAREG_%=:\n\t" \
    "s_nop 15\n\t" \
    "v_max3_f32 v239, v112, v113, v114\n\t" \
    "v_max3_f32 v239, v239, v115, v116\n\t" \
    "v_max3_f32 v239, v239, v117, v118\n\t" \
    "v_max3_f32 v239, v239, v119, v120\n\t" \
    "v_max3_f32 v239, v239, v121, v122\n\t" \
    "v_max3_f32 v239, v239, v123, v124\n\t" \
    "v_max3_f32 v239, v239, v125, v126\n\t" \
    "v_max_f32 v239, v239, v127\n\t" \
    "v_max_f32 v239, 0, v239\n\t" \
    "v_mov_b32 v240, v239\n\t" \
    "s_nop 1\n\t" \
    "v_permlane32_swap_b32 v239, v240\n\t" \
    "s_nop 1\n\t" \
    "v_max_f32 v239, v239, v240\n\t" \
    "v_add_f32 v238, v238, v239\n\t" \
    "v_exp_f32 v240, -v239\n\t" \
    "v_sub_f32 v241, 0, v238\n\t" \
    "v_sub_f32 v112, v112, v239\n\t" \
    "v_mov_b32 v96, v241\n\t" \
    "v_sub_f32 v113, v113, v239\n\t" \
    "v_mov_b32 v97, v241\n\t" \
    "v_sub_f32 v114, v114, v239\n\t" \
    "v_mov_b32 v98, v241\n\t" \
    "v_sub_f32 v115, v115, v239\n\t" \
    "v_mov_b32 v99, v241\n\t" \
    "v_sub_f32 v116, v116, v239\n\t" \
    "v_mov_b32 v100, v241\n\t" \
    "v_sub_f32 v117, v117, v239\n\t" \
    "v_mov_b32 v101, v241\n\t" \
    "v_sub_f32 v118, v118, v239\n\t" \
    "v_mov_b32 v102, v241\n\t" \
    "v_sub_f32 v119, v119, v239\n\t" \
    "v_mov_b32 v103, v241\n\t" \
    "v_sub_f32 v120, v120, v239\n\t" \
    "v_mov_b32 v104, v241\n\t" \
    "v_sub_f32 v121, v121, v239\n\t" \
    "v_mov_b32 v105, v241\n\t" \
    "v_sub_f32 v122, v122, v239\n\t" \
    "v_mov_b32 v106, v241\n\t" \
    "v_sub_f32 v123, v123, v239\n\t" \
    "v_mov_b32 v107, v241\n\t" \
    "v_sub_f32 v124, v124, v239\n\t" \
    "v_mov_b32 v108, v241\n\t" \
    "v_sub_f32 v125, v125, v239\n\t" \
    "v_mov_b32 v109, v241\n\t" \
    "v_sub_f32 v126, v126, v239\n\t" \
    "v_mov_b32 v110, v241\n\t" \
    "v_sub_f32 v127, v127, v239\n\t" \
    "v_mov_b32 v111, v241\n\t" \
    "v_exp_f32 v144, v112\n\t" \
    "v_exp_f32 v145, v113\n\t" \
    "v_exp_f32 v146, v114\n\t" \
    "v_exp_f32 v147, v115\n\t" \
    "v_exp_f32 v148, v116\n\t" \
    "v_exp_f32 v149, v117\n\t" \
    "v_exp_f32 v150, v118\n\t" \
    "v_exp_f32 v151, v119\n\t" \
    "v_exp_f32 v152, v120\n\t" \
    "v_exp_f32 v153, v121\n\t" \
    "v_exp_f32 v154, v122\n\t" \
    "v_exp_f32 v155, v123\n\t" \
    "v_exp_f32 v156, v124\n\t" \
    "v_exp_f32 v157, v125\n\t" \
    "v_exp_f32 v158, v126\n\t" \
    "v_exp_f32 v159, v127\n\t" \
    "v_mul_f32 v236, v236, v240\n\t" \
    "v_mul_f32 v0, v0, v240\n\t" \
    "v_mul_f32 v1, v1, v240\n\t" \
    "v_mul_f32 v2, v2, v240\n\t" \
    "v_mul_f32 v3, v3, v240\n\t" \
    "v_mul_f32 v4, v4, v240\n\t" \
    "v_mul_f32 v5, v5, v240\n\t" \
    "v_mul_f32 v6, v6, v240\n\t" \
    "v_mul_f32 v7, v7, v240\n\t" \
    "v_mul_f32 v8, v8, v240\n\t" \
    "v_mul_f32 v9, v9, v240\n\t" \
    "v_mul_f32 v10, v10, v240\n\t" \
    "v_mul_f32 v11, v11, v240\n\t" \
    "v_mul_f32 v12, v12, v240\n\t" \
    "v_mul_f32 v13, v13, v240\n\t" \
    "v_mul_f32 v14, v14, v240\n\t" \
    "v_mul_f32 v15, v15, v240\n\t" \
    "v_mul_f32 v16, v16, v240\n\t" \
    "v_mul_f32 v17, v17, v240\n\t" \
    "v_mul_f32 v18, v18, v240\n\t" \
    "v_mul_f32 v19, v19, v240\n\t" \
    "v_mul_f32 v20, v20, v240\n\t" \
    "v_mul_f32 v21, v21, v240\n\t" \
    "v_mul_f32 v22, v22, v240\n\t" \
    "v_mul_f32 v23, v23, v240\n\t" \
    "v_mul_f32 v24, v24, v240\n\t" \
    "v_mul_f32 v25, v25, v240\n\t" \
    "v_mul_f32 v26, v26, v240\n\t" \
    "v_mul_f32 v27, v27, v240\n\t" \
    "v_mul_f32 v28, v28, v240\n\t" \
    "v_mul_f32 v29, v29, v240\n\t" \
    "v_mul_f32 v30, v30, v240\n\t" \
    "v_mul_f32 v31, v31, v240\n\t" \
    "v_mul_f32 v32, v32, v240\n\t" \
    "v_mul_f32 v33, v33, v240\n\t" \
    "v_mul_f32 v34, v34, v240\n\t" \
    "v_mul_f32 v35, v35, v240\n\t" \
    "v_mul_f32 v36, v36, v240\n\t" \
    "v_mul_f32 v37, v37, v240\n\t" \
    "v_mul_f32 v38, v38, v240\n\t" \
    "v_mul_f32 v39, v39, v240\n\t" \
    "v_mul_f32 v40, v40, v240\n\t" \
    "v_mul_f32 v41, v41, v240\n\t" \
    "v_mul_f32 v42, v42, v240\n\t" \
    "v_mul_f32 v43, v43, v240\n\t" \
    "v_mul_f32 v44, v44, v240\n\t" \
    "v_mul_f32 v45, v45, v240\n\t" \
    "v_mul_f32 v46, v46, v240\n\t" \
    "v_mul_f32 v47, v47, v240\n\t" \
    "v_mul_f32 v48, v48, v240\n\t" \
    "v_mul_f32 v49, v49, v240\n\t" \
    "v_mul_f32 v50, v50, v240\n\t" \
    "v_mul_f32 v51, v51, v240\n\t" \
    "v_mul_f32 v52, v52, v240\n\t" \
    "v_mul_f32 v53, v53, v240\n\t" \
    "v_mul_f32 v54, v54, v240\n\t" \
    "v_mul_f32 v55, v55, v240\n\t" \
    "v_mul_f32 v56, v56, v240\n\t" \
    "v_mul_f32 v57, v57, v240\n\t" \
    "v_mul_f32 v58, v58, v240\n\t" \
    "v_mul_f32 v59, v59, v240\n\t" \
    "v_mul_f32 v60, v60, v240\n\t" \
    "v_mul_f32 v61, v61, v240\n\t" \
    "v_mul_f32 v62, v62, v240\n\t" \
    "v_mul_f32 v63, v63, v240\n\t" \
    "v_add_f32 v237, v144, v145\n\t" \
    "v_add_f32 v237, v237, v146\n\t" \
    "v_add_f32 v237, v237, v147\n\t" \
    "v_add_f32 v237, v237, v148\n\t" \
    "v_add_f32 v237, v237, v149\n\t" \
    "v_add_f32 v237, v237, v150\n\t" \
    "v_add_f32 v237, v237, v151\n\t" \
    "v_add_f32 v237, v237, v152\n\t" \
    "v_add_f32 v237, v237, v153\n\t" \
    "v_add_f32 v237, v237, v154\n\t" \
    "v_add_f32 v237, v237, v155\n\t" \
    "v_add_f32 v237, v237, v156\n\t" \
    "v_add_f32 v237, v237, v157\n\t" \
    "v_add_f32 v237, v237, v158\n\t" \
    "v_add_f32 v237, v237, v159\n\t" \
    "v_cvt_pk_bf16_f32 v160, v144, v145\n\t" \
    "v_cvt_pk_bf16_f32 v161, v146, v147\n\t" \
    "v_cvt_pk_bf16_f32 v162, v148, v149\n\t" \
    "v_cvt_pk_bf16_f32 v163, v150, v151\n\t" \
    "v_cvt_pk_bf16_f32 v164, v152, v153\n\t" \
    "v_cvt_pk_bf16_f32 v165, v154, v155\n\t" \
    "v_cvt_pk_bf16_f32 v166, v156, v157\n\t" \
    "v_cvt_pk_bf16_f32 v167, v158, v159\n\t" \
    "s_nop 1\n\t" \
    "s_branch BACKG_%=\n\t" \
    "END_%=:\n\t" \
    "s_cmp_lg_u32 %[dmaw], 0\n\t" \
    "s_cbranch_scc1 END2_%=\n\t" \
    "s_waitcnt vmcnt(0)\n\t" \
    "END2_%=:\n\t" \
    "s_waitcnt lgkmcnt(0)\n\t" \
    "s_nop 15\n\t"
#define K2Q_ASM \
    "s_mov_b64 s[60:61], %[a1]\n\t" \
    "s_mov_b64 s[62:63], %[b1]\n\t" \
    "s_mov_b32 s67, %[nl]\n\t" \
    "s_mov_b32 s68, 0\n\t" \
    "s_add_u32 m0, %[mA], s68\n\t" \
    "s_add_u32 s66, %[mB], s68\n\t" \
    "global_load_lds_dwordx4 %[va], s[60:61] offset:0\n\t" \
    "global_load_lds_dwordx4 %[va], s[60:61] offset:1024\n\t" \
    "global_load_lds_dwordx4 %[va], s[60:61] offset:2048\n\t" \
    "global_load_lds_dwordx4 %[va], s[60:61] offset:3072\n\t" \
    "s_mov_b32 m0, s66\n\t" \
    "s_add_u32 s64, s62, 0x40000\n\t" \
    "s_addc_u32 s65, s63, 0\n\t" \
    "global_load_lds_dwordx4 %[vb], s[62:63]\n\t" \
    "s_add_u32 m0, s66, 8192\n\t" \
    "s_add_u32 s60, s60, 0x4000\n\t" \
    "global_load_lds_dwordx4 %[vb], s[64:65]\n\t" \
    "s_addc_u32 s61, s61, 0\n\t" \
    "s_add_u32 s62, s62, 0x80000\n\t" \
    "s_addc_u32 s63, s63, 0\n\t" \
    "s_mov_b32 s68, 49152\n\t" \
    "s_add_u32 m0, %[mA], s68\n\t" \
    "s_add_u32 s66, %[mB], s68\n\t" \
    "global_load_lds_dwordx4 %[va], s[60:61] offset:0\n\t" \
    "global_load_lds_dwordx4 %[va], s[60:61] offset:1024\n\t" \
    "global_load_lds_dwordx4 %[va], s[60:61] offset:2048\n\t" \
    "global_load_lds_dwordx4 %[va], s[60:61] offset:3072\n\t" \
    "s_mov_b32 m0, s66\n\t" \
    "s_add_u32 s64, s62, 0x40000\n\t" \
    "s_addc_u32 s65, s63, 0\n\t" \
    "global_load_lds_dwordx4 %[vb], s[62:63]\n\t" \
    "s_add_u32 m0, s66, 8192\n\t" \
    "s_add_u32 s60, s60, 0x4000\n\t" \
    "global_load_lds_dwordx4 %[vb], s[64:65]\n\t" \
    "s_addc_u32 s61, s61, 0\n\t" \
    "s_add_u32 s62, s62, 0x80000\n\t" \
    "s_addc_u32 s63, s63, 0\n\t" \
    "s_mov_b32 s68, 98304\n\t" \
    "s_add_u32 m0, %[mA], s68\n\t" \
    "s_add_u32 s66, %[mB], s68\n\t" \
    "global_load_lds_dwordx4 %[va], s[60:61] offset:0\n\t" \
    "global_load_lds_dwordx4 %[va], s[60:61] offset:1024\n\t" \
    "global_load_lds_dwordx4 %[va], s[60:61] offset:2048\n\t" \
    "global_load_lds_dwordx4 %[va], s[60:61] offset:3072\n\t" \
    "s_mov_b32 m0, s66\n\t" \
    "s_add_u32 s64, s62, 0x40000\n\t" \
    "s_addc_u32 s65, s63, 0\n\t" \
    "global_load_lds_dwordx4 %[vb], s[62:63]\n\t" \
    "s_add_u32 m0, s66, 8192\n\t" \
    "s_add_u32 s60, s60, 0x4000\n\t" \
    "global_load_lds_dwordx4 %[vb], s[64:65]\n\t" \
    "s_addc_u32 s61, s61, 0\n\t" \
    "s_add_u32 s62, s62, 0x80000\n\t" \
    "s_addc_u32 s63, s63, 0\n\t" \
    "s_mov_b32 s68, 0\n\t" \
    "s_mov_b32 s69, 98304\n\t" \
    "v_mov_b32 v128, v134\n\t" \
    "v_mov_b32 v129, v135\n\t" \
    "v_mov_b32 v130, v136\n\t" \
    "v_add_u32 v131, 49152, v134\n\t" \
    "v_add_u32 v132, 49152, v135\n\t" \
    "v_add_u32 v133, 49152, v136\n\t" \
    "s_waitcnt vmcnt(12)\n\t" \
    "s_barrier\n\t" \
    "ds_read_b128 v[64:67], v128 offset:0\n\t" \
    "ds_read_b64_tr_b16 v[80:81], v129 offset:0\n\t" \
    "ds_read_b64_tr_b16 v[82:83], v129 offset:256\n\t" \
    "ds_read_b64_tr_b16 v[84:85], v130 offset:0\n\t" \
    "ds_read_b64_tr_b16 v[86:87], v130 offset:256\n\t" \
    "ds_read_b128 v[68:71], v128 offset:256\n\t" \
    "ds_read_b64_tr_b16 v[88:89], v129 offset:512\n\t" \
    "ds_read_b64_tr_b16 v[90:91], v129 offset:768\n\t" \
    "ds_read_b64_tr_b16 v[92:93], v130 offset:512\n\t" \
    "ds_read_b64_tr_b16 v[94:95], v130 offset:768\n\t" \
    "ds_read_b128 v[72:75], v128 offset:512\n\t" \
    "ds_read_b128 v[76:79], v128 offset:768\n\t" \
    "LOOP_%=:\n\t" \
    "s_add_u32 m0, %[mA], s68\n\t" \
    "s_add_u32 s66, %[mB], s68\n\t" \
    "global_load_lds_dwordx4 %[va], s[60:61] offset:0\n\t" \
    "global_load_lds_dwordx4 %[va], s[60:61] offset:1024\n\t" \
    "global_load_lds_dwordx4 %[va], s[60:61] offset:2048\n\t" \
    "global_load_lds_dwordx4 %[va], s[60:61] offset:3072\n\t" \
    "s_mov_b32 m0, s66\n\t" \
    "s_add_u32 s64, s62, 0x40000\n\t" \
    "s_addc_u32 s65, s63, 0\n\t" \
    "global_load_lds_dwordx4 %[vb], s[62:63]\n\t" \
    "s_add_u32 m0, s66, 8192\n\t" \
    "s_add_u32 s60, s60, 0x4000\n\t" \
    "global_load_lds_dwordx4 %[vb], s[64:65]\n\t" \
    "s_addc_u32 s61, s61, 0\n\t" \
    "s_add_u32 s62, s62, 0x80000\n\t" \
    "s_addc_u32 s63, s63, 0\n\t" \
    "v_mfma_f32_16x16x32_bf16 v[0:3], v[112:115], v[96:99], v[0:3]\n\t" \
    "ds_read_b128 v[64:67], v131 offset:0\n\t" \
    "ds_read_b64_tr_b16 v[80:81], v132 offset:0\n\t" \
    "v_mfma_f32_16x16x32_bf16 v[4:7], v[116:119], v[96:99], v[4:7]\n\t" \
    "ds_read_b64_tr_b16 v[82:83], v132 offset:256\n\t" \
    "ds_read_b64_tr_b16 v[84:85], v133 offset:0\n\t" \
    "v_mfma_f32_16x16x32_bf16 v[8:11], v[120:123], v[96:99], v[8:11]\n\t" \
    "ds_read_b64_tr_b16 v[86:87], v133 offset:256\n\t" \
    "ds_read_b128 v[68:71], v131 offset:256\n\t" \
    "v_mfma_f32_16x16x32_bf16 v[12:15], v[124:127], v[96:99], v[12:15]\n\t" \
    "ds_read_b64_tr_b16 v[88:89], v132 offset:512\n\t" \
    "ds_read_b64_tr_b16 v[90:91], v132 offset:768\n\t" \
    "v_mfma_f32_16x16x32_bf16 v[16:19], v[112:115], v[100:103], v[16:19]\n\t" \
    "ds_read_b64_tr_b16 v[92:93], v133 offset:512\n\t" \
    "ds_read_b64_tr_b16 v[94:95], v133 offset:768\n\t" \
    "v_mfma_f32_16x16x32_bf16 v[20:23], v[116:119], v[100:103], v[20:23]\n\t" \
    "ds_read_b128 v[72:75], v131 offset:512\n\t" \
    "ds_read_b128 v[76:79], v131 offset:768\n\t" \
    "v_mfma_f32_16x16x32_bf16 v[24:27], v[120:123], v[100:103], v[24:27]\n\t" \
    "s_add_u32 m0, %[mA], s68\n\t" \
    "s_add_u32 s66, %[mB], s68\n\t" \
    "global_load_lds_dwordx4 %[va], s[60:61] offset:0\n\t" \
    "v_mfma_f32_16x16x32_bf16 v[28:31], v[124:127], v[100:103], v[28:31]\n\t" \
    "global_load_lds_dwordx4 %[va], s[60:61] offset:1024\n\t" \
    "global_load_lds_dwordx4 %[va], s[60:61] offset:2048\n\t" \
    "global_load_lds_dwordx4 %[va], s[60:61] offset:3072\n\t" \
    "v_mfma_f32_16x16x32_bf16 v[32:35], v[112:115], v[104:107], v[32:35]\n\t" \
    "s_mov_b32 m0, s66\n\t" \
    "s_add_u32 s64, s62, 0x40000\n\t" \
    "s_addc_u32 s65, s63, 0\n\t" \
    "v_mfma_f32_16x16x32_bf16 v[36:39], v[116:119], v[104:107], v[36:39]\n\t" \
    "global_load_lds_dwordx4 %[vb], s[62:63]\n\t" \
    "s_add_u32 m0, s66, 8192\n\t" \
    "s_add_u32 s60, s60, 0x4000\n\t" \
    "v_mfma_f32_16x16x32_bf16 v[40:43], v[120:123], v[104:107], v[40:43]\n\t" \
    "global_load_lds_dwordx4 %[vb], s[64:65]\n\t" \
    "s_addc_u32 s61, s61, 0\n\t" \
    "s_add_u32 s62, s62, 0x80000\n\t" \
    "v_mfma_f32_16x16x32_bf16 v[44:47], v[124:127], v[104:107], v[44:47]\n\t" \
    "s_addc_u32 s63, s63, 0\n\t" \
    "v_mov_b32 v128, v131\n\t" \
    "v_mov_b32 v129, v132\n\t" \
    "v_mfma_f32_16x16x32_bf16 v[48:51], v[112:115], v[108:111], v[48:51]\n\t" \
    "v_mov_b32 v130, v133\n\t" \
    "v_add_u32 v131, s69, v134\n\t" \
    "v_add_u32 v132, s69, v135\n\t" \
    "v_add_u32 v133, s69, v136\n\t" \
    "v_mfma_f32_16x16x32_bf16 v[52:55], v[116:119], v[108:111], v[52:55]\n\t" \
    "s_add_u32 s68, s68, 49152\n\t" \
    "s_cmp_eq_u32 s68, 147456\n\t" \
    "s_cselect_b32 s68, 0, s68\n\t" \
    "s_add_u32 s69, s69, 49152\n\t" \
    "v_mfma_f32_16x16x32_bf16 v[56:59], v[120:123], v[108:111], v[56:59]\n\t" \
    "s_cmp_eq_u32 s69, 147456\n\t" \
    "s_cselect_b32 s69, 0, s69\n\t" \
    "v_mfma_f32_16x16x32_bf16 v[60:63], v[124:127], v[108:111], v[60:63]\n\t" \
    "s_sub_u32 s67, s67, 1\n\t" \
    "s_cmp_lg_u32 s67, 0\n\t" \
    "s_cbranch_scc1 LOOP_%=\n\t" \
    "s_waitcnt lgkmcnt(0)\n\t" \
    "v_mfma_f32_16x16x32_bf16 v[0:3], v[80:83], v[64:67], v[0:3]\n\t" \
    "ds_read_b128 v[96:99], v128 offset:8192\n\t" \
    "ds_read_b64_tr_b16 v[112:113], v129 offset:8192\n\t" \
    "v_mfma_f32_16x16x32_bf16 v[4:7], v[84:87], v[64:67], v[4:7]\n\t" \
    "ds_read_b64_tr_b16 v[114:115], v129 offset:8448\n\t" \
    "ds_read_b64_tr_b16 v[116:117], v130 offset:8192\n\t" \
    "v_mfma_f32_16x16x32_bf16 v[8:11], v[88:91], v[64:67], v[8:11]\n\t" \
    "ds_read_b64_tr_b16 v[118:119], v130 offset:8448\n\t" \
    "ds_read_b128 v[100:103], v128 offset:8448\n\t" \
    "v_mfma_f32_16x16x32_bf16 v[12:15], v[92:95], v[64:67], v[12:15]\n\t" \
    "ds_read_b64_tr_b16 v[120:121], v129 offset:8704\n\t" \
    "ds_read_b64_tr_b16 v[122:123], v129 offset:8960\n\t" \
    "v_mfma_f32_16x16x32_bf16 v[16:19], v[80:83], v[68:71], v[16:19]\n\t" \
    "ds_read_b64_tr_b16 v[124:125], v130 offset:8704\n\t" \
    "ds_read_b64_tr_b16 v[126:127], v130 offset:8960\n\t" \
    "v_mfma_f32_16x16x32_bf16 v[20:23], v[84:87], v[68:71], v[20:23]\n\t" \
    "ds_read_b128 v[104:107], v128 offset:8704\n\t" \
    "ds_read_b128 v[108:111], v128 offset:8960\n\t" \
    "v_mfma_f32_16x16x32_bf16 v[24:27], v[88:91], v[68:71], v[24:27]\n\t" \
    "v_mfma_f32_16x16x32_bf16 v[28:31], v[92:95], v[68:71], v[28:31]\n\t" \
    "v_mfma_f32_16x16x32_bf16 v[32:35], v[80:83], v[72:75], v[32:35]\n\t" \
    "v_mfma_f32_16x16x32_bf16 v[36:39], v[84:87], v[72:75], v[36:39]\n\t" \
    "v_mfma_f32_16x16x32_bf16 v[40:43], v[88:91], v[72:75], v[40:43]\n\t" \
    "v_mfma_f32_16x16x32_bf16 v[44:47], v[92:95], v[72:75], v[44:47]\n\t" \
    "v_mfma_f32_16x16x32_bf16 v[48:51], v[80:83], v[76:79], v[48:51]\n\t" \
    "v_mfma_f32_16x16x32_bf16 v[52:55], v[84:87], v[76:79], v[52:55]\n\t" \
    "v_mfma_f32_16x16x32_bf16 v[56:59], v[88:91], v[76:79], v[56:59]\n\t" \
    "v_mfma_f32_16x16x32_bf16 v[60:63], v[92:95], v[76:79], v[60:63]\n\t" \
    "s_waitcnt vmcnt(6) lgkmcnt(0)\n\t" \
    "s_barrier\n\t" \
    "v_mfma_f32_16x16x32_bf16 v[0:3], v[112:115], v[96:99], v[0:3]\n\t" \
    "ds_read_b128 v[64:67], v131 offset:0\n\t" \
    "ds_read_b64_tr_b16 v[80:81], v132 offset:0\n\t" \
    "v_mfma_f32_16x16x32_bf16 v[4:7], v[116:119], v[96:99], v[4:7]\n\t" \
    "ds_read_b64_tr_b16 v[82:83], v132 offset:256\n\t" \
    "ds_read_b64_tr_b16 v[84:85], v133 offset:0\n\t" \
    "v_mfma_f32_16x16x32_bf16 v[8:11], v[120:123], v[96:99], v[8:11]\n\t" \
    "ds_read_b64_tr_b16 v[86:87], v133 offset:256\n\t" \
    "ds_read_b128 v[68:71], v131 offset:256\n\t" \
    "v_mfma_f32_16x16x32_bf16 v[12:15], v[124:127], v[96:99], v[12:15]\n\t" \
    "ds_read_b64_tr_b16 v[88:89], v132 offset:512\n\t" \
    "ds_read_b64_tr_b16 v[90:91], v132 offset:768\n\t" \
    "v_mfma_f32_16x16x32_bf16 v[16:19], v[112:115], v[100:103], v[16:19]\n\t" \
    "ds_read_b64_tr_b16 v[92:93], v133 offset:512\n\t" \
    "ds_read_b64_tr_b16 v[94:95], v133 offset:768\n\t" \
    "v_mfma_f32_16x16x32_bf16 v[20:23], v[116:119], v[100:103], v[20:23]\n\t" \
    "ds_read_b128 v[72:75], v131 offset:512\n\t" \
    "ds_read_b128 v[76:79], v131 offset:768\n\t" \
    "v_mfma_f32_16x16x32_bf16 v[24:27], v[120:123], v[100:103], v[24:27]\n\t" \
    "v_mov_b32 v128, v131\n\t" \
    "v_mov_b32 v129, v132\n\t" \
    "v_mov_b32 v130, v133\n\t" \
    "v_mfma_f32_16x16x32_bf16 v[28:31], v[124:127], v[100:103], v[28:31]\n\t" \
    "v_add_u32 v131, s69, v134\n\t" \
    "v_add_u32 v132, s69, v135\n\t" \
    "v_add_u32 v133, s69, v136\n\t" \
    "v_mfma_f32_16x16x32_bf16 v[32:35], v[112:115], v[104:107], v[32:35]\n\t" \
    "s_add_u32 s68, s68, 49152\n\t" \
    "s_cmp_eq_u32 s68, 147456\n\t" \
    "s_cselect_b32 s68, 0, s68\n\t" \
    "v_mfma_f32_16x16x32_bf16 v[36:39], v[116:119], v[104:107], v[36:39]\n\t" \
    "s_add_u32 s69, s69, 49152\n\t" \
    "s_cmp_eq_u32 s69, 147456\n\t" \
    "s_cselect_b32 s69, 0, s69\n\t" \
    "v_mfma_f32_16x16x32_bf16 v[40:43], v[120:123], v[104:107], v[40:43]\n\t" \
    "v_mfma_f32_16x16x32_bf16 v[44:47], v[124:127], v[104:107], v[44:47]\n\t" \
    "v_mfma_f32_16x16x32_bf16 v[48:51], v[112:115], v[108:111], v[48:51]\n\t" \
    "v_mfma_f32_16x16x32_bf16 v[52:55], v[116:119], v[108:111], v[52:55]\n\t" \
    "v_mfma_f32_16x16x32_bf16 v[56:59], v[120:123], v[108:111], v[56:59]\n\t" \
    "v_mfma_f32_16x16x32_bf16 v[60:63], v[124:127], v[108:111], v[60:63]\n\t" \
    "s_waitcnt lgkmcnt(0)\n\t" \
    "v_mfma_f32_16x16x32_bf16 v[0:3], v[80:83], v[64:67], v[0:3]\n\t" \
    "ds_read_b128 v[96:99], v128 offset:8192\n\t" \
    "ds_read_b64_tr_b16 v[112:113], v129 offset:8192\n\t" \
    "v_mfma_f32_16x16x32_bf16 v[4:7], v[84:87], v[64:67], v[4:7]\n\t" \
    "ds_read_b64_tr_b16 v[114:115], v129 offset:8448\n\t" \
    "ds_read_b64_tr_b16 v[116:117], v130 offset:8192\n\t" \
    "v_mfma_f32_16x16x32_bf16 v[8:11], v[88:91], v[64:67], v[8:11]\n\t" \
    "ds_read_b64_tr_b16 v[118:119], v130 offset:8448\n\t" \
    "ds_read_b128 v[100:103], v128 offset:8448\n\t" \
    "v_mfma_f32_16x16x32_bf16 v[12:15], v[92:95], v[64:67], v[12:15]\n\t" \
    "ds_read_b64_tr_b16 v[120:121], v129 offset:8704\n\t" \
    "ds_read_b64_tr_b16 v[122:123], v129 offset:8960\n\t" \
    "v_mfma_f32_16x16x32_bf16 v[16:19], v[80:83], v[68:71], v[16:19]\n\t" \
    "ds_read_b64_tr_b16 v[124:125], v130 offset:8704\n\t" \
    "ds_read_b64_tr_b16 v[126:127], v130 offset:8960\n\t" \
    "v_mfma_f32_16x16x32_bf16 v[20:23], v[84:87], v[68:71], v[20:23]\n\t" \
    "ds_read_b128 v[104:107], v128 offset:8704\n\t" \
    "ds_read_b128 v[108:111], v128 offset:8960\n\t" \
    "v_mfma_f32_16x16x32_bf16 v[24:27], v[88:91], v[68:71], v[24:27]\n\t" \
    "v_mfma_f32_16x16x32_bf16 v[28:31], v[92:95], v[68:71], v[28:31]\n\t" \
    "v_mfma_f32_16x16x32_bf16 v[32:35], v[80:83], v[72:75], v[32:35]\n\t" \
    "v_mfma_f32_16x16x32_bf16 v[36:39], v[84:87], v[72:75], v[36:39]\n\t" \
    "v_mfma_f32_16x16x32_bf16 v[40:43], v[88:91], v[72:75], v[40:43]\n\t" \
    "v_mfma_f32_16x16x32_bf16 v[44:47], v[92:95], v[72:75], v[44:47]\n\t" \
    "v_mfma_f32_16x16x32_bf16 v[48:51], v[80:83], v[76:79], v[48:51]\n\t" \
    "v_mfma_f32_16x16x32_bf16 v[52:55], v[84:87], v[76:79], v[52:55]\n\t" \
    "v_mfma_f32_16x16x32_bf16 v[56:59], v[88:91], v[76:79], v[56:59]\n\t" \
    "v_mfma_f32_16x16x32_bf16 v[60:63], v[92:95], v[76:79], v[60:63]\n\t" \
    "s_waitcnt vmcnt(0) lgkmcnt(0)\n\t" \
    "s_barrier\n\t" \
    "v_mfma_f32_16x16x32_bf16 v[0:3], v[112:115], v[96:99], v[0:3]\n\t" \
    "ds_read_b128 v[64:67], v131 offset:0\n\t" \
    "ds_read_b64_tr_b16 v[80:81], v132 offset:0\n\t" \
    "v_mfma_f32_16x16x32_bf16 v[4:7], v[116:119], v[96:99], v[4:7]\n\t" \
    "ds_read_b64_tr_b16 v[82:83], v132 offset:256\n\t" \
    "ds_read_b64_tr_b16 v[84:85], v133 offset:0\n\t" \
    "v_mfma_f32_16x16x32_bf16 v[8:11], v[120:123], v[96:99], v[8:11]\n\t" \
    "ds_read_b64_tr_b16 v[86:87], v133 offset:256\n\t" \
    "ds_read_b128 v[68:71], v131 offset:256\n\t" \
    "v_mfma_f32_16x16x32_bf16 v[12:15], v[124:127], v[96:99], v[12:15]\n\t" \
    "ds_read_b64_tr_b16 v[88:89], v132 offset:512\n\t" \
    "ds_read_b64_tr_b16 v[90:91], v132 offset:768\n\t" \
    "v_mfma_f32_16x16x32_bf16 v[16:19], v[112:115], v[100:103], v[16:19]\n\t" \
    "ds_read_b64_tr_b16 v[92:93], v133 offset:512\n\t" \
    "ds_read_b64_tr_b16 v[94:95], v133 offset:768\n\t" \
    "v_mfma_f32_16x16x32_bf16 v[20:23], v[116:119], v[100:103], v[20:23]\n\t" \
    "ds_read_b128 v[72:75], v131 offset:512\n\t" \
    "ds_read_b128 v[76:79], v131 offset:768\n\t" \
    "v_mfma_f32_16x16x32_bf16 v[24:27], v[120:123], v[100:103], v[24:27]\n\t" \
    "v_mov_b32 v128, v131\n\t" \
    "v_mov_b32 v129, v132\n\t" \
    "v_mov_b32 v130, v133\n\t" \
    "v_mfma_f32_16x16x32_bf16 v[28:31], v[124:127], v[100:103], v[28:31]\n\t" \
    "v_add_u32 v131, s69, v134\n\t" \
    "v_add_u32 v132, s69, v135\n\t" \
    "v_add_u32 v133, s69, v136\n\t" \
    "v_mfma_f32_16x16x32_bf16 v[32:35], v[112:115], v[104:107], v[32:35]\n\t" \
    "s_add_u32 s68, s68, 49152\n\t" \
    "s_cmp_eq_u32 s68, 147456\n\t" \
    "s_cselect_b32 s68, 0, s68\n\t" \
    "v_mfma_f32_16x16x32_bf16 v[36:39], v[116:119], v[104:107], v[36:39]\n\t" \
    "s_add_u32 s69, s69, 49152\n\t" \
    "s_cmp_eq_u32 s69, 147456\n\t" \
    "s_cselect_b32 s69, 0, s69\n\t" \
    "v_mfma_f32_16x16x32_bf16 v[40:43], v[120:123], v[104:107], v[40:43]\n\t" \
    "v_mfma_f32_16x16x32_bf16 v[44:47], v[124:127], v[104:107], v[44:47]\n\t" \
    "v_mfma_f32_16x16x32_bf16 v[48:51], v[112:115], v[108:111], v[48:51]\n\t" \
    "v_mfma_f32_16x16x32_bf16 v[52:55], v[116:119], v[108:111], v[52:55]\n\t" \
    "v_mfma_f32_16x16x32_bf16 v[56:59], v[120:123], v[108:111], v[56:59]\n\t" \
    "v_mfma_f32_16x16x32_bf16 v[60:63], v[124:127], v[108:111], v[60:63]\n\t" \
    "s_waitcnt lgkmcnt(0)\n\t" \
    "v_mfma_f32_16x16x32_bf16 v[0:3], v[80:83], v[64:67], v[0:3]\n\t" \
    "ds_read_b128 v[96:99], v128 offset:8192\n\t" \
    "ds_read_b64_tr_b16 v[112:113], v129 offset:8192\n\t" \
    "v_mfma_f32_16x16x32_bf16 v[4:7], v[84:87], v[64:67], v[4:7]\n\t" \
    "ds_read_b64_tr_b16 v[114:115], v129 offset:8448\n\t" \
    "ds_read_b64_tr_b16 v[116:117], v130 offset:8192\n\t" \
    "v_mfma_f32_16x16x32_bf16 v[8:11], v[88:91], v[64:67], v[8:11]\n\t" \
    "ds_read_b64_tr_b16 v[118:119], v130 offset:8448\n\t" \
    "ds_read_b128 v[100:103], v128 offset:8448\n\t" \
    "v_mfma_f32_16x16x32_bf16 v[12:15], v[92:95], v[64:67], v[12:15]\n\t" \
    "ds_read_b64_tr_b16 v[120:121], v129 offset:8704\n\t" \
    "ds_read_b64_tr_b16 v[122:123], v129 offset:8960\n\t" \
    "v_mfma_f32_16x16x32_bf16 v[16:19], v[80:83], v[68:71], v[16:19]\n\t" \
    "ds_read_b64_tr_b16 v[124:125], v130 offset:8704\n\t" \
    "ds_read_b64_tr_b16 v[126:127], v130 offset:8960\n\t" \
    "v_mfma_f32_16x16x32_bf16 v[20:23], v[84:87], v[68:71], v[20:23]\n\t" \
    "ds_read_b128 v[104:107], v128 offset:8704\n\t" \
    "ds_read_b128 v[108:111], v128 offset:8960\n\t" \
    "v_mfma_f32_16x16x32_bf16 v[24:27], v[88:91], v[68:71], v[24:27]\n\t" \
    "v_mfma_f32_16x16x32_bf16 v[28:31], v[92:95], v[68:71], v[28:31]\n\t" \
    "v_mfma_f32_16x16x32_bf16 v[32:35], v[80:83], v[72:75], v[32:35]\n\t" \
    "v_mfma_f32_16x16x32_bf16 v[36:39], v[84:87], v[72:75], v[36:39]\n\t" \
    "v_mfma_f32_16x16x32_bf16 v[40:43], v[88:91], v[72:75], v[40:43]\n\t" \
    "v_mfma_f32_16x16x32_bf16 v[44:47], v[92:95], v[72:75], v[44:47]\n\t" \
    "v_mfma_f32_16x16x32_bf16 v[48:51], v[80:83], v[76:79], v[48:51]\n\t" \
    "v_mfma_f32_16x16x32_bf16 v[52:55], v[84:87], v[76:79], v[52:55]\n\t" \
    "v_mfma_f32_16x16x32_bf16 v[56:59], v[88:91], v[76:79], v[56:59]\n\t" \
    "v_mfma_f32_16x16x32_bf16 v[60:63], v[92:95], v[76:79], v[60:63]\n\t" \
    "s_waitcnt vmcnt(0) lgkmcnt(0)\n\t" \
    "s_barrier\n\t" \
    "v_mfma_f32_16x16x32_bf16 v[0:3], v[112:115], v[96:99], v[0:3]\n\t" \
    "v_mov_b32 v128, v131\n\t" \
    "v_mov_b32 v129, v132\n\t" \
    "v_mfma_f32_16x16x32_bf16 v[4:7], v[116:119], v[96:99], v[4:7]\n\t" \
    "v_mov_b32 v130, v133\n\t" \
    "v_add_u32 v131, s69, v134\n\t" \
    "v_mfma_f32_16x16x32_bf16 v[8:11], v[120:123], v[96:99], v[8:11]\n\t" \
    "v_add_u32 v132, s69, v135\n\t" \
    "v_add_u32 v133, s69, v136\n\t" \
    "v_mfma_f32_16x16x32_bf16 v[12:15], v[124:127], v[96:99], v[12:15]\n\t" \
    "s_add_u32 s68, s68, 49152\n\t" \
    "s_cmp_eq_u32 s68, 147456\n\t" \
    "v_mfma_f32_16x16x32_bf16 v[16:19], v[112:115], v[100:103], v[16:19]\n\t" \
    "s_cselect_b32 s68, 0, s68\n\t" \
    "s_add_u32 s69, s69, 49152\n\t" \
    "v_mfma_f32_16x16x32_bf16 v[20:23], v[116:119], v[100:103], v[20:23]\n\t" \
    "s_cmp_eq_u32 s69, 147456\n\t" \
    "s_cselect_b32 s69, 0, s69\n\t" \
    "v_mfma_f32_16x16x32_bf16 v[24:27], v[120:123], v[100:103], v[24:27]\n\t" \
    "v_mfma_f32_16x16x32_bf16 v[28:31], v[124:127], v[100:103], v[28:31]\n\t" \
    "v_mfma_f32_16x16x32_bf16 v[32:35], v[112:115], v[104:107], v[32:35]\n\t" \
    "v_mfma_f32_16x16x32_bf16 v[36:39], v[116:119], v[104:107], v[36:39]\n\t" \
    "v_mfma_f32_16x16x32_bf16 v[40:43], v[120:123], v[104:107], v[40:43]\n\t" \
    "v_mfma_f32_16x16x32_bf16 v[44:47], v[124:127], v[104:107], v[44:47]\n\t" \
    "v_mfma_f32_16x16x32_bf16 v[48:51], v[112:115], v[108:111], v[48:51]\n\t" \
    "v_mfma_f32_16x16x32_bf16 v[52:55], v[116:119], v[108:111], v[52:55]\n\t" \
    "v_mfma_f32_16x16x32_bf16 v[56:59], v[120:123], v[108:111], v[56:59]\n\t" \
    "v_mfma_f32_16x16x32_bf16 v[60:63], v[124:127], v[108:111], v[60:63]\n\t" \
    "s_nop 15\n\t"
#define MX3(a, b, c) __builtin_fmaxf(__builtin_fmaxf((a), (b)), (c))
DI float max16(const f32x16& p) {
    float a = MX3(p[0], p[1], p[2]), b = MX3(p[3], p[4], p[5]);
    a = MX3(a, p[6], p[7]); b = MX3(b, p[8], p[9]); a = MX3(a, p[10], p[11]); b = MX3(b, p[12], p[13]); a = MX3(a, p[14], p[15]);
    return __builtin_fmaxf(a, b);
}
DI float halfmax(float m) { auto rr = __builtin_amdgcn_permlane32_swap(__float_as_uint(m), __float_as_uint(m), false, false); return __builtin_fmaxf(__uint_as_float(rr[0]), __uint_as_float(rr[1])); }
DI void cmask(f32x16& p, int r32, int hi) {
#pragma unroll
    for (int r = 0; r < 16; ++r) { const int kv = 4 * hi + (r & 3) + 8 * (r >> 2); if (kv > r32) p[r] = -INFINITY; }
}
#define WAITBAR(N) asm volatile("s_waitcnt vmcnt(" #N ")\n\ts_barrier" ::: "memory")
typedef unsigned u32x16 __attribute__((ext_vector_type(16)));

__global__ __launch_bounds__(512, 2) void k2_attn(const char* __restrict__ xn2, const char* __restrict__ wq, const char* __restrict__ K, const char* __restrict__ VT, char* __restrict__ CTX, const char* __restrict__ wo, const char* __restrict__ w2) {
    extern __shared__ __attribute__((aligned(16))) unsigned char smem[];
    const lds_cptr lds = (lds_cptr)smem;
    const int tid = threadIdx.x, lane = tid & 63, r32 = lane & 31, hi = lane >> 5, wid = __builtin_amdgcn_readfirstlane(tid >> 6);
    const int xcd = blockIdx.x & 7, idx = blockIdx.x >> 3, h = K2_HEAD, ib = idx & 7;
    const int G = wid < 4 ? 4 * ib + wid : 63 - 4 * ib - (wid - 4);
    const int NT = 32 - 2 * ib;
    const char* Kh = K + (size_t)h * 32 * SLOT; const char* Vh = VT + (size_t)h * 32 * SLOT;
    const unsigned lds0 = (unsigned)(uintptr_t)smem;
    const unsigned dmaw = (unsigned)(wid - 4) >> 31;
    const unsigned dvo = (wid & 3) * 4096 + lane * 16, kdst = lds0 + LDS_K + (wid & 3) * 4096, vdst = lds0 + LDS_V + (wid & 3) * 4096;
#define DMA_T(base, dst, t, sl) do { const char* g_ = (base) + (size_t)(t) * SLOT; const unsigned l_ = (unsigned)__builtin_amdgcn_readfirstlane((dst) + ((sl) & 3) * SLOT); glds2(g_, dvo, l_); glds2(g_ + 2048, dvo, l_ + 2048); } while (0)
#define DMA_K(t, sl) DMA_T(Kh, kdst, t, sl)
#define DMA_V(t, sl) DMA_T(Vh, vdst, t, sl)
#define DMA_STEP(s) do { if (dmaw) { DMA_K(min((s) + 3, NT - 1), (s) + 3); DMA_V(min((s) + 2, NT - 1), (s) + 2); } } while (0)
    STAMP(2, 0);
    const unsigned cpf = code_pf(tid, 120);
    u32x16 qa, qb;
    {
        const int gr = wid >> 1, gc = wid & 1, fr = lane & 15, fq = lane >> 4;
        const char* a1 = xn2 + (size_t)((wid >> 2) ? 15 - ib : ib) * 16 * 16384 + (wid & 3) * 4096;
        const char* b1 = wq + (size_t)h * 256 + (size_t)(8 * (wid >> 1)) * (NQ * 2) + 128 * (wid & 1);
        const unsigned va = lane * 16, vb = ((lane & 31) >> 2) * (NQ * 2) + (32 * (lane >> 5) + 8 * ((lane & 3) ^ (wid & 2))) * 2;
        const unsigned mA = lds0 + (wid >> 2) * 16384 + (wid & 3) * 4096, mB = lds0 + 32768 + wid * 1024;
        const unsigned ra = lds0 + (gr >> 1) * 16384 + fq * 2048 + ((gr & 1) * 64 + fr) * 16;
        const unsigned rb0 = lds0 + 32768 + fq * 2048 + gc * 1024 + (fr >> 2) * 64 + (fq & 1) * 32 + (fr & 3) * 8, rb1 = rb0 ^ 32;
        f32x16 c0 = f32x16{}, c1 = f32x16{}, c2 = f32x16{}, c3 = f32x16{};
        asm volatile(K2Q_ASM
                     : "+&{v[0:15]}"(c0), "+&{v[16:31]}"(c1), "+&{v[32:47]}"(c2), "+&{v[48:63]}"(c3)
                     : "{v134}"(ra), "{v135}"(rb0), "{v136}"(rb1),
                       [va] "v"(va), [vb] "v"(vb), [a1] "s"(a1), [b1] "s"(b1), [nl] "s"(13), [mA] "s"(mA), [mB] "s"(mB)
                     : "v64", "v65", "v66", "v67", "v68", "v69", "v70", "v71", "v72", "v73", "v74", "v75", "v76", "v77", "v78", "v79", "v80", "v81", "v82", "v83", "v84", "v85", "v86", "v87", "v88", "v89", "v90", "v91", "v92", "v93", "v94", "v95", "v96", "v97", "v98", "v99", "v100", "v101", "v102", "v103", "v104", "v105", "v106", "v107", "v108", "v109", "v110", "v111", "v112", "v113", "v114", "v115", "v116", "v117", "v118", "v119", "v120", "v121", "v122", "v123", "v124", "v125", "v126", "v127", "v128", "v129", "v130", "v131", "v132", "v133", "s60", "s61", "s62", "s63", "s64", "s65", "s66", "s67", "s68", "s69", "scc", "memory");
        STAMP(2, 1);
        asm volatile("s_waitcnt lgkmcnt(0)\n\ts_barrier" ::: "memory");
        if (dmaw) { DMA_K(0, 0); DMA_V(0, 0); DMA_K(1, 1); DMA_K(2, 2); DMA_V(1, 1); }
        const int cg = ((fq & 1) << 1) | (fq >> 1);
        const float C2 = 0.12751743082459868f;
#define K2_QCH(c) ((c) < 4 ? 3 * SLOT + (c) * 4096 : LDS_V + 2 * SLOT + ((c) - 4) * 4096)
#define K2_QW(c, m) do { _Pragma("unroll") for (int np = 0; np < 2; ++np) { const int ch = 8 * gc + 4 * np + cg; \
            const u32x4 w = k1::pack_swap16((f32x4){c[8 * np], c[8 * np + 1], c[8 * np + 2], c[8 * np + 3]} * C2, (f32x4){c[8 * np + 4], c[8 * np + 5], c[8 * np + 6], c[8 * np + 7]} * C2); \
            *(LAS u32x4*)((lds_ptr)smem + K2_QCH(ch) + (64 * gr + 16 * (m) + fr) * 16) = w; } } while (0)
        K2_QW(c0, 0); K2_QW(c1, 1); K2_QW(c2, 2); K2_QW(c3, 3);
        asm volatile("s_waitcnt lgkmcnt(0)\n\ts_barrier" ::: "memory");
        const int qrow = (wid < 4 ? 32 * wid : 128 + 32 * (7 - wid)) + r32;
#pragma unroll
        for (int d0 = 0; d0 < 8; ++d0) { const int ch = 2 * d0 + hi; const u32x4 q = *(const LAS u32x4*)(lds + K2_QCH(ch) + qrow * 16);
#pragma unroll
            for (int e = 0; e < 4; ++e) { if (d0 < 4) qa[4 * d0 + e] = q[e]; else qb[4 * (d0 - 4) + e] = q[e]; } }
        asm volatile("s_waitcnt vmcnt(8) lgkmcnt(0)\n\ts_barrier" : "+v"(qa), "+v"(qb) :: "memory");
    }
    STAMP(2, 2);
    CODE_PF_DONE(cpf);
    const unsigned kp0 = lds0 + LDS_K + hi * 1024 + r32 * 16, vp0 = lds0 + LDS_V + hi * 2048 + r32 * 16;
    f32x16 o0 = f32x16{}, o1 = f32x16{}, o2 = f32x16{}, o3 = f32x16{}, negm, cA, cB, P = f32x16{};
    u32x16 kfa, kfb;
    float l_reg = 0.f, mhat;
    {
        f32x16 s = f32x16{};
#pragma unroll
        for (int j = 0; j < 8; ++j) {
            const bf16x8 kf = lds_rd(lds + LDS_K + hi * 1024 + r32 * 16 + j * 2048); const u32x4 kw = __builtin_bit_cast(u32x4, kf);
#pragma unroll
            for (int e = 0; e < 4; ++e) { if (j < 4) kfa[4 * j + e] = kw[e]; else kfb[4 * (j - 4) + e] = kw[e]; }
            const u32x4 qw = j < 4 ? (u32x4){qa[4 * j], qa[4 * j + 1], qa[4 * j + 2], qa[4 * j + 3]} : (u32x4){qb[4 * j - 16], qb[4 * j - 15], qb[4 * j - 14], qb[4 * j - 13]};
            s = MFMA32(kf, __builtin_bit_cast(bf16x8, qw), s);
        }
        if (G == 0) cmask(s, r32, hi);
        mhat = halfmax(max16(s));
#pragma unroll
        for (int r = 0; r < 16; ++r) { negm[r] = -mhat; cB[r] = -INFINITY; cA[r] = -INFINITY; }
    }
    const float thr = 65536.0f;
    int t = 0; unsigned soff = 0;
    unsigned vk = kp0, vkn = kp0 + SLOT, vv = vp0, vvp = vp0;
#define K2_STATE [o0] "+&{v[0:15]}"(o0), [o1] "+&{v[16:31]}"(o1), [o2] "+&{v[32:47]}"(o2), [o3] "+&{v[48:63]}"(o3), [negm] "+&{v[96:111]}"(negm), [ca] "+&{v[112:127]}"(cA), [cb] "+&{v[128:143]}"(cB), \
        [p] "+&{v[144:159]}"(P), [kfa] "+&{v[168:183]}"(kfa), [kfb] "+&{v[184:199]}"(kfb), [vk] "+&{v232}"(vk), [vkn] "+&{v233}"(vkn), [vv] "+&{v234}"(vv), [vvp] "+&{v235}"(vvp), [l] "+&{v236}"(l_reg), [mh] "+&{v238}"(mhat)
#define K2_IN [qa] "{v[64:79]}"(qa), [qb] "{v[80:95]}"(qb), [thr] "s"(thr)
#define K2_CLOB "v160", "v161", "v162", "v163", "v164", "v165", "v166", "v167", "v200", "v201", "v202", "v203", "v204", "v205", "v206", "v207", "v208", "v209", "v210", "v211", "v212", "v213", "v214", "v215", \
        "v216", "v217", "v218", "v219", "v220", "v221", "v222", "v223", "v224", "v225", "v226", "v227", "v228", "v229", "v230", "v231", "v237", "v239", "v240", "v241", "vcc", "scc", "memory"
    const unsigned pfg = blockIdx.x * 256 + (tid & 255), pfo = pfg * 128u, pfo2 = (pfg < 57344u ? pfg : 0u) * 128u;
    const int nloop = G >> 1;
    asm volatile(K2_ASM : K2_STATE, [t] "+&s"(t), [soff] "+&s"(soff)
                 : K2_IN, [n] "s"(nloop), [odd] "s"(G & 1), [dmaw] "s"(dmaw), [ntm1] "s"(NT - 1), [khl] "s"((unsigned)(uintptr_t)Kh), [khh] "s"((unsigned)((uintptr_t)Kh >> 32)),
                   [vhl] "s"((unsigned)(uintptr_t)Vh), [vhh] "s"((unsigned)((uintptr_t)Vh >> 32)), [kdst] "s"(kdst), [vdst] "s"(vdst), [dvo] "v"(dvo), [kp0] "v"(kp0), [vp0] "v"(vp0), [dq] "v"(r32 - 4 * hi),
                   [pfo] "v"(pfo), [pfo2] "v"(pfo2), [wo] "s"(wo), [w2] "s"(w2)
                 : K2_CLOB, "s60", "s61", "s62", "s63", "v250", "v251");
    STAMP(2, 3);
    {
        auto rr = __builtin_amdgcn_permlane32_swap(__float_as_uint(l_reg), __float_as_uint(l_reg), false, false);
        const float inv = 1.0f / (__uint_as_float(rr[0]) + __uint_as_float(rr[1]));
        char* op = CTX + ((size_t)((G >> 2) * 64 + 2 * h) * 8 + hi) * 2048 + ((G & 3) * 32 + r32) * 16;
#define K2_ST(o, db) do { _Pragma("unroll") for (int g = 0; g < 4; g += 2) { \
            const unsigned x0 = cvtpk(o[4 * g] * inv, o[4 * g + 1] * inv), x1 = cvtpk(o[4 * g + 2] * inv, o[4 * g + 3] * inv); \
            const unsigned y0 = cvtpk(o[4 * g + 4] * inv, o[4 * g + 5] * inv), y1 = cvtpk(o[4 * g + 6] * inv, o[4 * g + 7] * inv); \
            const auto r0 = __builtin_amdgcn_permlane32_swap(x0, y0, false, false), r1 = __builtin_amdgcn_permlane32_swap(x1, y1, false, false); \
            ST16(op + (size_t)(((db) >> 1) * 8 + 4 * ((db) & 1) + g) * 2048, ((u32x4){r0[0], r1[0], r0[1], r1[1]})); } } while (0)
        K2_ST(o0, 0); K2_ST(o1, 1); K2_ST(o2, 2); K2_ST(o3, 3);
    }
    WAITBAR(16); ++t;
    if (t < NT) { DMA_STEP(t); WAITBAR(16); ++t;
        for (; t < NT; ++t) { DMA_STEP(t); WAITBAR(8); } }
    STAMP(2, 4);
    WAIT_V(0);
    STAMP(2, 5);
}
}

namespace k3 {
constexpr int STG = 49152, LDS_BYTES = 3 * STG;
typedef __attribute__((address_space(1))) unsigned gu32;
#define K3_ASM \
    "s_mov_b64 s[60:61], %[a1]\n\t" \
    "s_mov_b64 s[62:63], %[b1]\n\t" \
    "s_mov_b32 s67, %[nl]\n\t" \
    "s_mov_b32 s68, 0\n\t" \
    "s_add_u32 m0, %[mA], s68\n\t" \
    "s_add_u32 s66, %[mB], s68\n\t" \
    "global_load_lds_dwordx4 %[va], s[60:61] offset:0\n\t" \
    "global_load_lds_dwordx4 %[va], s[60:61] offset:1024\n\t" \
    "global_load_lds_dwordx4 %[va], s[60:61] offset:2048\n\t" \
    "global_load_lds_dwordx4 %[va], s[60:61] offset:3072\n\t" \
    "s_mov_b32 m0, s66\n\t" \
    "s_add_u32 s64, s62, 0x10000\n\t" \
    "s_addc_u32 s65, s63, 0\n\t" \
    "global_load_lds_dwordx4 %[vb], s[62:63]\n\t" \
    "s_add_u32 m0, s66, 8192\n\t" \
    "s_add_u32 s60, s60, 0x4000\n\t" \
    "global_load_lds_dwordx4 %[vb], s[64:65]\n\t" \
    "s_addc_u32 s61, s61, 0\n\t" \
    "s_add_u32 s62, s62, 0x20000\n\t" \
    "s_addc_u32 s63, s63, 0\n\t" \
    "s_mov_b32 s68, 49152\n\t" \
    "s_add_u32 m0, %[mA], s68\n\t" \
    "s_add_u32 s66, %[mB], s68\n\t" \
    "global_load_lds_dwordx4 %[va], s[60:61] offset:0\n\t" \
    "global_load_lds_dwordx4 %[va], s[60:61] offset:1024\n\t" \
    "global_load_lds_dwordx4 %[va], s[60:61] offset:2048\n\t" \
    "global_load_lds_dwordx4 %[va], s[60:61] offset:3072\n\t" \
    "s_mov_b32 m0, s66\n\t" \
    "s_add_u32 s64, s62, 0x10000\n\t" \
    "s_addc_u32 s65, s63, 0\n\t" \
    "global_load_lds_dwordx4 %[vb], s[62:63]\n\t" \
    "s_add_u32 m0, s66, 8192\n\t" \
    "s_add_u32 s60, s60, 0x4000\n\t" \
    "global_load_lds_dwordx4 %[vb], s[64:65]\n\t" \
    "s_addc_u32 s61, s61, 0\n\t" \
    "s_add_u32 s62, s62, 0x20000\n\t" \
    "s_addc_u32 s63, s63, 0\n\t" \
    "s_mov_b32 s68, 98304\n\t" \
    "s_add_u32 m0, %[mA], s68\n\t" \
    "s_add_u32 s66, %[mB], s68\n\t" \
    "global_load_lds_dwordx4 %[va], s[60:61] offset:0\n\t" \
    "global_load_lds_dwordx4 %[va], s[60:61] offset:1024\n\t" \
    "global_load_lds_dwordx4 %[va], s[60:61] offset:2048\n\t" \
    "global_load_lds_dwordx4 %[va], s[60:61] offset:3072\n\t" \
    "s_mov_b32 m0, s66\n\t" \
    "s_add_u32 s64, s62, 0x10000\n\t" \
    "s_addc_u32 s65, s63, 0\n\t" \
    "global_load_lds_dwordx4 %[vb], s[62:63]\n\t" \
    "s_add_u32 m0, s66, 8192\n\t" \
    "s_add_u32 s60, s60, 0x4000\n\t" \
    "global_load_lds_dwordx4 %[vb], s[64:65]\n\t" \
    "s_addc_u32 s61, s61, 0\n\t" \
    "s_add_u32 s62, s62, 0x20000\n\t" \
    "s_addc_u32 s63, s63, 0\n\t" \
    "s_mov_b32 s68, 0\n\t" \
    "s_mov_b32 s69, 98304\n\t" \
    "v_mov_b32 v128, v134\n\t" \
    "v_mov_b32 v129, v135\n\t" \
    "v_mov_b32 v130, v136\n\t" \
    "v_add_u32 v131, 49152, v134\n\t" \
    "v_add_u32 v132, 49152, v135\n\t" \
    "v_add_u32 v133, 49152, v136\n\t" \
    "s_waitcnt vmcnt(12)\n\t" \
    "s_barrier\n\t" \
    "ds_read_b128 v[64:67], v128 offset:0\n\t" \
    "ds_read_b64_tr_b16 v[80:81], v129 offset:0\n\t" \
    "ds_read_b64_tr_b16 v[82:83], v129 offset:256\n\t" \
    "ds_read_b64_tr_b16 v[84:85], v130 offset:0\n\t" \
    "ds_read_b64_tr_b16 v[86:87], v130 offset:256\n\t" \
    "ds_read_b128 v[68:71], v128 offset:256\n\t" \
    "ds_read_b64_tr_b16 v[88:89], v129 offset:512\n\t" \
    "ds_read_b64_tr_b16 v[90:91], v129 offset:768\n\t" \
    "ds_read_b64_tr_b16 v[92:93], v130 offset:512\n\t" \
    "ds_read_b64_tr_b16 v[94:95], v130 offset:768\n\t" \
    "ds_read_b128 v[72:75], v128 offset:512\n\t" \
    "ds_read_b128 v[76:79], v128 offset:768\n\t" \
    "LOOP_%=:\n\t" \
    "s_add_u32 m0, %[mA], s68\n\t" \
    "s_add_u32 s66, %[mB], s68\n\t" \
    "global_load_lds_dwordx4 %[va], s[60:61] offset:0\n\t" \
    "global_load_lds_dwordx4 %[va], s[60:61] offset:1024\n\t" \
    "global_load_lds_dwordx4 %[va], s[60:61] offset:2048\n\t" \
    "global_load_lds_dwordx4 %[va], s[60:61] offset:3072\n\t" \
    "s_mov_b32 m0, s66\n\t" \
    "s_add_u32 s64, s62, 0x10000\n\t" \
    "s_addc_u32 s65, s63, 0\n\t" \
    "global_load_lds_dwordx4 %[vb], s[62:63]\n\t" \
    "s_add_u32 m0, s66, 8192\n\t" \
    "s_add_u32 s60, s60, 0x4000\n\t" \
    "global_load_lds_dwordx4 %[vb], s[64:65]\n\t" \
    "s_addc_u32 s61, s61, 0\n\t" \
    "s_add_u32 s62, s62, 0x20000\n\t" \
    "s_addc_u32 s63, s63, 0\n\t" \
    "v_mfma_f32_16x16x32_bf16 v[0:3], v[112:115], v[96:99], v[0:3]\n\t" \
    "ds_read_b128 v[64:67], v131 offset:0\n\t" \
    "ds_read_b64_tr_b16 v[80:81], v132 offset:0\n\t" \
    "v_mfma_f32_16x16x32_bf16 v[4:7], v[116:119], v[96:99], v[4:7]\n\t" \
    "ds_read_b64_tr_b16 v[82:83], v132 offset:256\n\t" \
    "ds_read_b64_tr_b16 v[84:85], v133 offset:0\n\t" \
    "v_mfma_f32_16x16x32_bf16 v[8:11], v[120:123], v[96:99], v[8:11]\n\t" \
    "ds_read_b64_tr_b16 v[86:87], v133 offset:256\n\t" \
    "ds_read_b128 v[68:71], v131 offset:256\n\t" \
    "v_mfma_f32_16x16x32_bf16 v[12:15], v[124:127], v[96:99], v[12:15]\n\t" \
    "ds_read_b64_tr_b16 v[88:89], v132 offset:512\n\t" \
    "ds_read_b64_tr_b16 v[90:91], v132 offset:768\n\t" \
    "v_mfma_f32_16x16x32_bf16 v[16:19], v[112:115], v[100:103], v[16:19]\n\t" \
    "ds_read_b64_tr_b16 v[92:93], v133 offset:512\n\t" \
    "ds_read_b64_tr_b16 v[94:95], v133 offset:768\n\t" \
    "v_mfma_f32_16x16x32_bf16 v[20:23], v[116:119], v[100:103], v[20:23]\n\t" \
    "ds_read_b128 v[72:75], v131 offset:512\n\t" \
    "ds_read_b128 v[76:79], v131 offset:768\n\t" \
    "v_mfma_f32_16x16x32_bf16 v[24:27], v[120:123], v[100:103], v[24:27]\n\t" \
    "s_add_u32 m0, %[mA], s68\n\t" \
    "s_add_u32 s66, %[mB], s68\n\t" \
    "global_load_lds_dwordx4 %[va], s[60:61] offset:0\n\t" \
    "v_mfma_f32_16x16x32_bf16 v[28:31], v[124:127], v[100:103], v[28:31]\n\t" \
    "global_load_lds_dwordx4 %[va], s[60:61] offset:1024\n\t" \
    "global_load_lds_dwordx4 %[va], s[60:61] offset:2048\n\t" \
    "global_load_lds_dwordx4 %[va], s[60:61] offset:3072\n\t" \
    "v_mfma_f32_16x16x32_bf16 v[32:35], v[112:115], v[104:107], v[32:35]\n\t" \
    "s_mov_b32 m0, s66\n\t" \
    "s_add_u32 s64, s62, 0x10000\n\t" \
    "s_addc_u32 s65, s63, 0\n\t" \
    "v_mfma_f32_16x16x32_bf16 v[36:39], v[116:119], v[104:107], v[36:39]\n\t" \
    "global_load_lds_dwordx4 %[vb], s[62:63]\n\t" \
    "s_add_u32 m0, s66, 8192\n\t" \
    "s_add_u32 s60, s60, 0x4000\n\t" \
    "v_mfma_f32_16x16x32_bf16 v[40:43], v[120:123], v[104:107], v[40:43]\n\t" \
    "global_load_lds_dwordx4 %[vb], s[64:65]\n\t" \
    "s_addc_u32 s61, s61, 0\n\t" \
    "s_add_u32 s62, s62, 0x20000\n\t" \
    "v_mfma_f32_16x16x32_bf16 v[44:47], v[124:127], v[104:107], v[44:47]\n\t" \
    "s_addc_u32 s63, s63, 0\n\t" \
    "v_mov_b32 v128, v131\n\t" \
    "v_mov_b32 v129, v132\n\t" \
    "v_mfma_f32_16x16x32_bf16 v[48:51], v[112:115], v[108:111], v[48:51]\n\t" \
    "v_mov_b32 v130, v133\n\t" \
    "v_add_u32 v131, s69, v134\n\t" \
    "v_add_u32 v132, s69, v135\n\t" \
    "v_add_u32 v133, s69, v136\n\t" \
    "v_mfma_f32_16x16x32_bf16 v[52:55], v[116:119], v[108:111], v[52:55]\n\t" \
    "s_add_u32 s68, s68, 49152\n\t" \
    "s_cmp_eq_u32 s68, 147456\n\t" \
    "s_cselect_b32 s68, 0, s68\n\t" \
    "s_add_u32 s69, s69, 49152\n\t" \
    "v_mfma_f32_16x16x32_bf16 v[56:59], v[120:123], v[108:111], v[56:59]\n\t" \
    "s_cmp_eq_u32 s69, 147456\n\t" \
    "s_cselect_b32 s69, 0, s69\n\t" \
    "v_mfma_f32_16x16x32_bf16 v[60:63], v[124:127], v[108:111], v[60:63]\n\t" \
    "s_sub_u32 s67, s67, 1\n\t" \
    "s_cmp_lg_u32 s67, 0\n\t" \
    "s_cbranch_scc1 LOOP_%=\n\t" \
    "s_waitcnt lgkmcnt(0)\n\t" \
    "v_mfma_f32_16x16x32_bf16 v[0:3], v[80:83], v[64:67], v[0:3]\n\t" \
    "ds_read_b128 v[96:99], v128 offset:8192\n\t" \
    "ds_read_b64_tr_b16 v[112:113], v129 offset:8192\n\t" \
    "v_mfma_f32_16x16x32_bf16 v[4:7], v[84:87], v[64:67], v[4:7]\n\t" \
    "ds_read_b64_tr_b16 v[114:115], v129 offset:8448\n\t" \
    "ds_read_b64_tr_b16 v[116:117], v130 offset:8192\n\t" \
    "v_mfma_f32_16x16x32_bf16 v[8:11], v[88:91], v[64:67], v[8:11]\n\t" \
    "ds_read_b64_tr_b16 v[118:119], v130 offset:8448\n\t" \
    "ds_read_b128 v[100:103], v128 offset:8448\n\t" \
    "v_mfma_f32_16x16x32_bf16 v[12:15], v[92:95], v[64:67], v[12:15]\n\t" \
    "ds_read_b64_tr_b16 v[120:121], v129 offset:8704\n\t" \
    "ds_read_b64_tr_b16 v[122:123], v129 offset:8960\n\t" \
    "v_mfma_f32_16x16x32_bf16 v[16:19], v[80:83], v[68:71], v[16:19]\n\t" \
    "ds_read_b64_tr_b16 v[124:125], v130 offset:8704\n\t" \
    "ds_read_b64_tr_b16 v[126:127], v130 offset:8960\n\t" \
    "v_mfma_f32_16x16x32_bf16 v[20:23], v[84:87], v[68:71], v[20:23]\n\t" \
    "ds_read_b128 v[104:107], v128 offset:8704\n\t" \
    "ds_read_b128 v[108:111], v128 offset:8960\n\t" \
    "v_mfma_f32_16x16x32_bf16 v[24:27], v[88:91], v[68:71], v[24:27]\n\t" \
    "v_mfma_f32_16x16x32_bf16 v[28:31], v[92:95], v[68:71], v[28:31]\n\t" \
    "v_mfma_f32_16x16x32_bf16 v[32:35], v[80:83], v[72:75], v[32:35]\n\t" \
    "v_mfma_f32_16x16x32_bf16 v[36:39], v[84:87], v[72:75], v[36:39]\n\t" \
    "v_mfma_f32_16x16x32_bf16 v[40:43], v[88:91], v[72:75], v[40:43]\n\t" \
    "v_mfma_f32_16x16x32_bf16 v[44:47], v[92:95], v[72:75], v[44:47]\n\t" \
    "v_mfma_f32_16x16x32_bf16 v[48:51], v[80:83], v[76:79], v[48:51]\n\t" \
    "v_mfma_f32_16x16x32_bf16 v[52:55], v[84:87], v[76:79], v[52:55]\n\t" \
    "v_mfma_f32_16x16x32_bf16 v[56:59], v[88:91], v[76:79], v[56:59]\n\t" \
    "v_mfma_f32_16x16x32_bf16 v[60:63], v[92:95], v[76:79], v[60:63]\n\t" \
    "s_waitcnt vmcnt(6) lgkmcnt(0)\n\t" \
    "s_barrier\n\t" \
    "v_mfma_f32_16x16x32_bf16 v[0:3], v[112:115], v[96:99], v[0:3]\n\t" \
    "ds_read_b128 v[64:67], v131 offset:0\n\t" \
    "ds_read_b64_tr_b16 v[80:81], v132 offset:0\n\t" \
    "v_mfma_f32_16x16x32_bf16 v[4:7], v[116:119], v[96:99], v[4:7]\n\t" \
    "ds_read_b64_tr_b16 v[82:83], v132 offset:256\n\t" \
    "ds_read_b64_tr_b16 v[84:85], v133 offset:0\n\t" \
    "v_mfma_f32_16x16x32_bf16 v[8:11], v[120:123], v[96:99], v[8:11]\n\t" \
    "ds_read_b64_tr_b16 v[86:87], v133 offset:256\n\t" \
    "ds_read_b128 v[68:71], v131 offset:256\n\t" \
    "v_mfma_f32_16x16x32_bf16 v[12:15], v[124:127], v[96:99], v[12:15]\n\t" \
    "ds_read_b64_tr_b16 v[88:89], v132 offset:512\n\t" \
    "ds_read_b64_tr_b16 v[90:91], v132 offset:768\n\t" \
    "v_mfma_f32_16x16x32_bf16 v[16:19], v[112:115], v[100:103], v[16:19]\n\t" \
    "ds_read_b64_tr_b16 v[92:93], v133 offset:512\n\t" \
    "ds_read_b64_tr_b16 v[94:95], v133 offset:768\n\t" \
    "v_mfma_f32_16x16x32_bf16 v[20:23], v[116:119], v[100:103], v[20:23]\n\t" \
    "ds_read_b128 v[72:75], v131 offset:512\n\t" \
    "ds_read_b128 v[76:79], v131 offset:768\n\t" \
    "v_mfma_f32_16x16x32_bf16 v[24:27], v[120:123], v[100:103], v[24:27]\n\t" \
    "v_mov_b32 v128, v131\n\t" \
    "v_mov_b32 v129, v132\n\t" \
    "v_mov_b32 v130, v133\n\t" \
    "v_mfma_f32_16x16x32_bf16 v[28:31], v[124:127], v[100:103], v[28:31]\n\t" \
    "v_add_u32 v131, s69, v134\n\t" \
    "v_add_u32 v132, s69, v135\n\t" \
    "v_add_u32 v133, s69, v136\n\t" \
    "v_mfma_f32_16x16x32_bf16 v[32:35], v[112:115], v[104:107], v[32:35]\n\t" \
    "s_add_u32 s68, s68, 49152\n\t" \
    "s_cmp_eq_u32 s68, 147456\n\t" \
    "s_cselect_b32 s68, 0, s68\n\t" \
    "v_mfma_f32_16x16x32_bf16 v[36:39], v[116:119], v[104:107], v[36:39]\n\t" \
    "s_add_u32 s69, s69, 49152\n\t" \
    "s_cmp_eq_u32 s69, 147456\n\t" \
    "s_cselect_b32 s69, 0, s69\n\t" \
    "v_mfma_f32_16x16x32_bf16 v[40:43], v[120:123], v[104:107], v[40:43]\n\t" \
    "v_mfma_f32_16x16x32_bf16 v[44:47], v[124:127], v[104:107], v[44:47]\n\t" \
    "v_mfma_f32_16x16x32_bf16 v[48:51], v[112:115], v[108:111], v[48:51]\n\t" \
    "v_mfma_f32_16x16x32_bf16 v[52:55], v[116:119], v[108:111], v[52:55]\n\t" \
    "v_mfma_f32_16x16x32_bf16 v[56:59], v[120:123], v[108:111], v[56:59]\n\t" \
    "v_mfma_f32_16x16x32_bf16 v[60:63], v[124:127], v[108:111], v[60:63]\n\t" \
    "s_waitcnt lgkmcnt(0)\n\t" \
    "v_mfma_f32_16x16x32_bf16 v[0:3], v[80:83], v[64:67], v[0:3]\n\t" \
    "ds_read_b128 v[96:99], v128 offset:8192\n\t" \
    "ds_read_b64_tr_b16 v[112:113], v129 offset:8192\n\t" \
    "v_mfma_f32_16x16x32_bf16 v[4:7], v[84:87], v[64:67], v[4:7]\n\t" \
    "ds_read_b64_tr_b16 v[114:115], v129 offset:8448\n\t" \
    "ds_read_b64_tr_b16 v[116:117], v130 offset:8192\n\t" \
    "v_mfma_f32_16x16x32_bf16 v[8:11], v[88:91], v[64:67], v[8:11]\n\t" \
    "ds_read_b64_tr_b16 v[118:119], v130 offset:8448\n\t" \
    "ds_read_b128 v[100:103], v128 offset:8448\n\t" \
    "v_mfma_f32_16x16x32_bf16 v[12:15], v[92:95], v[64:67], v[12:15]\n\t" \
    "ds_read_b64_tr_b16 v[120:121], v129 offset:8704\n\t" \
    "ds_read_b64_tr_b16 v[122:123], v129 offset:8960\n\t" \
    "v_mfma_f32_16x16x32_bf16 v[16:19], v[80:83], v[68:71], v[16:19]\n\t" \
    "ds_read_b64_tr_b16 v[124:125], v130 offset:8704\n\t" \
    "ds_read_b64_tr_b16 v[126:127], v130 offset:8960\n\t" \
    "v_mfma_f32_16x16x32_bf16 v[20:23], v[84:87], v[68:71], v[20:23]\n\t" \
    "ds_read_b128 v[104:107], v128 offset:8704\n\t" \
    "ds_read_b128 v[108:111], v128 offset:8960\n\t" \
    "v_mfma_f32_16x16x32_bf16 v[24:27], v[88:91], v[68:71], v[24:27]\n\t" \
    "v_mfma_f32_16x16x32_bf16 v[28:31], v[92:95], v[68:71], v[28:31]\n\t" \
    "v_mfma_f32_16x16x32_bf16 v[32:35], v[80:83], v[72:75], v[32:35]\n\t" \
    "v_mfma_f32_16x16x32_bf16 v[36:39], v[84:87], v[72:75], v[36:39]\n\t" \
    "v_mfma_f32_16x16x32_bf16 v[40:43], v[88:91], v[72:75], v[40:43]\n\t" \
    "v_mfma_f32_16x16x32_bf16 v[44:47], v[92:95], v[72:75], v[44:47]\n\t" \
    "v_mfma_f32_16x16x32_bf16 v[48:51], v[80:83], v[76:79], v[48:51]\n\t" \
    "v_mfma_f32_16x16x32_bf16 v[52:55], v[84:87], v[76:79], v[52:55]\n\t" \
    "v_mfma_f32_16x16x32_bf16 v[56:59], v[88:91], v[76:79], v[56:59]\n\t" \
    "v_mfma_f32_16x16x32_bf16 v[60:63], v[92:95], v[76:79], v[60:63]\n\t" \
    "s_waitcnt vmcnt(0) lgkmcnt(0)\n\t" \
    "s_barrier\n\t" \
    "v_mfma_f32_16x16x32_bf16 v[0:3], v[112:115], v[96:99], v[0:3]\n\t" \
    "ds_read_b128 v[64:67], v131 offset:0\n\t" \
    "ds_read_b64_tr_b16 v[80:81], v132 offset:0\n\t" \
    "v_mfma_f32_16x16x32_bf16 v[4:7], v[116:119], v[96:99], v[4:7]\n\t" \
    "ds_read_b64_tr_b16 v[82:83], v132 offset:256\n\t" \
    "ds_read_b64_tr_b16 v[84:85], v133 offset:0\n\t" \
    "v_mfma_f32_16x16x32_bf16 v[8:11], v[120:123], v[96:99], v[8:11]\n\t" \
    "ds_read_b64_tr_b16 v[86:87], v133 offset:256\n\t" \
    "ds_read_b128 v[68:71], v131 offset:256\n\t" \
    "v_mfma_f32_16x16x32_bf16 v[12:15], v[124:127], v[96:99], v[12:15]\n\t" \
    "ds_read_b64_tr_b16 v[88:89], v132 offset:512\n\t" \
    "ds_read_b64_tr_b16 v[90:91], v132 offset:768\n\t" \
    "v_mfma_f32_16x16x32_bf16 v[16:19], v[112:115], v[100:103], v[16:19]\n\t" \
    "ds_read_b64_tr_b16 v[92:93], v133 offset:512\n\t" \
    "ds_read_b64_tr_b16 v[94:95], v133 offset:768\n\t" \
    "v_mfma_f32_16x16x32_bf16 v[20:23], v[116:119], v[100:103], v[20:23]\n\t" \
    "ds_read_b128 v[72:75], v131 offset:512\n\t" \
    "ds_read_b128 v[76:79], v131 offset:768\n\t" \
    "v_mfma_f32_16x16x32_bf16 v[24:27], v[120:123], v[100:103], v[24:27]\n\t" \
    "v_mov_b32 v128, v131\n\t" \
    "v_mov_b32 v129, v132\n\t" \
    "v_mov_b32 v130, v133\n\t" \
    "v_mfma_f32_16x16x32_bf16 v[28:31], v[124:127], v[100:103], v[28:31]\n\t" \
    "v_add_u32 v131, s69, v134\n\t" \
    "v_add_u32 v132, s69, v135\n\t" \
    "v_add_u32 v133, s69, v136\n\t" \
    "v_mfma_f32_16x16x32_bf16 v[32:35], v[112:115], v[104:107], v[32:35]\n\t" \
    "s_add_u32 s68, s68, 49152\n\t" \
    "s_cmp_eq_u32 s68, 147456\n\t" \
    "s_cselect_b32 s68, 0, s68\n\t" \
    "v_mfma_f32_16x16x32_bf16 v[36:39], v[116:119], v[104:107], v[36:39]\n\t" \
    "s_add_u32 s69, s69, 49152\n\t" \
    "s_cmp_eq_u32 s69, 147456\n\t" \
    "s_cselect_b32 s69, 0, s69\n\t" \
    "v_mfma_f32_16x16x32_bf16 v[40:43], v[120:123], v[104:107], v[40:43]\n\t" \
    "v_mfma_f32_16x16x32_bf16 v[44:47], v[124:127], v[104:107], v[44:47]\n\t" \
    "v_mfma_f32_16x16x32_bf16 v[48:51], v[112:115], v[108:111], v[48:51]\n\t" \
    "v_mfma_f32_16x16x32_bf16 v[52:55], v[116:119], v[108:111], v[52:55]\n\t" \
    "v_mfma_f32_16x16x32_bf16 v[56:59], v[120:123], v[108:111], v[56:59]\n\t" \
    "v_mfma_f32_16x16x32_bf16 v[60:63], v[124:127], v[108:111], v[60:63]\n\t" \
    "s_waitcnt lgkmcnt(0)\n\t" \
    "v_mfma_f32_16x16x32_bf16 v[0:3], v[80:83], v[64:67], v[0:3]\n\t" \
    "ds_read_b128 v[96:99], v128 offset:8192\n\t" \
    "ds_read_b64_tr_b16 v[112:113], v129 offset:8192\n\t" \
    "v_mfma_f32_16x16x32_bf16 v[4:7], v[84:87], v[64:67], v[4:7]\n\t" \
    "ds_read_b64_tr_b16 v[114:115], v129 offset:8448\n\t" \
    "ds_read_b64_tr_b16 v[116:117], v130 offset:8192\n\t" \
    "v_mfma_f32_16x16x32_bf16 v[8:11], v[88:91], v[64:67], v[8:11]\n\t" \
    "ds_read_b64_tr_b16 v[118:119], v130 offset:8448\n\t" \
    "ds_read_b128 v[100:103], v128 offset:8448\n\t" \
    "v_mfma_f32_16x16x32_bf16 v[12:15], v[92:95], v[64:67], v[12:15]\n\t" \
    "ds_read_b64_tr_b16 v[120:121], v129 offset:8704\n\t" \
    "ds_read_b64_tr_b16 v[122:123], v129 offset:8960\n\t" \
    "v_mfma_f32_16x16x32_bf16 v[16:19], v[80:83], v[68:71], v[16:19]\n\t" \
    "ds_read_b64_tr_b16 v[124:125], v130 offset:8704\n\t" \
    "ds_read_b64_tr_b16 v[126:127], v130 offset:8960\n\t" \
    "v_mfma_f32_16x16x32_bf16 v[20:23], v[84:87], v[68:71], v[20:23]\n\t" \
    "ds_read_b128 v[104:107], v128 offset:8704\n\t" \
    "ds_read_b128 v[108:111], v128 offset:8960\n\t" \
    "v_mfma_f32_16x16x32_bf16 v[24:27], v[88:91], v[68:71], v[24:27]\n\t" \
    "v_mfma_f32_16x16x32_bf16 v[28:31], v[92:95], v[68:71], v[28:31]\n\t" \
    "v_mfma_f32_16x16x32_bf16 v[32:35], v[80:83], v[72:75], v[32:35]\n\t" \
    "v_mfma_f32_16x16x32_bf16 v[36:39], v[84:87], v[72:75], v[36:39]\n\t" \
    "v_mfma_f32_16x16x32_bf16 v[40:43], v[88:91], v[72:75], v[40:43]\n\t" \
    "v_mfma_f32_16x16x32_bf16 v[44:47], v[92:95], v[72:75], v[44:47]\n\t" \
    "v_mfma_f32_16x16x32_bf16 v[48:51], v[80:83], v[76:79], v[48:51]\n\t" \
    "v_mfma_f32_16x16x32_bf16 v[52:55], v[84:87], v[76:79], v[52:55]\n\t" \
    "v_mfma_f32_16x16x32_bf16 v[56:59], v[88:91], v[76:79], v[56:59]\n\t" \
    "v_mfma_f32_16x16x32_bf16 v[60:63], v[92:95], v[76:79], v[60:63]\n\t" \
    "s_waitcnt vmcnt(0) lgkmcnt(0)\n\t" \
    "s_barrier\n\t" \
    "v_mfma_f32_16x16x32_bf16 v[0:3], v[112:115], v[96:99], v[0:3]\n\t" \
    "v_mov_b32 v128, v131\n\t" \
    "v_mov_b32 v129, v132\n\t" \
    "v_mfma_f32_16x16x32_bf16 v[4:7], v[116:119], v[96:99], v[4:7]\n\t" \
    "v_mov_b32 v130, v133\n\t" \
    "v_add_u32 v131, s69, v134\n\t" \
    "v_mfma_f32_16x16x32_bf16 v[8:11], v[120:123], v[96:99], v[8:11]\n\t" \
    "v_add_u32 v132, s69, v135\n\t" \
    "v_add_u32 v133, s69, v136\n\t" \
    "v_mfma_f32_16x16x32_bf16 v[12:15], v[124:127], v[96:99], v[12:15]\n\t" \
    "s_add_u32 s68, s68, 49152\n\t" \
    "s_cmp_eq_u32 s68, 147456\n\t" \
    "v_mfma_f32_16x16x32_bf16 v[16:19], v[112:115], v[100:103], v[16:19]\n\t" \
    "s_cselect_b32 s68, 0, s68\n\t" \
    "s_add_u32 s69, s69, 49152\n\t" \
    "v_mfma_f32_16x16x32_bf16 v[20:23], v[116:119], v[100:103], v[20:23]\n\t" \
    "s_cmp_eq_u32 s69, 147456\n\t" \
    "s_cselect_b32 s69, 0, s69\n\t" \
    "v_mfma_f32_16x16x32_bf16 v[24:27], v[120:123], v[100:103], v[24:27]\n\t" \
    "v_mfma_f32_16x16x32_bf16 v[28:31], v[124:127], v[100:103], v[28:31]\n\t" \
    "v_mfma_f32_16x16x32_bf16 v[32:35], v[112:115], v[104:107], v[32:35]\n\t" \
    "v_mfma_f32_16x16x32_bf16 v[36:39], v[116:119], v[104:107], v[36:39]\n\t" \
    "v_mfma_f32_16x16x32_bf16 v[40:43], v[120:123], v[104:107], v[40:43]\n\t" \
    "v_mfma_f32_16x16x32_bf16 v[44:47], v[124:127], v[104:107], v[44:47]\n\t" \
    "v_mfma_f32_16x16x32_bf16 v[48:51], v[112:115], v[108:111], v[48:51]\n\t" \
    "v_mfma_f32_16x16x32_bf16 v[52:55], v[116:119], v[108:111], v[52:55]\n\t" \
    "v_mfma_f32_16x16x32_bf16 v[56:59], v[120:123], v[108:111], v[56:59]\n\t" \
    "v_mfma_f32_16x16x32_bf16 v[60:63], v[124:127], v[108:111], v[60:63]\n\t" \
    "s_nop 15\n\t"
DI u32x4 pack8(const f32x16& c, int n) { return (u32x4){cvtpk(c[4 * n], c[4 * n + 1]), cvtpk(c[4 * n + 2], c[4 * n + 3]), cvtpk(c[4 * n + 4], c[4 * n + 5]), cvtpk(c[4 * n + 6], c[4 * n + 7])}; }
DI void add8(f32x16& c, int n, u32x4 w) {
#pragma unroll
    for (int i = 0; i < 4; ++i) { c[4 * n + 2 * i] += bflo(w[i]); c[4 * n + 2 * i + 1] += bfhi(w[i]); }
}
DI void poll(unsigned* flags, int first, int count, int lane) {
    gu32* f = (gu32*)(flags + (first + (lane % count)) * 32);
    for (int spin = 0; spin < (1 << 20); ++spin) {
        if (__all(__hip_atomic_load(f, __ATOMIC_RELAXED, __HIP_MEMORY_SCOPE_AGENT) == 1u)) break;
        __builtin_amdgcn_s_sleep(1);
    }
}
__global__ __launch_bounds__(512, 2) void k3_out(const char* __restrict__ ctx, const char* __restrict__ H, const bf16_t* __restrict__ wo, const bf16_t* __restrict__ w2,
                                                 const bf16_t* __restrict__ b2, const bf16_t* __restrict__ x, float* part, unsigned* flags, bf16_t* __restrict__ out) {
    extern __shared__ __attribute__((aligned(16))) unsigned char smem[];
    const int tid = threadIdx.x, wid = __builtin_amdgcn_readfirstlane(tid >> 6), lane = tid & 63, wr = wid >> 1, wc = wid & 1, fr = lane & 15, fq = lane >> 4;
    const int xcd = blockIdx.x & 7, idx = blockIdx.x >> 3, sl = xcd >> 1, mt = 4 * (xcd & 1) + (idx >> 3), ntile = idx & 7, tile = mt * 8 + ntile;
    const unsigned lds0 = (unsigned)(uintptr_t)smem;
    const size_t aw = (size_t)(wid & 3) * 4096, bw = (size_t)ntile * 256 + (size_t)(8 * (wid >> 1)) * (DM * 2) + 128 * (wid & 1);
    const int kt0 = sl < 2 ? 32 * sl : 28 * (sl - 2);
    const char* a1 = (sl < 2 ? ctx + (size_t)(2 * mt + (wid >> 2)) * 64 * 16384 : H + (size_t)(2 * mt + (wid >> 2)) * 56 * 16384) + aw + (size_t)kt0 * 16384;
    const char* b1 = (const char*)(sl < 2 ? wo : w2) + bw + (size_t)kt0 * (64 * DM * 2);
    const int nl = (sl < 2 ? 32 : 28) - 3;
    const unsigned va = lane * 16, vb = ((lane & 31) >> 2) * (DM * 2) + (32 * (lane >> 5) + 8 * ((lane & 3) ^ (wid & 2))) * 2;
    const unsigned mA = lds0 + (wid >> 2) * 16384 + (wid & 3) * 4096, mB = lds0 + 32768 + wid * 1024;
    const unsigned ra = lds0 + (wr >> 1) * 16384 + fq * 2048 + ((wr & 1) * 64 + fr) * 16;
    const unsigned rb0 = lds0 + 32768 + fq * 2048 + wc * 1024 + (fr >> 2) * 64 + (fq & 1) * 32 + (fr & 3) * 8, rb1 = rb0 ^ 32;
    f32x16 c0 = f32x16{}, c1 = f32x16{}, c2 = f32x16{}, c3 = f32x16{};
    u32x2 xb[2][4], bb[4];
    if (sl >= 2) {
#pragma unroll
        for (int n = 0; n < 4; ++n) { bb[n] = *(const u32x2*)(b2 + ntile * 128 + wc * 64 + fq * 4 + n * 16);
#pragma unroll
            for (int m = 0; m < 2; ++m) xb[m][n] = *(const u32x2*)(x + (size_t)(mt * 256 + wr * 64 + (2 * (sl - 2) + m) * 16 + fr) * DM + ntile * 128 + wc * 64 + fq * 4 + n * 16); }
    } else {
#pragma unroll
        for (int n = 0; n < 4; ++n) { bb[n] = (u32x2){0u, 0u}; xb[0][n] = (u32x2){0u, 0u}; xb[1][n] = (u32x2){0u, 0u}; }
    }
    STAMP(3, 0);
    const unsigned cpf = code_pf(tid, 52);
    asm volatile(K3_ASM
                 : "+&{v[0:15]}"(c0), "+&{v[16:31]}"(c1), "+&{v[32:47]}"(c2), "+&{v[48:63]}"(c3)
                 : "{v134}"(ra), "{v135}"(rb0), "{v136}"(rb1),
                   [va] "v"(va), [vb] "v"(vb), [a1] "s"(a1), [b1] "s"(b1), [nl] "s"(nl), [mA] "s"(mA), [mB] "s"(mB)
                 : "v64", "v65", "v66", "v67", "v68", "v69", "v70", "v71", "v72", "v73", "v74", "v75", "v76", "v77", "v78", "v79", "v80", "v81", "v82", "v83", "v84", "v85", "v86", "v87", "v88", "v89", "v90", "v91", "v92", "v93", "v94", "v95", "v96", "v97", "v98", "v99", "v100", "v101", "v102", "v103", "v104", "v105", "v106", "v107", "v108", "v109", "v110", "v111", "v112", "v113", "v114", "v115", "v116", "v117", "v118", "v119", "v120", "v121", "v122", "v123", "v124", "v125", "v126", "v127", "v128", "v129", "v130", "v131", "v132", "v133", "s60", "s61", "s62", "s63", "s64", "s65", "s66", "s67", "s68", "s69", "scc", "memory");
    STAMP(3, 1);
    CODE_PF_DONE(cpf);
    const auto rs = __builtin_amdgcn_make_buffer_rsrc((void*)((char*)part + (size_t)tile * 4 * 65536), 0, 4 * 65536, 0x00020000);
#define K3_SEND(c, m) do { __builtin_amdgcn_raw_buffer_store_b128(pack8(c, 0), rs, tid * 16 + (m) * 16384 + sl * 65536, 0, 16); __builtin_amdgcn_raw_buffer_store_b128(pack8(c, 2), rs, tid * 16 + (m) * 16384 + 8192 + sl * 65536, 0, 16); } while (0)
#define K3_GET(src, m, p) __builtin_amdgcn_raw_buffer_load_b128(rs, tid * 16 + (m) * 16384 + (p) * 8192, (src) * 65536, 16)
    gu32* myflag = (gu32*)(flags + (tile * 4 + sl) * 32);
    if (sl < 2) {
        K3_SEND(c0, 0); K3_SEND(c1, 1); K3_SEND(c2, 2); K3_SEND(c3, 3);
        WAIT_V(0);
        __syncthreads();
        if (tid == 0) __hip_atomic_store(myflag, 1u, __ATOMIC_RELAXED, __HIP_MEMORY_SCOPE_AGENT);
        STAMP(3, 2);
        return;
    }
    f32x16 f0, f1;
    const int m0 = 2 * (sl - 2);
    if (sl == 2) { K3_SEND(c2, 2); K3_SEND(c3, 3); f0 = c0; f1 = c1; } else { K3_SEND(c0, 0); K3_SEND(c1, 1); f0 = c2; f1 = c3; }
    WAIT_V(0);
    __syncthreads();
    if (tid == 0) __hip_atomic_store(myflag, 1u, __ATOMIC_RELAXED, __HIP_MEMORY_SCOPE_AGENT);
    STAMP(3, 2);
#pragma unroll
    for (int n = 0; n < 4; ++n) {
        const float b0 = bflo(bb[n][0]), b1_ = bfhi(bb[n][0]), b2_ = bflo(bb[n][1]), b3 = bfhi(bb[n][1]);
        f0[4 * n] += bflo(xb[0][n][0]) + b0; f0[4 * n + 1] += bfhi(xb[0][n][0]) + b1_; f0[4 * n + 2] += bflo(xb[0][n][1]) + b2_; f0[4 * n + 3] += bfhi(xb[0][n][1]) + b3;
        f1[4 * n] += bflo(xb[1][n][0]) + b0; f1[4 * n + 1] += bfhi(xb[1][n][0]) + b1_; f1[4 * n + 2] += bflo(xb[1][n][1]) + b2_; f1[4 * n + 3] += bfhi(xb[1][n][1]) + b3;
    }
    if (wid == 0) poll(flags, tile * 4 + (5 - sl), 1, lane);
    asm volatile("" ::: "memory"); BAR(); asm volatile("" ::: "memory");
    { const u32x4 q0 = K3_GET(5 - sl, m0, 0), q1 = K3_GET(5 - sl, m0, 1), q2 = K3_GET(5 - sl, m0 + 1, 0), q3 = K3_GET(5 - sl, m0 + 1, 1);
      add8(f0, 0, q0); add8(f0, 2, q1); add8(f1, 0, q2); add8(f1, 2, q3); }
    STAMP(3, 3);
    if (wid == 0) poll(flags, tile * 4, 2, lane);
    asm volatile("" ::: "memory"); BAR(); asm volatile("" ::: "memory");
    STAMP(3, 4);
    u32x4 pa[2][2][2];
#pragma unroll
    for (int k = 0; k < 2; ++k)
#pragma unroll
        for (int m = 0; m < 2; ++m)
#pragma unroll
            for (int p = 0; p < 2; ++p) pa[k][m][p] = K3_GET(k, m0 + m, p);
#pragma unroll
    for (int k = 0; k < 2; ++k) { add8(f0, 0, pa[k][0][0]); add8(f0, 2, pa[k][0][1]); add8(f1, 0, pa[k][1][0]); add8(f1, 2, pa[k][1][1]); }
    const int row = mt * 256 + wr * 64 + m0 * 16 + fr;
    const int cg = ((fq & 1) << 1) | (fq >> 1);
    bf16_t* orow = out + (size_t)row * DM + ntile * 128 + wc * 64 + cg * 8;
#define K3_F4(c, n) (f32x4){c[4 * (n)], c[4 * (n) + 1], c[4 * (n) + 2], c[4 * (n) + 3]}
    *(u32x4*)(orow) = k1::pack_swap16(K3_F4(f0, 0), K3_F4(f0, 1)); *(u32x4*)(orow + 32) = k1::pack_swap16(K3_F4(f0, 2), K3_F4(f0, 3));
    *(u32x4*)(orow + 16 * DM) = k1::pack_swap16(K3_F4(f1, 0), K3_F4(f1, 1)); *(u32x4*)(orow + 16 * DM + 32) = k1::pack_swap16(K3_F4(f1, 2), K3_F4(f1, 3));
    STAMP(3, 5);
}
}

extern "C" void kernel_launch(void* const* d_in, const int* in_sizes, int n_in, void* d_out, int out_size, void* d_ws, size_t ws_size, hipStream_t stream) {
    const bf16_t* x = (const bf16_t*)d_in[0]; const bf16_t* ckv = (const bf16_t*)d_in[1];
    const bf16_t* wq = (const bf16_t*)d_in[2]; const bf16_t* wuk = (const bf16_t*)d_in[3]; const bf16_t* wuv = (const bf16_t*)d_in[4]; const bf16_t* wo = (const bf16_t*)d_in[5];
    const bf16_t* w1 = (const bf16_t*)d_in[7]; const bf16_t* b1 = (const bf16_t*)d_in[8]; const bf16_t* w2 = (const bf16_t*)d_in[9]; const bf16_t* b2 = (const bf16_t*)d_in[10];
    char* ws = (char*)d_ws;
    (void)hipFuncSetAttribute((const void*)k1::k1_proj, hipFuncAttributeMaxDynamicSharedMemorySize, k1::LDS_BYTES);
    (void)hipFuncSetAttribute((const void*)k2::k2_attn, hipFuncAttributeMaxDynamicSharedMemorySize, k2::LDS_BYTES);
    (void)hipFuncSetAttribute((const void*)k3::k3_out, hipFuncAttributeMaxDynamicSharedMemorySize, k3::LDS_BYTES);
    k0_ln<<<SEQ / 8, 512, 0, stream>>>(x, ws + WS_XN, (unsigned*)(ws + WS_FLAG));
    k1::k1_proj<<<256, 512, k1::LDS_BYTES, stream>>>(ckv, ws + WS_XN, wuk, wuv, w1, b1, ws + WS_K, ws + WS_VT, ws + WS_H);
    k2::k2_attn<<<256, 512, k2::LDS_BYTES, stream>>>(ws + WS_XN, (const char*)wq, ws + WS_K, ws + WS_VT, ws + WS_CTX, (const char*)wo, (const char*)w2);
    k3::k3_out<<<256, 512, k3::LDS_BYTES, stream>>>(ws + WS_CTX, ws + WS_H, wo, w2, b2, x, (float*)(ws + WS_PART), (unsigned*)(ws + WS_FLAG), (bf16_t*)d_out);
}
```

```cpp
#include <hip/hip_runtime.h>
#include <cstdint>

#define STAMP(k, i) do {} while (0)
#define LAS __attribute__((address_space(3)))
#define DI __device__ __forceinline__
typedef unsigned short bf16_t;
typedef short bf16x8 __attribute__((ext_vector_type(8)));
typedef short s16x4 __attribute__((ext_vector_type(4)));
typedef short v4i16_t __attribute__((ext_vector_type(4)));
typedef float f32x2 __attribute__((ext_vector_type(2)));
typedef float f32x4 __attribute__((ext_vector_type(4)));
typedef float f32x16 __attribute__((ext_vector_type(16)));
typedef unsigned u32x2 __attribute__((ext_vector_type(2)));
typedef unsigned u32x4 __attribute__((ext_vector_type(4)));
typedef __bf16 bf16x2_t __attribute__((ext_vector_type(2)));
typedef LAS unsigned char* lds_ptr;
typedef LAS const unsigned char* lds_cptr;

constexpr int SEQ = 2048, DM = 1024, NH = 32, HD = 128, DC = 512, FF = 3584, NQ = NH * HD;
constexpr size_t MiB = 1u << 20;
constexpr size_t WS_FLAG = 0;
constexpr size_t WS_XN = 1 * MiB;
constexpr size_t WS_K = 24 * MiB;
constexpr size_t WS_VT = 40 * MiB;
constexpr size_t WS_CTX = 56 * MiB;
constexpr size_t WS_H = 72 * MiB;
constexpr size_t WS_PART = 88 * MiB;

DI unsigned cvtpk(float lo, float hi) { f32x2 v = {lo, hi}; bf16x2_t b = __builtin_convertvector(v, bf16x2_t); return __builtin_bit_cast(unsigned, b); }
DI float bflo(unsigned w) { return __uint_as_float(w << 16); }
DI float bfhi(unsigned w) { return __uint_as_float(w & 0xffff0000u); }
DI s16x4 lds_tr(lds_cptr p) { return __builtin_bit_cast(s16x4, __builtin_amdgcn_ds_read_tr16_b64_v4i16((LAS v4i16_t*)p)); }
DI bf16x8 lds_rd(lds_cptr p) { return *(const LAS bf16x8*)p; }
DI bf16x8 cat8(s16x4 a, s16x4 b) { return __builtin_shufflevector(a, b, 0, 1, 2, 3, 4, 5, 6, 7); }
DI void st16_sc1(void* p, u32x4 v) { asm volatile("global_store_dwordx4 %0, %1, off sc1\n\ts_nop 1" :: "v"(p), "v"(v) : "memory"); }
#define ST16(p, v) st16_sc1((void*)(p), v)
template <bool WT> DI void st16(void* p, u32x4 v) { if constexpr (WT) st16_sc1(p, v); else *(u32x4*)p = v; }
DI unsigned code_pf(unsigned line, unsigned nlines) {
    unsigned long long pc; unsigned d;
    asm volatile("s_getpc_b64 %0" : "=s"(pc));
    asm volatile("global_load_dword %0, %1, %2" : "=v"(d) : "v"((line < nlines ? line : 0u) * 128u), "s"(pc) : "memory");
    return d;
}
#define CODE_PF_DONE(d) asm volatile("" :: "v"(d))
#define SBAR() __builtin_amdgcn_sched_barrier(0)
#define PIN(x) asm volatile("" : "+v"(x))
#define WAIT_V(n) asm volatile("s_waitcnt vmcnt(" #n ")" ::: "memory")
#define WAIT_L(n) asm volatile("s_waitcnt lgkmcnt(" #n ")" ::: "memory")
#define BAR() __builtin_amdgcn_s_barrier()
#define MFMA16(a, b, c) __builtin_amdgcn_mfma_f32_16x16x32_bf16(a, b, c, 0, 0, 0)
#define MFMA32(a, b, c) __builtin_amdgcn_mfma_f32_32x32x16_bf16(a, b, c, 0, 0, 0)
DI void glds(const char* sbase, unsigned voff, unsigned ldsaddr) {
    asm volatile("s_mov_b32 m0, %2\n\ts_nop 0\n\tglobal_load_lds_dwordx4 %0, %1" :: "v"(voff), "s"(sbase), "s"(ldsaddr) : "memory");
}
DI void glds2(const char* sbase, unsigned voff, unsigned ldsaddr) {
    asm volatile("s_mov_b32 m0, %2\n\ts_nop 0\n\tglobal_load_lds_dwordx4 %0, %1\n\tglobal_load_lds_dwordx4 %0, %1 offset:1024" :: "v"(voff), "s"(sbase), "s"(ldsaddr) : "memory");
}

DI float wave_sum(float v) {
    v += __builtin_bit_cast(float, __builtin_amdgcn_update_dpp(0, __builtin_bit_cast(int, v), 0x128, 0xf, 0xf, false));
    v += __builtin_bit_cast(float, __builtin_amdgcn_update_dpp(0, __builtin_bit_cast(int, v), 0x124, 0xf, 0xf, false));
    v += __builtin_bit_cast(float, __builtin_amdgcn_update_dpp(0, __builtin_bit_cast(int, v), 0x122, 0xf, 0xf, false));
    v += __builtin_bit_cast(float, __builtin_amdgcn_update_dpp(0, __builtin_bit_cast(int, v), 0x121, 0xf, 0xf, false));
    { const auto r = __builtin_amdgcn_permlane16_swap(__float_as_uint(v), __float_as_uint(v), false, false); v = __uint_as_float(r[0]) + __uint_as_float(r[1]); }
    { const auto r = __builtin_amdgcn_permlane32_swap(__float_as_uint(v), __float_as_uint(v), false, false); v = __uint_as_float(r[0]) + __uint_as_float(r[1]); }
    return v;
}
namespace k1 {
constexpr int HTB = 16384, LDS_LN = 8 * HTB, LDS_BYTES = 8 * HTB + 8 * 129 * 16;
struct Src {
    const char* a; const char* b0; const char* b1;
    unsigned aK, aH, aD;
    unsigned bK0, bD0, bK1, bD1;
    unsigned va, vb0, vb1;
};

DI f32x2 gelu_tanh2(f32x2 v) {
    const f32x2 z = v * (v * v * -0.10294324f + -2.3022082f);
    f32x2 e; e.x = __builtin_amdgcn_exp2f(z.x); e.y = __builtin_amdgcn_exp2f(z.y);
    e = e + 1.0f;
    f32x2 r; r.x = __builtin_amdgcn_rcpf(e.x); r.y = __builtin_amdgcn_rcpf(e.y);
    return v * r;
}

#define K1_SA(b, h) (((b) * 2 + (h)) * HTB)
#define K1_SB(b, h) ((4 + (b) * 2 + (h)) * HTB)
#define K1_STAGE(bufoff, gbase, voff, d2) do { glds((const char*)(gbase), voff, ldsw + (bufoff)); glds((const char*)(gbase) + (d2), voff, ldsw + (bufoff) + 8192); } while (0)
#define K1_LDA(dst, b, h) do { _Pragma("unroll") for (int m = 0; m < 4; ++m) { dst[m][0] = lds_rd(lds + K1_SA(b, h) + aoff0 + m * MSTEP); dst[m][1] = lds_rd(lds + K1_SA(b, h) + aoff1 + m * MSTEP); } } while (0)
#define K1_LDB(dst, b, h) do { _Pragma("unroll") for (int k = 0; k < 2; ++k) { \
        dst[0][k] = cat8(lds_tr(lds + K1_SB(b, h) + boff0 + k * 8192), lds_tr(lds + K1_SB(b, h) + boff0 + k * 8192 + 256)); \
        dst[1][k] = cat8(lds_tr(lds + K1_SB(b, h) + boff1 + k * 8192), lds_tr(lds + K1_SB(b, h) + boff1 + k * 8192 + 256)); } } while (0)
#define K1_MMA(ai, bj, At, Bt) do { __builtin_amdgcn_s_setprio(1); _Pragma("unroll") for (int m = 0; m < 4; ++m) _Pragma("unroll") for (int n = 0; n < 2; ++n) _Pragma("unroll") for (int k = 0; k < 2; ++k) \
        acc[ai][bj][m][n] = SWAP ? MFMA16(At[m][k], Bt[n][k], acc[ai][bj][m][n]) : MFMA16(Bt[n][k], At[m][k], acc[ai][bj][m][n]); __builtin_amdgcn_s_setprio(0); } while (0)

template <bool SWAP, int MSTEP>
DI void kloop(lds_ptr lds, f32x4 (&acc)[2][2][4][2], const Src& cur, const Src& nxt, int nt, unsigned ldsw, int aoff0, int aoff1, int boff0, int boff1) {
    bf16x8 At[4][2], B0[2][2], B1[2][2];
    for (int t = 0; t < nt; t += 2) {
        const bool last = (t == nt - 2);
        const char* a1h = cur.a + (size_t)(t + 1) * cur.aK + cur.aH;
        const char* a2 = last ? nxt.a : cur.a + (size_t)(t + 2) * cur.aK;
        const char* b20 = last ? nxt.b0 : cur.b0 + (size_t)(t + 2) * cur.bK0; const char* b21 = last ? nxt.b1 : cur.b1 + (size_t)(t + 2) * cur.bK1;
        const unsigned aK = last ? nxt.aK : cur.aK, aH = last ? nxt.aH : cur.aH, aD = last ? nxt.aD : cur.aD;
        const unsigned bK0 = last ? nxt.bK0 : cur.bK0, bD0 = last ? nxt.bD0 : cur.bD0, bK1 = last ? nxt.bK1 : cur.bK1, bD1 = last ? nxt.bD1 : cur.bD1;
        const unsigned va = last ? nxt.va : cur.va, vb0 = last ? nxt.vb0 : cur.vb0, vb1 = last ? nxt.vb1 : cur.vb1;
        const char* a3 = a2 + aK; const char* b30 = b20 + bK0; const char* b31 = b21 + bK1;
        K1_LDB(B0, 0, 0); K1_LDB(B1, 0, 1); SBAR(); K1_LDA(At, 0, 0); K1_STAGE(K1_SA(1, 1), a1h, cur.va, cur.aD);
        WAIT_V(8); WAIT_L(0); BAR(); K1_MMA(0, 0, At, B0); K1_MMA(0, 1, At, B1); BAR(); SBAR();
        K1_LDA(At, 0, 1); K1_STAGE(K1_SB(0, 0), b20, vb0, bD0); K1_STAGE(K1_SB(0, 1), b21, vb1, bD1); K1_STAGE(K1_SA(0, 0), a2, va, aD);
        WAIT_V(8); WAIT_L(0); BAR(); K1_MMA(1, 0, At, B0); K1_MMA(1, 1, At, B1); BAR(); SBAR();
        K1_LDB(B0, 1, 0); K1_LDB(B1, 1, 1); SBAR(); K1_LDA(At, 1, 0); K1_STAGE(K1_SA(0, 1), a2 + aH, va, aD);
        WAIT_V(8); WAIT_L(0); BAR(); K1_MMA(0, 0, At, B0); K1_MMA(0, 1, At, B1); BAR(); SBAR();
        K1_LDA(At, 1, 1); K1_STAGE(K1_SB(1, 0), b30, vb0, bD0); K1_STAGE(K1_SB(1, 1), b31, vb1, bD1); K1_STAGE(K1_SA(1, 0), a3, va, aD);
        WAIT_V(8); WAIT_L(0); BAR(); K1_MMA(1, 0, At, B0); K1_MMA(1, 1, At, B1); BAR(); SBAR();
    }
}
DI void prologue_a(const Src& u, unsigned ldsw) {
    K1_STAGE(K1_SB(0, 0), u.b0, u.vb0, u.bD0); K1_STAGE(K1_SB(0, 1), u.b1, u.vb1, u.bD1); K1_STAGE(K1_SA(0, 0), u.a, u.va, u.aD); K1_STAGE(K1_SA(0, 1), u.a + u.aH, u.va, u.aD);
}
DI void prologue_b(const Src& u, unsigned ldsw, int wr) {
    if (wr == 1) BAR();
    WAIT_V(2); BAR();
    K1_STAGE(K1_SB(1, 0), u.b0 + u.bK0, u.vb0, u.bD0); K1_STAGE(K1_SA(1, 0), u.a + u.aK, u.va, u.aD); K1_STAGE(K1_SB(1, 1), u.b1 + u.bK1, u.vb1, u.bD1);
    WAIT_V(6); BAR();
}

DI void zero_acc(f32x4 (&acc)[2][2][4][2]) {
#pragma unroll
    for (int a = 0; a < 2; ++a)
#pragma unroll
        for (int b = 0; b < 2; ++b)
#pragma unroll
            for (int m = 0; m < 4; ++m)
#pragma unroll
                for (int n = 0; n < 2; ++n) acc[a][b][m][n] = (f32x4){0.f, 0.f, 0.f, 0.f};
}
DI u32x4 pack_swap16(f32x4 v0, f32x4 v1) {
    const unsigned x0 = cvtpk(v0[0], v0[1]), x1 = cvtpk(v0[2], v0[3]), y0 = cvtpk(v1[0], v1[1]), y1 = cvtpk(v1[2], v1[3]);
    const auto r0 = __builtin_amdgcn_permlane16_swap(x0, y0, false, false), r1 = __builtin_amdgcn_permlane16_swap(x1, y1, false, false);
    return (u32x4){r0[0], r1[0], r0[1], r1[1]};
}
DI u32x4 pack_swap32(f32x4 v0, f32x4 v1) {
    const unsigned x0 = cvtpk(v0[0], v0[1]), x1 = cvtpk(v0[2], v0[3]), y0 = cvtpk(v1[0], v1[1]), y1 = cvtpk(v1[2], v1[3]);
    const auto r0 = __builtin_amdgcn_permlane32_swap(x0, y0, false, false), r1 = __builtin_amdgcn_permlane32_swap(x1, y1, false, false);
    return (u32x4){r0[0], r1[0], r0[1], r1[1]};
}
template <bool WT> DI void epi_qk(const f32x4 (&acc)[2][2][4][2], char* dst, int pm, int pn, float sc, int wr, int wc, int fr, int fq) {
    const int cg = ((fq & 1) << 1) | (fq >> 1);
    char* p = dst + (size_t)(2 * pn * 32 + 4 * pm + wr) * 16384 + (4 * wc + cg) * 1024 + fr * 16;
#pragma unroll
    for (int ai = 0; ai < 2; ++ai)
#pragma unroll
        for (int bj = 0; bj < 2; ++bj)
#pragma unroll
            for (int m = 0; m < 4; ++m)
                st16<WT>(p + (size_t)(bj * 32 + ai * 2) * 16384 + m * 256, pack_swap16(acc[ai][bj][m][0] * sc, acc[ai][bj][m][1] * sc));
}
template <bool WT> DI void epi_v(const f32x4 (&acc)[2][2][4][2], char* dst, int pm, int pn, int wr, int wc, int fr, int fq) {
    char* p = dst + (size_t)(2 * pn * 32 + 4 * pm + wr) * 16384 + fq * 2048 + (32 * wc + fr) * 16;
#pragma unroll
    for (int ai = 0; ai < 2; ++ai)
#pragma unroll
        for (int bj = 0; bj < 2; ++bj)
#pragma unroll
            for (int m = 0; m < 4; m += 2)
#pragma unroll
                for (int n = 0; n < 2; ++n)
                    st16<WT>(p + (size_t)(bj * 32 + ai * 2) * 16384 + m * 4096 + n * 256, pack_swap32(acc[ai][bj][m][n], acc[ai][bj][m + 1][n]));
}
__global__ __launch_bounds__(512, 2) void k1_kv(const bf16_t* __restrict__ x, const bf16_t* __restrict__ ckv, const bf16_t* __restrict__ wuk, const bf16_t* __restrict__ wuv,
                                                char* __restrict__ xn, char* __restrict__ Ko, char* __restrict__ VTo, unsigned* __restrict__ flags) {
    extern __shared__ __attribute__((aligned(16))) unsigned char smem[];
    lds_ptr lds = (lds_ptr)smem;
    const int tid = threadIdx.x, wid = __builtin_amdgcn_readfirstlane(tid >> 6), lane = tid & 63, wr = wid >> 2, wc = wid & 3, fr = lane & 15, fq = lane >> 4;
    const int xcd = blockIdx.x & 7, idx = blockIdx.x >> 3, pm = idx & 7, pj = xcd + 8 * (idx >> 3);
    const bool isV = pj >= 16;
    const unsigned ldsw = (unsigned)(uintptr_t)smem + (unsigned)wid * 1024u;
    const int aoff0 = (wr * 64 + fr) * 128 + ((fq ^ (fr >> 1)) * 16), aoff1 = aoff0 ^ 64;
    const int boff0 = fq * 2048 + wc * 512 + (fr >> 2) * 64 + (fq & 1) * 32 + (fr & 3) * 8, boff1 = boff0 ^ 32;
    const unsigned brow = 8 * (wid >> 1) + ((lane & 31) >> 2), bcol = (64 * (wid & 1) + 32 * (lane >> 5) + 8 * ((lane & 3) ^ (wid & 2))) * 2;
    Src u0;
    u0.a = (const char*)ckv + (size_t)pm * 256 * DC * 2; u0.aK = 128; u0.aH = 128 * DC * 2; u0.aD = 64 * DC * 2;
    u0.va = (tid >> 3) * (DC * 2) + (((tid & 7) ^ ((tid >> 4) & 7)) * 16);
    u0.b0 = (const char*)(isV ? wuv : wuk) + (pj & 15) * 512; u0.b1 = u0.b0 + 256; u0.bK0 = u0.bK1 = 64 * NQ * 2; u0.bD0 = u0.bD1 = 32 * NQ * 2; u0.vb0 = u0.vb1 = brow * (NQ * 2) + bcol;
    f32x4 acc[2][2][4][2];
    zero_acc(acc);
    STAMP(1, 0);
    const unsigned cpf = code_pf(tid, 200);
    const int row = blockIdx.x * 8 + wid;
    u32x4 v0, v1;
    { const char* src = (const char*)x + (size_t)row * DM * 2;
      asm volatile("global_load_dwordx4 %0, %2, %3\n\tglobal_load_dwordx4 %1, %2, %3 offset:1024" : "=&v"(v0), "=&v"(v1) : "v"(lane * 16), "s"(src) : "memory"); }
    prologue_a(u0, ldsw);
    prologue_b(u0, ldsw, wr);
    asm volatile("" : "+v"(v0), "+v"(v1));
    CODE_PF_DONE(cpf);
    STAMP(1, 1);
    if (isV) { kloop<true, 2048>(lds, acc, u0, u0, DC / 64, ldsw, aoff0, aoff1, boff0, boff1); STAMP(1, 2); if (wr == 0) BAR(); epi_v<true>(acc, VTo, pm, pj & 15, wr, wc, fr, fq); }
    else     { kloop<false, 2048>(lds, acc, u0, u0, DC / 64, ldsw, aoff0, aoff1, boff0, boff1); STAMP(1, 2); if (wr == 0) BAR(); epi_qk<true>(acc, Ko, pm, pj, 1.0f, wr, wc, fr, fq); }
    {
        float f[16];
#pragma unroll
        for (int i = 0; i < 4; ++i) { f[2 * i] = bflo(v0[i]); f[2 * i + 1] = bfhi(v0[i]); f[8 + 2 * i] = bflo(v1[i]); f[8 + 2 * i + 1] = bfhi(v1[i]); }
        float s = 0.f;
#pragma unroll
        for (int i = 0; i < 16; ++i) s += f[i];
        s = wave_sum(s);
        const float mean = s * (1.0f / DM);
        float q = 0.f;
#pragma unroll
        for (int i = 0; i < 16; ++i) { f[i] -= mean; q += f[i] * f[i]; }
        q = wave_sum(q);
        const float rstd = 1.0f / sqrtf(q * (1.0f / DM) + 1e-5f);
        u32x4 w0, w1;
#pragma unroll
        for (int i = 0; i < 4; ++i) { w0[i] = cvtpk(f[2 * i] * rstd, f[2 * i + 1] * rstd); w1[i] = cvtpk(f[8 + 2 * i] * rstd, f[8 + 2 * i + 1] * rstd); }
        LAS u32x4* tile = (LAS u32x4*)(lds + LDS_LN);
        tile[wid * 129 + lane] = w0; tile[wid * 129 + lane + 64] = w1;
        WAIT_L(0); BAR();
        const int r0 = (blockIdx.x * 8) & 127, mh = blockIdx.x >> 4, r = tid & 7;
#pragma unroll
        for (int j = 0; j < 2; ++j) { const int c128 = (tid >> 3) + 64 * j; ST16(xn + ((size_t)mh * 128 + c128) * 2048 + (r0 + r) * 16, tile[r * 129 + c128]); }
        if (blockIdx.x == 0 && tid < 256) flags[tid * 32] = 0u;
    }
    STAMP(1, 3);
    WAIT_V(0);
    STAMP(1, 4);
}
#undef K1_SA
#undef K1_SB
#undef K1_STAGE
#undef K1_LDA
#undef K1_LDB
#undef K1_MMA
}

namespace k2 {
#define K2_HEAD (2 * (xcd + 8 * (idx >> 4)) + ((idx >> 3) & 1))
constexpr int SLOT = 16384, LDS_K = 0, LDS_V = 4 * SLOT, LDS_BYTES = 9 * SLOT;
#define K2_ASM \
    "s_nop 1\n\t" \
    "s_cmp_eq_u32 %[n], 0\n\t" \
    "s_cbranch_scc1 TAIL_%=\n\t" \
    "LOOP_%=:\n\t" \
    "s_cmp_lg_u32 %[dmaw], 0\n\t" \
    "s_cbranch_scc1 DMAL_%=\n\t" \
    "DMABL_%=:\n\t" \
    "s_sub_u32 s60, %[t], 8\n\t" \
    "s_and_b32 s60, s60, 0xfffffffb\n\t" \
    "s_or_b32 s60, s60, %[dmaw]\n\t" \
    "s_cmp_eq_u32 s60, 0\n\t" \
    "s_cbranch_scc1 PF_%=\n\t" \
    "PFB_%=:\n\t" \
    "s_waitcnt lgkmcnt(0)\n\t" \
    "v_mfma_f32_32x32x16_bf16 v[112:127], v[168:171], v[64:67], v[96:111]\n\t" \
    "ds_read_b128 v[200:203], v235 offset:8192\n\t" \
    "ds_read_b128 v[204:207], v235 offset:8704\n\t" \
    "v_add_f32 v237, v144, v145\n\t" \
    "v_cvt_pk_bf16_f32 v160, v144, v145\n\t" \
    "v_mfma_f32_32x32x16_bf16 v[112:127], v[172:175], v[68:71], v[112:127]\n\t" \
    "ds_read_b128 v[208:211], v235 offset:9216\n\t" \
    "ds_read_b128 v[212:215], v235 offset:9728\n\t" \
    "v_add_f32 v237, v237, v146\n\t" \
    "v_add_f32 v237, v237, v147\n\t" \
    "v_cvt_pk_bf16_f32 v161, v146, v147\n\t" \
    "v_mfma_f32_32x32x16_bf16 v[112:127], v[176:179], v[72:75], v[112:127]\n\t" \
    "ds_read_b128 v[216:219], v235 offset:12288\n\t" \
    "ds_read_b128 v[220:223], v235 offset:12800\n\t" \
    "v_add_f32 v237, v237, v148\n\t" \
    "v_add_f32 v237, v237, v149\n\t" \
    "v_cvt_pk_bf16_f32 v162, v148, v149\n\t" \
    "v_mfma_f32_32x32x16_bf16 v[112:127], v[180:183], v[76:79], v[112:127]\n\t" \
    "ds_read_b128 v[224:227], v235 offset:13312\n\t" \
    "ds_read_b128 v[228:231], v235 offset:13824\n\t" \
    "v_add_f32 v237, v237, v150\n\t" \
    "v_add_f32 v237, v237, v151\n\t" \
    "v_cvt_pk_bf16_f32 v163, v150, v151\n\t" \
    "v_mfma_f32_32x32x16_bf16 v[112:127], v[184:187], v[80:83], v[112:127]\n\t" \
    "v_add_f32 v237, v237, v152\n\t" \
    "v_add_f32 v237, v237, v153\n\t" \
    "v_cvt_pk_bf16_f32 v164, v152, v153\n\t" \
    "v_mfma_f32_32x32x16_bf16 v[112:127], v[188:191], v[84:87], v[112:127]\n\t" \
    "v_add_f32 v237, v237, v154\n\t" \
    "v_add_f32 v237, v237, v155\n\t" \
    "v_cvt_pk_bf16_f32 v165, v154, v155\n\t" \
    "v_mfma_f32_32x32x16_bf16 v[112:127], v[192:195], v[88:91], v[112:127]\n\t" \
    "v_add_f32 v237, v237, v156\n\t" \
    "v_add_f32 v237, v237, v157\n\t" \
    "v_cvt_pk_bf16_f32 v166, v156, v157\n\t" \
    "v_mfma_f32_32x32x16_bf16 v[112:127], v[196:199], v[92:95], v[112:127]\n\t" \
    "v_add_f32 v237, v237, v158\n\t" \
    "v_add_f32 v237, v237, v159\n\t" \
    "v_cvt_pk_bf16_f32 v167, v158, v159\n\t" \
    "v_cmp_lt_f32 vcc, %[thr], v237\n\t" \
    "s_cbranch_vccnz RAREA_%=\n\t" \
    "BACKA_%=:\n\t" \
    "s_waitcnt lgkmcnt(0)\n\t" \
    "v_mfma_f32_32x32x16_bf16 v[0:15], v[200:203], v[160:163], v[0:15]\n\t" \
    "ds_read_b128 v[168:171], v232 offset:512\n\t" \
    "ds_read_b128 v[172:175], v232 offset:2560\n\t" \
    "v_add_f32 v236, v236, v237\n\t" \
    "v_mfma_f32_32x32x16_bf16 v[16:31], v[204:207], v[160:163], v[16:31]\n\t" \
    "ds_read_b128 v[176:179], v232 offset:4608\n\t" \
    "ds_read_b128 v[180:183], v232 offset:6656\n\t" \
    "v_exp_f32 v144, v112\n\t" \
    "v_exp_f32 v145, v113\n\t" \
    "v_mfma_f32_32x32x16_bf16 v[32:47], v[208:211], v[160:163], v[32:47]\n\t" \
    "ds_read_b128 v[184:187], v232 offset:8704\n\t" \
    "ds_read_b128 v[188:191], v232 offset:10752\n\t" \
    "v_exp_f32 v146, v114\n\t" \
    "v_exp_f32 v147, v115\n\t" \
    "v_mfma_f32_32x32x16_bf16 v[48:63], v[212:215], v[160:163], v[48:63]\n\t" \
    "ds_read_b128 v[192:195], v232 offset:12800\n\t" \
    "ds_read_b128 v[196:199], v232 offset:14848\n\t" \
    "v_exp_f32 v148, v116\n\t" \
    "v_exp_f32 v149, v117\n\t" \
    "v_mfma_f32_32x32x16_bf16 v[0:15], v[216:219], v[164:167], v[0:15]\n\t" \
    "v_exp_f32 v150, v118\n\t" \
    "v_exp_f32 v151, v119\n\t" \
    "v_exp_f32 v152, v120\n\t" \
    "v_mfma_f32_32x32x16_bf16 v[16:31], v[220:223], v[164:167], v[16:31]\n\t" \
    "v_exp_f32 v153, v121\n\t" \
    "v_exp_f32 v154, v122\n\t" \
    "v_exp_f32 v155, v123\n\t" \
    "v_mfma_f32_32x32x16_bf16 v[32:47], v[224:227], v[164:167], v[32:47]\n\t" \
    "v_exp_f32 v156, v124\n\t" \
    "v_exp_f32 v157, v125\n\t" \
    "v_mfma_f32_32x32x16_bf16 v[48:63], v[228:231], v[164:167], v[48:63]\n\t" \
    "v_exp_f32 v158, v126\n\t" \
    "v_exp_f32 v159, v127\n\t" \
    "s_waitcnt lgkmcnt(0)\n\t" \
    "v_mfma_f32_32x32x16_bf16 v[128:143], v[168:171], v[64:67], v[96:111]\n\t" \
    "ds_read_b128 v[200:203], v234 offset:0\n\t" \
    "ds_read_b128 v[204:207], v234 offset:512\n\t" \
    "v_add_f32 v237, v144, v145\n\t" \
    "v_cvt_pk_bf16_f32 v160, v144, v145\n\t" \
    "v_mfma_f32_32x32x16_bf16 v[128:143], v[172:175], v[68:71], v[128:143]\n\t" \
    "ds_read_b128 v[208:211], v234 offset:1024\n\t" \
    "ds_read_b128 v[212:215], v234 offset:1536\n\t" \
    "v_add_f32 v237, v237, v146\n\t" \
    "v_add_f32 v237, v237, v147\n\t" \
    "v_cvt_pk_bf16_f32 v161, v146, v147\n\t" \
    "v_mfma_f32_32x32x16_bf16 v[128:143], v[176:179], v[72:75], v[128:143]\n\t" \
    "ds_read_b128 v[216:219], v234 offset:4096\n\t" \
    "ds_read_b128 v[220:223], v234 offset:4608\n\t" \
    "v_add_f32 v237, v237, v148\n\t" \
    "v_add_f32 v237, v237, v149\n\t" \
    "v_cvt_pk_bf16_f32 v162, v148, v149\n\t" \
    "v_mfma_f32_32x32x16_bf16 v[128:143], v[180:183], v[76:79], v[128:143]\n\t" \
    "ds_read_b128 v[224:227], v234 offset:5120\n\t" \
    "ds_read_b128 v[228:231], v234 offset:5632\n\t" \
    "v_add_f32 v237, v237, v150\n\t" \
    "v_add_f32 v237, v237, v151\n\t" \
    "v_cvt_pk_bf16_f32 v163, v150, v151\n\t" \
    "v_mfma_f32_32x32x16_bf16 v[128:143], v[184:187], v[80:83], v[128:143]\n\t" \
    "v_add_f32 v237, v237, v152\n\t" \
    "v_add_f32 v237, v237, v153\n\t" \
    "v_cvt_pk_bf16_f32 v164, v152, v153\n\t" \
    "v_mfma_f32_32x32x16_bf16 v[128:143], v[188:191], v[84:87], v[128:143]\n\t" \
    "v_add_f32 v237, v237, v154\n\t" \
    "v_add_f32 v237, v237, v155\n\t" \
    "v_cvt_pk_bf16_f32 v165, v154, v155\n\t" \
    "v_mfma_f32_32x32x16_bf16 v[128:143], v[192:195], v[88:91], v[128:143]\n\t" \
    "v_add_f32 v237, v237, v156\n\t" \
    "v_add_f32 v237, v237, v157\n\t" \
    "v_cvt_pk_bf16_f32 v166, v156, v157\n\t" \
    "v_mfma_f32_32x32x16_bf16 v[128:143], v[196:199], v[92:95], v[128:143]\n\t" \
    "v_add_f32 v237, v237, v158\n\t" \
    "v_add_f32 v237, v237, v159\n\t" \
    "v_cvt_pk_bf16_f32 v167, v158, v159\n\t" \
    "v_cmp_lt_f32 vcc, %[thr], v237\n\t" \
    "s_cbranch_vccnz RAREB_%=\n\t" \
    "BACKB_%=:\n\t" \
    "s_waitcnt lgkmcnt(0)\n\t" \
    "v_mfma_f32_32x32x16_bf16 v[0:15], v[200:203], v[160:163], v[0:15]\n\t" \
    "ds_read_b128 v[168:171], v233 offset:0\n\t" \
    "ds_read_b128 v[172:175], v233 offset:2048\n\t" \
    "v_add_f32 v236, v236, v237\n\t" \
    "v_mfma_f32_32x32x16_bf16 v[16:31], v[204:207], v[160:163], v[16:31]\n\t" \
    "ds_read_b128 v[176:179], v233 offset:4096\n\t" \
    "ds_read_b128 v[180:183], v233 offset:6144\n\t" \
    "v_exp_f32 v144, v128\n\t" \
    "v_exp_f32 v145, v129\n\t" \
    "v_mfma_f32_32x32x16_bf16 v[32:47], v[208:211], v[160:163], v[32:47]\n\t" \
    "ds_read_b128 v[184:187], v233 offset:8192\n\t" \
    "ds_read_b128 v[188:191], v233 offset:10240\n\t" \
    "v_exp_f32 v146, v130\n\t" \
    "v_exp_f32 v147, v131\n\t" \
    "v_mfma_f32_32x32x16_bf16 v[48:63], v[212:215], v[160:163], v[48:63]\n\t" \
    "ds_read_b128 v[192:195], v233 offset:12288\n\t" \
    "ds_read_b128 v[196:199], v233 offset:14336\n\t" \
    "v_exp_f32 v148, v132\n\t" \
    "v_exp_f32 v149, v133\n\t" \
    "v_mfma_f32_32x32x16_bf16 v[0:15], v[216:219], v[164:167], v[0:15]\n\t" \
    "v_exp_f32 v150, v134\n\t" \
    "v_exp_f32 v151, v135\n\t" \
    "v_exp_f32 v152, v136\n\t" \
    "v_mfma_f32_32x32x16_bf16 v[16:31], v[220:223], v[164:167], v[16:31]\n\t" \
    "v_exp_f32 v153, v137\n\t" \
    "v_exp_f32 v154, v138\n\t" \
    "v_exp_f32 v155, v139\n\t" \
    "s_add_u32 %[soff], %[soff], 0x4000\n\t" \
    "s_and_b32 %[soff], %[soff], 0xffff\n\t" \
    "v_mfma_f32_32x32x16_bf16 v[32:47], v[224:227], v[164:167], v[32:47]\n\t" \
    "v_exp_f32 v156, v140\n\t" \
    "v_exp_f32 v157, v141\n\t" \
    "v_mov_b32 v235, v234\n\t" \
    "v_mov_b32 v232, v233\n\t" \
    "v_mfma_f32_32x32x16_bf16 v[48:63], v[228:231], v[164:167], v[48:63]\n\t" \
    "v_exp_f32 v158, v142\n\t" \
    "v_exp_f32 v159, v143\n\t" \
    "v_add_u32 v234, %[soff], %[vp0]\n\t" \
    "s_add_u32 s60, %[soff], 0x4000\n\t" \
    "s_and_b32 s60, s60, 0xffff\n\t" \
    "v_add_u32 v233, s60, %[kp0]\n\t" \
    "s_waitcnt vmcnt(8)\n\t" \
    "s_barrier\n\t" \
    "s_add_u32 %[t], %[t], 1\n\t" \
    "s_cmp_lt_u32 %[t], %[n]\n\t" \
    "s_cbranch_scc1 LOOP_%=\n\t" \
    "TAIL_%=:\n\t" \
    "s_cmp_lg_u32 %[dmaw], 0\n\t" \
    "s_cbranch_scc1 DMAT_%=\n\t" \
    "DMABT_%=:\n\t" \
    "s_cmp_eq_u32 %[odd], 0\n\t" \
    "s_cbranch_scc1 EVEN_%=\n\t" \
    "s_waitcnt lgkmcnt(0)\n\t" \
    "v_mfma_f32_32x32x16_bf16 v[112:127], v[168:171], v[64:67], v[96:111]\n\t" \
    "ds_read_b128 v[200:203], v235 offset:8192\n\t" \
    "ds_read_b128 v[204:207], v235 offset:8704\n\t" \
    "v_add_f32 v237, v144, v145\n\t" \
    "v_cvt_pk_bf16_f32 v160, v144, v145\n\t" \
    "v_mfma_f32_32x32x16_bf16 v[112:127], v[172:175], v[68:71], v[112:127]\n\t" \
    "ds_read_b128 v[208:211], v235 offset:9216\n\t" \
    "ds_read_b128 v[212:215], v235 offset:9728\n\t" \
    "v_add_f32 v237, v237, v146\n\t" \
    "v_add_f32 v237, v237, v147\n\t" \
    "v_cvt_pk_bf16_f32 v161, v146, v147\n\t" \
    "v_mfma_f32_32x32x16_bf16 v[112:127], v[176:179], v[72:75], v[112:127]\n\t" \
    "ds_read_b128 v[216:219], v235 offset:12288\n\t" \
    "ds_read_b128 v[220:223], v235 offset:12800\n\t" \
    "v_add_f32 v237, v237, v148\n\t" \
    "v_add_f32 v237, v237, v149\n\t" \
    "v_cvt_pk_bf16_f32 v162, v148, v149\n\t" \
    "v_mfma_f32_32x32x16_bf16 v[112:127], v[180:183], v[76:79], v[112:127]\n\t" \
    "ds_read_b128 v[224:227], v235 offset:13312\n\t" \
    "ds_read_b128 v[228:231], v235 offset:13824\n\t" \
    "v_add_f32 v237, v237, v150\n\t" \
    "v_add_f32 v237, v237, v151\n\t" \
    "v_cvt_pk_bf16_f32 v163, v150, v151\n\t" \
    "v_mfma_f32_32x32x16_bf16 v[112:127], v[184:187], v[80:83], v[112:127]\n\t" \
    "v_add_f32 v237, v237, v152\n\t" \
    "v_add_f32 v237, v237, v153\n\t" \
    "v_cvt_pk_bf16_f32 v164, v152, v153\n\t" \
    "v_mfma_f32_32x32x16_bf16 v[112:127], v[188:191], v[84:87], v[112:127]\n\t" \
    "v_add_f32 v237, v237, v154\n\t" \
    "v_add_f32 v237, v237, v155\n\t" \
    "v_cvt_pk_bf16_f32 v165, v154, v155\n\t" \
    "v_mfma_f32_32x32x16_bf16 v[112:127], v[192:195], v[88:91], v[112:127]\n\t" \
    "v_add_f32 v237, v237, v156\n\t" \
    "v_add_f32 v237, v237, v157\n\t" \
    "v_cvt_pk_bf16_f32 v166, v156, v157\n\t" \
    "v_mfma_f32_32x32x16_bf16 v[112:127], v[196:199], v[92:95], v[112:127]\n\t" \
    "v_add_f32 v237, v237, v158\n\t" \
    "v_add_f32 v237, v237, v159\n\t" \
    "v_cvt_pk_bf16_f32 v167, v158, v159\n\t" \
    "v_cmp_lt_f32 vcc, %[thr], v237\n\t" \
    "s_cbranch_vccnz RAREC_%=\n\t" \
    "BACKC_%=:\n\t" \
    "s_waitcnt lgkmcnt(0)\n\t" \
    "v_mfma_f32_32x32x16_bf16 v[0:15], v[200:203], v[160:163], v[0:15]\n\t" \
    "ds_read_b128 v[168:171], v232 offset:512\n\t" \
    "ds_read_b128 v[172:175], v232 offset:2560\n\t" \
    "v_add_f32 v236, v236, v237\n\t" \
    "v_mfma_f32_32x32x16_bf16 v[16:31], v[204:207], v[160:163], v[16:31]\n\t" \
    "ds_read_b128 v[176:179], v232 offset:4608\n\t" \
    "ds_read_b128 v[180:183], v232 offset:6656\n\t" \
    "v_exp_f32 v144, v112\n\t" \
    "v_exp_f32 v145, v113\n\t" \
    "v_mfma_f32_32x32x16_bf16 v[32:47], v[208:211], v[160:163], v[32:47]\n\t" \
    "ds_read_b128 v[184:187], v232 offset:8704\n\t" \
    "ds_read_b128 v[188:191], v232 offset:10752\n\t" \
    "v_exp_f32 v146, v114\n\t" \
    "v_exp_f32 v147, v115\n\t" \
    "v_mfma_f32_32x32x16_bf16 v[48:63], v[212:215], v[160:163], v[48:63]\n\t" \
    "ds_read_b128 v[192:195], v232 offset:12800\n\t" \
    "ds_read_b128 v[196:199], v232 offset:14848\n\t" \
    "v_exp_f32 v148, v116\n\t" \
    "v_exp_f32 v149, v117\n\t" \
    "v_mfma_f32_32x32x16_bf16 v[0:15], v[216:219], v[164:167], v[0:15]\n\t" \
    "v_exp_f32 v150, v118\n\t" \
    "v_exp_f32 v151, v119\n\t" \
    "v_exp_f32 v152, v120\n\t" \
    "v_mfma_f32_32x32x16_bf16 v[16:31], v[220:223], v[164:167], v[16:31]\n\t" \
    "v_exp_f32 v153, v121\n\t" \
    "v_exp_f32 v154, v122\n\t" \
    "v_exp_f32 v155, v123\n\t" \
    "v_mfma_f32_32x32x16_bf16 v[32:47], v[224:227], v[164:167], v[32:47]\n\t" \
    "v_exp_f32 v156, v124\n\t" \
    "v_exp_f32 v157, v125\n\t" \
    "v_mfma_f32_32x32x16_bf16 v[48:63], v[228:231], v[164:167], v[48:63]\n\t" \
    "v_exp_f32 v158, v126\n\t" \
    "v_exp_f32 v159, v127\n\t" \
    "v_mov_b32 v239, 0xff800000\n\t" \
    "v_cmp_gt_i32 vcc, 0, %[dq]\n\t" \
    "s_nop 1\n\t" \
    "v_cndmask_b32 v96, v96, v239, vcc\n\t" \
    "v_cmp_gt_i32 vcc, 1, %[dq]\n\t" \
    "s_nop 1\n\t" \
    "v_cndmask_b32 v97, v97, v239, vcc\n\t" \
    "v_cmp_gt_i32 vcc, 2, %[dq]\n\t" \
    "s_nop 1\n\t" \
    "v_cndmask_b32 v98, v98, v239, vcc\n\t" \
    "v_cmp_gt_i32 vcc, 3, %[dq]\n\t" \
    "s_nop 1\n\t" \
    "v_cndmask_b32 v99, v99, v239, vcc\n\t" \
    "v_cmp_gt_i32 vcc, 8, %[dq]\n\t" \
    "s_nop 1\n\t" \
    "v_cndmask_b32 v100, v100, v239, vcc\n\t" \
    "v_cmp_gt_i32 vcc, 9, %[dq]\n\t" \
    "s_nop 1\n\t" \
    "v_cndmask_b32 v101, v101, v239, vcc\n\t" \
    "v_cmp_gt_i32 vcc, 10, %[dq]\n\t" \
    "s_nop 1\n\t" \
    "v_cndmask_b32 v102, v102, v239, vcc\n\t" \
    "v_cmp_gt_i32 vcc, 11, %[dq]\n\t" \
    "s_nop 1\n\t" \
    "v_cndmask_b32 v103, v103, v239, vcc\n\t" \
    "v_cmp_gt_i32 vcc, 16, %[dq]\n\t" \
    "s_nop 1\n\t" \
    "v_cndmask_b32 v104, v104, v239, vcc\n\t" \
    "v_cmp_gt_i32 vcc, 17, %[dq]\n\t" \
    "s_nop 1\n\t" \
    "v_cndmask_b32 v105, v105, v239, vcc\n\t" \
    "v_cmp_gt_i32 vcc, 18, %[dq]\n\t" \
    "s_nop 1\n\t" \
    "v_cndmask_b32 v106, v106, v239, vcc\n\t" \
    "v_cmp_gt_i32 vcc, 19, %[dq]\n\t" \
    "s_nop 1\n\t" \
    "v_cndmask_b32 v107, v107, v239, vcc\n\t" \
    "v_cmp_gt_i32 vcc, 24, %[dq]\n\t" \
    "s_nop 1\n\t" \
    "v_cndmask_b32 v108, v108, v239, vcc\n\t" \
    "v_cmp_gt_i32 vcc, 25, %[dq]\n\t" \
    "s_nop 1\n\t" \
    "v_cndmask_b32 v109, v109, v239, vcc\n\t" \
    "v_cmp_gt_i32 vcc, 26, %[dq]\n\t" \
    "s_nop 1\n\t" \
    "v_cndmask_b32 v110, v110, v239, vcc\n\t" \
    "v_cmp_gt_i32 vcc, 27, %[dq]\n\t" \
    "s_nop 1\n\t" \
    "v_cndmask_b32 v111, v111, v239, vcc\n\t" \
    "s_nop 1\n\t" \
    "s_waitcnt lgkmcnt(0)\n\t" \
    "v_mfma_f32_32x32x16_bf16 v[128:143], v[168:171], v[64:67], v[96:111]\n\t" \
    "ds_read_b128 v[200:203], v234 offset:0\n\t" \
    "ds_read_b128 v[204:207], v234 offset:512\n\t" \
    "v_add_f32 v237, v144, v145\n\t" \
    "v_cvt_pk_bf16_f32 v160, v144, v145\n\t" \
    "v_mfma_f32_32x32x16_bf16 v[128:143], v[172:175], v[68:71], v[128:143]\n\t" \
    "ds_read_b128 v[208:211], v234 offset:1024\n\t" \
    "ds_read_b128 v[212:215], v234 offset:1536\n\t" \
    "v_add_f32 v237, v237, v146\n\t" \
    "v_add_f32 v237, v237, v147\n\t" \
    "v_cvt_pk_bf16_f32 v161, v146, v147\n\t" \
    "v_mfma_f32_32x32x16_bf16 v[128:143], v[176:179], v[72:75], v[128:143]\n\t" \
    "ds_read_b128 v[216:219], v234 offset:4096\n\t" \
    "ds_read_b128 v[220:223], v234 offset:4608\n\t" \
    "v_add_f32 v237, v237, v148\n\t" \
    "v_add_f32 v237, v237, v149\n\t" \
    "v_cvt_pk_bf16_f32 v162, v148, v149\n\t" \
    "v_mfma_f32_32x32x16_bf16 v[128:143], v[180:183], v[76:79], v[128:143]\n\t" \
    "ds_read_b128 v[224:227], v234 offset:5120\n\t" \
    "ds_read_b128 v[228:231], v234 offset:5632\n\t" \
    "v_add_f32 v237, v237, v150\n\t" \
    "v_add_f32 v237, v237, v151\n\t" \
    "v_cvt_pk_bf16_f32 v163, v150, v151\n\t" \
    "v_mfma_f32_32x32x16_bf16 v[128:143], v[184:187], v[80:83], v[128:143]\n\t" \
    "v_add_f32 v237, v237, v152\n\t" \
    "v_add_f32 v237, v237, v153\n\t" \
    "v_cvt_pk_bf16_f32 v164, v152, v153\n\t" \
    "v_mfma_f32_32x32x16_bf16 v[128:143], v[188:191], v[84:87], v[128:143]\n\t" \
    "v_add_f32 v237, v237, v154\n\t" \
    "v_add_f32 v237, v237, v155\n\t" \
    "v_cvt_pk_bf16_f32 v165, v154, v155\n\t" \
    "v_mfma_f32_32x32x16_bf16 v[128:143], v[192:195], v[88:91], v[128:143]\n\t" \
    "v_add_f32 v237, v237, v156\n\t" \
    "v_add_f32 v237, v237, v157\n\t" \
    "v_cvt_pk_bf16_f32 v166, v156, v157\n\t" \
    "v_mfma_f32_32x32x16_bf16 v[128:143], v[196:199], v[92:95], v[128:143]\n\t" \
    "v_add_f32 v237, v237, v158\n\t" \
    "v_add_f32 v237, v237, v159\n\t" \
    "v_cvt_pk_bf16_f32 v167, v158, v159\n\t" \
    "v_cmp_lt_f32 vcc, %[thr], v237\n\t" \
    "s_cbranch_vccnz RARED_%=\n\t" \
    "BACKD_%=:\n\t" \
    "s_waitcnt lgkmcnt(0)\n\t" \
    "v_mfma_f32_32x32x16_bf16 v[0:15], v[200:203], v[160:163], v[0:15]\n\t" \
    "ds_read_b128 v[168:171], v233 offset:0\n\t" \
    "ds_read_b128 v[172:175], v233 offset:2048\n\t" \
    "v_add_f32 v236, v236, v237\n\t" \
    "v_mfma_f32_32x32x16_bf16 v[16:31], v[204:207], v[160:163], v[16:31]\n\t" \
    "ds_read_b128 v[176:179], v233 offset:4096\n\t" \
    "ds_read_b128 v[180:183], v233 offset:6144\n\t" \
    "v_exp_f32 v144, v128\n\t" \
    "v_exp_f32 v145, v129\n\t" \
    "v_mfma_f32_32x32x16_bf16 v[32:47], v[208:211], v[160:163], v[32:47]\n\t" \
    "ds_read_b128 v[184:187], v233 offset:8192\n\t" \
    "ds_read_b128 v[188:191], v233 offset:10240\n\t" \
    "v_exp_f32 v146, v130\n\t" \
    "v_exp_f32 v147, v131\n\t" \
    "v_mfma_f32_32x32x16_bf16 v[48:63], v[212:215], v[160:163], v[48:63]\n\t" \
    "ds_read_b128 v[192:195], v233 offset:12288\n\t" \
    "ds_read_b128 v[196:199], v233 offset:14336\n\t" \
    "v_exp_f32 v148, v132\n\t" \
    "v_exp_f32 v149, v133\n\t" \
    "v_mfma_f32_32x32x16_bf16 v[0:15], v[216:219], v[164:167], v[0:15]\n\t" \
    "v_exp_f32 v150, v134\n\t" \
    "v_exp_f32 v151, v135\n\t" \
    "v_exp_f32 v152, v136\n\t" \
    "v_mfma_f32_32x32x16_bf16 v[16:31], v[220:223], v[164:167], v[16:31]\n\t" \
    "v_exp_f32 v153, v137\n\t" \
    "v_exp_f32 v154, v138\n\t" \
    "v_exp_f32 v155, v139\n\t" \
    "v_mfma_f32_32x32x16_bf16 v[32:47], v[224:227], v[164:167], v[32:47]\n\t" \
    "v_exp_f32 v156, v140\n\t" \
    "v_exp_f32 v157, v141\n\t" \
    "v_mfma_f32_32x32x16_bf16 v[48:63], v[228:231], v[164:167], v[48:63]\n\t" \
    "v_exp_f32 v158, v142\n\t" \
    "v_exp_f32 v159, v143\n\t" \
    "s_waitcnt lgkmcnt(0)\n\t" \
    "ds_read_b128 v[200:203], v234 offset:8192\n\t" \
    "ds_read_b128 v[204:207], v234 offset:8704\n\t" \
    "ds_read_b128 v[208:211], v234 offset:9216\n\t" \
    "ds_read_b128 v[212:215], v234 offset:9728\n\t" \
    "ds_read_b128 v[216:219], v234 offset:12288\n\t" \
    "ds_read_b128 v[220:223], v234 offset:12800\n\t" \
    "ds_read_b128 v[224:227], v234 offset:13312\n\t" \
    "ds_read_b128 v[228:231], v234 offset:13824\n\t" \
    "v_add_f32 v237, v144, v145\n\t" \
    "v_cvt_pk_bf16_f32 v160, v144, v145\n\t" \
    "v_add_f32 v237, v237, v146\n\t" \
    "v_add_f32 v237, v237, v147\n\t" \
    "v_cvt_pk_bf16_f32 v161, v146, v147\n\t" \
    "v_add_f32 v237, v237, v148\n\t" \
    "v_add_f32 v237, v237, v149\n\t" \
    "v_cvt_pk_bf16_f32 v162, v148, v149\n\t" \
    "v_add_f32 v237, v237, v150\n\t" \
    "v_add_f32 v237, v237, v151\n\t" \
    "v_cvt_pk_bf16_f32 v163, v150, v151\n\t" \
    "v_add_f32 v237, v237, v152\n\t" \
    "v_add_f32 v237, v237, v153\n\t" \
    "v_cvt_pk_bf16_f32 v164, v152, v153\n\t" \
    "v_add_f32 v237, v237, v154\n\t" \
    "v_add_f32 v237, v237, v155\n\t" \
    "v_cvt_pk_bf16_f32 v165, v154, v155\n\t" \
    "v_add_f32 v237, v237, v156\n\t" \
    "v_add_f32 v237, v237, v157\n\t" \
    "v_cvt_pk_bf16_f32 v166, v156, v157\n\t" \
    "v_add_f32 v237, v237, v158\n\t" \
    "v_add_f32 v237, v237, v159\n\t" \
    "v_cvt_pk_bf16_f32 v167, v158, v159\n\t" \
    "v_cmp_lt_f32 vcc, %[thr], v237\n\t" \
    "s_cbranch_vccnz RAREE_%=\n\t" \
    "BACKE_%=:\n\t" \
    "s_waitcnt lgkmcnt(0)\n\t" \
    "v_add_f32 v236, v236, v237\n\t" \
    "v_mfma_f32_32x32x16_bf16 v[0:15], v[200:203], v[160:163], v[0:15]\n\t" \
    "v_mfma_f32_32x32x16_bf16 v[16:31], v[204:207], v[160:163], v[16:31]\n\t" \
    "v_mfma_f32_32x32x16_bf16 v[32:47], v[208:211], v[160:163], v[32:47]\n\t" \
    "v_mfma_f32_32x32x16_bf16 v[48:63], v[212:215], v[160:163], v[48:63]\n\t" \
    "v_mfma_f32_32x32x16_bf16 v[0:15], v[216:219], v[164:167], v[0:15]\n\t" \
    "v_mfma_f32_32x32x16_bf16 v[16:31], v[220:223], v[164:167], v[16:31]\n\t" \
    "v_mfma_f32_32x32x16_bf16 v[32:47], v[224:227], v[164:167], v[32:47]\n\t" \
    "v_mfma_f32_32x32x16_bf16 v[48:63], v[228:231], v[164:167], v[48:63]\n\t" \
    "s_branch END_%=\n\t" \
    "EVEN_%=:\n\t" \
    "v_mov_b32 v239, 0xff800000\n\t" \
    "v_cmp_gt_i32 vcc, 0, %[dq]\n\t" \
    "s_nop 1\n\t" \
    "v_cndmask_b32 v96, v96, v239, vcc\n\t" \
    "v_cmp_gt_i32 vcc, 1, %[dq]\n\t" \
    "s_nop 1\n\t" \
    "v_cndmask_b32 v97, v97, v239, vcc\n\t" \
    "v_cmp_gt_i32 vcc, 2, %[dq]\n\t" \
    "s_nop 1\n\t" \
    "v_cndmask_b32 v98, v98, v239, vcc\n\t" \
    "v_cmp_gt_i32 vcc, 3, %[dq]\n\t" \
    "s_nop 1\n\t" \
    "v_cndmask_b32 v99, v99, v239, vcc\n\t" \
    "v_cmp_gt_i32 vcc, 8, %[dq]\n\t" \
    "s_nop 1\n\t" \
    "v_cndmask_b32 v100, v100, v239, vcc\n\t" \
    "v_cmp_gt_i32 vcc, 9, %[dq]\n\t" \
    "s_nop 1\n\t" \
    "v_cndmask_b32 v101, v101, v239, vcc\n\t" \
    "v_cmp_gt_i32 vcc, 10, %[dq]\n\t" \
    "s_nop 1\n\t" \
    "v_cndmask_b32 v102, v102, v239, vcc\n\t" \
    "v_cmp_gt_i32 vcc, 11, %[dq]\n\t" \
    "s_nop 1\n\t" \
    "v_cndmask_b32 v103, v103, v239, vcc\n\t" \
    "v_cmp_gt_i32 vcc, 16, %[dq]\n\t" \
    "s_nop 1\n\t" \
    "v_cndmask_b32 v104, v104, v239, vcc\n\t" \
    "v_cmp_gt_i32 vcc, 17, %[dq]\n\t" \
    "s_nop 1\n\t" \
    "v_cndmask_b32 v105, v105, v239, vcc\n\t" \
    "v_cmp_gt_i32 vcc, 18, %[dq]\n\t" \
    "s_nop 1\n\t" \
    "v_cndmask_b32 v106, v106, v239, vcc\n\t" \
    "v_cmp_gt_i32 vcc, 19, %[dq]\n\t" \
    "s_nop 1\n\t" \
    "v_cndmask_b32 v107, v107, v239, vcc\n\t" \
    "v_cmp_gt_i32 vcc, 24, %[dq]\n\t" \
    "s_nop 1\n\t" \
    "v_cndmask_b32 v108, v108, v239, vcc\n\t" \
    "v_cmp_gt_i32 vcc, 25, %[dq]\n\t" \
    "s_nop 1\n\t" \
    "v_cndmask_b32 v109, v109, v239, vcc\n\t" \
    "v_cmp_gt_i32 vcc, 26, %[dq]\n\t" \
    "s_nop 1\n\t" \
    "v_cndmask_b32 v110, v110, v239, vcc\n\t" \
    "v_cmp_gt_i32 vcc, 27, %[dq]\n\t" \
    "s_nop 1\n\t" \
    "v_cndmask_b32 v111, v111, v239, vcc\n\t" \
    "s_nop 1\n\t" \
    "s_waitcnt lgkmcnt(0)\n\t" \
    "v_mfma_f32_32x32x16_bf16 v[112:127], v[168:171], v[64:67], v[96:111]\n\t" \
    "ds_read_b128 v[200:203], v235 offset:8192\n\t" \
    "ds_read_b128 v[204:207], v235 offset:8704\n\t" \
    "v_add_f32 v237, v144, v145\n\t" \
    "v_cvt_pk_bf16_f32 v160, v144, v145\n\t" \
    "v_mfma_f32_32x32x16_bf16 v[112:127], v[172:175], v[68:71], v[112:127]\n\t" \
    "ds_read_b128 v[208:211], v235 offset:9216\n\t" \
    "ds_read_b128 v[212:215], v235 offset:9728\n\t" \
    "v_add_f32 v237, v237, v146\n\t" \
    "v_add_f32 v237, v237, v147\n\t" \
    "v_cvt_pk_bf16_f32 v161, v146, v147\n\t" \
    "v_mfma_f32_32x32x16_bf16 v[112:127], v[176:179], v[72:75], v[112:127]\n\t" \
    "ds_read_b128 v[216:219], v235 offset:12288\n\t" \
    "ds_read_b128 v[220:223], v235 offset:12800\n\t" \
    "v_add_f32 v237, v237, v148\n\t" \
    "v_add_f32 v237, v237, v149\n\t" \
    "v_cvt_pk_bf16_f32 v162, v148, v149\n\t" \
    "v_mfma_f32_32x32x16_bf16 v[112:127], v[180:183], v[76:79], v[112:127]\n\t" \
    "ds_read_b128 v[224:227], v235 offset:13312\n\t" \
    "ds_read_b128 v[228:231], v235 offset:13824\n\t" \
    "v_add_f32 v237, v237, v150\n\t" \
    "v_add_f32 v237, v237, v151\n\t" \
    "v_cvt_pk_bf16_f32 v163, v150, v151\n\t" \
    "v_mfma_f32_32x32x16_bf16 v[112:127], v[184:187], v[80:83], v[112:127]\n\t" \
    "v_add_f32 v237, v237, v152\n\t" \
    "v_add_f32 v237, v237, v153\n\t" \
    "v_cvt_pk_bf16_f32 v164, v152, v153\n\t" \
    "v_mfma_f32_32x32x16_bf16 v[112:127], v[188:191], v[84:87], v[112:127]\n\t" \
    "v_add_f32 v237, v237, v154\n\t" \
    "v_add_f32 v237, v237, v155\n\t" \
    "v_cvt_pk_bf16_f32 v165, v154, v155\n\t" \
    "v_mfma_f32_32x32x16_bf16 v[112:127], v[192:195], v[88:91], v[112:127]\n\t" \
    "v_add_f32 v237, v237, v156\n\t" \
    "v_add_f32 v237, v237, v157\n\t" \
    "v_cvt_pk_bf16_f32 v166, v156, v157\n\t" \
    "v_mfma_f32_32x32x16_bf16 v[112:127], v[196:199], v[92:95], v[112:127]\n\t" \
    "v_add_f32 v237, v237, v158\n\t" \
    "v_add_f32 v237, v237, v159\n\t" \
    "v_cvt_pk_bf16_f32 v167, v158, v159\n\t" \
    "v_cmp_lt_f32 vcc, %[thr], v237\n\t" \
    "s_cbranch_vccnz RAREF_%=\n\t" \
    "BACKF_%=:\n\t" \
    "s_waitcnt lgkmcnt(0)\n\t" \
    "v_mfma_f32_32x32x16_bf16 v[0:15], v[200:203], v[160:163], v[0:15]\n\t" \
    "ds_read_b128 v[168:171], v232 offset:512\n\t" \
    "ds_read_b128 v[172:175], v232 offset:2560\n\t" \
    "v_add_f32 v236, v236, v237\n\t" \
    "v_mfma_f32_32x32x16_bf16 v[16:31], v[204:207], v[160:163], v[16:31]\n\t" \
    "ds_read_b128 v[176:179], v232 offset:4608\n\t" \
    "ds_read_b128 v[180:183], v232 offset:6656\n\t" \
    "v_exp_f32 v144, v112\n\t" \
    "v_exp_f32 v145, v113\n\t" \
    "v_mfma_f32_32x32x16_bf16 v[32:47], v[208:211], v[160:163], v[32:47]\n\t" \
    "ds_read_b128 v[184:187], v232 offset:8704\n\t" \
    "ds_read_b128 v[188:191], v232 offset:10752\n\t" \
    "v_exp_f32 v146, v114\n\t" \
    "v_exp_f32 v147, v115\n\t" \
    "v_mfma_f32_32x32x16_bf16 v[48:63], v[212:215], v[160:163], v[48:63]\n\t" \
    "ds_read_b128 v[192:195], v232 offset:12800\n\t" \
    "ds_read_b128 v[196:199], v232 offset:14848\n\t" \
    "v_exp_f32 v148, v116\n\t" \
    "v_exp_f32 v149, v117\n\t" \
    "v_mfma_f32_32x32x16_bf16 v[0:15], v[216:219], v[164:167], v[0:15]\n\t" \
    "v_exp_f32 v150, v118\n\t" \
    "v_exp_f32 v151, v119\n\t" \
    "v_exp_f32 v152, v120\n\t" \
    "v_mfma_f32_32x32x16_bf16 v[16:31], v[220:223], v[164:167], v[16:31]\n\t" \
    "v_exp_f32 v153, v121\n\t" \
    "v_exp_f32 v154, v122\n\t" \
    "v_exp_f32 v155, v123\n\t" \
    "v_mfma_f32_32x32x16_bf16 v[32:47], v[224:227], v[164:167], v[32:47]\n\t" \
    "v_exp_f32 v156, v124\n\t" \
    "v_exp_f32 v157, v125\n\t" \
    "v_mfma_f32_32x32x16_bf16 v[48:63], v[228:231], v[164:167], v[48:63]\n\t" \
    "v_exp_f32 v158, v126\n\t" \
    "v_exp_f32 v159, v127\n\t" \
    "s_waitcnt lgkmcnt(0)\n\t" \
    "ds_read_b128 v[200:203], v234 offset:0\n\t" \
    "ds_read_b128 v[204:207], v234 offset:512\n\t" \
    "ds_read_b128 v[208:211], v234 offset:1024\n\t" \
    "ds_read_b128 v[212:215], v234 offset:1536\n\t" \
    "ds_read_b128 v[216:219], v234 offset:4096\n\t" \
    "ds_read_b128 v[220:223], v234 offset:4608\n\t" \
    "ds_read_b128 v[224:227], v234 offset:5120\n\t" \
    "ds_read_b128 v[228:231], v234 offset:5632\n\t" \
    "v_add_f32 v237, v144, v145\n\t" \
    "v_cvt_pk_bf16_f32 v160, v144, v145\n\t" \
    "v_add_f32 v237, v237, v146\n\t" \
    "v_add_f32 v237, v237, v147\n\t" \
    "v_cvt_pk_bf16_f32 v161, v146, v147\n\t" \
    "v_add_f32 v237, v237, v148\n\t" \
    "v_add_f32 v237, v237, v149\n\t" \
    "v_cvt_pk_bf16_f32 v162, v148, v149\n\t" \
    "v_add_f32 v237, v237, v150\n\t" \
    "v_add_f32 v237, v237, v151\n\t" \
    "v_cvt_pk_bf16_f32 v163, v150, v151\n\t" \
    "v_add_f32 v237, v237, v152\n\t" \
    "v_add_f32 v237, v237, v153\n\t" \
    "v_cvt_pk_bf16_f32 v164, v152, v153\n\t" \
    "v_add_f32 v237, v237, v154\n\t" \
    "v_add_f32 v237, v237, v155\n\t" \
    "v_cvt_pk_bf16_f32 v165, v154, v155\n\t" \
    "v_add_f32 v237, v237, v156\n\t" \
    "v_add_f32 v237, v237, v157\n\t" \
    "v_cvt_pk_bf16_f32 v166, v156, v157\n\t" \
    "v_add_f32 v237, v237, v158\n\t" \
    "v_add_f32 v237, v237, v159\n\t" \
    "v_cvt_pk_bf16_f32 v167, v158, v159\n\t" \
    "v_cmp_lt_f32 vcc, %[thr], v237\n\t" \
    "s_cbranch_vccnz RAREG_%=\n\t" \
    "BACKG_%=:\n\t" \
    "s_waitcnt lgkmcnt(0)\n\t" \
    "v_add_f32 v236, v236, v237\n\t" \
    "v_mfma_f32_32x32x16_bf16 v[0:15], v[200:203], v[160:163], v[0:15]\n\t" \
    "v_mfma_f32_32x32x16_bf16 v[16:31], v[204:207], v[160:163], v[16:31]\n\t" \
    "v_mfma_f32_32x32x16_bf16 v[32:47], v[208:211], v[160:163], v[32:47]\n\t" \
    "v_mfma_f32_32x32x16_bf16 v[48:63], v[212:215], v[160:163], v[48:63]\n\t" \
    "v_mfma_f32_32x32x16_bf16 v[0:15], v[216:219], v[164:167], v[0:15]\n\t" \
    "v_mfma_f32_32x32x16_bf16 v[16:31], v[220:223], v[164:167], v[16:31]\n\t" \
    "v_mfma_f32_32x32x16_bf16 v[32:47], v[224:227], v[164:167], v[32:47]\n\t" \
    "v_mfma_f32_32x32x16_bf16 v[48:63], v[228:231], v[164:167], v[48:63]\n\t" \
    "s_branch END_%=\n\t" \
    "PF_%=:\n\t" \
    "s_cmp_eq_u32 %[t], 8\n\t" \
    "s_cbranch_scc0 PF2_%=\n\t" \
    "global_load_dword v250, %[pfo], %[wo]\n\t" \
    "s_branch PFB_%=\n\t" \
    "PF2_%=:\n\t" \
    "global_load_dword v251, %[pfo2], %[w2]\n\t" \
    "s_branch PFB_%=\n\t" \
    "DMAL_%=:\n\t" \
    "s_add_u32 s60, %[t], 3\n\t" \
    "s_min_u32 s60, s60, %[ntm1]\n\t" \
    "s_lshl_b32 s60, s60, 14\n\t" \
    "s_add_u32 s62, %[khl], s60\n\t" \
    "s_addc_u32 s63, %[khh], 0\n\t" \
    "s_add_u32 s61, %[soff], 0xc000\n\t" \
    "s_and_b32 s61, s61, 0xffff\n\t" \
    "s_add_u32 m0, s61, %[kdst]\n\t" \
    "s_nop 0\n\t" \
    "global_load_lds_dwordx4 %[dvo], s[62:63] offset:0\n\t" \
    "global_load_lds_dwordx4 %[dvo], s[62:63] offset:1024\n\t" \
    "global_load_lds_dwordx4 %[dvo], s[62:63] offset:2048\n\t" \
    "global_load_lds_dwordx4 %[dvo], s[62:63] offset:3072\n\t" \
    "s_add_u32 s60, %[t], 2\n\t" \
    "s_min_u32 s60, s60, %[ntm1]\n\t" \
    "s_lshl_b32 s60, s60, 14\n\t" \
    "s_add_u32 s62, %[vhl], s60\n\t" \
    "s_addc_u32 s63, %[vhh], 0\n\t" \
    "s_add_u32 s61, %[soff], 0x8000\n\t" \
    "s_and_b32 s61, s61, 0xffff\n\t" \
    "s_add_u32 m0, s61, %[vdst]\n\t" \
    "s_nop 0\n\t" \
    "global_load_lds_dwordx4 %[dvo], s[62:63] offset:0\n\t" \
    "global_load_lds_dwordx4 %[dvo], s[62:63] offset:1024\n\t" \
    "global_load_lds_dwordx4 %[dvo], s[62:63] offset:2048\n\t" \
    "global_load_lds_dwordx4 %[dvo], s[62:63] offset:3072\n\t" \
    "s_branch DMABL_%=\n\t" \
    "DMAT_%=:\n\t" \
    "s_add_u32 s60, %[t], 3\n\t" \
    "s_min_u32 s60, s60, %[ntm1]\n\t" \
    "s_lshl_b32 s60, s60, 14\n\t" \
    "s_add_u32 s62, %[khl], s60\n\t" \
    "s_addc_u32 s63, %[khh], 0\n\t" \
    "s_add_u32 s61, %[soff], 0xc000\n\t" \
    "s_and_b32 s61, s61, 0xffff\n\t" \
    "s_add_u32 m0, s61, %[kdst]\n\t" \
    "s_nop 0\n\t" \
    "global_load_lds_dwordx4 %[dvo], s[62:63] offset:0\n\t" \
    "global_load_lds_dwordx4 %[dvo], s[62:63] offset:1024\n\t" \
    "global_load_lds_dwordx4 %[dvo], s[62:63] offset:2048\n\t" \
    "global_load_lds_dwordx4 %[dvo], s[62:63] offset:3072\n\t" \
    "s_add_u32 s60, %[t], 2\n\t" \
    "s_min_u32 s60, s60, %[ntm1]\n\t" \
    "s_lshl_b32 s60, s60, 14\n\t" \
    "s_add_u32 s62, %[vhl], s60\n\t" \
    "s_addc_u32 s63, %[vhh], 0\n\t" \
    "s_add_u32 s61, %[soff], 0x8000\n\t" \
    "s_and_b32 s61, s61, 0xffff\n\t" \
    "s_add_u32 m0, s61, %[vdst]\n\t" \
    "s_nop 0\n\t" \
    "global_load_lds_dwordx4 %[dvo], s[62:63] offset:0\n\t" \
    "global_load_lds_dwordx4 %[dvo], s[62:63] offset:1024\n\t" \
    "global_load_lds_dwordx4 %[dvo], s[62:63] offset:2048\n\t" \
    "global_load_lds_dwordx4 %[dvo], s[62:63] offset:3072\n\t" \
    "s_branch DMABT_%=\n\t" \
    "RAREA_%=:\n\t" \
    "s_nop 15\n\t" \
    "v_max3_f32 v239, v128, v129, v130\n\t" \
    "v_max3_f32 v239, v239, v131, v132\n\t" \
    "v_max3_f32 v239, v239, v133, v134\n\t" \
    "v_max3_f32 v239, v239, v135, v136\n\t" \
    "v_max3_f32 v239, v239, v137, v138\n\t" \
    "v_max3_f32 v239, v239, v139, v140\n\t" \
    "v_max3_f32 v239, v239, v141, v142\n\t" \
    "v_max_f32 v239, v239, v143\n\t" \
    "v_max_f32 v239, 0, v239\n\t" \
    "v_mov_b32 v240, v239\n\t" \
    "s_nop 1\n\t" \
    "v_permlane32_swap_b32 v239, v240\n\t" \
    "s_nop 1\n\t" \
    "v_max_f32 v239, v239, v240\n\t" \
    "v_add_f32 v238, v238, v239\n\t" \
    "v_exp_f32 v240, -v239\n\t" \
    "v_sub_f32 v241, 0, v238\n\t" \
    "v_sub_f32 v128, v128, v239\n\t" \
    "v_sub_f32 v112, v112, v239\n\t" \
    "v_mov_b32 v96, v241\n\t" \
    "v_sub_f32 v129, v129, v239\n\t" \
    "v_sub_f32 v113, v113, v239\n\t" \
    "v_mov_b32 v97, v241\n\t" \
    "v_sub_f32 v130, v130, v239\n\t" \
    "v_sub_f32 v114, v114, v239\n\t" \
    "v_mov_b32 v98, v241\n\t" \
    "v_sub_f32 v131, v131, v239\n\t" \
    "v_sub_f32 v115, v115, v239\n\t" \
    "v_mov_b32 v99, v241\n\t" \
    "v_sub_f32 v132, v132, v239\n\t" \
    "v_sub_f32 v116, v116, v239\n\t" \
    "v_mov_b32 v100, v241\n\t" \
    "v_sub_f32 v133, v133, v239\n\t" \
    "v_sub_f32 v117, v117, v239\n\t" \
    "v_mov_b32 v101, v241\n\t" \
    "v_sub_f32 v134, v134, v239\n\t" \
    "v_sub_f32 v118, v118, v239\n\t" \
    "v_mov_b32 v102, v241\n\t" \
    "v_sub_f32 v135, v135, v239\n\t" \
    "v_sub_f32 v119, v119, v239\n\t" \
    "v_mov_b32 v103, v241\n\t" \
    "v_sub_f32 v136, v136, v239\n\t" \
    "v_sub_f32 v120, v120, v239\n\t" \
    "v_mov_b32 v104, v241\n\t" \
    "v_sub_f32 v137, v137, v239\n\t" \
    "v_sub_f32 v121, v121, v239\n\t" \
    "v_mov_b32 v105, v241\n\t" \
    "v_sub_f32 v138, v138, v239\n\t" \
    "v_sub_f32 v122, v122, v239\n\t" \
    "v_mov_b32 v106, v241\n\t" \
    "v_sub_f32 v139, v139, v239\n\t" \
    "v_sub_f32 v123, v123, v239\n\t" \
    "v_mov_b32 v107, v241\n\t" \
    "v_sub_f32 v140, v140, v239\n\t" \
    "v_sub_f32 v124, v124, v239\n\t" \
    "v_mov_b32 v108, v241\n\t" \
    "v_sub_f32 v141, v141, v239\n\t" \
    "v_sub_f32 v125, v125, v239\n\t" \
    "v_mov_b32 v109, v241\n\t" \
    "v_sub_f32 v142, v142, v239\n\t" \
    "v_sub_f32 v126, v126, v239\n\t" \
    "v_mov_b32 v110, v241\n\t" \
    "v_sub_f32 v143, v143, v239\n\t" \
    "v_sub_f32 v127, v127, v239\n\t" \
    "v_mov_b32 v111, v241\n\t" \
    "v_exp_f32 v144, v128\n\t" \
    "v_exp_f32 v145, v129\n\t" \
    "v_exp_f32 v146, v130\n\t" \
    "v_exp_f32 v147, v131\n\t" \
    "v_exp_f32 v148, v132\n\t" \
    "v_exp_f32 v149, v133\n\t" \
    "v_exp_f32 v150, v134\n\t" \
    "v_exp_f32 v151, v135\n\t" \
    "v_exp_f32 v152, v136\n\t" \
    "v_exp_f32 v153, v137\n\t" \
    "v_exp_f32 v154, v138\n\t" \
    "v_exp_f32 v155, v139\n\t" \
    "v_exp_f32 v156, v140\n\t" \
    "v_exp_f32 v157, v141\n\t" \
    "v_exp_f32 v158, v142\n\t" \
    "v_exp_f32 v159, v143\n\t" \
    "v_mul_f32 v236, v236, v240\n\t" \
    "v_mul_f32 v0, v0, v240\n\t" \
    "v_mul_f32 v1, v1, v240\n\t" \
    "v_mul_f32 v2, v2, v240\n\t" \
    "v_mul_f32 v3, v3, v240\n\t" \
    "v_mul_f32 v4, v4, v240\n\t" \
    "v_mul_f32 v5, v5, v240\n\t" \
    "v_mul_f32 v6, v6, v240\n\t" \
    "v_mul_f32 v7, v7, v240\n\t" \
    "v_mul_f32 v8, v8, v240\n\t" \
    "v_mul_f32 v9, v9, v240\n\t" \
    "v_mul_f32 v10, v10, v240\n\t" \
    "v_mul_f32 v11, v11, v240\n\t" \
    "v_mul_f32 v12, v12, v240\n\t" \
    "v_mul_f32 v13, v13, v240\n\t" \
    "v_mul_f32 v14, v14, v240\n\t" \
    "v_mul_f32 v15, v15, v240\n\t" \
    "v_mul_f32 v16, v16, v240\n\t" \
    "v_mul_f32 v17, v17, v240\n\t" \
    "v_mul_f32 v18, v18, v240\n\t" \
    "v_mul_f32 v19, v19, v240\n\t" \
    "v_mul_f32 v20, v20, v240\n\t" \
    "v_mul_f32 v21, v21, v240\n\t" \
    "v_mul_f32 v22, v22, v240\n\t" \
    "v_mul_f32 v23, v23, v240\n\t" \
    "v_mul_f32 v24, v24, v240\n\t" \
    "v_mul_f32 v25, v25, v240\n\t" \
    "v_mul_f32 v26, v26, v240\n\t" \
    "v_mul_f32 v27, v27, v240\n\t" \
    "v_mul_f32 v28, v28, v240\n\t" \
    "v_mul_f32 v29, v29, v240\n\t" \
    "v_mul_f32 v30, v30, v240\n\t" \
    "v_mul_f32 v31, v31, v240\n\t" \
    "v_mul_f32 v32, v32, v240\n\t" \
    "v_mul_f32 v33, v33, v240\n\t" \
    "v_mul_f32 v34, v34, v240\n\t" \
    "v_mul_f32 v35, v35, v240\n\t" \
    "v_mul_f32 v36, v36, v240\n\t" \
    "v_mul_f32 v37, v37, v240\n\t" \
    "v_mul_f32 v38, v38, v240\n\t" \
    "v_mul_f32 v39, v39, v240\n\t" \
    "v_mul_f32 v40, v40, v240\n\t" \
    "v_mul_f32 v41, v41, v240\n\t" \
    "v_mul_f32 v42, v42, v240\n\t" \
    "v_mul_f32 v43, v43, v240\n\t" \
    "v_mul_f32 v44, v44, v240\n\t" \
    "v_mul_f32 v45, v45, v240\n\t" \
    "v_mul_f32 v46, v46, v240\n\t" \
    "v_mul_f32 v47, v47, v240\n\t" \
    "v_mul_f32 v48, v48, v240\n\t" \
    "v_mul_f32 v49, v49, v240\n\t" \
    "v_mul_f32 v50, v50, v240\n\t" \
    "v_mul_f32 v51, v51, v240\n\t" \
    "v_mul_f32 v52, v52, v240\n\t" \
    "v_mul_f32 v53, v53, v240\n\t" \
    "v_mul_f32 v54, v54, v240\n\t" \
    "v_mul_f32 v55, v55, v240\n\t" \
    "v_mul_f32 v56, v56, v240\n\t" \
    "v_mul_f32 v57, v57, v240\n\t" \
    "v_mul_f32 v58, v58, v240\n\t" \
    "v_mul_f32 v59, v59, v240\n\t" \
    "v_mul_f32 v60, v60, v240\n\t" \
    "v_mul_f32 v61, v61, v240\n\t" \
    "v_mul_f32 v62, v62, v240\n\t" \
    "v_mul_f32 v63, v63, v240\n\t" \
    "v_add_f32 v237, v144, v145\n\t" \
    "v_add_f32 v237, v237, v146\n\t" \
    "v_add_f32 v237, v237, v147\n\t" \
    "v_add_f32 v237, v237, v148\n\t" \
    "v_add_f32 v237, v237, v149\n\t" \
    "v_add_f32 v237, v237, v150\n\t" \
    "v_add_f32 v237, v237, v151\n\t" \
    "v_add_f32 v237, v237, v152\n\t" \
    "v_add_f32 v237, v237, v153\n\t" \
    "v_add_f32 v237, v237, v154\n\t" \
    "v_add_f32 v237, v237, v155\n\t" \
    "v_add_f32 v237, v237, v156\n\t" \
    "v_add_f32 v237, v237, v157\n\t" \
    "v_add_f32 v237, v237, v158\n\t" \
    "v_add_f32 v237, v237, v159\n\t" \
    "v_cvt_pk_bf16_f32 v160, v144, v145\n\t" \
    "v_cvt_pk_bf16_f32 v161, v146, v147\n\t" \
    "v_cvt_pk_bf16_f32 v162, v148, v149\n\t" \
    "v_cvt_pk_bf16_f32 v163, v150, v151\n\t" \
    "v_cvt_pk_bf16_f32 v164, v152, v153\n\t" \
    "v_cvt_pk_bf16_f32 v165, v154, v155\n\t" \
    "v_cvt_pk_bf16_f32 v166, v156, v157\n\t" \
    "v_cvt_pk_bf16_f32 v167, v158, v159\n\t" \
    "s_nop 1\n\t" \
    "s_branch BACKA_%=\n\t" \
    "RAREB_%=:\n\t" \
    "s_nop 15\n\t" \
    "v_max3_f32 v239, v112, v113, v114\n\t" \
    "v_max3_f32 v239, v239, v115, v116\n\t" \
    "v_max3_f32 v239, v239, v117, v118\n\t" \
    "v_max3_f32 v239, v239, v119, v120\n\t" \
    "v_max3_f32 v239, v239, v121, v122\n\t" \
    "v_max3_f32 v239, v239, v123, v124\n\t" \
    "v_max3_f32 v239, v239, v125, v126\n\t" \
    "v_max_f32 v239, v239, v127\n\t" \
    "v_max_f32 v239, 0, v239\n\t" \
    "v_mov_b32 v240, v239\n\t" \
    "s_nop 1\n\t" \
    "v_permlane32_swap_b32 v239, v240\n\t" \
    "s_nop 1\n\t" \
    "v_max_f32 v239, v239, v240\n\t" \
    "v_add_f32 v238, v238, v239\n\t" \
    "v_exp_f32 v240, -v239\n\t" \
    "v_sub_f32 v241, 0, v238\n\t" \
    "v_sub_f32 v112, v112, v239\n\t" \
    "v_sub_f32 v128, v128, v239\n\t" \
    "v_mov_b32 v96, v241\n\t" \
    "v_sub_f32 v113, v113, v239\n\t" \
    "v_sub_f32 v129, v129, v239\n\t" \
    "v_mov_b32 v97, v241\n\t" \
    "v_sub_f32 v114, v114, v239\n\t" \
    "v_sub_f32 v130, v130, v239\n\t" \
    "v_mov_b32 v98, v241\n\t" \
    "v_sub_f32 v115, v115, v239\n\t" \
    "v_sub_f32 v131, v131, v239\n\t" \
    "v_mov_b32 v99, v241\n\t" \
    "v_sub_f32 v116, v116, v239\n\t" \
    "v_sub_f32 v132, v132, v239\n\t" \
    "v_mov_b32 v100, v241\n\t" \
    "v_sub_f32 v117, v117, v239\n\t" \
    "v_sub_f32 v133, v133, v239\n\t" \
    "v_mov_b32 v101, v241\n\t" \
    "v_sub_f32 v118, v118, v239\n\t" \
    "v_sub_f32 v134, v134, v239\n\t" \
    "v_mov_b32 v102, v241\n\t" \
    "v_sub_f32 v119, v119, v239\n\t" \
    "v_sub_f32 v135, v135, v239\n\t" \
    "v_mov_b32 v103, v241\n\t" \
    "v_sub_f32 v120, v120, v239\n\t" \
    "v_sub_f32 v136, v136, v239\n\t" \
    "v_mov_b32 v104, v241\n\t" \
    "v_sub_f32 v121, v121, v239\n\t" \
    "v_sub_f32 v137, v137, v239\n\t" \
    "v_mov_b32 v105, v241\n\t" \
    "v_sub_f32 v122, v122, v239\n\t" \
    "v_sub_f32 v138, v138, v239\n\t" \
    "v_mov_b32 v106, v241\n\t" \
    "v_sub_f32 v123, v123, v239\n\t" \
    "v_sub_f32 v139, v139, v239\n\t" \
    "v_mov_b32 v107, v241\n\t" \
    "v_sub_f32 v124, v124, v239\n\t" \
    "v_sub_f32 v140, v140, v239\n\t" \
    "v_mov_b32 v108, v241\n\t" \
    "v_sub_f32 v125, v125, v239\n\t" \
    "v_sub_f32 v141, v141, v239\n\t" \
    "v_mov_b32 v109, v241\n\t" \
    "v_sub_f32 v126, v126, v239\n\t" \
    "v_sub_f32 v142, v142, v239\n\t" \
    "v_mov_b32 v110, v241\n\t" \
    "v_sub_f32 v127, v127, v239\n\t" \
    "v_sub_f32 v143, v143, v239\n\t" \
    "v_mov_b32 v111, v241\n\t" \
    "v_exp_f32 v144, v112\n\t" \
    "v_exp_f32 v145, v113\n\t" \
    "v_exp_f32 v146, v114\n\t" \
    "v_exp_f32 v147, v115\n\t" \
    "v_exp_f32 v148, v116\n\t" \
    "v_exp_f32 v149, v117\n\t" \
    "v_exp_f32 v150, v118\n\t" \
    "v_exp_f32 v151, v119\n\t" \
    "v_exp_f32 v152, v120\n\t" \
    "v_exp_f32 v153, v121\n\t" \
    "v_exp_f32 v154, v122\n\t" \
    "v_exp_f32 v155, v123\n\t" \
    "v_exp_f32 v156, v124\n\t" \
    "v_exp_f32 v157, v125\n\t" \
    "v_exp_f32 v158, v126\n\t" \
    "v_exp_f32 v159, v127\n\t" \
    "v_mul_f32 v236, v236, v240\n\t" \
    "v_mul_f32 v0, v0, v240\n\t" \
    "v_mul_f32 v1, v1, v240\n\t" \
    "v_mul_f32 v2, v2, v240\n\t" \
    "v_mul_f32 v3, v3, v240\n\t" \
    "v_mul_f32 v4, v4, v240\n\t" \
    "v_mul_f32 v5, v5, v240\n\t" \
    "v_mul_f32 v6, v6, v240\n\t" \
    "v_mul_f32 v7, v7, v240\n\t" \
    "v_mul_f32 v8, v8, v240\n\t" \
    "v_mul_f32 v9, v9, v240\n\t" \
    "v_mul_f32 v10, v10, v240\n\t" \
    "v_mul_f32 v11, v11, v240\n\t" \
    "v_mul_f32 v12, v12, v240\n\t" \
    "v_mul_f32 v13, v13, v240\n\t" \
    "v_mul_f32 v14, v14, v240\n\t" \
    "v_mul_f32 v15, v15, v240\n\t" \
    "v_mul_f32 v16, v16, v240\n\t" \
    "v_mul_f32 v17, v17, v240\n\t" \
    "v_mul_f32 v18, v18, v240\n\t" \
    "v_mul_f32 v19, v19, v240\n\t" \
    "v_mul_f32 v20, v20, v240\n\t" \
    "v_mul_f32 v21, v21, v240\n\t" \
    "v_mul_f32 v22, v22, v240\n\t" \
    "v_mul_f32 v23, v23, v240\n\t" \
    "v_mul_f32 v24, v24, v240\n\t" \
    "v_mul_f32 v25, v25, v240\n\t" \
    "v_mul_f32 v26, v26, v240\n\t" \
    "v_mul_f32 v27, v27, v240\n\t" \
    "v_mul_f32 v28, v28, v240\n\t" \
    "v_mul_f32 v29, v29, v240\n\t" \
    "v_mul_f32 v30, v30, v240\n\t" \
    "v_mul_f32 v31, v31, v240\n\t" \
    "v_mul_f32 v32, v32, v240\n\t" \
    "v_mul_f32 v33, v33, v240\n\t" \
    "v_mul_f32 v34, v34, v240\n\t" \
    "v_mul_f32 v35, v35, v240\n\t" \
    "v_mul_f32 v36, v36, v240\n\t" \
    "v_mul_f32 v37, v37, v240\n\t" \
    "v_mul_f32 v38, v38, v240\n\t" \
    "v_mul_f32 v39, v39, v240\n\t" \
    "v_mul_f32 v40, v40, v240\n\t" \
    "v_mul_f32 v41, v41, v240\n\t" \
    "v_mul_f32 v42, v42, v240\n\t" \
    "v_mul_f32 v43, v43, v240\n\t" \
    "v_mul_f32 v44, v44, v240\n\t" \
    "v_mul_f32 v45, v45, v240\n\t" \
    "v_mul_f32 v46, v46, v240\n\t" \
    "v_mul_f32 v47, v47, v240\n\t" \
    "v_mul_f32 v48, v48, v240\n\t" \
    "v_mul_f32 v49, v49, v240\n\t" \
    "v_mul_f32 v50, v50, v240\n\t" \
    "v_mul_f32 v51, v51, v240\n\t" \
    "v_mul_f32 v52, v52, v240\n\t" \
    "v_mul_f32 v53, v53, v240\n\t" \
    "v_mul_f32 v54, v54, v240\n\t" \
    "v_mul_f32 v55, v55, v240\n\t" \
    "v_mul_f32 v56, v56, v240\n\t" \
    "v_mul_f32 v57, v57, v240\n\t" \
    "v_mul_f32 v58, v58, v240\n\t" \
    "v_mul_f32 v59, v59, v240\n\t" \
    "v_mul_f32 v60, v60, v240\n\t" \
    "v_mul_f32 v61, v61, v240\n\t" \
    "v_mul_f32 v62, v62, v240\n\t" \
    "v_mul_f32 v63, v63, v240\n\t" \
    "v_add_f32 v237, v144, v145\n\t" \
    "v_add_f32 v237, v237, v146\n\t" \
    "v_add_f32 v237, v237, v147\n\t" \
    "v_add_f32 v237, v237, v148\n\t" \
    "v_add_f32 v237, v237, v149\n\t" \
    "v_add_f32 v237, v237, v150\n\t" \
    "v_add_f32 v237, v237, v151\n\t" \
    "v_add_f32 v237, v237, v152\n\t" \
    "v_add_f32 v237, v237, v153\n\t" \
    "v_add_f32 v237, v237, v154\n\t" \
    "v_add_f32 v237, v237, v155\n\t" \
    "v_add_f32 v237, v237, v156\n\t" \
    "v_add_f32 v237, v237, v157\n\t" \
    "v_add_f32 v237, v237, v158\n\t" \
    "v_add_f32 v237, v237, v159\n\t" \
    "v_cvt_pk_bf16_f32 v160, v144, v145\n\t" \
    "v_cvt_pk_bf16_f32 v161, v146, v147\n\t" \
    "v_cvt_pk_bf16_f32 v162, v148, v149\n\t" \
    "v_cvt_pk_bf16_f32 v163, v150, v151\n\t" \
    "v_cvt_pk_bf16_f32 v164, v152, v153\n\t" \
    "v_cvt_pk_bf16_f32 v165, v154, v155\n\t" \
    "v_cvt_pk_bf16_f32 v166, v156, v157\n\t" \
    "v_cvt_pk_bf16_f32 v167, v158, v159\n\t" \
    "s_nop 1\n\t" \
    "s_branch BACKB_%=\n\t" \
    "RAREC_%=:\n\t" \
    "s_nop 15\n\t" \
    "v_max3_f32 v239, v128, v129, v130\n\t" \
    "v_max3_f32 v239, v239, v131, v132\n\t" \
    "v_max3_f32 v239, v239, v133, v134\n\t" \
    "v_max3_f32 v239, v239, v135, v136\n\t" \
    "v_max3_f32 v239, v239, v137, v138\n\t" \
    "v_max3_f32 v239, v239, v139, v140\n\t" \
    "v_max3_f32 v239, v239, v141, v142\n\t" \
    "v_max_f32 v239, v239, v143\n\t" \
    "v_max_f32 v239, 0, v239\n\t" \
    "v_mov_b32 v240, v239\n\t" \
    "s_nop 1\n\t" \
    "v_permlane32_swap_b32 v239, v240\n\t" \
    "s_nop 1\n\t" \
    "v_max_f32 v239, v239, v240\n\t" \
    "v_add_f32 v238, v238, v239\n\t" \
    "v_exp_f32 v240, -v239\n\t" \
    "v_sub_f32 v241, 0, v238\n\t" \
    "v_sub_f32 v128, v128, v239\n\t" \
    "v_sub_f32 v112, v112, v239\n\t" \
    "v_mov_b32 v96, v241\n\t" \
    "v_sub_f32 v129, v129, v239\n\t" \
    "v_sub_f32 v113, v113, v239\n\t" \
    "v_mov_b32 v97, v241\n\t" \
    "v_sub_f32 v130, v130, v239\n\t" \
    "v_sub_f32 v114, v114, v239\n\t" \
    "v_mov_b32 v98, v241\n\t" \
    "v_sub_f32 v131, v131, v239\n\t" \
    "v_sub_f32 v115, v115, v239\n\t" \
    "v_mov_b32 v99, v241\n\t" \
    "v_sub_f32 v132, v132, v239\n\t" \
    "v_sub_f32 v116, v116, v239\n\t" \
    "v_mov_b32 v100, v241\n\t" \
    "v_sub_f32 v133, v133, v239\n\t" \
    "v_sub_f32 v117, v117, v239\n\t" \
    "v_mov_b32 v101, v241\n\t" \
    "v_sub_f32 v134, v134, v239\n\t" \
    "v_sub_f32 v118, v118, v239\n\t" \
    "v_mov_b32 v102, v241\n\t" \
    "v_sub_f32 v135, v135, v239\n\t" \
    "v_sub_f32 v119, v119, v239\n\t" \
    "v_mov_b32 v103, v241\n\t" \
    "v_sub_f32 v136, v136, v239\n\t" \
    "v_sub_f32 v120, v120, v239\n\t" \
    "v_mov_b32 v104, v241\n\t" \
    "v_sub_f32 v137, v137, v239\n\t" \
    "v_sub_f32 v121, v121, v239\n\t" \
    "v_mov_b32 v105, v241\n\t" \
    "v_sub_f32 v138, v138, v239\n\t" \
    "v_sub_f32 v122, v122, v239\n\t" \
    "v_mov_b32 v106, v241\n\t" \
    "v_sub_f32 v139, v139, v239\n\t" \
    "v_sub_f32 v123, v123, v239\n\t" \
    "v_mov_b32 v107, v241\n\t" \
    "v_sub_f32 v140, v140, v239\n\t" \
    "v_sub_f32 v124, v124, v239\n\t" \
    "v_mov_b32 v108, v241\n\t" \
    "v_sub_f32 v141, v141, v239\n\t" \
    "v_sub_f32 v125, v125, v239\n\t" \
    "v_mov_b32 v109, v241\n\t" \
    "v_sub_f32 v142, v142, v239\n\t" \
    "v_sub_f32 v126, v126, v239\n\t" \
    "v_mov_b32 v110, v241\n\t" \
    "v_sub_f32 v143, v143, v239\n\t" \
    "v_sub_f32 v127, v127, v239\n\t" \
    "v_mov_b32 v111, v241\n\t" \
    "v_exp_f32 v144, v128\n\t" \
    "v_exp_f32 v145, v129\n\t" \
    "v_exp_f32 v146, v130\n\t" \
    "v_exp_f32 v147, v131\n\t" \
    "v_exp_f32 v148, v132\n\t" \
    "v_exp_f32 v149, v133\n\t" \
    "v_exp_f32 v150, v134\n\t" \
    "v_exp_f32 v151, v135\n\t" \
    "v_exp_f32 v152, v136\n\t" \
    "v_exp_f32 v153, v137\n\t" \
    "v_exp_f32 v154, v138\n\t" \
    "v_exp_f32 v155, v139\n\t" \
    "v_exp_f32 v156, v140\n\t" \
    "v_exp_f32 v157, v141\n\t" \
    "v_exp_f32 v158, v142\n\t" \
    "v_exp_f32 v159, v143\n\t" \
    "v_mul_f32 v236, v236, v240\n\t" \
    "v_mul_f32 v0, v0, v240\n\t" \
    "v_mul_f32 v1, v1, v240\n\t" \
    "v_mul_f32 v2, v2, v240\n\t" \
    "v_mul_f32 v3, v3, v240\n\t" \
    "v_mul_f32 v4, v4, v240\n\t" \
    "v_mul_f32 v5, v5, v240\n\t" \
    "v_mul_f32 v6, v6, v240\n\t" \
    "v_mul_f32 v7, v7, v240\n\t" \
    "v_mul_f32 v8, v8, v240\n\t" \
    "v_mul_f32 v9, v9, v240\n\t" \
    "v_mul_f32 v10, v10, v240\n\t" \
    "v_mul_f32 v11, v11, v240\n\t" \
    "v_mul_f32 v12, v12, v240\n\t" \
    "v_mul_f32 v13, v13, v240\n\t" \
    "v_mul_f32 v14, v14, v240\n\t" \
    "v_mul_f32 v15, v15, v240\n\t" \
    "v_mul_f32 v16, v16, v240\n\t" \
    "v_mul_f32 v17, v17, v240\n\t" \
    "v_mul_f32 v18, v18, v240\n\t" \
    "v_mul_f32 v19, v19, v240\n\t" \
    "v_mul_f32 v20, v20, v240\n\t" \
    "v_mul_f32 v21, v21, v240\n\t" \
    "v_mul_f32 v22, v22, v240\n\t" \
    "v_mul_f32 v23, v23, v240\n\t" \
    "v_mul_f32 v24, v24, v240\n\t" \
    "v_mul_f32 v25, v25, v240\n\t" \
    "v_mul_f32 v26, v26, v240\n\t" \
    "v_mul_f32 v27, v27, v240\n\t" \
    "v_mul_f32 v28, v28, v240\n\t" \
    "v_mul_f32 v29, v29, v240\n\t" \
    "v_mul_f32 v30, v30, v240\n\t" \
    "v_mul_f32 v31, v31, v240\n\t" \
    "v_mul_f32 v32, v32, v240\n\t" \
    "v_mul_f32 v33, v33, v240\n\t" \
    "v_mul_f32 v34, v34, v240\n\t" \
    "v_mul_f32 v35, v35, v240\n\t" \
    "v_mul_f32 v36, v36, v240\n\t" \
    "v_mul_f32 v37, v37, v240\n\t" \
    "v_mul_f32 v38, v38, v240\n\t" \
    "v_mul_f32 v39, v39, v240\n\t" \
    "v_mul_f32 v40, v40, v240\n\t" \
    "v_mul_f32 v41, v41, v240\n\t" \
    "v_mul_f32 v42, v42, v240\n\t" \
    "v_mul_f32 v43, v43, v240\n\t" \
    "v_mul_f32 v44, v44, v240\n\t" \
    "v_mul_f32 v45, v45, v240\n\t" \
    "v_mul_f32 v46, v46, v240\n\t" \
    "v_mul_f32 v47, v47, v240\n\t" \
    "v_mul_f32 v48, v48, v240\n\t" \
    "v_mul_f32 v49, v49, v240\n\t" \
    "v_mul_f32 v50, v50, v240\n\t" \
    "v_mul_f32 v51, v51, v240\n\t" \
    "v_mul_f32 v52, v52, v240\n\t" \
    "v_mul_f32 v53, v53, v240\n\t" \
    "v_mul_f32 v54, v54, v240\n\t" \
    "v_mul_f32 v55, v55, v240\n\t" \
    "v_mul_f32 v56, v56, v240\n\t" \
    "v_mul_f32 v57, v57, v240\n\t" \
    "v_mul_f32 v58, v58, v240\n\t" \
    "v_mul_f32 v59, v59, v240\n\t" \
    "v_mul_f32 v60, v60, v240\n\t" \
    "v_mul_f32 v61, v61, v240\n\t" \
    "v_mul_f32 v62, v62, v240\n\t" \
    "v_mul_f32 v63, v63, v240\n\t" \
    "v_add_f32 v237, v144, v145\n\t" \
    "v_add_f32 v237, v237, v146\n\t" \
    "v_add_f32 v237, v237, v147\n\t" \
    "v_add_f32 v237, v237, v148\n\t" \
    "v_add_f32 v237, v237, v149\n\t" \
    "v_add_f32 v237, v237, v150\n\t" \
    "v_add_f32 v237, v237, v151\n\t" \
    "v_add_f32 v237, v237, v152\n\t" \
    "v_add_f32 v237, v237, v153\n\t" \
    "v_add_f32 v237, v237, v154\n\t" \
    "v_add_f32 v237, v237, v155\n\t" \
    "v_add_f32 v237, v237, v156\n\t" \
    "v_add_f32 v237, v237, v157\n\t" \
    "v_add_f32 v237, v237, v158\n\t" \
    "v_add_f32 v237, v237, v159\n\t" \
    "v_cvt_pk_bf16_f32 v160, v144, v145\n\t" \
    "v_cvt_pk_bf16_f32 v161, v146, v147\n\t" \
    "v_cvt_pk_bf16_f32 v162, v148, v149\n\t" \
    "v_cvt_pk_bf16_f32 v163, v150, v151\n\t" \
    "v_cvt_pk_bf16_f32 v164, v152, v153\n\t" \
    "v_cvt_pk_bf16_f32 v165, v154, v155\n\t" \
    "v_cvt_pk_bf16_f32 v166, v156, v157\n\t" \
    "v_cvt_pk_bf16_f32 v167, v158, v159\n\t" \
    "s_nop 1\n\t" \
    "s_branch BACKC_%=\n\t" \
    "RARED_%=:\n\t" \
    "s_nop 15\n\t" \
    "v_max3_f32 v239, v112, v113, v114\n\t" \
    "v_max3_f32 v239, v239, v115, v116\n\t" \
    "v_max3_f32 v239, v239, v117, v118\n\t" \
    "v_max3_f32 v239, v239, v119, v120\n\t" \
    "v_max3_f32 v239, v239, v121, v122\n\t" \
    "v_max3_f32 v239, v239, v123, v124\n\t" \
    "v_max3_f32 v239, v239, v125, v126\n\t" \
    "v_max_f32 v239, v239, v127\n\t" \
    "v_max_f32 v239, 0, v239\n\t" \
    "v_mov_b32 v240, v239\n\t" \
    "s_nop 1\n\t" \
    "v_permlane32_swap_b32 v239, v240\n\t" \
    "s_nop 1\n\t" \
    "v_max_f32 v239, v239, v240\n\t" \
    "v_add_f32 v238, v238, v239\n\t" \
    "v_exp_f32 v240, -v239\n\t" \
    "v_sub_f32 v241, 0, v238\n\t" \
    "v_sub_f32 v112, v112, v239\n\t" \
    "v_sub_f32 v128, v128, v239\n\t" \
    "v_mov_b32 v96, v241\n\t" \
    "v_sub_f32 v113, v113, v239\n\t" \
    "v_sub_f32 v129, v129, v239\n\t" \
    "v_mov_b32 v97, v241\n\t" \
    "v_sub_f32 v114, v114, v239\n\t" \
    "v_sub_f32 v130, v130, v239\n\t" \
    "v_mov_b32 v98, v241\n\t" \
    "v_sub_f32 v115, v115, v239\n\t" \
    "v_sub_f32 v131, v131, v239\n\t" \
    "v_mov_b32 v99, v241\n\t" \
    "v_sub_f32 v116, v116, v239\n\t" \
    "v_sub_f32 v132, v132, v239\n\t" \
    "v_mov_b32 v100, v241\n\t" \
    "v_sub_f32 v117, v117, v239\n\t" \
    "v_sub_f32 v133, v133, v239\n\t" \
    "v_mov_b32 v101, v241\n\t" \
    "v_sub_f32 v118, v118, v239\n\t" \
    "v_sub_f32 v134, v134, v239\n\t" \
    "v_mov_b32 v102, v241\n\t" \
    "v_sub_f32 v119, v119, v239\n\t" \
    "v_sub_f32 v135, v135, v239\n\t" \
    "v_mov_b32 v103, v241\n\t" \
    "v_sub_f32 v120, v120, v239\n\t" \
    "v_sub_f32 v136, v136, v239\n\t" \
    "v_mov_b32 v104, v241\n\t" \
    "v_sub_f32 v121, v121, v239\n\t" \
    "v_sub_f32 v137, v137, v239\n\t" \
    "v_mov_b32 v105, v241\n\t" \
    "v_sub_f32 v122, v122, v239\n\t" \
    "v_sub_f32 v138, v138, v239\n\t" \
    "v_mov_b32 v106, v241\n\t" \
    "v_sub_f32 v123, v123, v239\n\t" \
    "v_sub_f32 v139, v139, v239\n\t" \
    "v_mov_b32 v107, v241\n\t" \
    "v_sub_f32 v124, v124, v239\n\t" \
    "v_sub_f32 v140, v140, v239\n\t" \
    "v_mov_b32 v108, v241\n\t" \
    "v_sub_f32 v125, v125, v239\n\t" \
    "v_sub_f32 v141, v141, v239\n\t" \
    "v_mov_b32 v109, v241\n\t" \
    "v_sub_f32 v126, v126, v239\n\t" \
    "v_sub_f32 v142, v142, v239\n\t" \
    "v_mov_b32 v110, v241\n\t" \
    "v_sub_f32 v127, v127, v239\n\t" \
    "v_sub_f32 v143, v143, v239\n\t" \
    "v_mov_b32 v111, v241\n\t" \
    "v_exp_f32 v144, v112\n\t" \
    "v_exp_f32 v145, v113\n\t" \
    "v_exp_f32 v146, v114\n\t" \
    "v_exp_f32 v147, v115\n\t" \
    "v_exp_f32 v148, v116\n\t" \
    "v_exp_f32 v149, v117\n\t" \
    "v_exp_f32 v150, v118\n\t" \
    "v_exp_f32 v151, v119\n\t" \
    "v_exp_f32 v152, v120\n\t" \
    "v_exp_f32 v153, v121\n\t" \
    "v_exp_f32 v154, v122\n\t" \
    "v_exp_f32 v155, v123\n\t" \
    "v_exp_f32 v156, v124\n\t" \
    "v_exp_f32 v157, v125\n\t" \
    "v_exp_f32 v158, v126\n\t" \
    "v_exp_f32 v159, v127\n\t" \
    "v_mul_f32 v236, v236, v240\n\t" \
    "v_mul_f32 v0, v0, v240\n\t" \
    "v_mul_f32 v1, v1, v240\n\t" \
    "v_mul_f32 v2, v2, v240\n\t" \
    "v_mul_f32 v3, v3, v240\n\t" \
    "v_mul_f32 v4, v4, v240\n\t" \
    "v_mul_f32 v5, v5, v240\n\t" \
    "v_mul_f32 v6, v6, v240\n\t" \
    "v_mul_f32 v7, v7, v240\n\t" \
    "v_mul_f32 v8, v8, v240\n\t" \
    "v_mul_f32 v9, v9, v240\n\t" \
    "v_mul_f32 v10, v10, v240\n\t" \
    "v_mul_f32 v11, v11, v240\n\t" \
    "v_mul_f32 v12, v12, v240\n\t" \
    "v_mul_f32 v13, v13, v240\n\t" \
    "v_mul_f32 v14, v14, v240\n\t" \
    "v_mul_f32 v15, v15, v240\n\t" \
    "v_mul_f32 v16, v16, v240\n\t" \
    "v_mul_f32 v17, v17, v240\n\t" \
    "v_mul_f32 v18, v18, v240\n\t" \
    "v_mul_f32 v19, v19, v240\n\t" \
    "v_mul_f32 v20, v20, v240\n\t" \
    "v_mul_f32 v21, v21, v240\n\t" \
    "v_mul_f32 v22, v22, v240\n\t" \
    "v_mul_f32 v23, v23, v240\n\t" \
    "v_mul_f32 v24, v24, v240\n\t" \
    "v_mul_f32 v25, v25, v240\n\t" \
    "v_mul_f32 v26, v26, v240\n\t" \
    "v_mul_f32 v27, v27, v240\n\t" \
    "v_mul_f32 v28, v28, v240\n\t" \
    "v_mul_f32 v29, v29, v240\n\t" \
    "v_mul_f32 v30, v30, v240\n\t" \
    "v_mul_f32 v31, v31, v240\n\t" \
    "v_mul_f32 v32, v32, v240\n\t" \
    "v_mul_f32 v33, v33, v240\n\t" \
    "v_mul_f32 v34, v34, v240\n\t" \
    "v_mul_f32 v35, v35, v240\n\t" \
    "v_mul_f32 v36, v36, v240\n\t" \
    "v_mul_f32 v37, v37, v240\n\t" \
    "v_mul_f32 v38, v38, v240\n\t" \
    "v_mul_f32 v39, v39, v240\n\t" \
    "v_mul_f32 v40, v40, v240\n\t" \
    "v_mul_f32 v41, v41, v240\n\t" \
    "v_mul_f32 v42, v42, v240\n\t" \
    "v_mul_f32 v43, v43, v240\n\t" \
    "v_mul_f32 v44, v44, v240\n\t" \
    "v_mul_f32 v45, v45, v240\n\t" \
    "v_mul_f32 v46, v46, v240\n\t" \
    "v_mul_f32 v47, v47, v240\n\t" \
    "v_mul_f32 v48, v48, v240\n\t" \
    "v_mul_f32 v49, v49, v240\n\t" \
    "v_mul_f32 v50, v50, v240\n\t" \
    "v_mul_f32 v51, v51, v240\n\t" \
    "v_mul_f32 v52, v52, v240\n\t" \
    "v_mul_f32 v53, v53, v240\n\t" \
    "v_mul_f32 v54, v54, v240\n\t" \
    "v_mul_f32 v55, v55, v240\n\t" \
    "v_mul_f32 v56, v56, v240\n\t" \
    "v_mul_f32 v57, v57, v240\n\t" \
    "v_mul_f32 v58, v58, v240\n\t" \
    "v_mul_f32 v59, v59, v240\n\t" \
    "v_mul_f32 v60, v60, v240\n\t" \
    "v_mul_f32 v61, v61, v240\n\t" \
    "v_mul_f32 v62, v62, v240\n\t" \
    "v_mul_f32 v63, v63, v240\n\t" \
    "v_add_f32 v237, v144, v145\n\t" \
    "v_add_f32 v237, v237, v146\n\t" \
    "v_add_f32 v237, v237, v147\n\t" \
    "v_add_f32 v237, v237, v148\n\t" \
    "v_add_f32 v237, v237, v149\n\t" \
    "v_add_f32 v237, v237, v150\n\t" \
    "v_add_f32 v237, v237, v151\n\t" \
    "v_add_f32 v237, v237, v152\n\t" \
    "v_add_f32 v237, v237, v153\n\t" \
    "v_add_f32 v237, v237, v154\n\t" \
    "v_add_f32 v237, v237, v155\n\t" \
    "v_add_f32 v237, v237, v156\n\t" \
    "v_add_f32 v237, v237, v157\n\t" \
    "v_add_f32 v237, v237, v158\n\t" \
    "v_add_f32 v237, v237, v159\n\t" \
    "v_cvt_pk_bf16_f32 v160, v144, v145\n\t" \
    "v_cvt_pk_bf16_f32 v161, v146, v147\n\t" \
    "v_cvt_pk_bf16_f32 v162, v148, v149\n\t" \
    "v_cvt_pk_bf16_f32 v163, v150, v151\n\t" \
    "v_cvt_pk_bf16_f32 v164, v152, v153\n\t" \
    "v_cvt_pk_bf16_f32 v165, v154, v155\n\t" \
    "v_cvt_pk_bf16_f32 v166, v156, v157\n\t" \
    "v_cvt_pk_bf16_f32 v167, v158, v159\n\t" \
    "s_nop 1\n\t" \
    "s_branch BACKD_%=\n\t" \
    "RAREE_%=:\n\t" \
    "s_nop 15\n\t" \
    "v_max3_f32 v239, v128, v129, v130\n\t" \
    "v_max3_f32 v239, v239, v131, v132\n\t" \
    "v_max3_f32 v239, v239, v133, v134\n\t" \
    "v_max3_f32 v239, v239, v135, v136\n\t" \
    "v_max3_f32 v239, v239, v137, v138\n\t" \
    "v_max3_f32 v239, v239, v139, v140\n\t" \
    "v_max3_f32 v239, v239, v141, v142\n\t" \
    "v_max_f32 v239, v239, v143\n\t" \
    "v_max_f32 v239, 0, v239\n\t" \
    "v_mov_b32 v240, v239\n\t" \
    "s_nop 1\n\t" \
    "v_permlane32_swap_b32 v239, v240\n\t" \
    "s_nop 1\n\t" \
    "v_max_f32 v239, v239, v240\n\t" \
    "v_add_f32 v238, v238, v239\n\t" \
    "v_exp_f32 v240, -v239\n\t" \
    "v_sub_f32 v241, 0, v238\n\t" \
    "v_sub_f32 v128, v128, v239\n\t" \
    "v_mov_b32 v96, v241\n\t" \
    "v_sub_f32 v129, v129, v239\n\t" \
    "v_mov_b32 v97, v241\n\t" \
    "v_sub_f32 v130, v130, v239\n\t" \
    "v_mov_b32 v98, v241\n\t" \
    "v_sub_f32 v131, v131, v239\n\t" \
    "v_mov_b32 v99, v241\n\t" \
    "v_sub_f32 v132, v132, v239\n\t" \
    "v_mov_b32 v100, v241\n\t" \
    "v_sub_f32 v133, v133, v239\n\t" \
    "v_mov_b32 v101, v241\n\t" \
    "v_sub_f32 v134, v134, v239\n\t" \
    "v_mov_b32 v102, v241\n\t" \
    "v_sub_f32 v135, v135, v239\n\t" \
    "v_mov_b32 v103, v241\n\t" \
    "v_sub_f32 v136, v136, v239\n\t" \
    "v_mov_b32 v104, v241\n\t" \
    "v_sub_f32 v137, v137, v239\n\t" \
    "v_mov_b32 v105, v241\n\t" \
    "v_sub_f32 v138, v138, v239\n\t" \
    "v_mov_b32 v106, v241\n\t" \
    "v_sub_f32 v139, v139, v239\n\t" \
    "v_mov_b32 v107, v241\n\t" \
    "v_sub_f32 v140, v140, v239\n\t" \
    "v_mov_b32 v108, v241\n\t" \
    "v_sub_f32 v141, v141, v239\n\t" \
    "v_mov_b32 v109, v241\n\t" \
    "v_sub_f32 v142, v142, v239\n\t" \
    "v_mov_b32 v110, v241\n\t" \
    "v_sub_f32 v143, v143, v239\n\t" \
    "v_mov_b32 v111, v241\n\t" \
    "v_exp_f32 v144, v128\n\t" \
    "v_exp_f32 v145, v129\n\t" \
    "v_exp_f32 v146, v130\n\t" \
    "v_exp_f32 v147, v131\n\t" \
    "v_exp_f32 v148, v132\n\t" \
    "v_exp_f32 v149, v133\n\t" \
    "v_exp_f32 v150, v134\n\t" \
    "v_exp_f32 v151, v135\n\t" \
    "v_exp_f32 v152, v136\n\t" \
    "v_exp_f32 v153, v137\n\t" \
    "v_exp_f32 v154, v138\n\t" \
    "v_exp_f32 v155, v139\n\t" \
    "v_exp_f32 v156, v140\n\t" \
    "v_exp_f32 v157, v141\n\t" \
    "v_exp_f32 v158, v142\n\t" \
    "v_exp_f32 v159, v143\n\t" \
    "v_mul_f32 v236, v236, v240\n\t" \
    "v_mul_f32 v0, v0, v240\n\t" \
    "v_mul_f32 v1, v1, v240\n\t" \
    "v_mul_f32 v2, v2, v240\n\t" \
    "v_mul_f32 v3, v3, v240\n\t" \
    "v_mul_f32 v4, v4, v240\n\t" \
    "v_mul_f32 v5, v5, v240\n\t" \
    "v_mul_f32 v6, v6, v240\n\t" \
    "v_mul_f32 v7, v7, v240\n\t" \
    "v_mul_f32 v8, v8, v240\n\t" \
    "v_mul_f32 v9, v9, v240\n\t" \
    "v_mul_f32 v10, v10, v240\n\t" \
    "v_mul_f32 v11, v11, v240\n\t" \
    "v_mul_f32 v12, v12, v240\n\t" \
    "v_mul_f32 v13, v13, v240\n\t" \
    "v_mul_f32 v14, v14, v240\n\t" \
    "v_mul_f32 v15, v15, v240\n\t" \
    "v_mul_f32 v16, v16, v240\n\t" \
    "v_mul_f32 v17, v17, v240\n\t" \
    "v_mul_f32 v18, v18, v240\n\t" \
    "v_mul_f32 v19, v19, v240\n\t" \
    "v_mul_f32 v20, v20, v240\n\t" \
    "v_mul_f32 v21, v21, v240\n\t" \
    "v_mul_f32 v22, v22, v240\n\t" \
    "v_mul_f32 v23, v23, v240\n\t" \
    "v_mul_f32 v24, v24, v240\n\t" \
    "v_mul_f32 v25, v25, v240\n\t" \
    "v_mul_f32 v26, v26, v240\n\t" \
    "v_mul_f32 v27, v27, v240\n\t" \
    "v_mul_f32 v28, v28, v240\n\t" \
    "v_mul_f32 v29, v29, v240\n\t" \
    "v_mul_f32 v30, v30, v240\n\t" \
    "v_mul_f32 v31, v31, v240\n\t" \
    "v_mul_f32 v32, v32, v240\n\t" \
    "v_mul_f32 v33, v33, v240\n\t" \
    "v_mul_f32 v34, v34, v240\n\t" \
    "v_mul_f32 v35, v35, v240\n\t" \
    "v_mul_f32 v36, v36, v240\n\t" \
    "v_mul_f32 v37, v37, v240\n\t" \
    "v_mul_f32 v38, v38, v240\n\t" \
    "v_mul_f32 v39, v39, v240\n\t" \
    "v_mul_f32 v40, v40, v240\n\t" \
    "v_mul_f32 v41, v41, v240\n\t" \
    "v_mul_f32 v42, v42, v240\n\t" \
    "v_mul_f32 v43, v43, v240\n\t" \
    "v_mul_f32 v44, v44, v240\n\t" \
    "v_mul_f32 v45, v45, v240\n\t" \
    "v_mul_f32 v46, v46, v240\n\t" \
    "v_mul_f32 v47, v47, v240\n\t" \
    "v_mul_f32 v48, v48, v240\n\t" \
    "v_mul_f32 v49, v49, v240\n\t" \
    "v_mul_f32 v50, v50, v240\n\t" \
    "v_mul_f32 v51, v51, v240\n\t" \
    "v_mul_f32 v52, v52, v240\n\t" \
    "v_mul_f32 v53, v53, v240\n\t" \
    "v_mul_f32 v54, v54, v240\n\t" \
    "v_mul_f32 v55, v55, v240\n\t" \
    "v_mul_f32 v56, v56, v240\n\t" \
    "v_mul_f32 v57, v57, v240\n\t" \
    "v_mul_f32 v58, v58, v240\n\t" \
    "v_mul_f32 v59, v59, v240\n\t" \
    "v_mul_f32 v60, v60, v240\n\t" \
    "v_mul_f32 v61, v61, v240\n\t" \
    "v_mul_f32 v62, v62, v240\n\t" \
    "v_mul_f32 v63, v63, v240\n\t" \
    "v_add_f32 v237, v144, v145\n\t" \
    "v_add_f32 v237, v237, v146\n\t" \
    "v_add_f32 v237, v237, v147\n\t" \
    "v_add_f32 v237, v237, v148\n\t" \
    "v_add_f32 v237, v237, v149\n\t" \
    "v_add_f32 v237, v237, v150\n\t" \
    "v_add_f32 v237, v237, v151\n\t" \
    "v_add_f32 v237, v237, v152\n\t" \
    "v_add_f32 v237, v237, v153\n\t" \
    "v_add_f32 v237, v237, v154\n\t" \
    "v_add_f32 v237, v237, v155\n\t" \
    "v_add_f32 v237, v237, v156\n\t" \
    "v_add_f32 v237, v237, v157\n\t" \
    "v_add_f32 v237, v237, v158\n\t" \
    "v_add_f32 v237, v237, v159\n\t" \
    "v_cvt_pk_bf16_f32 v160, v144, v145\n\t" \
    "v_cvt_pk_bf16_f32 v161, v146, v147\n\t" \
    "v_cvt_pk_bf16_f32 v162, v148, v149\n\t" \
    "v_cvt_pk_bf16_f32 v163, v150, v151\n\t" \
    "v_cvt_pk_bf16_f32 v164, v152, v153\n\t" \
    "v_cvt_pk_bf16_f32 v165, v154, v155\n\t" \
    "v_cvt_pk_bf16_f32 v166, v156, v157\n\t" \
    "v_cvt_pk_bf16_f32 v167, v158, v159\n\t" \
    "s_nop 1\n\t" \
    "s_branch BACKE_%=\n\t" \
    "RAREF_%=:\n\t" \
    "s_nop 15\n\t" \
    "v_max3_f32 v239, v128, v129, v130\n\t" \
    "v_max3_f32 v239, v239, v131, v132\n\t" \
    "v_max3_f32 v239, v239, v133, v134\n\t" \
    "v_max3_f32 v239, v239, v135, v136\n\t" \
    "v_max3_f32 v239, v239, v137, v138\n\t" \
    "v_max3_f32 v239, v239, v139, v140\n\t" \
    "v_max3_f32 v239, v239, v141, v142\n\t" \
    "v_max_f32 v239, v239, v143\n\t" \
    "v_max_f32 v239, 0, v239\n\t" \
    "v_mov_b32 v240, v239\n\t" \
    "s_nop 1\n\t" \
    "v_permlane32_swap_b32 v239, v240\n\t" \
    "s_nop 1\n\t" \
    "v_max_f32 v239, v239, v240\n\t" \
    "v_add_f32 v238, v238, v239\n\t" \
    "v_exp_f32 v240, -v239\n\t" \
    "v_sub_f32 v241, 0, v238\n\t" \
    "v_sub_f32 v128, v128, v239\n\t" \
    "v_sub_f32 v112, v112, v239\n\t" \
    "v_mov_b32 v96, v241\n\t" \
    "v_sub_f32 v129, v129, v239\n\t" \
    "v_sub_f32 v113, v113, v239\n\t" \
    "v_mov_b32 v97, v241\n\t" \
    "v_sub_f32 v130, v130, v239\n\t" \
    "v_sub_f32 v114, v114, v239\n\t" \
    "v_mov_b32 v98, v241\n\t" \
    "v_sub_f32 v131, v131, v239\n\t" \
    "v_sub_f32 v115, v115, v239\n\t" \
    "v_mov_b32 v99, v241\n\t" \
    "v_sub_f32 v132, v132, v239\n\t" \
    "v_sub_f32 v116, v116, v239\n\t" \
    "v_mov_b32 v100, v241\n\t" \
    "v_sub_f32 v133, v133, v239\n\t" \
    "v_sub_f32 v117, v117, v239\n\t" \
    "v_mov_b32 v101, v241\n\t" \
    "v_sub_f32 v134, v134, v239\n\t" \
    "v_sub_f32 v118, v118, v239\n\t" \
    "v_mov_b32 v102, v241\n\t" \
    "v_sub_f32 v135, v135, v239\n\t" \
    "v_sub_f32 v119, v119, v239\n\t" \
    "v_mov_b32 v103, v241\n\t" \
    "v_sub_f32 v136, v136, v239\n\t" \
    "v_sub_f32 v120, v120, v239\n\t" \
    "v_mov_b32 v104, v241\n\t" \
    "v_sub_f32 v137, v137, v239\n\t" \
    "v_sub_f32 v121, v121, v239\n\t" \
    "v_mov_b32 v105, v241\n\t" \
    "v_sub_f32 v138, v138, v239\n\t" \
    "v_sub_f32 v122, v122, v239\n\t" \
    "v_mov_b32 v106, v241\n\t" \
    "v_sub_f32 v139, v139, v239\n\t" \
    "v_sub_f32 v123, v123, v239\n\t" \
    "v_mov_b32 v107, v241\n\t" \
    "v_sub_f32 v140, v140, v239\n\t" \
    "v_sub_f32 v124, v124, v239\n\t" \
    "v_mov_b32 v108, v241\n\t" \
    "v_sub_f32 v141, v141, v239\n\t" \
    "v_sub_f32 v125, v125, v239\n\t" \
    "v_mov_b32 v109, v241\n\t" \
    "v_sub_f32 v142, v142, v239\n\t" \
    "v_sub_f32 v126, v126, v239\n\t" \
    "v_mov_b32 v110, v241\n\t" \
    "v_sub_f32 v143, v143, v239\n\t" \
    "v_sub_f32 v127, v127, v239\n\t" \
    "v_mov_b32 v111, v241\n\t" \
    "v_exp_f32 v144, v128\n\t" \
    "v_exp_f32 v145, v129\n\t" \
    "v_exp_f32 v146, v130\n\t" \
    "v_exp_f32 v147, v131\n\t" \
    "v_exp_f32 v148, v132\n\t" \
    "v_exp_f32 v149, v133\n\t" \
    "v_exp_f32 v150, v134\n\t" \
    "v_exp_f32 v151, v135\n\t" \
    "v_exp_f32 v152, v136\n\t" \
    "v_exp_f32 v153, v137\n\t" \
    "v_exp_f32 v154, v138\n\t" \
    "v_exp_f32 v155, v139\n\t" \
    "v_exp_f32 v156, v140\n\t" \
    "v_exp_f32 v157, v141\n\t" \
    "v_exp_f32 v158, v142\n\t" \
    "v_exp_f32 v159, v143\n\t" \
    "v_mul_f32 v236, v236, v240\n\t" \
    "v_mul_f32 v0, v0, v240\n\t" \
    "v_mul_f32 v1, v1, v240\n\t" \
    "v_mul_f32 v2, v2, v240\n\t" \
    "v_mul_f32 v3, v3, v240\n\t" \
    "v_mul_f32 v4, v4, v240\n\t" \
    "v_mul_f32 v5, v5, v240\n\t" \
    "v_mul_f32 v6, v6, v240\n\t" \
    "v_mul_f32 v7, v7, v240\n\t" \
    "v_mul_f32 v8, v8, v240\n\t" \
    "v_mul_f32 v9, v9, v240\n\t" \
    "v_mul_f32 v10, v10, v240\n\t" \
    "v_mul_f32 v11, v11, v240\n\t" \
    "v_mul_f32 v12, v12, v240\n\t" \
    "v_mul_f32 v13, v13, v240\n\t" \
    "v_mul_f32 v14, v14, v240\n\t" \
    "v_mul_f32 v15, v15, v240\n\t" \
    "v_mul_f32 v16, v16, v240\n\t" \
    "v_mul_f32 v17, v17, v240\n\t" \
    "v_mul_f32 v18, v18, v240\n\t" \
    "v_mul_f32 v19, v19, v240\n\t" \
    "v_mul_f32 v20, v20, v240\n\t" \
    "v_mul_f32 v21, v21, v240\n\t" \
    "v_mul_f32 v22, v22, v240\n\t" \
    "v_mul_f32 v23, v23, v240\n\t" \
    "v_mul_f32 v24, v24, v240\n\t" \
    "v_mul_f32 v25, v25, v240\n\t" \
    "v_mul_f32 v26, v26, v240\n\t" \
    "v_mul_f32 v27, v27, v240\n\t" \
    "v_mul_f32 v28, v28, v240\n\t" \
    "v_mul_f32 v29, v29, v240\n\t" \
    "v_mul_f32 v30, v30, v240\n\t" \
    "v_mul_f32 v31, v31, v240\n\t" \
    "v_mul_f32 v32, v32, v240\n\t" \
    "v_mul_f32 v33, v33, v240\n\t" \
    "v_mul_f32 v34, v34, v240\n\t" \
    "v_mul_f32 v35, v35, v240\n\t" \
    "v_mul_f32 v36, v36, v240\n\t" \
    "v_mul_f32 v37, v37, v240\n\t" \
    "v_mul_f32 v38, v38, v240\n\t" \
    "v_mul_f32 v39, v39, v240\n\t" \
    "v_mul_f32 v40, v40, v240\n\t" \
    "v_mul_f32 v41, v41, v240\n\t" \
    "v_mul_f32 v42, v42, v240\n\t" \
    "v_mul_f32 v43, v43, v240\n\t" \
    "v_mul_f32 v44, v44, v240\n\t" \
    "v_mul_f32 v45, v45, v240\n\t" \
    "v_mul_f32 v46, v46, v240\n\t" \
    "v_mul_f32 v47, v47, v240\n\t" \
    "v_mul_f32 v48, v48, v240\n\t" \
    "v_mul_f32 v49, v49, v240\n\t" \
    "v_mul_f32 v50, v50, v240\n\t" \
    "v_mul_f32 v51, v51, v240\n\t" \
    "v_mul_f32 v52, v52, v240\n\t" \
    "v_mul_f32 v53, v53, v240\n\t" \
    "v_mul_f32 v54, v54, v240\n\t" \
    "v_mul_f32 v55, v55, v240\n\t" \
    "v_mul_f32 v56, v56, v240\n\t" \
    "v_mul_f32 v57, v57, v240\n\t" \
    "v_mul_f32 v58, v58, v240\n\t" \
    "v_mul_f32 v59, v59, v240\n\t" \
    "v_mul_f32 v60, v60, v240\n\t" \
    "v_mul_f32 v61, v61, v240\n\t" \
    "v_mul_f32 v62, v62, v240\n\t" \
    "v_mul_f32 v63, v63, v240\n\t" \
    "v_add_f32 v237, v144, v145\n\t" \
    "v_add_f32 v237, v237, v146\n\t" \
    "v_add_f32 v237, v237, v147\n\t" \
    "v_add_f32 v237, v237, v148\n\t" \
    "v_add_f32 v237, v237, v149\n\t" \
    "v_add_f32 v237, v237, v150\n\t" \
    "v_add_f32 v237, v237, v151\n\t" \
    "v_add_f32 v237, v237, v152\n\t" \
    "v_add_f32 v237, v237, v153\n\t" \
    "v_add_f32 v237, v237, v154\n\t" \
    "v_add_f32 v237, v237, v155\n\t" \
    "v_add_f32 v237, v237, v156\n\t" \
    "v_add_f32 v237, v237, v157\n\t" \
    "v_add_f32 v237, v237, v158\n\t" \
    "v_add_f32 v237, v237, v159\n\t" \
    "v_cvt_pk_bf16_f32 v160, v144, v145\n\t" \
    "v_cvt_pk_bf16_f32 v161, v146, v147\n\t" \
    "v_cvt_pk_bf16_f32 v162, v148, v149\n\t" \
    "v_cvt_pk_bf16_f32 v163, v150, v151\n\t" \
    "v_cvt_pk_bf16_f32 v164, v152, v153\n\t" \
    "v_cvt_pk_bf16_f32 v165, v154, v155\n\t" \
    "v_cvt_pk_bf16_f32 v166, v156, v157\n\t" \
    "v_cvt_pk_bf16_f32 v167, v158, v159\n\t" \
    "s_nop 1\n\t" \
    "s_branch BACKF_%=\n\t" \
    "RAREG_%=:\n\t" \
    "s_nop 15\n\t" \
    "v_max3_f32 v239, v112, v113, v114\n\t" \
    "v_max3_f32 v239, v239, v115, v116\n\t" \
    "v_max3_f32 v239, v239, v117, v118\n\t" \
    "v_max3_f32 v239, v239, v119, v120\n\t" \
    "v_max3_f32 v239, v239, v121, v122\n\t" \
    "v_max3_f32 v239, v239, v123, v124\n\t" \
    "v_max3_f32 v239, v239, v125, v126\n\t" \
    "v_max_f32 v239, v239, v127\n\t" \
    "v_max_f32 v239, 0, v239\n\t" \
    "v_mov_b32 v240, v239\n\t" \
    "s_nop 1\n\t" \
    "v_permlane32_swap_b32 v239, v240\n\t" \
    "s_nop 1\n\t" \
    "v_max_f32 v239, v239, v240\n\t" \
    "v_add_f32 v238, v238, v239\n\t" \
    "v_exp_f32 v240, -v239\n\t" \
    "v_sub_f32 v241, 0, v238\n\t" \
    "v_sub_f32 v112, v112, v239\n\t" \
    "v_mov_b32 v96, v241\n\t" \
    "v_sub_f32 v113, v113, v239\n\t" \
    "v_mov_b32 v97, v241\n\t" \
    "v_sub_f32 v114, v114, v239\n\t" \
    "v_mov_b32 v98, v241\n\t" \
    "v_sub_f32 v115, v115, v239\n\t" \
    "v_mov_b32 v99, v241\n\t" \
    "v_sub_f32 v116, v116, v239\n\t" \
    "v_mov_b32 v100, v241\n\t" \
    "v_sub_f32 v117, v117, v239\n\t" \
    "v_mov_b32 v101, v241\n\t" \
    "v_sub_f32 v118, v118, v239\n\t" \
    "v_mov_b32 v102, v241\n\t" \
    "v_sub_f32 v119, v119, v239\n\t" \
    "v_mov_b32 v103, v241\n\t" \
    "v_sub_f32 v120, v120, v239\n\t" \
    "v_mov_b32 v104, v241\n\t" \
    "v_sub_f32 v121, v121, v239\n\t" \
    "v_mov_b32 v105, v241\n\t" \
    "v_sub_f32 v122, v122, v239\n\t" \
    "v_mov_b32 v106, v241\n\t" \
    "v_sub_f32 v123, v123, v239\n\t" \
    "v_mov_b32 v107, v241\n\t" \
    "v_sub_f32 v124, v124, v239\n\t" \
    "v_mov_b32 v108, v241\n\t" \
    "v_sub_f32 v125, v125, v239\n\t" \
    "v_mov_b32 v109, v241\n\t" \
    "v_sub_f32 v126, v126, v239\n\t" \
    "v_mov_b32 v110, v241\n\t" \
    "v_sub_f32 v127, v127, v239\n\t" \
    "v_mov_b32 v111, v241\n\t" \
    "v_exp_f32 v144, v112\n\t" \
    "v_exp_f32 v145, v113\n\t" \
    "v_exp_f32 v146, v114\n\t" \
    "v_exp_f32 v147, v115\n\t" \
    "v_exp_f32 v148, v116\n\t" \
    "v_exp_f32 v149, v117\n\t" \
    "v_exp_f32 v150, v118\n\t" \
    "v_exp_f32 v151, v119\n\t" \
    "v_exp_f32 v152, v120\n\t" \
    "v_exp_f32 v153, v121\n\t" \
    "v_exp_f32 v154, v122\n\t" \
    "v_exp_f32 v155, v123\n\t" \
    "v_exp_f32 v156, v124\n\t" \
    "v_exp_f32 v157, v125\n\t" \
    "v_exp_f32 v158, v126\n\t" \
    "v_exp_f32 v159, v127\n\t" \
    "v_mul_f32 v236, v236, v240\n\t" \
    "v_mul_f32 v0, v0, v240\n\t" \
    "v_mul_f32 v1, v1, v240\n\t" \
    "v_mul_f32 v2, v2, v240\n\t" \
    "v_mul_f32 v3, v3, v240\n\t" \
    "v_mul_f32 v4, v4, v240\n\t" \
    "v_mul_f32 v5, v5, v240\n\t" \
    "v_mul_f32 v6, v6, v240\n\t" \
    "v_mul_f32 v7, v7, v240\n\t" \
    "v_mul_f32 v8, v8, v240\n\t" \
    "v_mul_f32 v9, v9, v240\n\t" \
    "v_mul_f32 v10, v10, v240\n\t" \
    "v_mul_f32 v11, v11, v240\n\t" \
    "v_mul_f32 v12, v12, v240\n\t" \
    "v_mul_f32 v13, v13, v240\n\t" \
    "v_mul_f32 v14, v14, v240\n\t" \
    "v_mul_f32 v15, v15, v240\n\t" \
    "v_mul_f32 v16, v16, v240\n\t" \
    "v_mul_f32 v17, v17, v240\n\t" \
    "v_mul_f32 v18, v18, v240\n\t" \
    "v_mul_f32 v19, v19, v240\n\t" \
    "v_mul_f32 v20, v20, v240\n\t" \
    "v_mul_f32 v21, v21, v240\n\t" \
    "v_mul_f32 v22, v22, v240\n\t" \
    "v_mul_f32 v23, v23, v240\n\t" \
    "v_mul_f32 v24, v24, v240\n\t" \
    "v_mul_f32 v25, v25, v240\n\t" \
    "v_mul_f32 v26, v26, v240\n\t" \
    "v_mul_f32 v27, v27, v240\n\t" \
    "v_mul_f32 v28, v28, v240\n\t" \
    "v_mul_f32 v29, v29, v240\n\t" \
    "v_mul_f32 v30, v30, v240\n\t" \
    "v_mul_f32 v31, v31, v240\n\t" \
    "v_mul_f32 v32, v32, v240\n\t" \
    "v_mul_f32 v33, v33, v240\n\t" \
    "v_mul_f32 v34, v34, v240\n\t" \
    "v_mul_f32 v35, v35, v240\n\t" \
    "v_mul_f32 v36, v36, v240\n\t" \
    "v_mul_f32 v37, v37, v240\n\t" \
    "v_mul_f32 v38, v38, v240\n\t" \
    "v_mul_f32 v39, v39, v240\n\t" \
    "v_mul_f32 v40, v40, v240\n\t" \
    "v_mul_f32 v41, v41, v240\n\t" \
    "v_mul_f32 v42, v42, v240\n\t" \
    "v_mul_f32 v43, v43, v240\n\t" \
    "v_mul_f32 v44, v44, v240\n\t" \
    "v_mul_f32 v45, v45, v240\n\t" \
    "v_mul_f32 v46, v46, v240\n\t" \
    "v_mul_f32 v47, v47, v240\n\t" \
    "v_mul_f32 v48, v48, v240\n\t" \
    "v_mul_f32 v49, v49, v240\n\t" \
    "v_mul_f32 v50, v50, v240\n\t" \
    "v_mul_f32 v51, v51, v240\n\t" \
    "v_mul_f32 v52, v52, v240\n\t" \
    "v_mul_f32 v53, v53, v240\n\t" \
    "v_mul_f32 v54, v54, v240\n\t" \
    "v_mul_f32 v55, v55, v240\n\t" \
    "v_mul_f32 v56, v56, v240\n\t" \
    "v_mul_f32 v57, v57, v240\n\t" \
    "v_mul_f32 v58, v58, v240\n\t" \
    "v_mul_f32 v59, v59, v240\n\t" \
    "v_mul_f32 v60, v60, v240\n\t" \
    "v_mul_f32 v61, v61, v240\n\t" \
    "v_mul_f32 v62, v62, v240\n\t" \
    "v_mul_f32 v63, v63, v240\n\t" \
    "v_add_f32 v237, v144, v145\n\t" \
    "v_add_f32 v237, v237, v146\n\t" \
    "v_add_f32 v237, v237, v147\n\t" \
    "v_add_f32 v237, v237, v148\n\t" \
    "v_add_f32 v237, v237, v149\n\t" \
    "v_add_f32 v237, v237, v150\n\t" \
    "v_add_f32 v237, v237, v151\n\t" \
    "v_add_f32 v237, v237, v152\n\t" \
    "v_add_f32 v237, v237, v153\n\t" \
    "v_add_f32 v237, v237, v154\n\t" \
    "v_add_f32 v237, v237, v155\n\t" \
    "v_add_f32 v237, v237, v156\n\t" \
    "v_add_f32 v237, v237, v157\n\t" \
    "v_add_f32 v237, v237, v158\n\t" \
    "v_add_f32 v237, v237, v159\n\t" \
    "v_cvt_pk_bf16_f32 v160, v144, v145\n\t" \
    "v_cvt_pk_bf16_f32 v161, v146, v147\n\t" \
    "v_cvt_pk_bf16_f32 v162, v148, v149\n\t" \
    "v_cvt_pk_bf16_f32 v163, v150, v151\n\t" \
    "v_cvt_pk_bf16_f32 v164, v152, v153\n\t" \
    "v_cvt_pk_bf16_f32 v165, v154, v155\n\t" \
    "v_cvt_pk_bf16_f32 v166, v156, v157\n\t" \
    "v_cvt_pk_bf16_f32 v167, v158, v159\n\t" \
    "s_nop 1\n\t" \
    "s_branch BACKG_%=\n\t" \
    "END_%=:\n\t" \
    "s_cmp_lg_u32 %[dmaw], 0\n\t" \
    "s_cbranch_scc1 END2_%=\n\t" \
    "s_waitcnt vmcnt(0)\n\t" \
    "END2_%=:\n\t" \
    "s_waitcnt lgkmcnt(0)\n\t" \
    "s_nop 15\n\t"
#define MX3(a, b, c) __builtin_fmaxf(__builtin_fmaxf((a), (b)), (c))
DI float max16(const f32x16& p) {
    float a = MX3(p[0], p[1], p[2]), b = MX3(p[3], p[4], p[5]);
    a = MX3(a, p[6], p[7]); b = MX3(b, p[8], p[9]); a = MX3(a, p[10], p[11]); b = MX3(b, p[12], p[13]); a = MX3(a, p[14], p[15]);
    return __builtin_fmaxf(a, b);
}
DI float halfmax(float m) { auto rr = __builtin_amdgcn_permlane32_swap(__float_as_uint(m), __float_as_uint(m), false, false); return __builtin_fmaxf(__uint_as_float(rr[0]), __uint_as_float(rr[1])); }
DI void cmask(f32x16& p, int r32, int hi) {
#pragma unroll
    for (int r = 0; r < 16; ++r) { const int kv = 4 * hi + (r & 3) + 8 * (r >> 2); if (kv > r32) p[r] = -INFINITY; }
}
#define WAITBAR(N) asm volatile("s_waitcnt vmcnt(" #N ")\n\ts_barrier" ::: "memory")
typedef unsigned u32x16 __attribute__((ext_vector_type(16)));

__global__ __launch_bounds__(512, 2) void k2_attn(const char* __restrict__ xn, const char* __restrict__ wq, const char* __restrict__ w1, const bf16_t* __restrict__ b1, const char* __restrict__ K, const char* __restrict__ VT,
                                                  char* __restrict__ Ho, char* __restrict__ CTX, const char* __restrict__ wo, const char* __restrict__ w2) {
    extern __shared__ __attribute__((aligned(16))) unsigned char smem[];
    const lds_cptr lds = (lds_cptr)smem;
    const int tid = threadIdx.x, lane = tid & 63, r32 = lane & 31, hi = lane >> 5, wid = __builtin_amdgcn_readfirstlane(tid >> 6);
    const int xcd = blockIdx.x & 7, idx = blockIdx.x >> 3, h = K2_HEAD, ib = idx & 7;
    const int G = wid < 4 ? 4 * ib + wid : 63 - 4 * ib - (wid - 4);
    const int NT = 32 - 2 * ib;
    const char* Kh = K + (size_t)h * 32 * SLOT; const char* Vh = VT + (size_t)h * 32 * SLOT;
    const unsigned lds0 = (unsigned)(uintptr_t)smem;
    const unsigned dmaw = (unsigned)(wid - 4) >> 31;
    const unsigned dvo = (wid & 3) * 4096 + lane * 16, kdst = lds0 + LDS_K + (wid & 3) * 4096, vdst = lds0 + LDS_V + (wid & 3) * 4096;
#define DMA_T(base, dst, t, sl) do { const char* g_ = (base) + (size_t)(t) * SLOT; const unsigned l_ = (unsigned)__builtin_amdgcn_readfirstlane((dst) + ((sl) & 3) * SLOT); glds2(g_, dvo, l_); glds2(g_ + 2048, dvo, l_ + 2048); } while (0)
#define DMA_K(t, sl) DMA_T(Kh, kdst, t, sl)
#define DMA_V(t, sl) DMA_T(Vh, vdst, t, sl)
#define DMA_STEP(s) do { if (dmaw) { DMA_K(min((s) + 3, NT - 1), (s) + 3); DMA_V(min((s) + 2, NT - 1), (s) + 2); } } while (0)
    STAMP(2, 0);
    const unsigned cpf = code_pf(tid, 140);
    u32x16 qa, qb;
    {
        const int wr = wid >> 2, wc = wid & 3, fr = lane & 15, fq = lane >> 4;
        const bool hasH = h < FF / 128;
        const unsigned ldsw = lds0 + (unsigned)wid * 1024u;
        const int hoff = fq * 2048 + (wr * 64 + fr) * 16;
        const int boff0 = fq * 2048 + wc * 512 + (fr >> 2) * 64 + (fq & 1) * 32 + (fr & 3) * 8, boff1 = boff0 ^ 32;
        const unsigned brow = 8 * (wid >> 1) + ((lane & 31) >> 2), bcol = (64 * (wid & 1) + 32 * (lane >> 5) + 8 * ((lane & 3) ^ (wid & 2))) * 2;
        k1::Src u;
        u.a = xn + (size_t)ib * 16 * 16384; u.aK = 16384; u.aH = (15 - 2 * ib) * 16 * 16384; u.aD = 8192; u.va = tid * 16;
        u.b0 = wq + h * 256; u.bK0 = 64 * NQ * 2; u.bD0 = 32 * NQ * 2; u.vb0 = brow * (NQ * 2) + bcol;
        if (hasH) { u.b1 = w1 + h * 256; u.bK1 = 64 * FF * 2; u.bD1 = 32 * FF * 2; u.vb1 = brow * (FF * 2) + bcol; }
        else      { u.b1 = u.b0; u.bK1 = u.bK0; u.bD1 = u.bD0; u.vb1 = u.vb0; }
        f32x4 acc[2][2][4][2];
        k1::zero_acc(acc);
        k1::prologue_a(u, ldsw);
        k1::prologue_b(u, ldsw, wr);
        k1::Src nx;
        nx.a = Kh; nx.aK = 2 * SLOT; nx.aH = SLOT; nx.aD = 8192; nx.va = tid * 16;
        nx.b0 = Vh; nx.b1 = Vh + SLOT; nx.bK0 = nx.bK1 = 0; nx.bD0 = nx.bD1 = 8192; nx.vb0 = nx.vb1 = tid * 16;
        k1::kloop<false, 256>((lds_ptr)smem, acc, u, nx, DM / 64, ldsw, hoff, hoff + 8192, boff0, boff1);
        STAMP(2, 1);
        f32x4 bv[2];
#pragma unroll
        for (int n = 0; n < 2; ++n) { const u32x2 w = *(const u32x2*)(b1 + (hasH ? h : 0) * 128 + wc * 32 + n * 16 + fq * 4); bv[n] = (f32x4){bflo(w[0]), bfhi(w[0]), bflo(w[1]), bfhi(w[1])}; PIN(bv[n]); }
        if (wr == 0) BAR();
        asm volatile("s_waitcnt vmcnt(0) lgkmcnt(0)\n\ts_barrier" ::: "memory");
        const int cg = ((fq & 1) << 1) | (fq >> 1);
        const float C2 = 0.12751743082459868f;
#define K2_QCH(c) ((c) < 4 ? 3 * SLOT + (c) * 4096 : LDS_V + 2 * SLOT + ((c) - 4) * 4096)
#pragma unroll
        for (int ai = 0; ai < 2; ++ai)
#pragma unroll
            for (int m = 0; m < 4; ++m)
                *(LAS u32x4*)((lds_ptr)smem + K2_QCH(4 * wc + cg) + (128 * ai + 64 * wr + 16 * m + fr) * 16) = k1::pack_swap16(acc[ai][0][m][0] * C2, acc[ai][0][m][1] * C2);
        asm volatile("s_waitcnt lgkmcnt(0)\n\ts_barrier" ::: "memory");
        const int qrow = (wid < 4 ? 32 * wid : 128 + 32 * (7 - wid)) + r32;
#pragma unroll
        for (int d0 = 0; d0 < 8; ++d0) { const int ch = 2 * d0 + hi; const u32x4 q = *(const LAS u32x4*)(lds + K2_QCH(ch) + qrow * 16);
#pragma unroll
            for (int e = 0; e < 4; ++e) { if (d0 < 4) qa[4 * d0 + e] = q[e]; else qb[4 * (d0 - 4) + e] = q[e]; } }
        if (hasH) {
            char* hp = Ho + ((size_t)(2 * h + (wc >> 1)) * 8 + 4 * (wc & 1) + cg) * 2048 + (64 * wr + fr) * 16;
#pragma unroll
            for (int ai = 0; ai < 2; ++ai)
#pragma unroll
                for (int m = 0; m < 4; ++m) {
                    f32x4 v0 = acc[ai][1][m][0] + bv[0], v1 = acc[ai][1][m][1] + bv[1];
                    const f32x2 a = k1::gelu_tanh2((f32x2){v0[0], v0[1]}), b = k1::gelu_tanh2((f32x2){v0[2], v0[3]}), c = k1::gelu_tanh2((f32x2){v1[0], v1[1]}), d = k1::gelu_tanh2((f32x2){v1[2], v1[3]});
                    *(u32x4*)(hp + (size_t)(ai ? 15 - ib : ib) * 56 * 16384 + m * 256) = k1::pack_swap16((f32x4){a.x, a.y, b.x, b.y}, (f32x4){c.x, c.y, d.x, d.y});
                }
        }
        asm volatile("s_waitcnt lgkmcnt(0)\n\ts_barrier" : "+v"(qa), "+v"(qb) :: "memory");
    }
    STAMP(2, 2);
    CODE_PF_DONE(cpf);
    const unsigned kp0 = lds0 + LDS_K + hi * 1024 + r32 * 16, vp0 = lds0 + LDS_V + hi * 2048 + r32 * 16;
    f32x16 o0 = f32x16{}, o1 = f32x16{}, o2 = f32x16{}, o3 = f32x16{}, negm, cA, cB, P = f32x16{};
    u32x16 kfa, kfb;
    float l_reg = 0.f, mhat;
    {
        f32x16 s = f32x16{};
#pragma unroll
        for (int j = 0; j < 8; ++j) {
            const bf16x8 kf = lds_rd(lds + LDS_K + hi * 1024 + r32 * 16 + j * 2048); const u32x4 kw = __builtin_bit_cast(u32x4, kf);
#pragma unroll
            for (int e = 0; e < 4; ++e) { if (j < 4) kfa[4 * j + e] = kw[e]; else kfb[4 * (j - 4) + e] = kw[e]; }
            const u32x4 qw = j < 4 ? (u32x4){qa[4 * j], qa[4 * j + 1], qa[4 * j + 2], qa[4 * j + 3]} : (u32x4){qb[4 * j - 16], qb[4 * j - 15], qb[4 * j - 14], qb[4 * j - 13]};
            s = MFMA32(kf, __builtin_bit_cast(bf16x8, qw), s);
        }
        if (G == 0) cmask(s, r32, hi);
        mhat = halfmax(max16(s));
#pragma unroll
        for (int r = 0; r < 16; ++r) { negm[r] = -mhat; cB[r] = -INFINITY; cA[r] = -INFINITY; }
    }
    const float thr = 65536.0f;
    int t = 0; unsigned soff = 0;
    unsigned vk = kp0, vkn = kp0 + SLOT, vv = vp0, vvp = vp0;
#define K2_STATE [o0] "+&{v[0:15]}"(o0), [o1] "+&{v[16:31]}"(o1), [o2] "+&{v[32:47]}"(o2), [o3] "+&{v[48:63]}"(o3), [negm] "+&{v[96:111]}"(negm), [ca] "+&{v[112:127]}"(cA), [cb] "+&{v[128:143]}"(cB), \
        [p] "+&{v[144:159]}"(P), [kfa] "+&{v[168:183]}"(kfa), [kfb] "+&{v[184:199]}"(kfb), [vk] "+&{v232}"(vk), [vkn] "+&{v233}"(vkn), [vv] "+&{v234}"(vv), [vvp] "+&{v235}"(vvp), [l] "+&{v236}"(l_reg), [mh] "+&{v238}"(mhat)
#define K2_IN [qa] "{v[64:79]}"(qa), [qb] "{v[80:95]}"(qb), [thr] "s"(thr)
#define K2_CLOB "v160", "v161", "v162", "v163", "v164", "v165", "v166", "v167", "v200", "v201", "v202", "v203", "v204", "v205", "v206", "v207", "v208", "v209", "v210", "v211", "v212", "v213", "v214", "v215", \
        "v216", "v217", "v218", "v219", "v220", "v221", "v222", "v223", "v224", "v225", "v226", "v227", "v228", "v229", "v230", "v231", "v237", "v239", "v240", "v241", "vcc", "scc", "memory"
    const unsigned pfg = blockIdx.x * 256 + (tid & 255), pfo = pfg * 128u, pfo2 = (pfg < 57344u ? pfg : 0u) * 128u;
    const int nloop = G >> 1;
    asm volatile(K2_ASM : K2_STATE, [t] "+&s"(t), [soff] "+&s"(soff)
                 : K2_IN, [n] "s"(nloop), [odd] "s"(G & 1), [dmaw] "s"(dmaw), [ntm1] "s"(NT - 1), [khl] "s"((unsigned)(uintptr_t)Kh), [khh] "s"((unsigned)((uintptr_t)Kh >> 32)),
                   [vhl] "s"((unsigned)(uintptr_t)Vh), [vhh] "s"((unsigned)((uintptr_t)Vh >> 32)), [kdst] "s"(kdst), [vdst] "s"(vdst), [dvo] "v"(dvo), [kp0] "v"(kp0), [vp0] "v"(vp0), [dq] "v"(r32 - 4 * hi),
                   [pfo] "v"(pfo), [pfo2] "v"(pfo2), [wo] "s"(wo), [w2] "s"(w2)
                 : K2_CLOB, "s60", "s61", "s62", "s63", "v250", "v251");
    STAMP(2, 3);
    {
        auto rr = __builtin_amdgcn_permlane32_swap(__float_as_uint(l_reg), __float_as_uint(l_reg), false, false);
        const float inv = 1.0f / (__uint_as_float(rr[0]) + __uint_as_float(rr[1]));
        char* op = CTX + ((size_t)((G >> 2) * 64 + 2 * h) * 8 + hi) * 2048 + ((G & 3) * 32 + r32) * 16;
#define K2_ST(o, db) do { _Pragma("unroll") for (int g = 0; g < 4; g += 2) { \
            const unsigned x0 = cvtpk(o[4 * g] * inv, o[4 * g + 1] * inv), x1 = cvtpk(o[4 * g + 2] * inv, o[4 * g + 3] * inv); \
            const unsigned y0 = cvtpk(o[4 * g + 4] * inv, o[4 * g + 5] * inv), y1 = cvtpk(o[4 * g + 6] * inv, o[4 * g + 7] * inv); \
            const auto r0 = __builtin_amdgcn_permlane32_swap(x0, y0, false, false), r1 = __builtin_amdgcn_permlane32_swap(x1, y1, false, false); \
            ST16(op + (size_t)(((db) >> 1) * 8 + 4 * ((db) & 1) + g) * 2048, ((u32x4){r0[0], r1[0], r0[1], r1[1]})); } } while (0)
        K2_ST(o0, 0); K2_ST(o1, 1); K2_ST(o2, 2); K2_ST(o3, 3);
    }
    WAITBAR(16); ++t;
    if (t < NT) { DMA_STEP(t); WAITBAR(16); ++t;
        for (; t < NT; ++t) { DMA_STEP(t); WAITBAR(8); } }
    STAMP(2, 4);
    WAIT_V(0);
    STAMP(2, 5);
}
}

namespace k3 {
constexpr int STG = 49152, LDS_BYTES = 3 * STG;
typedef __attribute__((address_space(1))) unsigned gu32;
#define K3_ASM \
    "s_mov_b64 s[60:61], %[a1]\n\t" \
    "s_mov_b64 s[62:63], %[b1]\n\t" \
    "s_mov_b32 s67, %[nl]\n\t" \
    "s_mov_b32 s70, %[nl2]\n\t" \
    "s_mov_b32 s68, 0\n\t" \
    "s_add_u32 m0, %[mA], s68\n\t" \
    "s_add_u32 s66, %[mB], s68\n\t" \
    "global_load_lds_dwordx4 %[va], s[60:61] offset:0\n\t" \
    "global_load_lds_dwordx4 %[va], s[60:61] offset:1024\n\t" \
    "global_load_lds_dwordx4 %[va], s[60:61] offset:2048\n\t" \
    "global_load_lds_dwordx4 %[va], s[60:61] offset:3072\n\t" \
    "s_mov_b32 m0, s66\n\t" \
    "s_add_u32 s64, s62, 0x10000\n\t" \
    "s_addc_u32 s65, s63, 0\n\t" \
    "global_load_lds_dwordx4 %[vb], s[62:63]\n\t" \
    "s_add_u32 m0, s66, 8192\n\t" \
    "s_add_u32 s60, s60, 0x4000\n\t" \
    "global_load_lds_dwordx4 %[vb], s[64:65]\n\t" \
    "s_addc_u32 s61, s61, 0\n\t" \
    "s_add_u32 s62, s62, 0x20000\n\t" \
    "s_addc_u32 s63, s63, 0\n\t" \
    "s_mov_b32 s68, 49152\n\t" \
    "s_add_u32 m0, %[mA], s68\n\t" \
    "s_add_u32 s66, %[mB], s68\n\t" \
    "global_load_lds_dwordx4 %[va], s[60:61] offset:0\n\t" \
    "global_load_lds_dwordx4 %[va], s[60:61] offset:1024\n\t" \
    "global_load_lds_dwordx4 %[va], s[60:61] offset:2048\n\t" \
    "global_load_lds_dwordx4 %[va], s[60:61] offset:3072\n\t" \
    "s_mov_b32 m0, s66\n\t" \
    "s_add_u32 s64, s62, 0x10000\n\t" \
    "s_addc_u32 s65, s63, 0\n\t" \
    "global_load_lds_dwordx4 %[vb], s[62:63]\n\t" \
    "s_add_u32 m0, s66, 8192\n\t" \
    "s_add_u32 s60, s60, 0x4000\n\t" \
    "global_load_lds_dwordx4 %[vb], s[64:65]\n\t" \
    "s_addc_u32 s61, s61, 0\n\t" \
    "s_add_u32 s62, s62, 0x20000\n\t" \
    "s_addc_u32 s63, s63, 0\n\t" \
    "s_mov_b32 s68, 98304\n\t" \
    "s_add_u32 m0, %[mA], s68\n\t" \
    "s_add_u32 s66, %[mB], s68\n\t" \
    "global_load_lds_dwordx4 %[va], s[60:61] offset:0\n\t" \
    "global_load_lds_dwordx4 %[va], s[60:61] offset:1024\n\t" \
    "global_load_lds_dwordx4 %[va], s[60:61] offset:2048\n\t" \
    "global_load_lds_dwordx4 %[va], s[60:61] offset:3072\n\t" \
    "s_mov_b32 m0, s66\n\t" \
    "s_add_u32 s64, s62, 0x10000\n\t" \
    "s_addc_u32 s65, s63, 0\n\t" \
    "global_load_lds_dwordx4 %[vb], s[62:63]\n\t" \
    "s_add_u32 m0, s66, 8192\n\t" \
    "s_add_u32 s60, s60, 0x4000\n\t" \
    "global_load_lds_dwordx4 %[vb], s[64:65]\n\t" \
    "s_addc_u32 s61, s61, 0\n\t" \
    "s_add_u32 s62, s62, 0x20000\n\t" \
    "s_addc_u32 s63, s63, 0\n\t" \
    "s_mov_b32 s68, 0\n\t" \
    "s_mov_b32 s69, 98304\n\t" \
    "v_mov_b32 v128, v134\n\t" \
    "v_mov_b32 v129, v135\n\t" \
    "v_mov_b32 v130, v136\n\t" \
    "v_add_u32 v131, 49152, v134\n\t" \
    "v_add_u32 v132, 49152, v135\n\t" \
    "v_add_u32 v133, 49152, v136\n\t" \
    "s_waitcnt vmcnt(12)\n\t" \
    "s_barrier\n\t" \
    "ds_read_b128 v[64:67], v128 offset:0\n\t" \
    "ds_read_b64_tr_b16 v[80:81], v129 offset:0\n\t" \
    "ds_read_b64_tr_b16 v[82:83], v129 offset:256\n\t" \
    "ds_read_b64_tr_b16 v[84:85], v130 offset:0\n\t" \
    "ds_read_b64_tr_b16 v[86:87], v130 offset:256\n\t" \
    "ds_read_b128 v[68:71], v128 offset:256\n\t" \
    "ds_read_b64_tr_b16 v[88:89], v129 offset:512\n\t" \
    "ds_read_b64_tr_b16 v[90:91], v129 offset:768\n\t" \
    "ds_read_b64_tr_b16 v[92:93], v130 offset:512\n\t" \
    "ds_read_b64_tr_b16 v[94:95], v130 offset:768\n\t" \
    "ds_read_b128 v[72:75], v128 offset:512\n\t" \
    "ds_read_b128 v[76:79], v128 offset:768\n\t" \
    "LOOP_%=:\n\t" \
    "s_waitcnt lgkmcnt(0)\n\t" \
    "v_mfma_f32_16x16x32_bf16 v[0:3], v[80:83], v[64:67], v[0:3]\n\t" \
    "ds_read_b128 v[96:99], v128 offset:8192\n\t" \
    "ds_read_b64_tr_b16 v[112:113], v129 offset:8192\n\t" \
    "v_mfma_f32_16x16x32_bf16 v[4:7], v[84:87], v[64:67], v[4:7]\n\t" \
    "ds_read_b64_tr_b16 v[114:115], v129 offset:8448\n\t" \
    "ds_read_b64_tr_b16 v[116:117], v130 offset:8192\n\t" \
    "v_mfma_f32_16x16x32_bf16 v[8:11], v[88:91], v[64:67], v[8:11]\n\t" \
    "ds_read_b64_tr_b16 v[118:119], v130 offset:8448\n\t" \
    "ds_read_b128 v[100:103], v128 offset:8448\n\t" \
    "v_mfma_f32_16x16x32_bf16 v[12:15], v[92:95], v[64:67], v[12:15]\n\t" \
    "ds_read_b64_tr_b16 v[120:121], v129 offset:8704\n\t" \
    "ds_read_b64_tr_b16 v[122:123], v129 offset:8960\n\t" \
    "v_mfma_f32_16x16x32_bf16 v[16:19], v[80:83], v[68:71], v[16:19]\n\t" \
    "ds_read_b64_tr_b16 v[124:125], v130 offset:8704\n\t" \
    "ds_read_b64_tr_b16 v[126:127], v130 offset:8960\n\t" \
    "v_mfma_f32_16x16x32_bf16 v[20:23], v[84:87], v[68:71], v[20:23]\n\t" \
    "ds_read_b128 v[104:107], v128 offset:8704\n\t" \
    "ds_read_b128 v[108:111], v128 offset:8960\n\t" \
    "v_mfma_f32_16x16x32_bf16 v[24:27], v[88:91], v[68:71], v[24:27]\n\t" \
    "v_mfma_f32_16x16x32_bf16 v[28:31], v[92:95], v[68:71], v[28:31]\n\t" \
    "v_mfma_f32_16x16x32_bf16 v[32:35], v[80:83], v[72:75], v[32:35]\n\t" \
    "v_mfma_f32_16x16x32_bf16 v[36:39], v[84:87], v[72:75], v[36:39]\n\t" \
    "v_mfma_f32_16x16x32_bf16 v[40:43], v[88:91], v[72:75], v[40:43]\n\t" \
    "v_mfma_f32_16x16x32_bf16 v[44:47], v[92:95], v[72:75], v[44:47]\n\t" \
    "v_mfma_f32_16x16x32_bf16 v[48:51], v[80:83], v[76:79], v[48:51]\n\t" \
    "v_mfma_f32_16x16x32_bf16 v[52:55], v[84:87], v[76:79], v[52:55]\n\t" \
    "v_mfma_f32_16x16x32_bf16 v[56:59], v[88:91], v[76:79], v[56:59]\n\t" \
    "v_mfma_f32_16x16x32_bf16 v[60:63], v[92:95], v[76:79], v[60:63]\n\t" \
    "s_waitcnt vmcnt(6) lgkmcnt(0)\n\t" \
    "s_barrier\n\t" \
    "v_mfma_f32_16x16x32_bf16 v[0:3], v[112:115], v[96:99], v[0:3]\n\t" \
    "ds_read_b128 v[64:67], v131 offset:0\n\t" \
    "ds_read_b64_tr_b16 v[80:81], v132 offset:0\n\t" \
    "v_mfma_f32_16x16x32_bf16 v[4:7], v[116:119], v[96:99], v[4:7]\n\t" \
    "ds_read_b64_tr_b16 v[82:83], v132 offset:256\n\t" \
    "ds_read_b64_tr_b16 v[84:85], v133 offset:0\n\t" \
    "v_mfma_f32_16x16x32_bf16 v[8:11], v[120:123], v[96:99], v[8:11]\n\t" \
    "ds_read_b64_tr_b16 v[86:87], v133 offset:256\n\t" \
    "ds_read_b128 v[68:71], v131 offset:256\n\t" \
    "v_mfma_f32_16x16x32_bf16 v[12:15], v[124:127], v[96:99], v[12:15]\n\t" \
    "ds_read_b64_tr_b16 v[88:89], v132 offset:512\n\t" \
    "ds_read_b64_tr_b16 v[90:91], v132 offset:768\n\t" \
    "v_mfma_f32_16x16x32_bf16 v[16:19], v[112:115], v[100:103], v[16:19]\n\t" \
    "ds_read_b64_tr_b16 v[92:93], v133 offset:512\n\t" \
    "ds_read_b64_tr_b16 v[94:95], v133 offset:768\n\t" \
    "v_mfma_f32_16x16x32_bf16 v[20:23], v[116:119], v[100:103], v[20:23]\n\t" \
    "ds_read_b128 v[72:75], v131 offset:512\n\t" \
    "ds_read_b128 v[76:79], v131 offset:768\n\t" \
    "v_mfma_f32_16x16x32_bf16 v[24:27], v[120:123], v[100:103], v[24:27]\n\t" \
    "s_add_u32 m0, %[mA], s68\n\t" \
    "s_add_u32 s66, %[mB], s68\n\t" \
    "global_load_lds_dwordx4 %[va], s[60:61] offset:0\n\t" \
    "v_mfma_f32_16x16x32_bf16 v[28:31], v[124:127], v[100:103], v[28:31]\n\t" \
    "global_load_lds_dwordx4 %[va], s[60:61] offset:1024\n\t" \
    "global_load_lds_dwordx4 %[va], s[60:61] offset:2048\n\t" \
    "global_load_lds_dwordx4 %[va], s[60:61] offset:3072\n\t" \
    "v_mfma_f32_16x16x32_bf16 v[32:35], v[112:115], v[104:107], v[32:35]\n\t" \
    "s_mov_b32 m0, s66\n\t" \
    "s_add_u32 s64, s62, 0x10000\n\t" \
    "s_addc_u32 s65, s63, 0\n\t" \
    "v_mfma_f32_16x16x32_bf16 v[36:39], v[116:119], v[104:107], v[36:39]\n\t" \
    "global_load_lds_dwordx4 %[vb], s[62:63]\n\t" \
    "s_add_u32 m0, s66, 8192\n\t" \
    "s_add_u32 s60, s60, 0x4000\n\t" \
    "v_mfma_f32_16x16x32_bf16 v[40:43], v[120:123], v[104:107], v[40:43]\n\t" \
    "global_load_lds_dwordx4 %[vb], s[64:65]\n\t" \
    "s_addc_u32 s61, s61, 0\n\t" \
    "s_add_u32 s62, s62, 0x20000\n\t" \
    "v_mfma_f32_16x16x32_bf16 v[44:47], v[124:127], v[104:107], v[44:47]\n\t" \
    "s_addc_u32 s63, s63, 0\n\t" \
    "v_mov_b32 v128, v131\n\t" \
    "v_mov_b32 v129, v132\n\t" \
    "v_mfma_f32_16x16x32_bf16 v[48:51], v[112:115], v[108:111], v[48:51]\n\t" \
    "v_mov_b32 v130, v133\n\t" \
    "v_add_u32 v131, s69, v134\n\t" \
    "v_add_u32 v132, s69, v135\n\t" \
    "v_add_u32 v133, s69, v136\n\t" \
    "v_mfma_f32_16x16x32_bf16 v[52:55], v[116:119], v[108:111], v[52:55]\n\t" \
    "s_add_u32 s68, s68, 49152\n\t" \
    "s_cmp_eq_u32 s68, 147456\n\t" \
    "s_cselect_b32 s68, 0, s68\n\t" \
    "s_add_u32 s69, s69, 49152\n\t" \
    "v_mfma_f32_16x16x32_bf16 v[56:59], v[120:123], v[108:111], v[56:59]\n\t" \
    "s_cmp_eq_u32 s69, 147456\n\t" \
    "s_cselect_b32 s69, 0, s69\n\t" \
    "v_mfma_f32_16x16x32_bf16 v[60:63], v[124:127], v[108:111], v[60:63]\n\t" \
    "s_sub_u32 s67, s67, 1\n\t" \
    "s_cmp_lg_u32 s67, 0\n\t" \
    "s_cbranch_scc1 LOOP_%=\n\t" \
    "s_cmp_eq_u32 s70, 0\n\t" \
    "s_cbranch_scc1 FIN_%=\n\t" \
    "s_mov_b32 s67, s70\n\t" \
    "s_mov_b32 s70, 0\n\t" \
    "s_mov_b64 s[60:61], %[a2]\n\t" \
    "s_mov_b64 s[62:63], %[b2]\n\t" \
    "s_branch LOOP_%=\n\t" \
    "FIN_%=:\n\t" \
    "s_waitcnt lgkmcnt(0)\n\t" \
    "v_mfma_f32_16x16x32_bf16 v[0:3], v[80:83], v[64:67], v[0:3]\n\t" \
    "ds_read_b128 v[96:99], v128 offset:8192\n\t" \
    "ds_read_b64_tr_b16 v[112:113], v129 offset:8192\n\t" \
    "v_mfma_f32_16x16x32_bf16 v[4:7], v[84:87], v[64:67], v[4:7]\n\t" \
    "ds_read_b64_tr_b16 v[114:115], v129 offset:8448\n\t" \
    "ds_read_b64_tr_b16 v[116:117], v130 offset:8192\n\t" \
    "v_mfma_f32_16x16x32_bf16 v[8:11], v[88:91], v[64:67], v[8:11]\n\t" \
    "ds_read_b64_tr_b16 v[118:119], v130 offset:8448\n\t" \
    "ds_read_b128 v[100:103], v128 offset:8448\n\t" \
    "v_mfma_f32_16x16x32_bf16 v[12:15], v[92:95], v[64:67], v[12:15]\n\t" \
    "ds_read_b64_tr_b16 v[120:121], v129 offset:8704\n\t" \
    "ds_read_b64_tr_b16 v[122:123], v129 offset:8960\n\t" \
    "v_mfma_f32_16x16x32_bf16 v[16:19], v[80:83], v[68:71], v[16:19]\n\t" \
    "ds_read_b64_tr_b16 v[124:125], v130 offset:8704\n\t" \
    "ds_read_b64_tr_b16 v[126:127], v130 offset:8960\n\t" \
    "v_mfma_f32_16x16x32_bf16 v[20:23], v[84:87], v[68:71], v[20:23]\n\t" \
    "ds_read_b128 v[104:107], v128 offset:8704\n\t" \
    "ds_read_b128 v[108:111], v128 offset:8960\n\t" \
    "v_mfma_f32_16x16x32_bf16 v[24:27], v[88:91], v[68:71], v[24:27]\n\t" \
    "v_mfma_f32_16x16x32_bf16 v[28:31], v[92:95], v[68:71], v[28:31]\n\t" \
    "v_mfma_f32_16x16x32_bf16 v[32:35], v[80:83], v[72:75], v[32:35]\n\t" \
    "v_mfma_f32_16x16x32_bf16 v[36:39], v[84:87], v[72:75], v[36:39]\n\t" \
    "v_mfma_f32_16x16x32_bf16 v[40:43], v[88:91], v[72:75], v[40:43]\n\t" \
    "v_mfma_f32_16x16x32_bf16 v[44:47], v[92:95], v[72:75], v[44:47]\n\t" \
    "v_mfma_f32_16x16x32_bf16 v[48:51], v[80:83], v[76:79], v[48:51]\n\t" \
    "v_mfma_f32_16x16x32_bf16 v[52:55], v[84:87], v[76:79], v[52:55]\n\t" \
    "v_mfma_f32_16x16x32_bf16 v[56:59], v[88:91], v[76:79], v[56:59]\n\t" \
    "v_mfma_f32_16x16x32_bf16 v[60:63], v[92:95], v[76:79], v[60:63]\n\t" \
    "s_waitcnt vmcnt(6) lgkmcnt(0)\n\t" \
    "s_barrier\n\t" \
    "v_mfma_f32_16x16x32_bf16 v[0:3], v[112:115], v[96:99], v[0:3]\n\t" \
    "ds_read_b128 v[64:67], v131 offset:0\n\t" \
    "ds_read_b64_tr_b16 v[80:81], v132 offset:0\n\t" \
    "v_mfma_f32_16x16x32_bf16 v[4:7], v[116:119], v[96:99], v[4:7]\n\t" \
    "ds_read_b64_tr_b16 v[82:83], v132 offset:256\n\t" \
    "ds_read_b64_tr_b16 v[84:85], v133 offset:0\n\t" \
    "v_mfma_f32_16x16x32_bf16 v[8:11], v[120:123], v[96:99], v[8:11]\n\t" \
    "ds_read_b64_tr_b16 v[86:87], v133 offset:256\n\t" \
    "ds_read_b128 v[68:71], v131 offset:256\n\t" \
    "v_mfma_f32_16x16x32_bf16 v[12:15], v[124:127], v[96:99], v[12:15]\n\t" \
    "ds_read_b64_tr_b16 v[88:89], v132 offset:512\n\t" \
    "ds_read_b64_tr_b16 v[90:91], v132 offset:768\n\t" \
    "v_mfma_f32_16x16x32_bf16 v[16:19], v[112:115], v[100:103], v[16:19]\n\t" \
    "ds_read_b64_tr_b16 v[92:93], v133 offset:512\n\t" \
    "ds_read_b64_tr_b16 v[94:95], v133 offset:768\n\t" \
    "v_mfma_f32_16x16x32_bf16 v[20:23], v[116:119], v[100:103], v[20:23]\n\t" \
    "ds_read_b128 v[72:75], v131 offset:512\n\t" \
    "ds_read_b128 v[76:79], v131 offset:768\n\t" \
    "v_mfma_f32_16x16x32_bf16 v[24:27], v[120:123], v[100:103], v[24:27]\n\t" \
    "v_mov_b32 v128, v131\n\t" \
    "v_mov_b32 v129, v132\n\t" \
    "v_mov_b32 v130, v133\n\t" \
    "v_mfma_f32_16x16x32_bf16 v[28:31], v[124:127], v[100:103], v[28:31]\n\t" \
    "v_add_u32 v131, s69, v134\n\t" \
    "v_add_u32 v132, s69, v135\n\t" \
    "v_add_u32 v133, s69, v136\n\t" \
    "v_mfma_f32_16x16x32_bf16 v[32:35], v[112:115], v[104:107], v[32:35]\n\t" \
    "s_add_u32 s68, s68, 49152\n\t" \
    "s_cmp_eq_u32 s68, 147456\n\t" \
    "s_cselect_b32 s68, 0, s68\n\t" \
    "v_mfma_f32_16x16x32_bf16 v[36:39], v[116:119], v[104:107], v[36:39]\n\t" \
    "s_add_u32 s69, s69, 49152\n\t" \
    "s_cmp_eq_u32 s69, 147456\n\t" \
    "s_cselect_b32 s69, 0, s69\n\t" \
    "v_mfma_f32_16x16x32_bf16 v[40:43], v[120:123], v[104:107], v[40:43]\n\t" \
    "v_mfma_f32_16x16x32_bf16 v[44:47], v[124:127], v[104:107], v[44:47]\n\t" \
    "v_mfma_f32_16x16x32_bf16 v[48:51], v[112:115], v[108:111], v[48:51]\n\t" \
    "v_mfma_f32_16x16x32_bf16 v[52:55], v[116:119], v[108:111], v[52:55]\n\t" \
    "v_mfma_f32_16x16x32_bf16 v[56:59], v[120:123], v[108:111], v[56:59]\n\t" \
    "v_mfma_f32_16x16x32_bf16 v[60:63], v[124:127], v[108:111], v[60:63]\n\t" \
    "s_waitcnt lgkmcnt(0)\n\t" \
    "v_mfma_f32_16x16x32_bf16 v[0:3], v[80:83], v[64:67], v[0:3]\n\t" \
    "ds_read_b128 v[96:99], v128 offset:8192\n\t" \
    "ds_read_b64_tr_b16 v[112:113], v129 offset:8192\n\t" \
    "v_mfma_f32_16x16x32_bf16 v[4:7], v[84:87], v[64:67], v[4:7]\n\t" \
    "ds_read_b64_tr_b16 v[114:115], v129 offset:8448\n\t" \
    "ds_read_b64_tr_b16 v[116:117], v130 offset:8192\n\t" \
    "v_mfma_f32_16x16x32_bf16 v[8:11], v[88:91], v[64:67], v[8:11]\n\t" \
    "ds_read_b64_tr_b16 v[118:119], v130 offset:8448\n\t" \
    "ds_read_b128 v[100:103], v128 offset:8448\n\t" \
    "v_mfma_f32_16x16x32_bf16 v[12:15], v[92:95], v[64:67], v[12:15]\n\t" \
    "ds_read_b64_tr_b16 v[120:121], v129 offset:8704\n\t" \
    "ds_read_b64_tr_b16 v[122:123], v129 offset:8960\n\t" \
    "v_mfma_f32_16x16x32_bf16 v[16:19], v[80:83], v[68:71], v[16:19]\n\t" \
    "ds_read_b64_tr_b16 v[124:125], v130 offset:8704\n\t" \
    "ds_read_b64_tr_b16 v[126:127], v130 offset:8960\n\t" \
    "v_mfma_f32_16x16x32_bf16 v[20:23], v[84:87], v[68:71], v[20:23]\n\t" \
    "ds_read_b128 v[104:107], v128 offset:8704\n\t" \
    "ds_read_b128 v[108:111], v128 offset:8960\n\t" \
    "v_mfma_f32_16x16x32_bf16 v[24:27], v[88:91], v[68:71], v[24:27]\n\t" \
    "v_mfma_f32_16x16x32_bf16 v[28:31], v[92:95], v[68:71], v[28:31]\n\t" \
    "v_mfma_f32_16x16x32_bf16 v[32:35], v[80:83], v[72:75], v[32:35]\n\t" \
    "v_mfma_f32_16x16x32_bf16 v[36:39], v[84:87], v[72:75], v[36:39]\n\t" \
    "v_mfma_f32_16x16x32_bf16 v[40:43], v[88:91], v[72:75], v[40:43]\n\t" \
    "v_mfma_f32_16x16x32_bf16 v[44:47], v[92:95], v[72:75], v[44:47]\n\t" \
    "v_mfma_f32_16x16x32_bf16 v[48:51], v[80:83], v[76:79], v[48:51]\n\t" \
    "v_mfma_f32_16x16x32_bf16 v[52:55], v[84:87], v[76:79], v[52:55]\n\t" \
    "v_mfma_f32_16x16x32_bf16 v[56:59], v[88:91], v[76:79], v[56:59]\n\t" \
    "v_mfma_f32_16x16x32_bf16 v[60:63], v[92:95], v[76:79], v[60:63]\n\t" \
    "s_waitcnt vmcnt(0) lgkmcnt(0)\n\t" \
    "s_barrier\n\t" \
    "v_mfma_f32_16x16x32_bf16 v[0:3], v[112:115], v[96:99], v[0:3]\n\t" \
    "ds_read_b128 v[64:67], v131 offset:0\n\t" \
    "ds_read_b64_tr_b16 v[80:81], v132 offset:0\n\t" \
    "v_mfma_f32_16x16x32_bf16 v[4:7], v[116:119], v[96:99], v[4:7]\n\t" \
    "ds_read_b64_tr_b16 v[82:83], v132 offset:256\n\t" \
    "ds_read_b64_tr_b16 v[84:85], v133 offset:0\n\t" \
    "v_mfma_f32_16x16x32_bf16 v[8:11], v[120:123], v[96:99], v[8:11]\n\t" \
    "ds_read_b64_tr_b16 v[86:87], v133 offset:256\n\t" \
    "ds_read_b128 v[68:71], v131 offset:256\n\t" \
    "v_mfma_f32_16x16x32_bf16 v[12:15], v[124:127], v[96:99], v[12:15]\n\t" \
    "ds_read_b64_tr_b16 v[88:89], v132 offset:512\n\t" \
    "ds_read_b64_tr_b16 v[90:91], v132 offset:768\n\t" \
    "v_mfma_f32_16x16x32_bf16 v[16:19], v[112:115], v[100:103], v[16:19]\n\t" \
    "ds_read_b64_tr_b16 v[92:93], v133 offset:512\n\t" \
    "ds_read_b64_tr_b16 v[94:95], v133 offset:768\n\t" \
    "v_mfma_f32_16x16x32_bf16 v[20:23], v[116:119], v[100:103], v[20:23]\n\t" \
    "ds_read_b128 v[72:75], v131 offset:512\n\t" \
    "ds_read_b128 v[76:79], v131 offset:768\n\t" \
    "v_mfma_f32_16x16x32_bf16 v[24:27], v[120:123], v[100:103], v[24:27]\n\t" \
    "v_mov_b32 v128, v131\n\t" \
    "v_mov_b32 v129, v132\n\t" \
    "v_mov_b32 v130, v133\n\t" \
    "v_mfma_f32_16x16x32_bf16 v[28:31], v[124:127], v[100:103], v[28:31]\n\t" \
    "v_add_u32 v131, s69, v134\n\t" \
    "v_add_u32 v132, s69, v135\n\t" \
    "v_add_u32 v133, s69, v136\n\t" \
    "v_mfma_f32_16x16x32_bf16 v[32:35], v[112:115], v[104:107], v[32:35]\n\t" \
    "s_add_u32 s68, s68, 49152\n\t" \
    "s_cmp_eq_u32 s68, 147456\n\t" \
    "s_cselect_b32 s68, 0, s68\n\t" \
    "v_mfma_f32_16x16x32_bf16 v[36:39], v[116:119], v[104:107], v[36:39]\n\t" \
    "s_add_u32 s69, s69, 49152\n\t" \
    "s_cmp_eq_u32 s69, 147456\n\t" \
    "s_cselect_b32 s69, 0, s69\n\t" \
    "v_mfma_f32_16x16x32_bf16 v[40:43], v[120:123], v[104:107], v[40:43]\n\t" \
    "v_mfma_f32_16x16x32_bf16 v[44:47], v[124:127], v[104:107], v[44:47]\n\t" \
    "v_mfma_f32_16x16x32_bf16 v[48:51], v[112:115], v[108:111], v[48:51]\n\t" \
    "v_mfma_f32_16x16x32_bf16 v[52:55], v[116:119], v[108:111], v[52:55]\n\t" \
    "v_mfma_f32_16x16x32_bf16 v[56:59], v[120:123], v[108:111], v[56:59]\n\t" \
    "v_mfma_f32_16x16x32_bf16 v[60:63], v[124:127], v[108:111], v[60:63]\n\t" \
    "s_waitcnt lgkmcnt(0)\n\t" \
    "v_mfma_f32_16x16x32_bf16 v[0:3], v[80:83], v[64:67], v[0:3]\n\t" \
    "ds_read_b128 v[96:99], v128 offset:8192\n\t" \
    "ds_read_b64_tr_b16 v[112:113], v129 offset:8192\n\t" \
    "v_mfma_f32_16x16x32_bf16 v[4:7], v[84:87], v[64:67], v[4:7]\n\t" \
    "ds_read_b64_tr_b16 v[114:115], v129 offset:8448\n\t" \
    "ds_read_b64_tr_b16 v[116:117], v130 offset:8192\n\t" \
    "v_mfma_f32_16x16x32_bf16 v[8:11], v[88:91], v[64:67], v[8:11]\n\t" \
    "ds_read_b64_tr_b16 v[118:119], v130 offset:8448\n\t" \
    "ds_read_b128 v[100:103], v128 offset:8448\n\t" \
    "v_mfma_f32_16x16x32_bf16 v[12:15], v[92:95], v[64:67], v[12:15]\n\t" \
    "ds_read_b64_tr_b16 v[120:121], v129 offset:8704\n\t" \
    "ds_read_b64_tr_b16 v[122:123], v129 offset:8960\n\t" \
    "v_mfma_f32_16x16x32_bf16 v[16:19], v[80:83], v[68:71], v[16:19]\n\t" \
    "ds_read_b64_tr_b16 v[124:125], v130 offset:8704\n\t" \
    "ds_read_b64_tr_b16 v[126:127], v130 offset:8960\n\t" \
    "v_mfma_f32_16x16x32_bf16 v[20:23], v[84:87], v[68:71], v[20:23]\n\t" \
    "ds_read_b128 v[104:107], v128 offset:8704\n\t" \
    "ds_read_b128 v[108:111], v128 offset:8960\n\t" \
    "v_mfma_f32_16x16x32_bf16 v[24:27], v[88:91], v[68:71], v[24:27]\n\t" \
    "v_mfma_f32_16x16x32_bf16 v[28:31], v[92:95], v[68:71], v[28:31]\n\t" \
    "v_mfma_f32_16x16x32_bf16 v[32:35], v[80:83], v[72:75], v[32:35]\n\t" \
    "v_mfma_f32_16x16x32_bf16 v[36:39], v[84:87], v[72:75], v[36:39]\n\t" \
    "v_mfma_f32_16x16x32_bf16 v[40:43], v[88:91], v[72:75], v[40:43]\n\t" \
    "v_mfma_f32_16x16x32_bf16 v[44:47], v[92:95], v[72:75], v[44:47]\n\t" \
    "v_mfma_f32_16x16x32_bf16 v[48:51], v[80:83], v[76:79], v[48:51]\n\t" \
    "v_mfma_f32_16x16x32_bf16 v[52:55], v[84:87], v[76:79], v[52:55]\n\t" \
    "v_mfma_f32_16x16x32_bf16 v[56:59], v[88:91], v[76:79], v[56:59]\n\t" \
    "v_mfma_f32_16x16x32_bf16 v[60:63], v[92:95], v[76:79], v[60:63]\n\t" \
    "s_waitcnt vmcnt(0) lgkmcnt(0)\n\t" \
    "s_barrier\n\t" \
    "v_mfma_f32_16x16x32_bf16 v[0:3], v[112:115], v[96:99], v[0:3]\n\t" \
    "v_mov_b32 v128, v131\n\t" \
    "v_mov_b32 v129, v132\n\t" \
    "v_mfma_f32_16x16x32_bf16 v[4:7], v[116:119], v[96:99], v[4:7]\n\t" \
    "v_mov_b32 v130, v133\n\t" \
    "v_add_u32 v131, s69, v134\n\t" \
    "v_mfma_f32_16x16x32_bf16 v[8:11], v[120:123], v[96:99], v[8:11]\n\t" \
    "v_add_u32 v132, s69, v135\n\t" \
    "v_add_u32 v133, s69, v136\n\t" \
    "v_mfma_f32_16x16x32_bf16 v[12:15], v[124:127], v[96:99], v[12:15]\n\t" \
    "s_add_u32 s68, s68, 49152\n\t" \
    "s_cmp_eq_u32 s68, 147456\n\t" \
    "v_mfma_f32_16x16x32_bf16 v[16:19], v[112:115], v[100:103], v[16:19]\n\t" \
    "s_cselect_b32 s68, 0, s68\n\t" \
    "s_add_u32 s69, s69, 49152\n\t" \
    "v_mfma_f32_16x16x32_bf16 v[20:23], v[116:119], v[100:103], v[20:23]\n\t" \
    "s_cmp_eq_u32 s69, 147456\n\t" \
    "s_cselect_b32 s69, 0, s69\n\t" \
    "v_mfma_f32_16x16x32_bf16 v[24:27], v[120:123], v[100:103], v[24:27]\n\t" \
    "v_mfma_f32_16x16x32_bf16 v[28:31], v[124:127], v[100:103], v[28:31]\n\t" \
    "v_mfma_f32_16x16x32_bf16 v[32:35], v[112:115], v[104:107], v[32:35]\n\t" \
    "v_mfma_f32_16x16x32_bf16 v[36:39], v[116:119], v[104:107], v[36:39]\n\t" \
    "v_mfma_f32_16x16x32_bf16 v[40:43], v[120:123], v[104:107], v[40:43]\n\t" \
    "v_mfma_f32_16x16x32_bf16 v[44:47], v[124:127], v[104:107], v[44:47]\n\t" \
    "v_mfma_f32_16x16x32_bf16 v[48:51], v[112:115], v[108:111], v[48:51]\n\t" \
    "v_mfma_f32_16x16x32_bf16 v[52:55], v[116:119], v[108:111], v[52:55]\n\t" \
    "v_mfma_f32_16x16x32_bf16 v[56:59], v[120:123], v[108:111], v[56:59]\n\t" \
    "v_mfma_f32_16x16x32_bf16 v[60:63], v[124:127], v[108:111], v[60:63]\n\t" \
    "s_nop 15\n\t"
DI u32x4 pack8(const f32x16& c, int n) { return (u32x4){cvtpk(c[4 * n], c[4 * n + 1]), cvtpk(c[4 * n + 2], c[4 * n + 3]), cvtpk(c[4 * n + 4], c[4 * n + 5]), cvtpk(c[4 * n + 6], c[4 * n + 7])}; }
DI void add8(f32x16& c, int n, u32x4 w) {
#pragma unroll
    for (int i = 0; i < 4; ++i) { c[4 * n + 2 * i] += bflo(w[i]); c[4 * n + 2 * i + 1] += bfhi(w[i]); }
}
DI void poll(unsigned* flags, int first, int count, int lane) {
    gu32* f = (gu32*)(flags + (first + (lane % count)) * 32);
    for (int spin = 0; spin < (1 << 20); ++spin) {
        if (__all(__hip_atomic_load(f, __ATOMIC_RELAXED, __HIP_MEMORY_SCOPE_AGENT) == 1u)) break;
        __builtin_amdgcn_s_sleep(1);
    }
}
__global__ __launch_bounds__(512, 2) void k3_out(const char* __restrict__ ctx, const char* __restrict__ H, const bf16_t* __restrict__ wo, const bf16_t* __restrict__ w2,
                                                 const bf16_t* __restrict__ b2, const bf16_t* __restrict__ x, float* part, unsigned* flags, bf16_t* __restrict__ out) {
    extern __shared__ __attribute__((aligned(16))) unsigned char smem[];
    const int tid = threadIdx.x, wid = __builtin_amdgcn_readfirstlane(tid >> 6), lane = tid & 63, wr = wid >> 1, wc = wid & 1, fr = lane & 15, fq = lane >> 4;
    const int xcd = blockIdx.x & 7, idx = blockIdx.x >> 3, sl = xcd >> 1, mt = 4 * (xcd & 1) + (idx >> 3), ntile = idx & 7, tile = mt * 8 + ntile;
    const unsigned lds0 = (unsigned)(uintptr_t)smem;
    const size_t aw = (size_t)(wid & 3) * 4096, bw = (size_t)ntile * 256 + (size_t)(8 * (wid >> 1)) * (DM * 2) + 128 * (wid & 1);
    const char* ctxw = ctx + (size_t)(2 * mt + (wid >> 2)) * 64 * 16384 + aw; const char* hw = H + (size_t)(2 * mt + (wid >> 2)) * 56 * 16384 + aw;
    const char* wow = (const char*)wo + bw; const char* w2w = (const char*)w2 + bw;
    const int kt0 = sl == 0 ? 0 : sl == 1 ? 30 : sl == 2 ? 60 : 26;
    const char* a1 = (sl == 3 ? hw : ctxw) + (size_t)kt0 * 16384; const char* b1 = (sl == 3 ? w2w : wow) + (size_t)kt0 * (64 * DM * 2);
    const int nl = sl == 2 ? 1 : 27, nl2 = sl == 2 ? 26 : 0;
    const unsigned va = lane * 16, vb = ((lane & 31) >> 2) * (DM * 2) + (32 * (lane >> 5) + 8 * ((lane & 3) ^ (wid & 2))) * 2;
    const unsigned mA = lds0 + (wid >> 2) * 16384 + (wid & 3) * 4096, mB = lds0 + 32768 + wid * 1024;
    const unsigned ra = lds0 + (wr >> 1) * 16384 + fq * 2048 + ((wr & 1) * 64 + fr) * 16;
    const unsigned rb0 = lds0 + 32768 + fq * 2048 + wc * 1024 + (fr >> 2) * 64 + (fq & 1) * 32 + (fr & 3) * 8, rb1 = rb0 ^ 32;
    f32x16 c0 = f32x16{}, c1 = f32x16{}, c2 = f32x16{}, c3 = f32x16{};
    u32x2 xb[4], bb[4];
#pragma unroll
    for (int n = 0; n < 4; ++n) { bb[n] = *(const u32x2*)(b2 + ntile * 128 + wc * 64 + fq * 4 + n * 16);
        xb[n] = *(const u32x2*)(x + (size_t)(mt * 256 + wr * 64 + sl * 16 + fr) * DM + ntile * 128 + wc * 64 + fq * 4 + n * 16); }
    STAMP(3, 0);
    const unsigned cpf = code_pf(tid, 36);
    asm volatile(K3_ASM
                 : "+&{v[0:15]}"(c0), "+&{v[16:31]}"(c1), "+&{v[32:47]}"(c2), "+&{v[48:63]}"(c3)
                 : "{v134}"(ra), "{v135}"(rb0), "{v136}"(rb1),
                   [va] "v"(va), [vb] "v"(vb), [a1] "s"(a1), [b1] "s"(b1), [nl] "s"(nl), [mA] "s"(mA), [mB] "s"(mB), [a2] "s"(hw), [b2] "s"(w2w), [nl2] "s"(nl2)
                 : "v64", "v65", "v66", "v67", "v68", "v69", "v70", "v71", "v72", "v73", "v74", "v75", "v76", "v77", "v78", "v79", "v80", "v81", "v82", "v83", "v84", "v85", "v86", "v87", "v88", "v89", "v90", "v91", "v92", "v93", "v94", "v95", "v96", "v97", "v98", "v99", "v100", "v101", "v102", "v103", "v104", "v105", "v106", "v107", "v108", "v109", "v110", "v111", "v112", "v113", "v114", "v115", "v116", "v117", "v118", "v119", "v120", "v121", "v122", "v123", "v124", "v125", "v126", "v127", "v128", "v129", "v130", "v131", "v132", "v133", "s60", "s61", "s62", "s63", "s64", "s65", "s66", "s67", "s68", "s69", "s70", "scc", "memory");
    STAMP(3, 1);
    CODE_PF_DONE(cpf);
    const auto rs = __builtin_amdgcn_make_buffer_rsrc((void*)((char*)part + (size_t)tile * 4 * 65536), 0, 4 * 65536, 0x00020000);
#define K3_SEND(c, m) do { __builtin_amdgcn_raw_buffer_store_b128(pack8(c, 0), rs, tid * 16 + (m) * 16384 + sl * 65536, 0, 16); __builtin_amdgcn_raw_buffer_store_b128(pack8(c, 2), rs, tid * 16 + (m) * 16384 + 8192 + sl * 65536, 0, 16); } while (0)
#define K3_GET(src, m, p) __builtin_amdgcn_raw_buffer_load_b128(rs, tid * 16 + (m) * 16384 + (p) * 8192, (src) * 65536, 16)
    gu32* myflag = (gu32*)(flags + (tile * 4 + sl) * 32);
    if (sl != 0) K3_SEND(c0, 0);
    if (sl != 1) K3_SEND(c1, 1);
    if (sl != 2) K3_SEND(c2, 2);
    if (sl != 3) K3_SEND(c3, 3);
    f32x16 f0 = sl == 0 ? c0 : sl == 1 ? c1 : sl == 2 ? c2 : c3;
    WAIT_V(0);
    __syncthreads();
    if (tid == 0) __hip_atomic_store(myflag, 1u, __ATOMIC_RELAXED, __HIP_MEMORY_SCOPE_AGENT);
    STAMP(3, 2);
#pragma unroll
    for (int n = 0; n < 4; ++n) {
        f0[4 * n] += bflo(xb[n][0]) + bflo(bb[n][0]); f0[4 * n + 1] += bfhi(xb[n][0]) + bfhi(bb[n][0]);
        f0[4 * n + 2] += bflo(xb[n][1]) + bflo(bb[n][1]); f0[4 * n + 3] += bfhi(xb[n][1]) + bfhi(bb[n][1]);
    }
    if (wid == 0) poll(flags, tile * 4, 4, lane);
    asm volatile("" ::: "memory"); BAR(); asm volatile("" ::: "memory");
    STAMP(3, 3);
    u32x4 pa[3][2];
#pragma unroll
    for (int k = 0; k < 3; ++k) { const int src = (sl + 1 + k) & 3; pa[k][0] = K3_GET(src, sl, 0); pa[k][1] = K3_GET(src, sl, 1); }
#pragma unroll
    for (int k = 0; k < 3; ++k) { add8(f0, 0, pa[k][0]); add8(f0, 2, pa[k][1]); }
    const int row = mt * 256 + wr * 64 + sl * 16 + fr;
    const int cg = ((fq & 1) << 1) | (fq >> 1);
    bf16_t* orow = out + (size_t)row * DM + ntile * 128 + wc * 64 + cg * 8;
#define K3_F4(c, n) (f32x4){c[4 * (n)], c[4 * (n) + 1], c[4 * (n) + 2], c[4 * (n) + 3]}
    *(u32x4*)(orow) = k1::pack_swap16(K3_F4(f0, 0), K3_F4(f0, 1)); *(u32x4*)(orow + 32) = k1::pack_swap16(K3_F4(f0, 2), K3_F4(f0, 3));
    STAMP(3, 4);
}
}

extern "C" void kernel_launch(void* const* d_in, const int* in_sizes, int n_in, void* d_out, int out_size, void* d_ws, size_t ws_size, hipStream_t stream) {
    const bf16_t* x = (const bf16_t*)d_in[0]; const bf16_t* ckv = (const bf16_t*)d_in[1];
    const bf16_t* wq = (const bf16_t*)d_in[2]; const bf16_t* wuk = (const bf16_t*)d_in[3]; const bf16_t* wuv = (const bf16_t*)d_in[4]; const bf16_t* wo = (const bf16_t*)d_in[5];
    const bf16_t* w1 = (const bf16_t*)d_in[7]; const bf16_t* b1 = (const bf16_t*)d_in[8]; const bf16_t* w2 = (const bf16_t*)d_in[9]; const bf16_t* b2 = (const bf16_t*)d_in[10];
    char* ws = (char*)d_ws;
    (void)hipFuncSetAttribute((const void*)k1::k1_kv, hipFuncAttributeMaxDynamicSharedMemorySize, k1::LDS_BYTES);
    (void)hipFuncSetAttribute((const void*)k2::k2_attn, hipFuncAttributeMaxDynamicSharedMemorySize, k2::LDS_BYTES);
    (void)hipFuncSetAttribute((const void*)k3::k3_out, hipFuncAttributeMaxDynamicSharedMemorySize, k3::LDS_BYTES);
    k1::k1_kv<<<256, 512, k1::LDS_BYTES, stream>>>(x, ckv, wuk, wuv, ws + WS_XN, ws + WS_K, ws + WS_VT, (unsigned*)(ws + WS_FLAG));
    k2::k2_attn<<<256, 512, k2::LDS_BYTES, stream>>>(ws + WS_XN, (const char*)wq, (const char*)w1, b1, ws + WS_K, ws + WS_VT, ws + WS_H, ws + WS_CTX, (const char*)wo, (const char*)w2);
    k3::k3_out<<<256, 512, k3::LDS_BYTES, stream>>>(ws + WS_CTX, ws + WS_H, wo, w2, b2, x, (float*)(ws + WS_PART), (unsigned*)(ws + WS_FLAG), (bf16_t*)d_out);
}
```
